```python
import torch

def _softmax(x, axis=-1):
    return torch.softmax(x, dim=axis)
def _gelu(x):
    return torch.nn.functional.gelu(x, approximate="tanh")
def _silu(x):
    return torch.nn.functional.silu(x)
def _sigmoid(x):
    return torch.sigmoid(x)
def _layernorm(x, axis=-1, eps=1e-5):
    m = x.mean(dim=axis, keepdim=True)
    v = x.var(dim=axis, keepdim=True, unbiased=False)
    return (x - m) / torch.sqrt(v + eps)
def _scatter_add(tgt, idx, src, axis=0):
    return tgt.clone().index_add_(axis, idx, src)
def _segment_sum(src, seg, n):
    out = torch.zeros((n,) + src.shape[1:], dtype=src.dtype)
    return out.index_add_(0, seg, src)
def _pad(x, pw):
    flat = tuple(v for pair in reversed(pw) for v in pair)
    return torch.nn.functional.pad(x, flat)

kernel_name = 'gated_mix__grouped_kv_mqa__moe_arch_mod__prompt'

def setup_inputs():

    _w = (lambda a: (lambda t: t.to(torch.bfloat16) if t.is_floating_point() else t)(torch.as_tensor(a)))
    def _A_setup():
        B, S, D, H = 1, 2048, 512, 32
        Dh = D // H
        return {
            "x": _w((lambda *s: torch.randn(*s, dtype=torch.bfloat16) * 0.05)(B, S, D)),
            "wq": _w((lambda *s: torch.randn(*s, dtype=torch.bfloat16) * 1.6)(D, H * Dh)),
            "wk": _w((lambda *s: torch.randn(*s, dtype=torch.bfloat16) * 1.6)(D, Dh)),
            "wv": _w((lambda *s: torch.randn(*s, dtype=torch.bfloat16) * 0.1)(D, Dh)),
            "wo": _w((lambda *s: torch.randn(*s, dtype=torch.bfloat16) * 0.1)(H * Dh, D)),
        }
    _A_in = _A_setup()

    def _B_setup():
        B, S, D, F, C = 1, 2048, 512, 1792, 1
        return {
            "x": _w((lambda *s: torch.randn(*s, dtype=torch.bfloat16) * 0.05)(B, S, D)),
            "w_r": _w((lambda *s: torch.randn(*s, dtype=torch.bfloat16) * 3.2)(D)),
            "w_gate": _w((lambda *s: torch.randn(*s, dtype=torch.bfloat16) * 0.2)(D, F)),
            "w_up": _w((lambda *s: torch.randn(*s, dtype=torch.bfloat16) * 0.2)(D, F)),
            "w_down": _w((lambda *s: torch.randn(*s, dtype=torch.bfloat16) * 0.2)(F, D)),
        }
    _B_in = _B_setup()

    return {
        "A_x": _A_in["x"],
        "A_wq": _A_in["wq"],
        "A_wk": _A_in["wk"],
        "A_wv": _A_in["wv"],
        "A_wo": _A_in["wo"],
        "B_x": _B_in["x"],
        "B_w_r": _B_in["w_r"],
        "B_w_gate": _B_in["w_gate"],
        "B_w_up": _B_in["w_up"],
        "B_w_down": _B_in["w_down"],
        "gate_w": _w((lambda *s: torch.randn(*s, dtype=torch.bfloat16) * 0.05)(512) * 16.0),
    }

def reference(A_x, A_wq, A_wk, A_wv, A_wo, B_x, B_w_r, B_w_gate, B_w_up, B_w_down, gate_w):
    def _A_ref(x, wq, wk, wv, wo):
        B, S, D = x.shape
        H = 32
        Dh = D // H
        q = (lambda x, *d: x.permute(*d))((x @ wq).reshape(B, S, H, Dh), 0, 2, 1, 3)
        k = x @ wk
        v = x @ wv
        scores = torch.einsum("bhqd,bkd->bhqk", q, k) * (Dh ** -0.5)
        p = _softmax(scores, axis=-1)
        o = torch.einsum("bhqk,bkd->bhqd", p, v)
        return (lambda x, *d: x.permute(*d))(o, 0, 2, 1, 3).reshape(B, S, H * Dh) @ wo

    def _B_ref(x, w_r, w_gate, w_up, w_down):
        B, S, D = x.shape
        C = 1
        r = x @ w_r
        idx = torch.argsort(-r, axis=-1)[:, :C]
        idx_e = (lambda x, n, axis: torch.repeat_interleave(x, n, dim=axis))(idx.reshape(B, C, 1), D, axis=2)
        x_sel = (lambda x, i, axis: torch.take_along_dim(x, i, dim=axis))(x, idx_e, axis=1)
        g = _silu(x_sel @ w_gate)
        u = x_sel @ w_up
        y = (g * u) @ w_down
        r_sel = _sigmoid((lambda x, i, axis: torch.take_along_dim(x, i, dim=axis))(r, idx, axis=1))
        y = y * r_sel.reshape(B, C, 1)
        b_off = (torch.arange(B) * S).reshape(B, 1)
        flat_idx = (idx + b_off).reshape(B * C)
        zero = (x * 0.0).reshape(B * S, D)
        upd = _scatter_add(zero, flat_idx, y.reshape(B * C, D), axis=0)
        return x + upd.reshape(B, S, D)

    x = A_x
    g = _sigmoid((x * gate_w).sum(-1, keepdims=True))
    a = _A_ref(x, A_wq, A_wk, A_wv, A_wo)
    b = _B_ref(x, B_w_r, B_w_gate, B_w_up, B_w_down)
    return g * a + (1 - g) * b
```

```cpp
#include <hip/hip_runtime.h>
#include <stdint.h>

typedef unsigned int u32;
typedef unsigned long long u64;
typedef unsigned short u16;
typedef u32 u32x2 __attribute__((ext_vector_type(2)));
typedef u32 u32x4 __attribute__((ext_vector_type(4)));
typedef float f32x2 __attribute__((ext_vector_type(2)));
typedef float f32x4 __attribute__((ext_vector_type(4)));
typedef float f32x16 __attribute__((ext_vector_type(16)));
typedef __bf16 bf16x2 __attribute__((ext_vector_type(2)));
typedef __bf16 bf16x8 __attribute__((ext_vector_type(8)));
typedef short s16x4 __attribute__((ext_vector_type(4)));
#define LDSP __attribute__((address_space(3)))

constexpr u32 WS_E   = 0;
constexpr u32 WS_F1  = 4096;
constexpr u32 WS_F2  = 20480;
constexpr u32 WS_G   = 24576;
constexpr u32 WS_KV  = 65536;
constexpr u32 WS_O   = 262144;
constexpr u32 WS_Y   = 2621440;
constexpr u32 WS_DBG = 4194304;

constexpr u32 L_K   = 0;
constexpr u32 L_V   = 65536;
constexpr u32 L_ONE = 131072;
constexpr u32 L_W   = 135168;
constexpr u32 L_M   = 151552;
constexpr u32 L_P1  = L_M;
constexpr u32 L_PD  = L_M + 8192;
constexpr u32 L_FP  = L_M + 8704;
constexpr u32 L_YP  = L_M + 9216;
constexpr u32 L_CT  = L_M + 11264;
constexpr u32 L_END = 163840;

constexpr u32 SPIN_MAX = 1u << 22;

#define LD128(a) (*(const LDSP u32x4*)(lds + (a)))
#define ST128(a, v) (*(LDSP u32x4*)(lds + (a)) = (v))
#define LD32(a) (*(const LDSP u32*)(lds + (a)))
#define ST32(a, v) (*(LDSP u32*)(lds + (a)) = (v))
#define LDF(a) (*(const LDSP float*)(lds + (a)))
#define STF(a, v) (*(LDSP float*)(lds + (a)) = (v))

__device__ __forceinline__ u32x2 trread(LDSP char* lds, u32 a) {
    typedef short v4s __attribute__((ext_vector_type(4)));
    v4s r = __builtin_amdgcn_ds_read_tr16_b64_v4i16((LDSP v4s*)(lds + a));
    return __builtin_bit_cast(u32x2, r);
}
__device__ __forceinline__ bf16x8 asbf(u32x4 v) { return __builtin_bit_cast(bf16x8, v); }
__device__ __forceinline__ u32x4 cat(u32x2 a, u32x2 b) { u32x4 r = {a[0], a[1], b[0], b[1]}; return r; }
__device__ __forceinline__ u32 pk(float a, float b) {
    f32x2 f = {a, b};
    return __builtin_bit_cast(u32, __builtin_convertvector(f, bf16x2));
}
__device__ __forceinline__ float bfr(float a) {
    return __uint_as_float(pk(a, a) & 0xffff0000u);
}
__device__ __forceinline__ float lo16(u32 v) { return __uint_as_float(v << 16); }
__device__ __forceinline__ float hi16(u32 v) { return __uint_as_float(v & 0xffff0000u); }
__device__ __forceinline__ float sigm(float z) { return __builtin_amdgcn_rcpf(1.0f + __builtin_amdgcn_exp2f(-1.4426950408889634f * z)); }

__device__ __forceinline__ void dma16(const void* base, u32 voff, u32 ldsaddr) {
    asm volatile("s_mov_b32 m0, %0\n\ts_nop 0\n\tglobal_load_lds_dwordx4 %1, %2" :: "s"(ldsaddr), "v"(voff), "s"(base) : "memory");
}
__device__ __forceinline__ void dma16c(const void* base, u32 voff, u32 ldsaddr) {
    asm volatile("s_mov_b32 m0, %0\n\ts_nop 0\n\tglobal_load_lds_dwordx4 %1, %2 sc1" :: "s"(ldsaddr), "v"(voff), "s"(base) : "memory");
}
#define GLD128(dst, base, voff, OFF)  asm volatile("global_load_dwordx4 %0, %1, %2 offset:" #OFF : "=v"(dst) : "v"(voff), "s"(base) : "memory")
#define GLD128C(dst, base, voff, OFF) asm volatile("global_load_dwordx4 %0, %1, %2 offset:" #OFF " sc1" : "=v"(dst) : "v"(voff), "s"(base) : "memory")
#define GLD64C(dst, base, voff, OFF)  asm volatile("global_load_dwordx2 %0, %1, %2 offset:" #OFF " sc1" : "=v"(dst) : "v"(voff), "s"(base) : "memory")
#define GLD32C(dst, base, voff, OFF)  asm volatile("global_load_dword %0, %1, %2 offset:" #OFF " sc1" : "=v"(dst) : "v"(voff), "s"(base) : "memory")
#define GLD16(dst, base, voff, OFF)   asm volatile("global_load_ushort %0, %1, %2 offset:" #OFF : "=v"(dst) : "v"(voff), "s"(base) : "memory")
#define GST128C(base, voff, val) asm volatile("global_store_dwordx4 %0, %1, %2 sc1\n\ts_nop 1" :: "v"(voff), "v"(val), "s"(base) : "memory")
#define GST64C(base, voff, val)  asm volatile("global_store_dwordx2 %0, %1, %2 sc1" :: "v"(voff), "v"(val), "s"(base) : "memory")
#define GST32C(base, voff, val)  asm volatile("global_store_dword %0, %1, %2 sc1" :: "v"(voff), "v"(val), "s"(base) : "memory")
#define VMWAIT(N) asm volatile("s_waitcnt vmcnt(" #N ")" ::: "memory")
#define BAR() asm volatile("s_waitcnt lgkmcnt(0)\n\ts_barrier" ::: "memory")

#define STAMP(i)
#define STAMPDUMP()

__device__ __forceinline__ u32 wave_max_u32(u32 v) {
    v = max(v, (u32)__builtin_amdgcn_update_dpp(0, (int)v, 0x111, 0xf, 0xf, false));
    v = max(v, (u32)__builtin_amdgcn_update_dpp(0, (int)v, 0x112, 0xf, 0xf, false));
    v = max(v, (u32)__builtin_amdgcn_update_dpp(0, (int)v, 0x114, 0xf, 0xf, false));
    v = max(v, (u32)__builtin_amdgcn_update_dpp(0, (int)v, 0x118, 0xf, 0xf, false));
    v = max(v, (u32)__builtin_amdgcn_update_dpp(0, (int)v, 0x142, 0xa, 0xf, false));
    v = max(v, (u32)__builtin_amdgcn_update_dpp(0, (int)v, 0x143, 0xc, 0xf, false));
    return (u32)__builtin_amdgcn_readlane((int)v, 63);
}
#define ATT_BODY4 \
    "v_exp_f32 v64, v64\n" \
    "v_exp_f32 v65, v65\n" \
    "v_smfmac_f32_32x32x32_bf16 v[144:159], v[188:191], v[120:127], v192\n" \
    "ds_read_b128 v[184:187], v194 offset:1024\n" \
    "v_exp_f32 v66, v66\n" \
    "v_exp_f32 v67, v67\n" \
    "v_exp_f32 v68, v68\n" \
    "v_exp_f32 v69, v69\n" \
    "s_waitcnt lgkmcnt(1)\n" \
    "v_mfma_f32_32x32x16_bf16 v[80:95], v[172:175], v[160:163], 0\n" \
    "ds_read_b128 v[188:191], v194 offset:1536\n" \
    "v_exp_f32 v70, v70\n" \
    "v_exp_f32 v71, v71\n" \
    "v_exp_f32 v72, v72\n" \
    "v_exp_f32 v73, v73\n" \
    "v_smfmac_f32_32x32x32_bf16 v[128:143], v[176:179], v[64:71], v192\n" \
    "v_exp_f32 v74, v74\n" \
    "v_exp_f32 v75, v75\n" \
    "v_exp_f32 v76, v76\n" \
    "v_exp_f32 v77, v77\n" \
    "v_exp_f32 v78, v78\n" \
    "v_exp_f32 v79, v79\n" \
    "v_exp_f32 v96, v96\n" \
    "v_exp_f32 v97, v97\n" \
    "v_smfmac_f32_32x32x32_bf16 v[128:143], v[180:183], v[72:79], v192\n" \
    "v_exp_f32 v98, v98\n" \
    "v_exp_f32 v99, v99\n" \
    "v_exp_f32 v100, v100\n" \
    "v_exp_f32 v101, v101\n" \
    "v_mfma_f32_32x32x16_bf16 v[112:127], v[172:175], v[164:167], 0\n" \
    "ds_read_b128 v[168:171], v193 offset:2048\n" \
    "v_exp_f32 v102, v102\n" \
    "v_exp_f32 v103, v103\n" \
    "v_exp_f32 v104, v104\n" \
    "v_exp_f32 v105, v105\n" \
    "v_smfmac_f32_32x32x32_bf16 v[144:159], v[176:179], v[96:103], v192\n" \
    "v_exp_f32 v106, v106\n" \
    "v_exp_f32 v107, v107\n" \
    "v_exp_f32 v108, v108\n" \
    "v_exp_f32 v109, v109\n" \
    "v_exp_f32 v110, v110\n" \
    "v_exp_f32 v111, v111\n" \
    "v_exp_f32 v80, v80\n" \
    "v_exp_f32 v81, v81\n" \
    "v_smfmac_f32_32x32x32_bf16 v[144:159], v[180:183], v[104:111], v192\n" \
    "ds_read_b128 v[176:179], v194 offset:2048\n" \
    "v_exp_f32 v82, v82\n" \
    "v_exp_f32 v83, v83\n" \
    "v_exp_f32 v84, v84\n" \
    "v_exp_f32 v85, v85\n" \
    "s_waitcnt lgkmcnt(1)\n" \
    "v_mfma_f32_32x32x16_bf16 v[64:79], v[168:171], v[160:163], 0\n" \
    "ds_read_b128 v[180:183], v194 offset:2560\n" \
    "v_exp_f32 v86, v86\n" \
    "v_exp_f32 v87, v87\n" \
    "v_exp_f32 v88, v88\n" \
    "v_exp_f32 v89, v89\n" \
    "v_smfmac_f32_32x32x32_bf16 v[128:143], v[184:187], v[80:87], v192\n" \
    "v_exp_f32 v90, v90\n" \
    "v_exp_f32 v91, v91\n" \
    "v_exp_f32 v92, v92\n" \
    "v_exp_f32 v93, v93\n" \
    "v_exp_f32 v94, v94\n" \
    "v_exp_f32 v95, v95\n" \
    "v_exp_f32 v112, v112\n" \
    "v_exp_f32 v113, v113\n" \
    "v_smfmac_f32_32x32x32_bf16 v[128:143], v[188:191], v[88:95], v192\n" \
    "v_exp_f32 v114, v114\n" \
    "v_exp_f32 v115, v115\n" \
    "v_exp_f32 v116, v116\n" \
    "v_exp_f32 v117, v117\n" \
    "v_mfma_f32_32x32x16_bf16 v[96:111], v[168:171], v[164:167], 0\n" \
    "ds_read_b128 v[172:175], v193 offset:3072\n" \
    "v_exp_f32 v118, v118\n" \
    "v_exp_f32 v119, v119\n" \
    "v_exp_f32 v120, v120\n" \
    "v_exp_f32 v121, v121\n" \
    "v_smfmac_f32_32x32x32_bf16 v[144:159], v[184:187], v[112:119], v192\n" \
    "v_exp_f32 v122, v122\n" \
    "v_exp_f32 v123, v123\n" \
    "v_exp_f32 v124, v124\n" \
    "v_exp_f32 v125, v125\n" \
    "v_exp_f32 v126, v126\n" \
    "v_exp_f32 v127, v127\n" \
    "v_exp_f32 v64, v64\n" \
    "v_exp_f32 v65, v65\n" \
    "v_smfmac_f32_32x32x32_bf16 v[144:159], v[188:191], v[120:127], v192\n" \
    "ds_read_b128 v[184:187], v194 offset:3072\n" \
    "v_exp_f32 v66, v66\n" \
    "v_exp_f32 v67, v67\n" \
    "v_exp_f32 v68, v68\n" \
    "v_exp_f32 v69, v69\n" \
    "s_waitcnt lgkmcnt(1)\n" \
    "v_mfma_f32_32x32x16_bf16 v[80:95], v[172:175], v[160:163], 0\n" \
    "ds_read_b128 v[188:191], v194 offset:3584\n" \
    "v_exp_f32 v70, v70\n" \
    "v_exp_f32 v71, v71\n" \
    "v_exp_f32 v72, v72\n" \
    "v_exp_f32 v73, v73\n" \
    "v_smfmac_f32_32x32x32_bf16 v[128:143], v[176:179], v[64:71], v192\n" \
    "v_exp_f32 v74, v74\n" \
    "v_exp_f32 v75, v75\n" \
    "v_exp_f32 v76, v76\n" \
    "v_exp_f32 v77, v77\n" \
    "v_exp_f32 v78, v78\n" \
    "v_exp_f32 v79, v79\n" \
    "v_exp_f32 v96, v96\n" \
    "v_exp_f32 v97, v97\n" \
    "v_smfmac_f32_32x32x32_bf16 v[128:143], v[180:183], v[72:79], v192\n" \
    "v_exp_f32 v98, v98\n" \
    "v_exp_f32 v99, v99\n" \
    "v_exp_f32 v100, v100\n" \
    "v_exp_f32 v101, v101\n" \
    "v_mfma_f32_32x32x16_bf16 v[112:127], v[172:175], v[164:167], 0\n" \
    "ds_read_b128 v[168:171], v193 offset:4096\n" \
    "v_exp_f32 v102, v102\n" \
    "v_exp_f32 v103, v103\n" \
    "v_exp_f32 v104, v104\n" \
    "v_exp_f32 v105, v105\n" \
    "v_smfmac_f32_32x32x32_bf16 v[144:159], v[176:179], v[96:103], v192\n" \
    "v_exp_f32 v106, v106\n" \
    "v_exp_f32 v107, v107\n" \
    "v_exp_f32 v108, v108\n" \
    "v_exp_f32 v109, v109\n" \
    "v_exp_f32 v110, v110\n" \
    "v_exp_f32 v111, v111\n" \
    "v_exp_f32 v80, v80\n" \
    "v_exp_f32 v81, v81\n" \
    "v_smfmac_f32_32x32x32_bf16 v[144:159], v[180:183], v[104:111], v192\n" \
    "ds_read_b128 v[176:179], v194 offset:4096\n" \
    "v_exp_f32 v82, v82\n" \
    "v_exp_f32 v83, v83\n" \
    "v_exp_f32 v84, v84\n" \
    "v_exp_f32 v85, v85\n" \
    "s_waitcnt lgkmcnt(1)\n" \
    "v_mfma_f32_32x32x16_bf16 v[64:79], v[168:171], v[160:163], 0\n" \
    "ds_read_b128 v[180:183], v194 offset:4608\n" \
    "v_exp_f32 v86, v86\n" \
    "v_exp_f32 v87, v87\n" \
    "v_exp_f32 v88, v88\n" \
    "v_exp_f32 v89, v89\n" \
    "v_smfmac_f32_32x32x32_bf16 v[128:143], v[184:187], v[80:87], v192\n" \
    "v_exp_f32 v90, v90\n" \
    "v_exp_f32 v91, v91\n" \
    "v_exp_f32 v92, v92\n" \
    "v_exp_f32 v93, v93\n" \
    "v_exp_f32 v94, v94\n" \
    "v_exp_f32 v95, v95\n" \
    "v_exp_f32 v112, v112\n" \
    "v_exp_f32 v113, v113\n" \
    "v_smfmac_f32_32x32x32_bf16 v[128:143], v[188:191], v[88:95], v192\n" \
    "v_exp_f32 v114, v114\n" \
    "v_exp_f32 v115, v115\n" \
    "v_exp_f32 v116, v116\n" \
    "v_exp_f32 v117, v117\n" \
    "v_mfma_f32_32x32x16_bf16 v[96:111], v[168:171], v[164:167], 0\n" \
    "ds_read_b128 v[172:175], v193 offset:5120\n" \
    "v_exp_f32 v118, v118\n" \
    "v_exp_f32 v119, v119\n" \
    "v_exp_f32 v120, v120\n" \
    "v_exp_f32 v121, v121\n" \
    "v_smfmac_f32_32x32x32_bf16 v[144:159], v[184:187], v[112:119], v192\n" \
    "v_exp_f32 v122, v122\n" \
    "v_exp_f32 v123, v123\n" \
    "v_exp_f32 v124, v124\n" \
    "v_exp_f32 v125, v125\n" \
    "v_exp_f32 v126, v126\n" \
    "v_exp_f32 v127, v127\n" \
    "v_add_u32 v193, 4096, v193\n" \
    "v_add_u32 v194, v195, v194\n"

__device__ __forceinline__ void slow_block(const char* kv, u32x4 q, int lane, float* o, float& lsum) {
    const u32 koff = lane * 16;
    const u32 r32 = lane & 31;
    float m = -3.0e38f;
    for (int t = 0; t < 64; ++t) {
        u32x4 kf;
        GLD128C(kf, kv, koff + t * 2048, 0);
        asm volatile("s_waitcnt vmcnt(0)" : "+v"(kf) :: "memory");
        f32x16 s = {0};
        s = __builtin_amdgcn_mfma_f32_32x32x16_bf16(asbf(kf), asbf(q), s, 0, 0, 0);
#pragma unroll
        for (int i = 0; i < 16; ++i) m = fmaxf(m, s[i]);
    }
    {
        auto sw = __builtin_amdgcn_permlane32_swap(__float_as_uint(m), __float_as_uint(m), false, false);
        m = fmaxf(__uint_as_float(sw[0]), __uint_as_float(sw[1]));
    }
    f32x16 acc = {0};
    const u32 voff = 1024 + (lane >> 5) * 256 + (lane & 15) * 16;
    for (int t = 0; t < 64; ++t) {
        u32x4 kf, v0, v1;
        GLD128C(kf, kv, koff + t * 2048, 0);
        GLD128C(v0, kv, voff + t * 2048, 0);
        GLD128C(v1, kv, voff + t * 2048, 512);
        asm volatile("s_waitcnt vmcnt(0)" : "+v"(kf), "+v"(v0), "+v"(v1) :: "memory");
        if (r32 >= 16) { v0 = (u32x4){0x3f803f80u, 0x3f803f80u, 0x3f803f80u, 0x3f803f80u}; v1 = v0; }
        f32x16 s = {0};
        s = __builtin_amdgcn_mfma_f32_32x32x16_bf16(asbf(kf), asbf(q), s, 0, 0, 0);
        u32 p[8];
#pragma unroll
        for (int i = 0; i < 8; ++i)
            p[i] = pk(__builtin_amdgcn_exp2f(s[2 * i] - m), __builtin_amdgcn_exp2f(s[2 * i + 1] - m));
#pragma unroll
        for (int st = 0; st < 2; ++st) {
            u32x4 vv = st ? v1 : v0;
            auto a = __builtin_amdgcn_permlane32_swap(vv[0], vv[2], false, false);
            auto c = __builtin_amdgcn_permlane32_swap(vv[1], vv[3], false, false);
            u32x4 af = {a[0], c[0], a[1], c[1]};
            u32x4 bf = {p[4 * st], p[4 * st + 1], p[4 * st + 2], p[4 * st + 3]};
            acc = __builtin_amdgcn_mfma_f32_32x32x16_bf16(asbf(af), asbf(bf), acc, 0, 0, 0);
        }
    }
#pragma unroll
    for (int i = 0; i < 8; ++i) o[i] = acc[i];
    lsum = acc[8];
}

__global__ void __launch_bounds__(512, 2)
fused_kernel(const u16* __restrict__ x, const u16* __restrict__ wq, const u16* __restrict__ wk, const u16* __restrict__ wv,
             const u16* __restrict__ wo, const u16* __restrict__ wr, const u16* __restrict__ wg, const u16* __restrict__ wu,
             const u16* __restrict__ wd, const u16* __restrict__ gw, u16* __restrict__ out, char* __restrict__ ws)
{
    __shared__ __attribute__((aligned(1024))) char smem[L_END];
    LDSP char* lds = (LDSP char*)smem;
    const u32 lb = (u32)(size_t)lds;
    const int tid = threadIdx.x;
    const int lane = tid & 63;
    const int w = __builtin_amdgcn_readfirstlane(tid >> 6);
    const int b = blockIdx.x;
    const int xcd = b & 7, jj = b >> 3;
    const int tb = xcd * 4 + (jj >> 3), hg = jj & 7;
    const int g = lane >> 4, l15 = lane & 15, h = lane >> 5, l31 = lane & 31;
    const bool isK = hg < 4;
    const bool ffn = jj < 28;
    const int col0 = (xcd * 28 + jj) * 8;
    STAMP(1);

    u32 epo;
    { u32 z = 0; GLD32C(epo, ws, z, 0); }
    const int T0 = tb * 64 + (hg & 3) * 16;
    u32x4 xf0, xf1, vf0, vf1;
    {
        const u16* wsrc = isK ? wk : wv;
#pragma unroll
        for (int i = 0; i < 2; ++i) {
            u32 slot = 64 * i + lane, pos = slot >> 3, B = pos ^ ((pos >> 2) & 1);
            u32 row = 4 * B + ((slot >> 1) & 3);
            dma16(wsrc, (64 * w + row) * 32 + (slot & 1) * 16, lb + L_W + w * 2048 + i * 1024);
        }
        u32 xo = (T0 + l15) * 1024 + (64 * w + 16 * g) * 2;
        GLD128(xf0, x, xo, 0);
        GLD128(xf1, x, xo, 16);
        const u16* vec = isK ? wr : gw;
        u32 vo = (64 * w + 16 * g) * 2;
        GLD128(vf0, vec, vo, 0);
        GLD128(vf1, vec, vo, 16);
    }
    if (w < 7) {
        for (int p = w; p < 128; p += 7) {
            if (p < 64) {
                dma16(x, (tb * 64 + p) * 1024 + ((lane ^ (p & 15)) << 4), lb + L_K + p * 1024);
            } else {
                int G = p - 64;
                dma16(wq, (8 * G + ((lane >> 2) & 7)) * 1024 + hg * 128 + (lane >> 5) * 64 + (lane & 3) * 16, lb + L_V + G * 1024);
            }
        }
    }
    if (tid < 256) ST128(L_ONE + tid * 16, ((u32x4){0x3f803f80u, 0x3f803f80u, 0x3f803f80u, 0x3f803f80u}));
    asm volatile("s_cmp_eq_u32 %[w], 7\n\ts_cbranch_scc1 1f\n\ts_waitcnt vmcnt(18)\n\ts_branch 2f\n1:\n\ts_waitcnt vmcnt(0)\n2:"
                 : "+v"(xf0), "+v"(xf1), "+v"(vf0), "+v"(vf1), "+v"(epo) : [w] "s"(w) : "memory", "scc");
    STAMP(2);
    {
        const u32 ta = L_W + w * 2048 + l15 * 8;
        const u32 gx = g & 1;
        u32x4 wf0 = cat(trread(lds, ta + ((4 * g + 0) ^ gx) * 128), trread(lds, ta + ((4 * g + 1) ^ gx) * 128));
        u32x4 wf1 = cat(trread(lds, ta + ((4 * g + 2) ^ gx) * 128), trread(lds, ta + ((4 * g + 3) ^ gx) * 128));
        f32x4 acc = {0, 0, 0, 0}, dacc = {0, 0, 0, 0};
        if (isK) {
            acc = __builtin_amdgcn_mfma_f32_16x16x32_bf16(asbf(wf0), asbf(xf0), acc, 0, 0, 0);
            acc = __builtin_amdgcn_mfma_f32_16x16x32_bf16(asbf(wf1), asbf(xf1), acc, 0, 0, 0);
        } else {
            acc = __builtin_amdgcn_mfma_f32_16x16x32_bf16(asbf(xf0), asbf(wf0), acc, 0, 0, 0);
            acc = __builtin_amdgcn_mfma_f32_16x16x32_bf16(asbf(xf1), asbf(wf1), acc, 0, 0, 0);
        }
        dacc = __builtin_amdgcn_mfma_f32_16x16x32_bf16(asbf(vf0), asbf(xf0), dacc, 0, 0, 0);
        dacc = __builtin_amdgcn_mfma_f32_16x16x32_bf16(asbf(vf1), asbf(xf1), dacc, 0, 0, 0);
        ST128(L_P1 + w * 1024 + lane * 16, __builtin_bit_cast(u32x4, acc));
        if (g == 0) STF(L_PD + w * 64 + l15 * 4, dacc[0]);
    }
    BAR();
    STAMP(3);
    u32 epoch = 0;
    if (w == 7) {
        epoch = epo + 1;
        f32x4 s = __builtin_bit_cast(f32x4, LD128(L_P1 + lane * 16));
        float d = LDF(L_PD + l15 * 4);
#pragma unroll
        for (int i = 1; i < 8; ++i) {
            s += __builtin_bit_cast(f32x4, LD128(L_P1 + i * 1024 + lane * 16));
            d += LDF(L_PD + i * 64 + l15 * 4);
        }
        u32x2 pv = {pk(s[0], s[1]), pk(s[2], s[3])};
        const int tile = T0 >> 5, sub = (T0 >> 4) & 1;
        u32 key = 0;
        if (isK) {
            u32 off = WS_KV + tile * 2048 + ((g & 1) * 32 + sub * 16 + l15) * 16 + (g >> 1) * 8;
            GST64C(ws, off, pv);
            u32 rb = pk(d, d) & 0xffffu;
            u32 ord = (rb & 0x8000u) ? (~rb & 0xffffu) : (rb | 0x8000u);
            key = wave_max_u32((ord << 16) | (0xffffu - (u32)(T0 + l15)));
        } else {
            u32 off = WS_KV + tile * 2048 + 1024 + sub * 512 + (g >> 1) * 256 + l15 * 16 + (g & 1) * 8;
            GST64C(ws, off, pv);
            if (g == 0) { u32 go = WS_G + (T0 + l15) * 4; u32 dv = __float_as_uint(d); GST32C(ws, go, dv); }
        }
        VMWAIT(0);
        if (lane < 8) { u32x2 fl = {key, epoch}; u32 fo = WS_F1 + lane * 2048 + b * 8; GST64C(ws, fo, fl); }
        STAMP(4);
    } else {
        VMWAIT(0);
    }
    BAR();
    STAMP(5);

    u32x4 qx, qy;
    if (w < 4) {
        const int th = w & 1, hp = w >> 1;
        f32x16 acc = {0};
        const int tok = th * 32 + l31;
        const u32 abase = L_V + h * 1024 + hp * 512 + ((l15 >> 2) * 64) + ((lane >> 4) & 1) * 32 + (l15 & 3) * 8;
        const u32 bbase = L_K + tok * 1024;
#pragma unroll
        for (int s = 0; s < 32; ++s) {
            u32x4 af = cat(trread(lds, abase + s * 2048), trread(lds, abase + s * 2048 + 256));
            u32x4 bf = LD128(bbase + (s >> 3) * 256 + ((((2 * s + h) & 15) ^ (tok & 15)) << 4));
            acc = __builtin_amdgcn_mfma_f32_32x32x16_bf16(asbf(af), asbf(bf), acc, 0, 0, 0);
        }
        const float qs = 0.25f * 1.4426950408889634f;
#pragma unroll
        for (int i = 0; i < 4; ++i) {
            u32 a = pk(acc[2 * i], acc[2 * i + 1]), c = pk(acc[8 + 2 * i], acc[8 + 2 * i + 1]);
            qx[i] = pk(lo16(a) * qs, hi16(a) * qs);
            qy[i] = pk(lo16(c) * qs, hi16(c) * qs);
        }
        ST128(L_P1 + w * 2048 + lane * 16, qx);
        ST128(L_P1 + w * 2048 + 1024 + lane * 16, qy);
        STAMP(6);
    } else {
        if (w < 6 && ffn) {
#pragma unroll
            for (int i = 0; i < 8; ++i) {
                int pc = (w - 4) * 8 + i;
                u32 slot = 64 * (pc & 1) + lane, pos = slot >> 3, B = pos ^ ((pos >> 2) & 1);
                u32 row = 64 * (pc >> 1) + 4 * B + ((slot >> 1) & 3);
                u64 src = (u64)((slot & 1) ? wu : wg) + (u64)row * 3584 + col0 * 2;
                asm volatile("s_mov_b32 m0, %0\n\ts_nop 0\n\tglobal_load_lds_dwordx4 %1, off" :: "s"(lb + L_W + pc * 1024), "v"(src) : "memory");
            }
        }
        if (w == 7) {
            u32 fo = WS_F1 + xcd * 2048 + lane * 32;
            u32x4 f0, f1;
            u32 spins = 0;
            while (true) {
                GLD128C(f0, ws, fo, 0);
                GLD128C(f1, ws, fo, 16);
                asm volatile("s_waitcnt vmcnt(0)" : "+v"(f0), "+v"(f1) :: "memory");
                bool ok = (f0[1] == epoch) & (f0[3] == epoch) & (f1[1] == epoch) & (f1[3] == epoch);
                if (__all(ok) || ++spins > SPIN_MAX) break;
                __builtin_amdgcn_s_sleep(1);
            }
            u32 key = wave_max_u32(max(max(f0[0], f0[2]), max(f1[0], f1[2])));
            if (lane == 0) {
                ST32(L_CT, key); ST32(L_CT + 4, epoch);
                if (b == 0) { u32 z = 0; GST32C(ws, z, epoch); }
            }
            STAMP(7);
        }
        if (w < 6) VMWAIT(0);
    }
    BAR();
    STAMP(8);
    const u32 key = __builtin_amdgcn_readfirstlane(LD32(L_CT));
    epoch = __builtin_amdgcn_readfirstlane(LD32(L_CT + 4));
    const int sel = 0xffff - (key & 0xffff);
    const u32 rord = key >> 16;
    const float rsel = __uint_as_float(((rord & 0x8000u) ? (rord & 0x7fffu) : (~rord & 0xffffu)) << 16);

    u32x4 xs0, xs1;
    {
        u32 so = (sel & 2047) * 1024 + (64 * w + 16 * g) * 2;
        GLD128(xs0, x, so, 0);
        GLD128(xs1, x, so, 16);
    }
    const int hf = w >> 2, jw = w & 3;
    {
        const char* kvb = ws + WS_KV;
#pragma unroll
        for (int r = 0; r < 16; ++r) {
            int m = 4 * r + jw;
            int isV = ((m & 1) == 0 && m != 0) || m == 63;
            int t = m == 63 ? 31 : (isV ? (m >> 1) - 1 : (m + 1) >> 1);
            t += 32 * hf;
            dma16c(kvb, t * 2048 + isV * 1024 + lane * 16, lb + (isV ? L_V : L_K) + t * 1024);
        }
    }
    u32 wd0, wd1, wd2, wd3, wd4, wd5, wd6, wd7;
    {
        const u32 wdo = (xcd * 28 + min(jj, 27)) * 8192 + (64 * w + lane) * 2, o2 = wdo + 4096;
        GLD16(wd0, wd, wdo, 0); GLD16(wd1, wd, wdo, 1024); GLD16(wd2, wd, wdo, 2048); GLD16(wd3, wd, wdo, 3072);
        GLD16(wd4, wd, o2, 0); GLD16(wd5, wd, o2, 1024); GLD16(wd6, wd, o2, 2048); GLD16(wd7, wd, o2, 3072);
    }
    if (w >= 4) {
        qx = LD128(L_P1 + (w - 4) * 2048 + lane * 16);
        qy = LD128(L_P1 + (w - 4) * 2048 + 1024 + lane * 16);
    }
    asm volatile("s_waitcnt vmcnt(21)" : "+v"(xs0), "+v"(xs1) :: "memory");
    STAMP(9);
    if (ffn) {
        const u32 ta = L_W + w * 2048 + l15 * 8;
        const u32 gx = g & 1;
        u32x4 wf0 = cat(trread(lds, ta + ((4 * g + 0) ^ gx) * 128), trread(lds, ta + ((4 * g + 1) ^ gx) * 128));
        u32x4 wf1 = cat(trread(lds, ta + ((4 * g + 2) ^ gx) * 128), trread(lds, ta + ((4 * g + 3) ^ gx) * 128));
        f32x4 acc = {0, 0, 0, 0};
        acc = __builtin_amdgcn_mfma_f32_16x16x32_bf16(asbf(wf0), asbf(xs0), acc, 0, 0, 0);
        acc = __builtin_amdgcn_mfma_f32_16x16x32_bf16(asbf(wf1), asbf(xs1), acc, 0, 0, 0);
        if (l15 == 0) ST128(L_FP + w * 64 + g * 16, __builtin_bit_cast(u32x4, acc));
    }
    BAR();
    STAMP(10);
    STAMP(11);

    f32x16 SX0, SX1, SY0, SY1, OX, OY;
    u32x4 K0, K1, V00, V01, V10, V11;
    u32 idx = 0xDDDDu;
    u32 ka = lb + L_K + hf * 32768 + lane * 16;
    const bool vlane = l31 < 16;
    u32 va = vlane ? (lb + L_V + hf * 32768 + h * 256 + l15 * 16) : (lb + L_ONE - 1024 + h * 256 + l15 * 16);
    u32 vinc = vlane ? 4096u : 0u;
    {
        u32 va0 = vlane ? va : va + 1024;
        asm volatile(
            "ds_read_b128 v[168:171], v193\n"
            "ds_read_b128 v[172:175], v193 offset:1024\n"
            "ds_read_b128 v[176:179], %[va0]\n"
            "ds_read_b128 v[180:183], %[va0] offset:512\n"
            "v_mov_b32 v128, 0\n v_mov_b32 v129, 0\n v_mov_b32 v130, 0\n v_mov_b32 v131, 0\n"
            "v_mov_b32 v132, 0\n v_mov_b32 v133, 0\n v_mov_b32 v134, 0\n v_mov_b32 v135, 0\n"
            "v_mov_b32 v136, 0\n v_mov_b32 v137, 0\n v_mov_b32 v138, 0\n v_mov_b32 v139, 0\n"
            "v_mov_b32 v140, 0\n v_mov_b32 v141, 0\n v_mov_b32 v142, 0\n v_mov_b32 v143, 0\n"
            "v_mov_b32 v144, 0\n v_mov_b32 v145, 0\n v_mov_b32 v146, 0\n v_mov_b32 v147, 0\n"
            "v_mov_b32 v148, 0\n v_mov_b32 v149, 0\n v_mov_b32 v150, 0\n v_mov_b32 v151, 0\n"
            "v_mov_b32 v152, 0\n v_mov_b32 v153, 0\n v_mov_b32 v154, 0\n v_mov_b32 v155, 0\n"
            "v_mov_b32 v156, 0\n v_mov_b32 v157, 0\n v_mov_b32 v158, 0\n v_mov_b32 v159, 0\n"
            "v_mov_b32 v120, 0\n v_mov_b32 v121, 0\n v_mov_b32 v122, 0\n v_mov_b32 v123, 0\n"
            "v_mov_b32 v124, 0\n v_mov_b32 v125, 0\n v_mov_b32 v126, 0\n v_mov_b32 v127, 0\n"
            "v_mov_b32 v188, 0\n v_mov_b32 v189, 0\n v_mov_b32 v190, 0\n v_mov_b32 v191, 0\n"
            "s_waitcnt lgkmcnt(0)\n"
            "v_mfma_f32_32x32x16_bf16 v[64:79], v[168:171], v[160:163], 0\n"
            "v_mfma_f32_32x32x16_bf16 v[96:111], v[168:171], v[164:167], 0\n"
            "s_nop 7\n s_nop 7\n"
            : "={v[64:79]}"(SX0), "={v[80:95]}"(SX1), "={v[96:111]}"(SY0), "={v[112:127]}"(SY1),
              "={v[128:143]}"(OX), "={v[144:159]}"(OY),
              "={v[168:171]}"(K0), "={v[172:175]}"(K1), "={v[176:179]}"(V00), "={v[180:183]}"(V01),
              "={v[184:187]}"(V10), "={v[188:191]}"(V11)
            : "{v[160:163]}"(qx), "{v[164:167]}"(qy), "{v193}"(ka), [va0] "v"(va0)
            : "memory");
    }
#define ATT_RUN(N) do { u32 cnt_; asm volatile("s_mov_b32 %[cnt], " #N "\n1:\n" ATT_BODY4 "s_sub_u32 %[cnt], %[cnt], 1\ns_cmp_lg_u32 %[cnt], 0\ns_cbranch_scc1 1b\n" \
            : "+{v[64:79]}"(SX0), "+{v[80:95]}"(SX1), "+{v[96:111]}"(SY0), "+{v[112:127]}"(SY1), \
              "+{v[128:143]}"(OX), "+{v[144:159]}"(OY), \
              "+{v[168:171]}"(K0), "+{v[172:175]}"(K1), "+{v[176:179]}"(V00), "+{v[180:183]}"(V01), \
              "+{v[184:187]}"(V10), "+{v[188:191]}"(V11), "+{v193}"(ka), "+{v194}"(va), [cnt] "=&s"(cnt_) \
            : "{v[160:163]}"(qx), "{v[164:167]}"(qy), "{v192}"(idx), "{v195}"(vinc) \
            : "memory", "scc"); } while (0)
    ATT_RUN(1);
    VMWAIT(19); BAR();
    ATT_RUN(1);
    asm volatile("s_waitcnt vmcnt(0)" : "+v"(wd0), "+v"(wd1), "+v"(wd2), "+v"(wd3), "+v"(wd4), "+v"(wd5), "+v"(wd6), "+v"(wd7) :: "memory");
    BAR();
    if (ffn) {
        float sum = LDF(L_FP + l15 * 4);
#pragma unroll
        for (int i = 1; i < 8; ++i) sum += LDF(L_FP + i * 64 + l15 * 4);
        sum = bfr(sum);
        float up = __shfl(sum, (lane & 7) + 8);
        float gt = __shfl(sum, lane & 7);
        float hv = bfr(bfr(gt * sigm(gt)) * up);
        u32 hb = __float_as_uint(hv);
        u32 h01 = ((u32)__builtin_amdgcn_readlane(hb, 0) >> 16) | ((u32)__builtin_amdgcn_readlane(hb, 1) & 0xffff0000u);
        u32 h23 = ((u32)__builtin_amdgcn_readlane(hb, 2) >> 16) | ((u32)__builtin_amdgcn_readlane(hb, 3) & 0xffff0000u);
        u32 h45 = ((u32)__builtin_amdgcn_readlane(hb, 4) >> 16) | ((u32)__builtin_amdgcn_readlane(hb, 5) & 0xffff0000u);
        u32 h67 = ((u32)__builtin_amdgcn_readlane(hb, 6) >> 16) | ((u32)__builtin_amdgcn_readlane(hb, 7) & 0xffff0000u);
        float y = lo16(h01) * lo16(wd0) + hi16(h01) * lo16(wd1) + lo16(h23) * lo16(wd2) + hi16(h23) * lo16(wd3)
                + lo16(h45) * lo16(wd4) + hi16(h45) * lo16(wd5) + lo16(h67) * lo16(wd6) + hi16(h67) * lo16(wd7);
        u32x2 gr = {__float_as_uint(y), epoch};
        u32 yo = WS_Y + (xcd * 28 + jj) * 4096 + (64 * w + lane) * 8;
        GST64C(ws, yo, gr);
    }
    ATT_RUN(2);
    BAR();
#pragma unroll
    for (int i = 0; i < 8; ++i) {
        int q8 = w * 8 + i;
        u32 row = 8 * q8 + (lane >> 3), slot = lane & 7;
        u32 f = ((row >> 1) & 1) | (((row >> 3) & 1) << 1);
        u32 c = (((slot >> 1) ^ f) << 1) | (slot & 1);
        int reg = q8 >> 4;
        u32 dst = ((reg & 2) ? L_V : L_K) + (reg & 1) * 32768 + (q8 & 15) * 1024;
        dma16(wo, row * 1024 + hg * 128 + c * 16, lb + dst);
    }
    ATT_RUN(4);
    asm volatile(
        "s_nop 1\n"
        "v_smfmac_f32_32x32x32_bf16 v[144:159], v[188:191], v[120:127], v192\n"
        "s_nop 7\n s_nop 7\n s_nop 3\n s_waitcnt lgkmcnt(0)\n"
        : "+{v[144:159]}"(OY), "+{v[128:143]}"(OX), "+{v[112:127]}"(SY1), "+{v[188:191]}"(V11)
        : "{v192}"(idx) : "memory");
    STAMP(12);

    const int th = w & 1, hp = (w >> 1) & 1, blk = w >> 2;
    {
        f32x16& mine = blk ? OY : OX;
        f32x16& give = blk ? OX : OY;
        const u32 cw = L_W + w * 2304, cr = L_W + (w ^ 4) * 2304;
        ST128(cw + lane * 16, ((u32x4){__float_as_uint(give[0]), __float_as_uint(give[1]), __float_as_uint(give[2]), __float_as_uint(give[3])}));
        ST128(cw + 1024 + lane * 16, ((u32x4){__float_as_uint(give[4]), __float_as_uint(give[5]), __float_as_uint(give[6]), __float_as_uint(give[7])}));
        STF(cw + 2048 + lane * 4, give[8]);
        BAR();
        f32x4 p0 = __builtin_bit_cast(f32x4, LD128(cr + lane * 16));
        f32x4 p1 = __builtin_bit_cast(f32x4, LD128(cr + 1024 + lane * 16));
        float l = LDF(cr + 2048 + lane * 4) + mine[8];
        float o[8];
#pragma unroll
        for (int i = 0; i < 4; ++i) { o[i] = mine[i] + p0[i]; o[4 + i] = mine[4 + i] + p1[i]; }
        {
            auto sw = __builtin_amdgcn_permlane32_swap(__float_as_uint(l), __float_as_uint(l), false, false);
            l = __uint_as_float(sw[0]);
        }
        float amax = fmaxf(fmaxf(fmaxf(fabsf(o[0]), fabsf(o[1])), fmaxf(fabsf(o[2]), fabsf(o[3]))),
                           fmaxf(fmaxf(fabsf(o[4]), fabsf(o[5])), fmaxf(fabsf(o[6]), fabsf(o[7]))));
        bool safe = (l >= 8.6736174e-19f) && (l <= 1.2676506e30f) && (amax <= 3.0e38f);
        if (!__all(safe)) {
            slow_block(ws + WS_KV, blk ? qy : qx, lane, o, l);
            auto sw = __builtin_amdgcn_permlane32_swap(__float_as_uint(l), __float_as_uint(l), false, false);
            l = __uint_as_float(sw[0]);
        }
        const float ri = __builtin_amdgcn_rcpf(l);
        u32 A0 = pk(o[0] * ri, o[1] * ri), A1 = pk(o[2] * ri, o[3] * ri);
        u32 B0 = pk(o[4] * ri, o[5] * ri), B1 = pk(o[6] * ri, o[7] * ri);
        auto s0 = __builtin_amdgcn_permlane32_swap(A0, B0, false, false);
        auto s1 = __builtin_amdgcn_permlane32_swap(A1, B1, false, false);
        u32x4 ov = {s0[0], s1[0], s0[1], s1[1]};
        const int head = hg * 4 + hp * 2 + blk;
        const int tl = th * 32 + l31;
        u32 oo = WS_O + ((tb * 4 + (tl >> 4)) * 16 + (head >> 1)) * 1024 + (((head & 1) * 2 + h) * 16 + (tl & 15)) * 16;
        GST128C(ws, oo, ov);
        VMWAIT(0);
    }
    BAR();
    if (tid == 0) { u32x2 fl = {0u, epoch}; u32 fo = WS_F2 + tb * 128 + hg * 8; GST64C(ws, fo, fl); }
    STAMP(13);

    const int tq = w & 3, kh = w >> 2;
    const int tokg = tb * 64 + tq * 16 + l15;
    const int ncol = hg * 64 + kh * 32 + (g & 1) * 16 + (g >> 1) * 8;
    const bool owner = (sel >> 6) == tb;
    if (owner) {
        u32x2 gr[28];
        const u32 yo = WS_Y + (28 * w) * 4096 + (hg * 64 + lane) * 8;
        u32 spins = 0;
        while (true) {
#pragma unroll
            for (int i = 0; i < 28; ++i) { u32 a = yo + i * 4096; GLD64C(gr[i], ws, a, 0); }
            asm volatile("s_waitcnt vmcnt(0)" : "+v"(gr[0]), "+v"(gr[1]), "+v"(gr[2]), "+v"(gr[3]), "+v"(gr[4]), "+v"(gr[5]), "+v"(gr[6]), "+v"(gr[7]), "+v"(gr[8]), "+v"(gr[9]), "+v"(gr[10]), "+v"(gr[11]), "+v"(gr[12]), "+v"(gr[13]), "+v"(gr[14]), "+v"(gr[15]), "+v"(gr[16]), "+v"(gr[17]), "+v"(gr[18]), "+v"(gr[19]), "+v"(gr[20]), "+v"(gr[21]), "+v"(gr[22]), "+v"(gr[23]), "+v"(gr[24]), "+v"(gr[25]), "+v"(gr[26]), "+v"(gr[27]) :: "memory");
            bool ok = true;
#pragma unroll
            for (int i = 0; i < 28; ++i) ok &= (gr[i][1] == epoch);
            if (__all(ok) || ++spins > SPIN_MAX) break;
        }
        float ys = 0.f;
#pragma unroll
        for (int i = 0; i < 28; ++i) ys += __uint_as_float(gr[i][0]);
        STF(L_YP + w * 256 + lane * 4, ys);
    }
    u32x4 af[4][8];
    {
        const u32 f = ((l15 >> 3) & 1) | ((g & 1) << 1);
        const u32 rb = (kh ? L_V : L_K) + (8 * g + (l15 >> 2)) * 128 + (l15 & 3) * 8;
#pragma unroll
        for (int nb = 0; nb < 4; ++nb) {
            const u32 a = rb + ((nb ^ f) * 32);
#pragma unroll
            for (int st = 0; st < 8; ++st) {
                const u32 o_ = (st >> 2) * 32768 + (st & 3) * 4096;
                af[nb][st] = cat(trread(lds, a + o_), trread(lds, a + o_ + 512));
            }
        }
    }
    STAMP(14);
    {
        u32 fo = WS_F2 + tb * 128 + (kh * 4 + (lane & 3)) * 8;
        u32x2 fl; u32 spins = 0;
        while (true) {
            GLD64C(fl, ws, fo, 0);
            asm volatile("s_waitcnt vmcnt(0)" : "+v"(fl) :: "memory");
            if (__all(fl[1] == epoch) || ++spins > SPIN_MAX) break;
            __builtin_amdgcn_s_sleep(1);
        }
    }
    STAMP(15);
    u32x4 xe; u32 gl;
    {
        u32 xo = tokg * 1024 + ncol * 2;
        GLD128(xe, x, xo, 0);
        u32 go = WS_G + tokg * 4;
        GLD32C(gl, ws, go, 0);
    }
    u32x4 of0, of1, of2, of3, of4, of5, of6, of7;
    {
        u32 oo = WS_O + ((tb * 4 + tq) * 16 + 8 * kh) * 1024 + lane * 16;
        GLD128C(of0, ws, oo, 0); GLD128C(of1, ws, oo, 1024); GLD128C(of2, ws, oo, 2048); GLD128C(of3, ws, oo, 3072);
        u32 o2 = oo + 4096;
        GLD128C(of4, ws, o2, 0); GLD128C(of5, ws, o2, 1024); GLD128C(of6, ws, o2, 2048); GLD128C(of7, ws, o2, 3072);
    }
    f32x4 acc[4] = {{0, 0, 0, 0}, {0, 0, 0, 0}, {0, 0, 0, 0}, {0, 0, 0, 0}};
#define OSTEP(N, OF, ST) \
    asm volatile("s_waitcnt vmcnt(" #N ")" : "+v"(OF) :: "memory"); \
    _Pragma("unroll") for (int nb = 0; nb < 4; ++nb) acc[nb] = __builtin_amdgcn_mfma_f32_16x16x32_bf16(asbf(af[nb][ST]), asbf(OF), acc[nb], 0, 0, 0);
    asm volatile("s_waitcnt vmcnt(8)" : "+v"(xe), "+v"(gl) :: "memory");
    OSTEP(7, of0, 0) OSTEP(6, of1, 1) OSTEP(5, of2, 2) OSTEP(4, of3, 3)
    OSTEP(3, of4, 4) OSTEP(2, of5, 5) OSTEP(1, of6, 6) OSTEP(0, of7, 7)
    STAMP(16);
    {
        f32x4 k0 = kh ? acc[2] : acc[0], k1 = kh ? acc[3] : acc[1];
        f32x4 g0 = kh ? acc[0] : acc[2], g1 = kh ? acc[1] : acc[3];
        ST128(L_W + w * 2048 + lane * 16, __builtin_bit_cast(u32x4, g0));
        ST128(L_W + w * 2048 + 1024 + lane * 16, __builtin_bit_cast(u32x4, g1));
        BAR();
        k0 += __builtin_bit_cast(f32x4, LD128(L_W + (w ^ 4) * 2048 + lane * 16));
        k1 += __builtin_bit_cast(f32x4, LD128(L_W + (w ^ 4) * 2048 + 1024 + lane * 16));
        float a[8];
#pragma unroll
        for (int i = 0; i < 4; ++i) {
            auto sw = __builtin_amdgcn_permlane16_swap(__float_as_uint(k0[i]), __float_as_uint(k1[i]), false, false);
            a[i] = __uint_as_float(sw[0]); a[4 + i] = __uint_as_float(sw[1]);
        }
        const float gt = bfr(sigm(bfr(__uint_as_float(gl))));
        const float og = bfr(1.0f - gt);
        float bb[8];
#pragma unroll
        for (int i = 0; i < 4; ++i) { bb[2 * i] = lo16(xe[i]); bb[2 * i + 1] = hi16(xe[i]); }
        if (owner && tokg == sel) {
            const float sg = bfr(sigm(rsel));
            const int c0 = kh * 32 + (g & 1) * 16 + (g >> 1) * 8;
#pragma unroll
            for (int i = 0; i < 8; ++i) {
                float y = 0.f;
#pragma unroll
                for (int p = 0; p < 8; ++p) y += LDF(L_YP + p * 256 + (c0 + i) * 4);
                bb[i] = bfr(bb[i] + bfr(bfr(y) * sg));
            }
        }
        u32x4 ov;
#pragma unroll
        for (int i = 0; i < 4; ++i)
            ov[i] = pk(bfr(gt * bfr(a[2 * i])) + bfr(og * bb[2 * i]), bfr(gt * bfr(a[2 * i + 1])) + bfr(og * bb[2 * i + 1]));
        *(u32x4*)((char*)out + (size_t)tokg * 1024 + ncol * 2) = ov;
    }
    STAMP(17); STAMPDUMP();
}

extern "C" void kernel_launch(void* const* d_in, const int* in_sizes, int n_in, void* d_out, int out_size,
                              void* d_ws, size_t ws_size, hipStream_t stream) {
    (void)in_sizes; (void)n_in; (void)out_size; (void)ws_size;
    fused_kernel<<<256, 512, 0, stream>>>((const u16*)d_in[0], (const u16*)d_in[1], (const u16*)d_in[2], (const u16*)d_in[3],
                                          (const u16*)d_in[4], (const u16*)d_in[6], (const u16*)d_in[7], (const u16*)d_in[8],
                                          (const u16*)d_in[9], (const u16*)d_in[10], (u16*)d_out, (char*)d_ws);
}
```

```cpp
#include <hip/hip_runtime.h>
#include <stdint.h>

typedef unsigned int u32;
typedef unsigned long long u64;
typedef unsigned short u16;
typedef u32 u32x2 __attribute__((ext_vector_type(2)));
typedef u32 u32x4 __attribute__((ext_vector_type(4)));
typedef float f32x2 __attribute__((ext_vector_type(2)));
typedef float f32x4 __attribute__((ext_vector_type(4)));
typedef float f32x16 __attribute__((ext_vector_type(16)));
typedef __bf16 bf16x2 __attribute__((ext_vector_type(2)));
typedef __bf16 bf16x8 __attribute__((ext_vector_type(8)));
typedef short s16x4 __attribute__((ext_vector_type(4)));
#define LDSP __attribute__((address_space(3)))

constexpr u32 WS_E   = 0;
constexpr u32 WS_F1  = 4096;
constexpr u32 WS_F2  = 20480;
constexpr u32 WS_G   = 24576;
constexpr u32 WS_KV  = 65536;
constexpr u32 WS_O   = 262144;
constexpr u32 WS_Y   = 2621440;
constexpr u32 WS_DBG = 4194304;

constexpr u32 L_K   = 0;
constexpr u32 L_V   = 65536;
constexpr u32 L_ONE = 131072;
constexpr u32 L_W   = 135168;
constexpr u32 L_M   = 151552;
constexpr u32 L_P1  = L_M;
constexpr u32 L_PD  = L_M + 8192;
constexpr u32 L_FP  = L_M + 8704;
constexpr u32 L_YP  = L_M + 9216;
constexpr u32 L_CT  = L_M + 11264;
constexpr u32 L_END = 163840;

constexpr u32 SPIN_MAX = 1u << 22;

#define LD128(a) (*(const LDSP u32x4*)(lds + (a)))
#define ST128(a, v) (*(LDSP u32x4*)(lds + (a)) = (v))
#define LD32(a) (*(const LDSP u32*)(lds + (a)))
#define ST32(a, v) (*(LDSP u32*)(lds + (a)) = (v))
#define LDF(a) (*(const LDSP float*)(lds + (a)))
#define STF(a, v) (*(LDSP float*)(lds + (a)) = (v))

__device__ __forceinline__ u32x2 trread(LDSP char* lds, u32 a) {
    typedef short v4s __attribute__((ext_vector_type(4)));
    v4s r = __builtin_amdgcn_ds_read_tr16_b64_v4i16((LDSP v4s*)(lds + a));
    return __builtin_bit_cast(u32x2, r);
}
__device__ __forceinline__ bf16x8 asbf(u32x4 v) { return __builtin_bit_cast(bf16x8, v); }
__device__ __forceinline__ u32x4 cat(u32x2 a, u32x2 b) { u32x4 r = {a[0], a[1], b[0], b[1]}; return r; }
__device__ __forceinline__ u32 pk(float a, float b) {
    f32x2 f = {a, b};
    return __builtin_bit_cast(u32, __builtin_convertvector(f, bf16x2));
}
__device__ __forceinline__ float bfr(float a) {
    return __uint_as_float(pk(a, a) & 0xffff0000u);
}
__device__ __forceinline__ float lo16(u32 v) { return __uint_as_float(v << 16); }
__device__ __forceinline__ float hi16(u32 v) { return __uint_as_float(v & 0xffff0000u); }
__device__ __forceinline__ float sigm(float z) { return __builtin_amdgcn_rcpf(1.0f + __builtin_amdgcn_exp2f(-1.4426950408889634f * z)); }

__device__ __forceinline__ void dma16(const void* base, u32 voff, u32 ldsaddr) {
    asm volatile("s_mov_b32 m0, %0\n\ts_nop 0\n\tglobal_load_lds_dwordx4 %1, %2" :: "s"(ldsaddr), "v"(voff), "s"(base) : "memory");
}
__device__ __forceinline__ void dma16c(const void* base, u32 voff, u32 ldsaddr) {
    asm volatile("s_mov_b32 m0, %0\n\ts_nop 0\n\tglobal_load_lds_dwordx4 %1, %2 sc1" :: "s"(ldsaddr), "v"(voff), "s"(base) : "memory");
}
#define GLD128(dst, base, voff, OFF)  asm volatile("global_load_dwordx4 %0, %1, %2 offset:" #OFF : "=v"(dst) : "v"(voff), "s"(base) : "memory")
#define GLD128C(dst, base, voff, OFF) asm volatile("global_load_dwordx4 %0, %1, %2 offset:" #OFF " sc1" : "=v"(dst) : "v"(voff), "s"(base) : "memory")
#define GLD64C(dst, base, voff, OFF)  asm volatile("global_load_dwordx2 %0, %1, %2 offset:" #OFF " sc1" : "=v"(dst) : "v"(voff), "s"(base) : "memory")
#define GLD32C(dst, base, voff, OFF)  asm volatile("global_load_dword %0, %1, %2 offset:" #OFF " sc1" : "=v"(dst) : "v"(voff), "s"(base) : "memory")
#define GLD16(dst, base, voff, OFF)   asm volatile("global_load_ushort %0, %1, %2 offset:" #OFF : "=v"(dst) : "v"(voff), "s"(base) : "memory")
#define GST128C(base, voff, val) asm volatile("global_store_dwordx4 %0, %1, %2 sc1\n\ts_nop 1" :: "v"(voff), "v"(val), "s"(base) : "memory")
#define GST64C(base, voff, val)  asm volatile("global_store_dwordx2 %0, %1, %2 sc1" :: "v"(voff), "v"(val), "s"(base) : "memory")
#define GST32C(base, voff, val)  asm volatile("global_store_dword %0, %1, %2 sc1" :: "v"(voff), "v"(val), "s"(base) : "memory")
#define VMWAIT(N) asm volatile("s_waitcnt vmcnt(" #N ")" ::: "memory")
#define BAR() asm volatile("s_waitcnt lgkmcnt(0)\n\ts_barrier" ::: "memory")

#define STAMP(i)
#define STAMPC(i)
#define STAMPDUMP()

__device__ __forceinline__ u32 wave_max_u32(u32 v) {
    v = max(v, (u32)__builtin_amdgcn_update_dpp(0, (int)v, 0x111, 0xf, 0xf, false));
    v = max(v, (u32)__builtin_amdgcn_update_dpp(0, (int)v, 0x112, 0xf, 0xf, false));
    v = max(v, (u32)__builtin_amdgcn_update_dpp(0, (int)v, 0x114, 0xf, 0xf, false));
    v = max(v, (u32)__builtin_amdgcn_update_dpp(0, (int)v, 0x118, 0xf, 0xf, false));
    v = max(v, (u32)__builtin_amdgcn_update_dpp(0, (int)v, 0x142, 0xa, 0xf, false));
    v = max(v, (u32)__builtin_amdgcn_update_dpp(0, (int)v, 0x143, 0xc, 0xf, false));
    return (u32)__builtin_amdgcn_readlane((int)v, 63);
}
#define ATT_BODY4 \
    "v_exp_f32 v64, v64\n" \
    "v_exp_f32 v65, v65\n" \
    "v_smfmac_f32_32x32x32_bf16 v[144:159], v[188:191], v[120:127], v192\n" \
    "ds_read_b128 v[184:187], v194 offset:1024\n" \
    "v_exp_f32 v66, v66\n" \
    "v_exp_f32 v67, v67\n" \
    "v_exp_f32 v68, v68\n" \
    "v_exp_f32 v69, v69\n" \
    "s_waitcnt lgkmcnt(1)\n" \
    "v_mfma_f32_32x32x16_bf16 v[80:95], v[172:175], v[160:163], 0\n" \
    "ds_read_b128 v[188:191], v194 offset:1536\n" \
    "v_exp_f32 v70, v70\n" \
    "v_exp_f32 v71, v71\n" \
    "v_exp_f32 v72, v72\n" \
    "v_exp_f32 v73, v73\n" \
    "v_smfmac_f32_32x32x32_bf16 v[128:143], v[176:179], v[64:71], v192\n" \
    "v_exp_f32 v74, v74\n" \
    "v_exp_f32 v75, v75\n" \
    "v_exp_f32 v76, v76\n" \
    "v_exp_f32 v77, v77\n" \
    "v_exp_f32 v78, v78\n" \
    "v_exp_f32 v79, v79\n" \
    "v_exp_f32 v96, v96\n" \
    "v_exp_f32 v97, v97\n" \
    "v_smfmac_f32_32x32x32_bf16 v[128:143], v[180:183], v[72:79], v192\n" \
    "v_exp_f32 v98, v98\n" \
    "v_exp_f32 v99, v99\n" \
    "v_exp_f32 v100, v100\n" \
    "v_exp_f32 v101, v101\n" \
    "v_mfma_f32_32x32x16_bf16 v[112:127], v[172:175], v[164:167], 0\n" \
    "ds_read_b128 v[168:171], v193 offset:2048\n" \
    "v_exp_f32 v102, v102\n" \
    "v_exp_f32 v103, v103\n" \
    "v_exp_f32 v104, v104\n" \
    "v_exp_f32 v105, v105\n" \
    "v_smfmac_f32_32x32x32_bf16 v[144:159], v[176:179], v[96:103], v192\n" \
    "v_exp_f32 v106, v106\n" \
    "v_exp_f32 v107, v107\n" \
    "v_exp_f32 v108, v108\n" \
    "v_exp_f32 v109, v109\n" \
    "v_exp_f32 v110, v110\n" \
    "v_exp_f32 v111, v111\n" \
    "v_exp_f32 v80, v80\n" \
    "v_exp_f32 v81, v81\n" \
    "v_smfmac_f32_32x32x32_bf16 v[144:159], v[180:183], v[104:111], v192\n" \
    "ds_read_b128 v[176:179], v194 offset:2048\n" \
    "v_exp_f32 v82, v82\n" \
    "v_exp_f32 v83, v83\n" \
    "v_exp_f32 v84, v84\n" \
    "v_exp_f32 v85, v85\n" \
    "s_waitcnt lgkmcnt(1)\n" \
    "v_mfma_f32_32x32x16_bf16 v[64:79], v[168:171], v[160:163], 0\n" \
    "ds_read_b128 v[180:183], v194 offset:2560\n" \
    "v_exp_f32 v86, v86\n" \
    "v_exp_f32 v87, v87\n" \
    "v_exp_f32 v88, v88\n" \
    "v_exp_f32 v89, v89\n" \
    "v_smfmac_f32_32x32x32_bf16 v[128:143], v[184:187], v[80:87], v192\n" \
    "v_exp_f32 v90, v90\n" \
    "v_exp_f32 v91, v91\n" \
    "v_exp_f32 v92, v92\n" \
    "v_exp_f32 v93, v93\n" \
    "v_exp_f32 v94, v94\n" \
    "v_exp_f32 v95, v95\n" \
    "v_exp_f32 v112, v112\n" \
    "v_exp_f32 v113, v113\n" \
    "v_smfmac_f32_32x32x32_bf16 v[128:143], v[188:191], v[88:95], v192\n" \
    "v_exp_f32 v114, v114\n" \
    "v_exp_f32 v115, v115\n" \
    "v_exp_f32 v116, v116\n" \
    "v_exp_f32 v117, v117\n" \
    "v_mfma_f32_32x32x16_bf16 v[96:111], v[168:171], v[164:167], 0\n" \
    "ds_read_b128 v[172:175], v193 offset:3072\n" \
    "v_exp_f32 v118, v118\n" \
    "v_exp_f32 v119, v119\n" \
    "v_exp_f32 v120, v120\n" \
    "v_exp_f32 v121, v121\n" \
    "v_smfmac_f32_32x32x32_bf16 v[144:159], v[184:187], v[112:119], v192\n" \
    "v_exp_f32 v122, v122\n" \
    "v_exp_f32 v123, v123\n" \
    "v_exp_f32 v124, v124\n" \
    "v_exp_f32 v125, v125\n" \
    "v_exp_f32 v126, v126\n" \
    "v_exp_f32 v127, v127\n" \
    "v_exp_f32 v64, v64\n" \
    "v_exp_f32 v65, v65\n" \
    "v_smfmac_f32_32x32x32_bf16 v[144:159], v[188:191], v[120:127], v192\n" \
    "ds_read_b128 v[184:187], v194 offset:3072\n" \
    "v_exp_f32 v66, v66\n" \
    "v_exp_f32 v67, v67\n" \
    "v_exp_f32 v68, v68\n" \
    "v_exp_f32 v69, v69\n" \
    "s_waitcnt lgkmcnt(1)\n" \
    "v_mfma_f32_32x32x16_bf16 v[80:95], v[172:175], v[160:163], 0\n" \
    "ds_read_b128 v[188:191], v194 offset:3584\n" \
    "v_exp_f32 v70, v70\n" \
    "v_exp_f32 v71, v71\n" \
    "v_exp_f32 v72, v72\n" \
    "v_exp_f32 v73, v73\n" \
    "v_smfmac_f32_32x32x32_bf16 v[128:143], v[176:179], v[64:71], v192\n" \
    "v_exp_f32 v74, v74\n" \
    "v_exp_f32 v75, v75\n" \
    "v_exp_f32 v76, v76\n" \
    "v_exp_f32 v77, v77\n" \
    "v_exp_f32 v78, v78\n" \
    "v_exp_f32 v79, v79\n" \
    "v_exp_f32 v96, v96\n" \
    "v_exp_f32 v97, v97\n" \
    "v_smfmac_f32_32x32x32_bf16 v[128:143], v[180:183], v[72:79], v192\n" \
    "v_exp_f32 v98, v98\n" \
    "v_exp_f32 v99, v99\n" \
    "v_exp_f32 v100, v100\n" \
    "v_exp_f32 v101, v101\n" \
    "v_mfma_f32_32x32x16_bf16 v[112:127], v[172:175], v[164:167], 0\n" \
    "ds_read_b128 v[168:171], v193 offset:4096\n" \
    "v_exp_f32 v102, v102\n" \
    "v_exp_f32 v103, v103\n" \
    "v_exp_f32 v104, v104\n" \
    "v_exp_f32 v105, v105\n" \
    "v_smfmac_f32_32x32x32_bf16 v[144:159], v[176:179], v[96:103], v192\n" \
    "v_exp_f32 v106, v106\n" \
    "v_exp_f32 v107, v107\n" \
    "v_exp_f32 v108, v108\n" \
    "v_exp_f32 v109, v109\n" \
    "v_exp_f32 v110, v110\n" \
    "v_exp_f32 v111, v111\n" \
    "v_exp_f32 v80, v80\n" \
    "v_exp_f32 v81, v81\n" \
    "v_smfmac_f32_32x32x32_bf16 v[144:159], v[180:183], v[104:111], v192\n" \
    "ds_read_b128 v[176:179], v194 offset:4096\n" \
    "v_exp_f32 v82, v82\n" \
    "v_exp_f32 v83, v83\n" \
    "v_exp_f32 v84, v84\n" \
    "v_exp_f32 v85, v85\n" \
    "s_waitcnt lgkmcnt(1)\n" \
    "v_mfma_f32_32x32x16_bf16 v[64:79], v[168:171], v[160:163], 0\n" \
    "ds_read_b128 v[180:183], v194 offset:4608\n" \
    "v_exp_f32 v86, v86\n" \
    "v_exp_f32 v87, v87\n" \
    "v_exp_f32 v88, v88\n" \
    "v_exp_f32 v89, v89\n" \
    "v_smfmac_f32_32x32x32_bf16 v[128:143], v[184:187], v[80:87], v192\n" \
    "v_exp_f32 v90, v90\n" \
    "v_exp_f32 v91, v91\n" \
    "v_exp_f32 v92, v92\n" \
    "v_exp_f32 v93, v93\n" \
    "v_exp_f32 v94, v94\n" \
    "v_exp_f32 v95, v95\n" \
    "v_exp_f32 v112, v112\n" \
    "v_exp_f32 v113, v113\n" \
    "v_smfmac_f32_32x32x32_bf16 v[128:143], v[188:191], v[88:95], v192\n" \
    "v_exp_f32 v114, v114\n" \
    "v_exp_f32 v115, v115\n" \
    "v_exp_f32 v116, v116\n" \
    "v_exp_f32 v117, v117\n" \
    "v_mfma_f32_32x32x16_bf16 v[96:111], v[168:171], v[164:167], 0\n" \
    "ds_read_b128 v[172:175], v193 offset:5120\n" \
    "v_exp_f32 v118, v118\n" \
    "v_exp_f32 v119, v119\n" \
    "v_exp_f32 v120, v120\n" \
    "v_exp_f32 v121, v121\n" \
    "v_smfmac_f32_32x32x32_bf16 v[144:159], v[184:187], v[112:119], v192\n" \
    "v_exp_f32 v122, v122\n" \
    "v_exp_f32 v123, v123\n" \
    "v_exp_f32 v124, v124\n" \
    "v_exp_f32 v125, v125\n" \
    "v_exp_f32 v126, v126\n" \
    "v_exp_f32 v127, v127\n" \
    "v_add_u32 v193, 4096, v193\n" \
    "v_add_u32 v194, v195, v194\n"

__device__ __forceinline__ void slow_block(const char* kv, u32x4 q, int lane, float* o, float& lsum) {
    const u32 koff = lane * 16;
    const u32 r32 = lane & 31;
    float m = -3.0e38f;
    for (int t = 0; t < 64; ++t) {
        u32x4 kf;
        GLD128C(kf, kv, koff + t * 2048, 0);
        asm volatile("s_waitcnt vmcnt(0)" : "+v"(kf) :: "memory");
        f32x16 s = {0};
        s = __builtin_amdgcn_mfma_f32_32x32x16_bf16(asbf(kf), asbf(q), s, 0, 0, 0);
#pragma unroll
        for (int i = 0; i < 16; ++i) m = fmaxf(m, s[i]);
    }
    {
        auto sw = __builtin_amdgcn_permlane32_swap(__float_as_uint(m), __float_as_uint(m), false, false);
        m = fmaxf(__uint_as_float(sw[0]), __uint_as_float(sw[1]));
    }
    f32x16 acc = {0};
    const u32 voff = 1024 + (lane >> 5) * 256 + (lane & 15) * 16;
    for (int t = 0; t < 64; ++t) {
        u32x4 kf, v0, v1;
        GLD128C(kf, kv, koff + t * 2048, 0);
        GLD128C(v0, kv, voff + t * 2048, 0);
        GLD128C(v1, kv, voff + t * 2048, 512);
        asm volatile("s_waitcnt vmcnt(0)" : "+v"(kf), "+v"(v0), "+v"(v1) :: "memory");
        if (r32 >= 16) { v0 = (u32x4){0x3f803f80u, 0x3f803f80u, 0x3f803f80u, 0x3f803f80u}; v1 = v0; }
        f32x16 s = {0};
        s = __builtin_amdgcn_mfma_f32_32x32x16_bf16(asbf(kf), asbf(q), s, 0, 0, 0);
        u32 p[8];
#pragma unroll
        for (int i = 0; i < 8; ++i)
            p[i] = pk(__builtin_amdgcn_exp2f(s[2 * i] - m), __builtin_amdgcn_exp2f(s[2 * i + 1] - m));
#pragma unroll
        for (int st = 0; st < 2; ++st) {
            u32x4 vv = st ? v1 : v0;
            auto a = __builtin_amdgcn_permlane32_swap(vv[0], vv[2], false, false);
            auto c = __builtin_amdgcn_permlane32_swap(vv[1], vv[3], false, false);
            u32x4 af = {a[0], c[0], a[1], c[1]};
            u32x4 bf = {p[4 * st], p[4 * st + 1], p[4 * st + 2], p[4 * st + 3]};
            acc = __builtin_amdgcn_mfma_f32_32x32x16_bf16(asbf(af), asbf(bf), acc, 0, 0, 0);
        }
    }
#pragma unroll
    for (int i = 0; i < 8; ++i) o[i] = acc[i];
    lsum = acc[8];
}

__global__ void __launch_bounds__(512, 2)
fused_kernel(const u16* __restrict__ x, const u16* __restrict__ wq, const u16* __restrict__ wk, const u16* __restrict__ wv,
             const u16* __restrict__ wo, const u16* __restrict__ wr, const u16* __restrict__ wg, const u16* __restrict__ wu,
             const u16* __restrict__ wd, const u16* __restrict__ gw, u16* __restrict__ out, char* __restrict__ ws)
{
    __shared__ __attribute__((aligned(1024))) char smem[L_END];
    LDSP char* lds = (LDSP char*)smem;
    const u32 lb = (u32)(size_t)lds;
    const int tid = threadIdx.x;
    const int lane = tid & 63;
    const int w = __builtin_amdgcn_readfirstlane(tid >> 6);
    const int b = blockIdx.x;
    const int xcd = b & 7, jj = b >> 3;
    const int tb = xcd * 4 + (jj >> 3), hg = jj & 7;
    const int g = lane >> 4, l15 = lane & 15, h = lane >> 5, l31 = lane & 31;
    const bool isK = hg < 4;
    const bool ffn = jj < 28;
    const int col0 = (xcd * 28 + jj) * 8;
    STAMP(1); STAMPC(18);

    u32 epo;
    { u32 z = 0; GLD32C(epo, ws, z, 0); }
    const int T0 = tb * 64 + (hg & 3) * 16;
    u32x4 xf0, xf1, vf0, vf1;
    {
        const u16* wsrc = isK ? wk : wv;
#pragma unroll
        for (int i = 0; i < 2; ++i) {
            u32 slot = 64 * i + lane, pos = slot >> 3, B = pos ^ ((pos >> 2) & 1);
            u32 row = 4 * B + ((slot >> 1) & 3);
            dma16(wsrc, (64 * w + row) * 32 + (slot & 1) * 16, lb + L_W + w * 2048 + i * 1024);
        }
        u32 xo = (T0 + l15) * 1024 + (64 * w + 16 * g) * 2;
        GLD128(xf0, x, xo, 0);
        GLD128(xf1, x, xo, 16);
        const u16* vec = isK ? wr : gw;
        u32 vo = (64 * w + 16 * g) * 2;
        GLD128(vf0, vec, vo, 0);
        GLD128(vf1, vec, vo, 16);
    }
    if (w < 7) {
        for (int p = w; p < 128; p += 7) {
            if (p < 64) {
                dma16(x, (tb * 64 + p) * 1024 + ((lane ^ (p & 15)) << 4), lb + L_K + p * 1024);
            } else {
                int G = p - 64;
                dma16(wq, (8 * G + ((lane >> 2) & 7)) * 1024 + hg * 128 + (lane >> 5) * 64 + (lane & 3) * 16, lb + L_V + G * 1024);
            }
        }
    }
    if (tid < 256) { u32 o1 = (tid & 15) == 0 ? 0x3f803f80u : 0u; ST128(L_ONE + tid * 16, ((u32x4){o1, o1, o1, o1})); }
    asm volatile("s_cmp_eq_u32 %[w], 7\n\ts_cbranch_scc1 1f\n\ts_waitcnt vmcnt(18)\n\ts_branch 2f\n1:\n\ts_waitcnt vmcnt(0)\n2:"
                 : "+v"(xf0), "+v"(xf1), "+v"(vf0), "+v"(vf1), "+v"(epo) : [w] "s"(w) : "memory", "scc");
    STAMP(2);
    {
        const u32 ta = L_W + w * 2048 + l15 * 8;
        const u32 gx = g & 1;
        u32x4 wf0 = cat(trread(lds, ta + ((4 * g + 0) ^ gx) * 128), trread(lds, ta + ((4 * g + 1) ^ gx) * 128));
        u32x4 wf1 = cat(trread(lds, ta + ((4 * g + 2) ^ gx) * 128), trread(lds, ta + ((4 * g + 3) ^ gx) * 128));
        f32x4 acc = {0, 0, 0, 0}, dacc = {0, 0, 0, 0};
        if (isK) {
            acc = __builtin_amdgcn_mfma_f32_16x16x32_bf16(asbf(wf0), asbf(xf0), acc, 0, 0, 0);
            acc = __builtin_amdgcn_mfma_f32_16x16x32_bf16(asbf(wf1), asbf(xf1), acc, 0, 0, 0);
        } else {
            acc = __builtin_amdgcn_mfma_f32_16x16x32_bf16(asbf(xf0), asbf(wf0), acc, 0, 0, 0);
            acc = __builtin_amdgcn_mfma_f32_16x16x32_bf16(asbf(xf1), asbf(wf1), acc, 0, 0, 0);
        }
        dacc = __builtin_amdgcn_mfma_f32_16x16x32_bf16(asbf(vf0), asbf(xf0), dacc, 0, 0, 0);
        dacc = __builtin_amdgcn_mfma_f32_16x16x32_bf16(asbf(vf1), asbf(xf1), dacc, 0, 0, 0);
        ST128(L_P1 + w * 1024 + lane * 16, __builtin_bit_cast(u32x4, acc));
        if (g == 0) STF(L_PD + w * 64 + l15 * 4, dacc[0]);
    }
    BAR();
    STAMP(3);
    u32 epoch = 0;
    if (w == 7) {
        epoch = epo + 1;
        f32x4 s = __builtin_bit_cast(f32x4, LD128(L_P1 + lane * 16));
        float d = LDF(L_PD + l15 * 4);
#pragma unroll
        for (int i = 1; i < 8; ++i) {
            s += __builtin_bit_cast(f32x4, LD128(L_P1 + i * 1024 + lane * 16));
            d += LDF(L_PD + i * 64 + l15 * 4);
        }
        u32x2 pv = {pk(s[0], s[1]), pk(s[2], s[3])};
        const int tile = T0 >> 5, sub = (T0 >> 4) & 1;
        u32 key = 0;
        if (isK) {
            u32 off = WS_KV + tile * 2048 + ((g & 1) * 32 + sub * 16 + l15) * 16 + (g >> 1) * 8;
            GST64C(ws, off, pv);
            u32 rb = pk(d, d) & 0xffffu;
            u32 ord = (rb & 0x8000u) ? (~rb & 0xffffu) : (rb | 0x8000u);
            key = wave_max_u32((ord << 16) | (0xffffu - (u32)(T0 + l15)));
        } else {
            u32 off = WS_KV + tile * 2048 + 1024 + sub * 512 + (g >> 1) * 256 + l15 * 16 + (g & 1) * 8;
            GST64C(ws, off, pv);
            if (g == 0) { u32 go = WS_G + (T0 + l15) * 4; u32 dv = __float_as_uint(d); GST32C(ws, go, dv); }
        }
        VMWAIT(0);
        if (lane < 8) { u32x2 fl = {key, epoch}; u32 fo = WS_F1 + lane * 2048 + b * 8; GST64C(ws, fo, fl); }
        STAMP(4);
    } else {
        VMWAIT(0);
    }
    BAR();
    STAMP(5);

    u32x4 qx, qy;
    if (w < 4) {
        const int th = w & 1, hp = w >> 1;
        f32x16 acc = {0};
        const int tok = th * 32 + l31;
        const u32 abase = L_V + h * 1024 + hp * 512 + ((l15 >> 2) * 64) + ((lane >> 4) & 1) * 32 + (l15 & 3) * 8;
        const u32 bbase = L_K + tok * 1024;
#pragma unroll
        for (int s = 0; s < 32; ++s) {
            u32x4 af = cat(trread(lds, abase + s * 2048), trread(lds, abase + s * 2048 + 256));
            u32x4 bf = LD128(bbase + (s >> 3) * 256 + ((((2 * s + h) & 15) ^ (tok & 15)) << 4));
            acc = __builtin_amdgcn_mfma_f32_32x32x16_bf16(asbf(af), asbf(bf), acc, 0, 0, 0);
        }
        const float qs = 0.25f * 1.4426950408889634f;
#pragma unroll
        for (int i = 0; i < 4; ++i) {
            u32 a = pk(acc[2 * i], acc[2 * i + 1]), c = pk(acc[8 + 2 * i], acc[8 + 2 * i + 1]);
            qx[i] = pk(lo16(a) * qs, hi16(a) * qs);
            qy[i] = pk(lo16(c) * qs, hi16(c) * qs);
        }
        ST128(L_P1 + w * 2048 + lane * 16, qx);
        ST128(L_P1 + w * 2048 + 1024 + lane * 16, qy);
        STAMP(6);
    } else {
        if (w < 6 && ffn) {
#pragma unroll
            for (int i = 0; i < 8; ++i) {
                int pc = (w - 4) * 8 + i;
                u32 slot = 64 * (pc & 1) + lane, pos = slot >> 3, B = pos ^ ((pos >> 2) & 1);
                u32 row = 64 * (pc >> 1) + 4 * B + ((slot >> 1) & 3);
                u64 src = (u64)((slot & 1) ? wu : wg) + (u64)row * 3584 + col0 * 2;
                asm volatile("s_mov_b32 m0, %0\n\ts_nop 0\n\tglobal_load_lds_dwordx4 %1, off" :: "s"(lb + L_W + pc * 1024), "v"(src) : "memory");
            }
        }
        if (w == 7) {
            u32 fo = WS_F1 + xcd * 2048 + lane * 32;
            u32x4 f0, f1;
            u32 spins = 0;
            while (true) {
                GLD128C(f0, ws, fo, 0);
                GLD128C(f1, ws, fo, 16);
                asm volatile("s_waitcnt vmcnt(0)" : "+v"(f0), "+v"(f1) :: "memory");
                bool ok = (f0[1] == epoch) & (f0[3] == epoch) & (f1[1] == epoch) & (f1[3] == epoch);
                if (__all(ok) || ++spins > SPIN_MAX) break;
                __builtin_amdgcn_s_sleep(1);
            }
            u32 key = wave_max_u32(max(max(f0[0], f0[2]), max(f1[0], f1[2])));
            if (lane == 0) {
                ST32(L_CT, key); ST32(L_CT + 4, epoch);
                if (b == 0) { u32 z = 0; GST32C(ws, z, epoch); }
            }
            STAMP(7);
        }
        if (w < 6) VMWAIT(0);
    }
    BAR();
    STAMP(8);
    const u32 key = __builtin_amdgcn_readfirstlane(LD32(L_CT));
    epoch = __builtin_amdgcn_readfirstlane(LD32(L_CT + 4));
    const int sel = 0xffff - (key & 0xffff);
    const u32 rord = key >> 16;
    const float rsel = __uint_as_float(((rord & 0x8000u) ? (rord & 0x7fffu) : (~rord & 0xffffu)) << 16);

    u32x4 xs0, xs1;
    {
        u32 so = (sel & 2047) * 1024 + (64 * w + 16 * g) * 2;
        GLD128(xs0, x, so, 0);
        GLD128(xs1, x, so, 16);
    }
    const int hf = w >> 2, jw = w & 3;
    {
        const char* kvb = ws + WS_KV;
#pragma unroll
        for (int r = 0; r < 16; ++r) {
            int m = 4 * r + jw;
            int isV = ((m & 1) == 0 && m != 0) || m == 63;
            int t = m == 63 ? 31 : (isV ? (m >> 1) - 1 : (m + 1) >> 1);
            t += 32 * hf;
            dma16c(kvb, t * 2048 + isV * 1024 + lane * 16, lb + (isV ? L_V : L_K) + t * 1024);
        }
    }
    u32 wd0, wd1, wd2, wd3, wd4, wd5, wd6, wd7;
    {
        const u32 wdo = (xcd * 28 + min(jj, 27)) * 8192 + (64 * w + lane) * 2, o2 = wdo + 4096;
        GLD16(wd0, wd, wdo, 0); GLD16(wd1, wd, wdo, 1024); GLD16(wd2, wd, wdo, 2048); GLD16(wd3, wd, wdo, 3072);
        GLD16(wd4, wd, o2, 0); GLD16(wd5, wd, o2, 1024); GLD16(wd6, wd, o2, 2048); GLD16(wd7, wd, o2, 3072);
    }
    if (w >= 4) {
        qx = LD128(L_P1 + (w - 4) * 2048 + lane * 16);
        qy = LD128(L_P1 + (w - 4) * 2048 + 1024 + lane * 16);
    }
    asm volatile("s_waitcnt vmcnt(21)" : "+v"(xs0), "+v"(xs1) :: "memory");
    STAMP(9);
    if (ffn) {
        const u32 ta = L_W + w * 2048 + l15 * 8;
        const u32 gx = g & 1;
        u32x4 wf0 = cat(trread(lds, ta + ((4 * g + 0) ^ gx) * 128), trread(lds, ta + ((4 * g + 1) ^ gx) * 128));
        u32x4 wf1 = cat(trread(lds, ta + ((4 * g + 2) ^ gx) * 128), trread(lds, ta + ((4 * g + 3) ^ gx) * 128));
        f32x4 acc = {0, 0, 0, 0};
        acc = __builtin_amdgcn_mfma_f32_16x16x32_bf16(asbf(wf0), asbf(xs0), acc, 0, 0, 0);
        acc = __builtin_amdgcn_mfma_f32_16x16x32_bf16(asbf(wf1), asbf(xs1), acc, 0, 0, 0);
        if (l15 == 0) ST128(L_FP + w * 64 + g * 16, __builtin_bit_cast(u32x4, acc));
    }
    BAR();
    STAMP(10); STAMPC(19);

    f32x16 SX0, SX1, SY0, SY1, OX, OY;
    u32x4 K0, K1, V00, V01, V10, V11;
    u32 idx = 0xDDDDu;
    u32 ka = lb + L_K + hf * 32768 + lane * 16;
    const bool vlane = l31 < 16;
    u32 va = vlane ? (lb + L_V + hf * 32768 + h * 256 + l15 * 16) : (lb + L_ONE - 1024 + h * 256 + l15 * 16);
    u32 vinc = vlane ? 4096u : 0u;
    {
        u32 va0 = vlane ? va : va + 1024;
        asm volatile(
            "ds_read_b128 v[168:171], v193\n"
            "ds_read_b128 v[172:175], v193 offset:1024\n"
            "ds_read_b128 v[176:179], %[va0]\n"
            "ds_read_b128 v[180:183], %[va0] offset:512\n"
            "v_mov_b32 v128, 0\n v_mov_b32 v129, 0\n v_mov_b32 v130, 0\n v_mov_b32 v131, 0\n"
            "v_mov_b32 v132, 0\n v_mov_b32 v133, 0\n v_mov_b32 v134, 0\n v_mov_b32 v135, 0\n"
            "v_mov_b32 v136, 0\n v_mov_b32 v137, 0\n v_mov_b32 v138, 0\n v_mov_b32 v139, 0\n"
            "v_mov_b32 v140, 0\n v_mov_b32 v141, 0\n v_mov_b32 v142, 0\n v_mov_b32 v143, 0\n"
            "v_mov_b32 v144, 0\n v_mov_b32 v145, 0\n v_mov_b32 v146, 0\n v_mov_b32 v147, 0\n"
            "v_mov_b32 v148, 0\n v_mov_b32 v149, 0\n v_mov_b32 v150, 0\n v_mov_b32 v151, 0\n"
            "v_mov_b32 v152, 0\n v_mov_b32 v153, 0\n v_mov_b32 v154, 0\n v_mov_b32 v155, 0\n"
            "v_mov_b32 v156, 0\n v_mov_b32 v157, 0\n v_mov_b32 v158, 0\n v_mov_b32 v159, 0\n"
            "v_mov_b32 v120, 0\n v_mov_b32 v121, 0\n v_mov_b32 v122, 0\n v_mov_b32 v123, 0\n"
            "v_mov_b32 v124, 0\n v_mov_b32 v125, 0\n v_mov_b32 v126, 0\n v_mov_b32 v127, 0\n"
            "v_mov_b32 v188, 0\n v_mov_b32 v189, 0\n v_mov_b32 v190, 0\n v_mov_b32 v191, 0\n"
            "s_waitcnt lgkmcnt(0)\n"
            "v_mfma_f32_32x32x16_bf16 v[64:79], v[168:171], v[160:163], 0\n"
            "v_mfma_f32_32x32x16_bf16 v[96:111], v[168:171], v[164:167], 0\n"
            "s_nop 7\n s_nop 7\n"
            : "={v[64:79]}"(SX0), "={v[80:95]}"(SX1), "={v[96:111]}"(SY0), "={v[112:127]}"(SY1),
              "={v[128:143]}"(OX), "={v[144:159]}"(OY),
              "={v[168:171]}"(K0), "={v[172:175]}"(K1), "={v[176:179]}"(V00), "={v[180:183]}"(V01),
              "={v[184:187]}"(V10), "={v[188:191]}"(V11)
            : "{v[160:163]}"(qx), "{v[164:167]}"(qy), "{v193}"(ka), [va0] "v"(va0)
            : "memory");
    }
#define ATT_RUN(N) do { u32 cnt_; asm volatile("s_mov_b32 %[cnt], " #N "\n1:\n" ATT_BODY4 "s_sub_u32 %[cnt], %[cnt], 1\ns_cmp_lg_u32 %[cnt], 0\ns_cbranch_scc1 1b\n" \
            : "+{v[64:79]}"(SX0), "+{v[80:95]}"(SX1), "+{v[96:111]}"(SY0), "+{v[112:127]}"(SY1), \
              "+{v[128:143]}"(OX), "+{v[144:159]}"(OY), \
              "+{v[168:171]}"(K0), "+{v[172:175]}"(K1), "+{v[176:179]}"(V00), "+{v[180:183]}"(V01), \
              "+{v[184:187]}"(V10), "+{v[188:191]}"(V11), "+{v193}"(ka), "+{v194}"(va), [cnt] "=&s"(cnt_) \
            : "{v[160:163]}"(qx), "{v[164:167]}"(qy), "{v192}"(idx), "{v195}"(vinc) \
            : "memory", "scc"); } while (0)
    ATT_RUN(1);
    VMWAIT(19); BAR();
    ATT_RUN(1);
    asm volatile("s_waitcnt vmcnt(0)" : "+v"(wd0), "+v"(wd1), "+v"(wd2), "+v"(wd3), "+v"(wd4), "+v"(wd5), "+v"(wd6), "+v"(wd7) :: "memory");
    BAR();
    if (ffn) {
        float sum = LDF(L_FP + l15 * 4);
#pragma unroll
        for (int i = 1; i < 8; ++i) sum += LDF(L_FP + i * 64 + l15 * 4);
        sum = bfr(sum);
        float up = __shfl(sum, (lane & 7) + 8);
        float gt = __shfl(sum, lane & 7);
        float hv = bfr(bfr(gt * sigm(gt)) * up);
        u32 hb = __float_as_uint(hv);
        u32 h01 = ((u32)__builtin_amdgcn_readlane(hb, 0) >> 16) | ((u32)__builtin_amdgcn_readlane(hb, 1) & 0xffff0000u);
        u32 h23 = ((u32)__builtin_amdgcn_readlane(hb, 2) >> 16) | ((u32)__builtin_amdgcn_readlane(hb, 3) & 0xffff0000u);
        u32 h45 = ((u32)__builtin_amdgcn_readlane(hb, 4) >> 16) | ((u32)__builtin_amdgcn_readlane(hb, 5) & 0xffff0000u);
        u32 h67 = ((u32)__builtin_amdgcn_readlane(hb, 6) >> 16) | ((u32)__builtin_amdgcn_readlane(hb, 7) & 0xffff0000u);
        float y = lo16(h01) * lo16(wd0) + hi16(h01) * lo16(wd1) + lo16(h23) * lo16(wd2) + hi16(h23) * lo16(wd3)
                + lo16(h45) * lo16(wd4) + hi16(h45) * lo16(wd5) + lo16(h67) * lo16(wd6) + hi16(h67) * lo16(wd7);
        u32x2 gr = {__float_as_uint(y), epoch};
        u32 yo = WS_Y + (xcd * 28 + jj) * 4096 + (64 * w + lane) * 8;
        GST64C(ws, yo, gr);
    }
    ATT_RUN(2);
    BAR();
#pragma unroll
    for (int i = 0; i < 8; ++i) {
        int q8 = w * 8 + i;
        u32 row = 8 * q8 + (lane >> 3), slot = lane & 7;
        u32 f = ((row >> 1) & 1) | (((row >> 3) & 1) << 1);
        u32 c = (((slot >> 1) ^ f) << 1) | (slot & 1);
        int reg = q8 >> 4;
        u32 dst = ((reg & 2) ? L_V : L_K) + (reg & 1) * 32768 + (q8 & 15) * 1024;
        dma16(wo, row * 1024 + hg * 128 + c * 16, lb + dst);
    }
    ATT_RUN(4);
    asm volatile(
        "s_nop 1\n"
        "v_smfmac_f32_32x32x32_bf16 v[144:159], v[188:191], v[120:127], v192\n"
        "s_nop 7\n s_nop 7\n s_nop 3\n s_waitcnt lgkmcnt(0)\n"
        : "+{v[144:159]}"(OY), "+{v[128:143]}"(OX), "+{v[112:127]}"(SY1), "+{v[188:191]}"(V11)
        : "{v192}"(idx) : "memory");
    STAMP(12); STAMPC(20);

    const int th = w & 1, hp = (w >> 1) & 1, blk = w >> 2;
    {
        f32x16& mine = blk ? OY : OX;
        f32x16& give = blk ? OX : OY;
        const u32 cw = L_W + w * 2304, cr = L_W + (w ^ 4) * 2304;
        ST128(cw + lane * 16, ((u32x4){__float_as_uint(give[0]), __float_as_uint(give[1]), __float_as_uint(give[2]), __float_as_uint(give[3])}));
        ST128(cw + 1024 + lane * 16, ((u32x4){__float_as_uint(give[4]), __float_as_uint(give[5]), __float_as_uint(give[6]), __float_as_uint(give[7])}));
        STF(cw + 2048 + lane * 4, give[8]);
        BAR();
        f32x4 p0 = __builtin_bit_cast(f32x4, LD128(cr + lane * 16));
        f32x4 p1 = __builtin_bit_cast(f32x4, LD128(cr + 1024 + lane * 16));
        float l = LDF(cr + 2048 + lane * 4) + mine[8];
        float o[8];
#pragma unroll
        for (int i = 0; i < 4; ++i) { o[i] = mine[i] + p0[i]; o[4 + i] = mine[4 + i] + p1[i]; }
        {
            auto sw = __builtin_amdgcn_permlane32_swap(__float_as_uint(l), __float_as_uint(l), false, false);
            l = __uint_as_float(sw[0]);
        }
        float amax = fmaxf(fmaxf(fmaxf(fabsf(o[0]), fabsf(o[1])), fmaxf(fabsf(o[2]), fabsf(o[3]))),
                           fmaxf(fmaxf(fabsf(o[4]), fabsf(o[5])), fmaxf(fabsf(o[6]), fabsf(o[7]))));
        bool safe = (l >= 8.6736174e-19f) && (l <= 1.2676506e30f) && (amax <= 3.0e38f);
        if (!__all(safe)) {
            slow_block(ws + WS_KV, blk ? qy : qx, lane, o, l);
            auto sw = __builtin_amdgcn_permlane32_swap(__float_as_uint(l), __float_as_uint(l), false, false);
            l = __uint_as_float(sw[0]);
        }
        const float ri = __builtin_amdgcn_rcpf(l);
        u32 A0 = pk(o[0] * ri, o[1] * ri), A1 = pk(o[2] * ri, o[3] * ri);
        u32 B0 = pk(o[4] * ri, o[5] * ri), B1 = pk(o[6] * ri, o[7] * ri);
        auto s0 = __builtin_amdgcn_permlane32_swap(A0, B0, false, false);
        auto s1 = __builtin_amdgcn_permlane32_swap(A1, B1, false, false);
        u32x4 ov = {s0[0], s1[0], s0[1], s1[1]};
        const int head = hg * 4 + hp * 2 + blk;
        const int tl = th * 32 + l31;
        u32 oo = WS_O + ((tb * 4 + (tl >> 4)) * 16 + (head >> 1)) * 1024 + (((head & 1) * 2 + h) * 16 + (tl & 15)) * 16;
        GST128C(ws, oo, ov);
        VMWAIT(0);
    }
    BAR();
    if (tid == 0) { u32x2 fl = {0u, epoch}; u32 fo = WS_F2 + tb * 128 + hg * 8; GST64C(ws, fo, fl); }
    STAMP(13);

    const int tq = w & 3, kh = w >> 2;
    const int tokg = tb * 64 + tq * 16 + l15;
    const int ncol = hg * 64 + kh * 32 + (g & 1) * 16 + (g >> 1) * 8;
    const bool owner = (sel >> 6) == tb;
    if (owner) {
        u32x2 gr[28];
        const u32 yo = WS_Y + (28 * w) * 4096 + (hg * 64 + lane) * 8;
        u32 spins = 0;
        while (true) {
#pragma unroll
            for (int i = 0; i < 28; ++i) { u32 a = yo + i * 4096; GLD64C(gr[i], ws, a, 0); }
            asm volatile("s_waitcnt vmcnt(0)" : "+v"(gr[0]), "+v"(gr[1]), "+v"(gr[2]), "+v"(gr[3]), "+v"(gr[4]), "+v"(gr[5]), "+v"(gr[6]), "+v"(gr[7]), "+v"(gr[8]), "+v"(gr[9]), "+v"(gr[10]), "+v"(gr[11]), "+v"(gr[12]), "+v"(gr[13]), "+v"(gr[14]), "+v"(gr[15]), "+v"(gr[16]), "+v"(gr[17]), "+v"(gr[18]), "+v"(gr[19]), "+v"(gr[20]), "+v"(gr[21]), "+v"(gr[22]), "+v"(gr[23]), "+v"(gr[24]), "+v"(gr[25]), "+v"(gr[26]), "+v"(gr[27]) :: "memory");
            bool ok = true;
#pragma unroll
            for (int i = 0; i < 28; ++i) ok &= (gr[i][1] == epoch);
            if (__all(ok) || ++spins > SPIN_MAX) break;
        }
        float ys = 0.f;
#pragma unroll
        for (int i = 0; i < 28; ++i) ys += __uint_as_float(gr[i][0]);
        STF(L_YP + w * 256 + lane * 4, ys);
    }
    u32x4 af[4][8];
    {
        const u32 f = ((l15 >> 3) & 1) | ((g & 1) << 1);
        const u32 rb = (kh ? L_V : L_K) + (8 * g + (l15 >> 2)) * 128 + (l15 & 3) * 8;
#pragma unroll
        for (int nb = 0; nb < 4; ++nb) {
            const u32 a = rb + ((nb ^ f) * 32);
#pragma unroll
            for (int st = 0; st < 8; ++st) {
                const u32 o_ = (st >> 2) * 32768 + (st & 3) * 4096;
                af[nb][st] = cat(trread(lds, a + o_), trread(lds, a + o_ + 512));
            }
        }
    }
    STAMP(14);
    {
        u32 fo = WS_F2 + tb * 128 + (kh * 4 + (lane & 3)) * 8;
        u32x2 fl; u32 spins = 0;
        while (true) {
            GLD64C(fl, ws, fo, 0);
            asm volatile("s_waitcnt vmcnt(0)" : "+v"(fl) :: "memory");
            if (__all(fl[1] == epoch) || ++spins > SPIN_MAX) break;
            __builtin_amdgcn_s_sleep(1);
        }
    }
    STAMP(15);
    u32x4 xe; u32 gl;
    {
        u32 xo = tokg * 1024 + ncol * 2;
        GLD128(xe, x, xo, 0);
        u32 go = WS_G + tokg * 4;
        GLD32C(gl, ws, go, 0);
    }
    u32x4 of0, of1, of2, of3, of4, of5, of6, of7;
    {
        u32 oo = WS_O + ((tb * 4 + tq) * 16 + 8 * kh) * 1024 + lane * 16;
        GLD128C(of0, ws, oo, 0); GLD128C(of1, ws, oo, 1024); GLD128C(of2, ws, oo, 2048); GLD128C(of3, ws, oo, 3072);
        u32 o2 = oo + 4096;
        GLD128C(of4, ws, o2, 0); GLD128C(of5, ws, o2, 1024); GLD128C(of6, ws, o2, 2048); GLD128C(of7, ws, o2, 3072);
    }
    f32x4 acc[4] = {{0, 0, 0, 0}, {0, 0, 0, 0}, {0, 0, 0, 0}, {0, 0, 0, 0}};
#define OSTEP(N, OF, ST) \
    asm volatile("s_waitcnt vmcnt(" #N ")" : "+v"(OF) :: "memory"); \
    _Pragma("unroll") for (int nb = 0; nb < 4; ++nb) acc[nb] = __builtin_amdgcn_mfma_f32_16x16x32_bf16(asbf(af[nb][ST]), asbf(OF), acc[nb], 0, 0, 0);
    asm volatile("s_waitcnt vmcnt(8)" : "+v"(xe), "+v"(gl) :: "memory");
    OSTEP(7, of0, 0) OSTEP(6, of1, 1) OSTEP(5, of2, 2) OSTEP(4, of3, 3)
    OSTEP(3, of4, 4) OSTEP(2, of5, 5) OSTEP(1, of6, 6) OSTEP(0, of7, 7)
    STAMP(16);
    {
        f32x4 k0 = kh ? acc[2] : acc[0], k1 = kh ? acc[3] : acc[1];
        f32x4 g0 = kh ? acc[0] : acc[2], g1 = kh ? acc[1] : acc[3];
        ST128(L_W + w * 2048 + lane * 16, __builtin_bit_cast(u32x4, g0));
        ST128(L_W + w * 2048 + 1024 + lane * 16, __builtin_bit_cast(u32x4, g1));
        BAR();
        k0 += __builtin_bit_cast(f32x4, LD128(L_W + (w ^ 4) * 2048 + lane * 16));
        k1 += __builtin_bit_cast(f32x4, LD128(L_W + (w ^ 4) * 2048 + 1024 + lane * 16));
        float a[8];
#pragma unroll
        for (int i = 0; i < 4; ++i) {
            auto sw = __builtin_amdgcn_permlane16_swap(__float_as_uint(k0[i]), __float_as_uint(k1[i]), false, false);
            a[i] = __uint_as_float(sw[0]); a[4 + i] = __uint_as_float(sw[1]);
        }
        const float gt = bfr(sigm(bfr(__uint_as_float(gl))));
        const float og = bfr(1.0f - gt);
        float bb[8];
#pragma unroll
        for (int i = 0; i < 4; ++i) { bb[2 * i] = lo16(xe[i]); bb[2 * i + 1] = hi16(xe[i]); }
        if (owner && tokg == sel) {
            const float sg = bfr(sigm(rsel));
            const int c0 = kh * 32 + (g & 1) * 16 + (g >> 1) * 8;
#pragma unroll
            for (int i = 0; i < 8; ++i) {
                float y = 0.f;
#pragma unroll
                for (int p = 0; p < 8; ++p) y += LDF(L_YP + p * 256 + (c0 + i) * 4);
                bb[i] = bfr(bb[i] + bfr(bfr(y) * sg));
            }
        }
        u32x4 ov;
#pragma unroll
        for (int i = 0; i < 4; ++i)
            ov[i] = pk(bfr(gt * bfr(a[2 * i])) + bfr(og * bb[2 * i]), bfr(gt * bfr(a[2 * i + 1])) + bfr(og * bb[2 * i + 1]));
        { u32 oo_ = tokg * 1024 + ncol * 2; asm volatile("global_store_dwordx4 %0, %1, %2 nt\n\ts_nop 1" :: "v"(oo_), "v"(ov), "s"(out) : "memory"); }
    }
    STAMP(17); STAMPC(21); STAMPDUMP();
}

extern "C" void kernel_launch(void* const* d_in, const int* in_sizes, int n_in, void* d_out, int out_size,
                              void* d_ws, size_t ws_size, hipStream_t stream) {
    (void)in_sizes; (void)n_in; (void)out_size; (void)ws_size;
    fused_kernel<<<256, 512, 0, stream>>>((const u16*)d_in[0], (const u16*)d_in[1], (const u16*)d_in[2], (const u16*)d_in[3],
                                          (const u16*)d_in[4], (const u16*)d_in[6], (const u16*)d_in[7], (const u16*)d_in[8],
                                          (const u16*)d_in[9], (const u16*)d_in[10], (u16*)d_out, (char*)d_ws);
}
```

```cpp
#include <hip/hip_runtime.h>
#include <stdint.h>

typedef unsigned int u32;
typedef unsigned long long u64;
typedef unsigned short u16;
typedef u32 u32x2 __attribute__((ext_vector_type(2)));
typedef u32 u32x4 __attribute__((ext_vector_type(4)));
typedef float f32x2 __attribute__((ext_vector_type(2)));
typedef float f32x4 __attribute__((ext_vector_type(4)));
typedef float f32x16 __attribute__((ext_vector_type(16)));
typedef __bf16 bf16x2 __attribute__((ext_vector_type(2)));
typedef __bf16 bf16x8 __attribute__((ext_vector_type(8)));
typedef short s16x4 __attribute__((ext_vector_type(4)));
#define LDSP __attribute__((address_space(3)))

constexpr u32 WS_E   = 0;
constexpr u32 WS_F1  = 4096;
constexpr u32 WS_F2  = 20480;
constexpr u32 WS_G   = 24576;
constexpr u32 WS_KV  = 65536;
constexpr u32 WS_O   = 262144;
constexpr u32 WS_Y   = 2621440;
constexpr u32 WS_DBG = 4194304;

constexpr u32 L_K   = 0;
constexpr u32 L_V   = 65536;
constexpr u32 L_ONE = 131072;
constexpr u32 L_W   = 135168;
constexpr u32 L_M   = 151552;
constexpr u32 L_P1  = L_M;
constexpr u32 L_PD  = L_M + 8192;
constexpr u32 L_FP  = L_M + 8704;
constexpr u32 L_YP  = L_M + 9216;
constexpr u32 L_CT  = L_M + 11264;
constexpr u32 L_END = 163840;

constexpr u32 SPIN_MAX = 1u << 22;

#define LD128(a) (*(const LDSP u32x4*)(lds + (a)))
#define ST128(a, v) (*(LDSP u32x4*)(lds + (a)) = (v))
#define LD32(a) (*(const LDSP u32*)(lds + (a)))
#define ST32(a, v) (*(LDSP u32*)(lds + (a)) = (v))
#define LDF(a) (*(const LDSP float*)(lds + (a)))
#define STF(a, v) (*(LDSP float*)(lds + (a)) = (v))

__device__ __forceinline__ u32x2 trread(LDSP char* lds, u32 a) {
    typedef short v4s __attribute__((ext_vector_type(4)));
    v4s r = __builtin_amdgcn_ds_read_tr16_b64_v4i16((LDSP v4s*)(lds + a));
    return __builtin_bit_cast(u32x2, r);
}
__device__ __forceinline__ bf16x8 asbf(u32x4 v) { return __builtin_bit_cast(bf16x8, v); }
__device__ __forceinline__ u32x4 cat(u32x2 a, u32x2 b) { u32x4 r = {a[0], a[1], b[0], b[1]}; return r; }
__device__ __forceinline__ u32 pk(float a, float b) {
    f32x2 f = {a, b};
    return __builtin_bit_cast(u32, __builtin_convertvector(f, bf16x2));
}
__device__ __forceinline__ float bfr(float a) {
    return __uint_as_float(pk(a, a) & 0xffff0000u);
}
__device__ __forceinline__ float lo16(u32 v) { return __uint_as_float(v << 16); }
__device__ __forceinline__ float hi16(u32 v) { return __uint_as_float(v & 0xffff0000u); }
__device__ __forceinline__ float sigm(float z) { return __builtin_amdgcn_rcpf(1.0f + __builtin_amdgcn_exp2f(-1.4426950408889634f * z)); }

__device__ __forceinline__ void dma16(const void* base, u32 voff, u32 ldsaddr) {
    asm volatile("s_mov_b32 m0, %0\n\ts_nop 0\n\tglobal_load_lds_dwordx4 %1, %2" :: "s"(ldsaddr), "v"(voff), "s"(base) : "memory");
}
__device__ __forceinline__ void dma16c(const void* base, u32 voff, u32 ldsaddr) {
    asm volatile("s_mov_b32 m0, %0\n\ts_nop 0\n\tglobal_load_lds_dwordx4 %1, %2 sc1" :: "s"(ldsaddr), "v"(voff), "s"(base) : "memory");
}
#define GLD128(dst, base, voff, OFF)  asm volatile("global_load_dwordx4 %0, %1, %2 offset:" #OFF : "=v"(dst) : "v"(voff), "s"(base) : "memory")
#define GLD128C(dst, base, voff, OFF) asm volatile("global_load_dwordx4 %0, %1, %2 offset:" #OFF " sc1" : "=v"(dst) : "v"(voff), "s"(base) : "memory")
#define GLD64C(dst, base, voff, OFF)  asm volatile("global_load_dwordx2 %0, %1, %2 offset:" #OFF " sc1" : "=v"(dst) : "v"(voff), "s"(base) : "memory")
#define GLD32C(dst, base, voff, OFF)  asm volatile("global_load_dword %0, %1, %2 offset:" #OFF " sc1" : "=v"(dst) : "v"(voff), "s"(base) : "memory")
#define GLD16(dst, base, voff, OFF)   asm volatile("global_load_ushort %0, %1, %2 offset:" #OFF : "=v"(dst) : "v"(voff), "s"(base) : "memory")
#define GST128C(base, voff, val) asm volatile("global_store_dwordx4 %0, %1, %2 sc1\n\ts_nop 1" :: "v"(voff), "v"(val), "s"(base) : "memory")
#define GST64C(base, voff, val)  asm volatile("global_store_dwordx2 %0, %1, %2 sc1" :: "v"(voff), "v"(val), "s"(base) : "memory")
#define GST32C(base, voff, val)  asm volatile("global_store_dword %0, %1, %2 sc1" :: "v"(voff), "v"(val), "s"(base) : "memory")
#define VMWAIT(N) asm volatile("s_waitcnt vmcnt(" #N ")" ::: "memory")
#define BAR() asm volatile("s_waitcnt lgkmcnt(0)\n\ts_barrier" ::: "memory")

#define STAMP(i)
#define STAMPC(i)
#define STAMPDUMP()

__device__ __forceinline__ u32 wave_max_u32(u32 v) {
    v = max(v, (u32)__builtin_amdgcn_update_dpp(0, (int)v, 0x111, 0xf, 0xf, false));
    v = max(v, (u32)__builtin_amdgcn_update_dpp(0, (int)v, 0x112, 0xf, 0xf, false));
    v = max(v, (u32)__builtin_amdgcn_update_dpp(0, (int)v, 0x114, 0xf, 0xf, false));
    v = max(v, (u32)__builtin_amdgcn_update_dpp(0, (int)v, 0x118, 0xf, 0xf, false));
    v = max(v, (u32)__builtin_amdgcn_update_dpp(0, (int)v, 0x142, 0xa, 0xf, false));
    v = max(v, (u32)__builtin_amdgcn_update_dpp(0, (int)v, 0x143, 0xc, 0xf, false));
    return (u32)__builtin_amdgcn_readlane((int)v, 63);
}
#define ATT_BODY4 \
    "v_exp_f32 v64, v64\n" \
    "v_exp_f32 v65, v65\n" \
    "v_smfmac_f32_32x32x32_bf16 v[144:159], v[188:191], v[120:127], v192\n" \
    "ds_read_b128 v[184:187], v194 offset:1024\n" \
    "v_exp_f32 v66, v66\n" \
    "v_exp_f32 v67, v67\n" \
    "v_exp_f32 v68, v68\n" \
    "v_exp_f32 v69, v69\n" \
    "s_waitcnt lgkmcnt(1)\n" \
    "v_mfma_f32_32x32x16_bf16 v[80:95], v[172:175], v[160:163], 0\n" \
    "ds_read_b128 v[188:191], v194 offset:1536\n" \
    "v_exp_f32 v70, v70\n" \
    "v_exp_f32 v71, v71\n" \
    "v_exp_f32 v72, v72\n" \
    "v_exp_f32 v73, v73\n" \
    "v_smfmac_f32_32x32x32_bf16 v[128:143], v[176:179], v[64:71], v192\n" \
    "v_exp_f32 v74, v74\n" \
    "v_exp_f32 v75, v75\n" \
    "v_exp_f32 v76, v76\n" \
    "v_exp_f32 v77, v77\n" \
    "v_exp_f32 v78, v78\n" \
    "v_exp_f32 v79, v79\n" \
    "v_exp_f32 v96, v96\n" \
    "v_exp_f32 v97, v97\n" \
    "v_smfmac_f32_32x32x32_bf16 v[128:143], v[180:183], v[72:79], v192\n" \
    "v_exp_f32 v98, v98\n" \
    "v_exp_f32 v99, v99\n" \
    "v_exp_f32 v100, v100\n" \
    "v_exp_f32 v101, v101\n" \
    "v_mfma_f32_32x32x16_bf16 v[112:127], v[172:175], v[164:167], 0\n" \
    "ds_read_b128 v[168:171], v193 offset:2048\n" \
    "v_exp_f32 v102, v102\n" \
    "v_exp_f32 v103, v103\n" \
    "v_exp_f32 v104, v104\n" \
    "v_exp_f32 v105, v105\n" \
    "v_smfmac_f32_32x32x32_bf16 v[144:159], v[176:179], v[96:103], v192\n" \
    "v_exp_f32 v106, v106\n" \
    "v_exp_f32 v107, v107\n" \
    "v_exp_f32 v108, v108\n" \
    "v_exp_f32 v109, v109\n" \
    "v_exp_f32 v110, v110\n" \
    "v_exp_f32 v111, v111\n" \
    "v_exp_f32 v80, v80\n" \
    "v_exp_f32 v81, v81\n" \
    "v_smfmac_f32_32x32x32_bf16 v[144:159], v[180:183], v[104:111], v192\n" \
    "ds_read_b128 v[176:179], v194 offset:2048\n" \
    "v_exp_f32 v82, v82\n" \
    "v_exp_f32 v83, v83\n" \
    "v_exp_f32 v84, v84\n" \
    "v_exp_f32 v85, v85\n" \
    "s_waitcnt lgkmcnt(1)\n" \
    "v_mfma_f32_32x32x16_bf16 v[64:79], v[168:171], v[160:163], 0\n" \
    "ds_read_b128 v[180:183], v194 offset:2560\n" \
    "v_exp_f32 v86, v86\n" \
    "v_exp_f32 v87, v87\n" \
    "v_exp_f32 v88, v88\n" \
    "v_exp_f32 v89, v89\n" \
    "v_smfmac_f32_32x32x32_bf16 v[128:143], v[184:187], v[80:87], v192\n" \
    "v_exp_f32 v90, v90\n" \
    "v_exp_f32 v91, v91\n" \
    "v_exp_f32 v92, v92\n" \
    "v_exp_f32 v93, v93\n" \
    "v_exp_f32 v94, v94\n" \
    "v_exp_f32 v95, v95\n" \
    "v_exp_f32 v112, v112\n" \
    "v_exp_f32 v113, v113\n" \
    "v_smfmac_f32_32x32x32_bf16 v[128:143], v[188:191], v[88:95], v192\n" \
    "v_exp_f32 v114, v114\n" \
    "v_exp_f32 v115, v115\n" \
    "v_exp_f32 v116, v116\n" \
    "v_exp_f32 v117, v117\n" \
    "v_mfma_f32_32x32x16_bf16 v[96:111], v[168:171], v[164:167], 0\n" \
    "ds_read_b128 v[172:175], v193 offset:3072\n" \
    "v_exp_f32 v118, v118\n" \
    "v_exp_f32 v119, v119\n" \
    "v_exp_f32 v120, v120\n" \
    "v_exp_f32 v121, v121\n" \
    "v_smfmac_f32_32x32x32_bf16 v[144:159], v[184:187], v[112:119], v192\n" \
    "v_exp_f32 v122, v122\n" \
    "v_exp_f32 v123, v123\n" \
    "v_exp_f32 v124, v124\n" \
    "v_exp_f32 v125, v125\n" \
    "v_exp_f32 v126, v126\n" \
    "v_exp_f32 v127, v127\n" \
    "v_exp_f32 v64, v64\n" \
    "v_exp_f32 v65, v65\n" \
    "v_smfmac_f32_32x32x32_bf16 v[144:159], v[188:191], v[120:127], v192\n" \
    "ds_read_b128 v[184:187], v194 offset:3072\n" \
    "v_exp_f32 v66, v66\n" \
    "v_exp_f32 v67, v67\n" \
    "v_exp_f32 v68, v68\n" \
    "v_exp_f32 v69, v69\n" \
    "s_waitcnt lgkmcnt(1)\n" \
    "v_mfma_f32_32x32x16_bf16 v[80:95], v[172:175], v[160:163], 0\n" \
    "ds_read_b128 v[188:191], v194 offset:3584\n" \
    "v_exp_f32 v70, v70\n" \
    "v_exp_f32 v71, v71\n" \
    "v_exp_f32 v72, v72\n" \
    "v_exp_f32 v73, v73\n" \
    "v_smfmac_f32_32x32x32_bf16 v[128:143], v[176:179], v[64:71], v192\n" \
    "v_exp_f32 v74, v74\n" \
    "v_exp_f32 v75, v75\n" \
    "v_exp_f32 v76, v76\n" \
    "v_exp_f32 v77, v77\n" \
    "v_exp_f32 v78, v78\n" \
    "v_exp_f32 v79, v79\n" \
    "v_exp_f32 v96, v96\n" \
    "v_exp_f32 v97, v97\n" \
    "v_smfmac_f32_32x32x32_bf16 v[128:143], v[180:183], v[72:79], v192\n" \
    "v_exp_f32 v98, v98\n" \
    "v_exp_f32 v99, v99\n" \
    "v_exp_f32 v100, v100\n" \
    "v_exp_f32 v101, v101\n" \
    "v_mfma_f32_32x32x16_bf16 v[112:127], v[172:175], v[164:167], 0\n" \
    "ds_read_b128 v[168:171], v193 offset:4096\n" \
    "v_exp_f32 v102, v102\n" \
    "v_exp_f32 v103, v103\n" \
    "v_exp_f32 v104, v104\n" \
    "v_exp_f32 v105, v105\n" \
    "v_smfmac_f32_32x32x32_bf16 v[144:159], v[176:179], v[96:103], v192\n" \
    "v_exp_f32 v106, v106\n" \
    "v_exp_f32 v107, v107\n" \
    "v_exp_f32 v108, v108\n" \
    "v_exp_f32 v109, v109\n" \
    "v_exp_f32 v110, v110\n" \
    "v_exp_f32 v111, v111\n" \
    "v_exp_f32 v80, v80\n" \
    "v_exp_f32 v81, v81\n" \
    "v_smfmac_f32_32x32x32_bf16 v[144:159], v[180:183], v[104:111], v192\n" \
    "ds_read_b128 v[176:179], v194 offset:4096\n" \
    "v_exp_f32 v82, v82\n" \
    "v_exp_f32 v83, v83\n" \
    "v_exp_f32 v84, v84\n" \
    "v_exp_f32 v85, v85\n" \
    "s_waitcnt lgkmcnt(1)\n" \
    "v_mfma_f32_32x32x16_bf16 v[64:79], v[168:171], v[160:163], 0\n" \
    "ds_read_b128 v[180:183], v194 offset:4608\n" \
    "v_exp_f32 v86, v86\n" \
    "v_exp_f32 v87, v87\n" \
    "v_exp_f32 v88, v88\n" \
    "v_exp_f32 v89, v89\n" \
    "v_smfmac_f32_32x32x32_bf16 v[128:143], v[184:187], v[80:87], v192\n" \
    "v_exp_f32 v90, v90\n" \
    "v_exp_f32 v91, v91\n" \
    "v_exp_f32 v92, v92\n" \
    "v_exp_f32 v93, v93\n" \
    "v_exp_f32 v94, v94\n" \
    "v_exp_f32 v95, v95\n" \
    "v_exp_f32 v112, v112\n" \
    "v_exp_f32 v113, v113\n" \
    "v_smfmac_f32_32x32x32_bf16 v[128:143], v[188:191], v[88:95], v192\n" \
    "v_exp_f32 v114, v114\n" \
    "v_exp_f32 v115, v115\n" \
    "v_exp_f32 v116, v116\n" \
    "v_exp_f32 v117, v117\n" \
    "v_mfma_f32_32x32x16_bf16 v[96:111], v[168:171], v[164:167], 0\n" \
    "ds_read_b128 v[172:175], v193 offset:5120\n" \
    "v_exp_f32 v118, v118\n" \
    "v_exp_f32 v119, v119\n" \
    "v_exp_f32 v120, v120\n" \
    "v_exp_f32 v121, v121\n" \
    "v_smfmac_f32_32x32x32_bf16 v[144:159], v[184:187], v[112:119], v192\n" \
    "v_exp_f32 v122, v122\n" \
    "v_exp_f32 v123, v123\n" \
    "v_exp_f32 v124, v124\n" \
    "v_exp_f32 v125, v125\n" \
    "v_exp_f32 v126, v126\n" \
    "v_exp_f32 v127, v127\n" \
    "v_add_u32 v193, 4096, v193\n" \
    "v_add_u32 v194, v195, v194\n"

__device__ __forceinline__ void slow_block(const char* kv, u32x4 q, int lane, float* o, float& lsum) {
    const u32 koff = lane * 16;
    const u32 r32 = lane & 31;
    float m = -3.0e38f;
    for (int t = 0; t < 64; ++t) {
        u32x4 kf;
        GLD128C(kf, kv, koff + t * 2048, 0);
        asm volatile("s_waitcnt vmcnt(0)" : "+v"(kf) :: "memory");
        f32x16 s = {0};
        s = __builtin_amdgcn_mfma_f32_32x32x16_bf16(asbf(kf), asbf(q), s, 0, 0, 0);
#pragma unroll
        for (int i = 0; i < 16; ++i) m = fmaxf(m, s[i]);
    }
    {
        auto sw = __builtin_amdgcn_permlane32_swap(__float_as_uint(m), __float_as_uint(m), false, false);
        m = fmaxf(__uint_as_float(sw[0]), __uint_as_float(sw[1]));
    }
    f32x16 acc = {0};
    const u32 voff = 1024 + (lane >> 5) * 256 + (lane & 15) * 16;
    for (int t = 0; t < 64; ++t) {
        u32x4 kf, v0, v1;
        GLD128C(kf, kv, koff + t * 2048, 0);
        GLD128C(v0, kv, voff + t * 2048, 0);
        GLD128C(v1, kv, voff + t * 2048, 512);
        asm volatile("s_waitcnt vmcnt(0)" : "+v"(kf), "+v"(v0), "+v"(v1) :: "memory");
        if (r32 >= 16) { v0 = (u32x4){0x3f803f80u, 0x3f803f80u, 0x3f803f80u, 0x3f803f80u}; v1 = v0; }
        f32x16 s = {0};
        s = __builtin_amdgcn_mfma_f32_32x32x16_bf16(asbf(kf), asbf(q), s, 0, 0, 0);
        u32 p[8];
#pragma unroll
        for (int i = 0; i < 8; ++i)
            p[i] = pk(__builtin_amdgcn_exp2f(s[2 * i] - m), __builtin_amdgcn_exp2f(s[2 * i + 1] - m));
#pragma unroll
        for (int st = 0; st < 2; ++st) {
            u32x4 vv = st ? v1 : v0;
            auto a = __builtin_amdgcn_permlane32_swap(vv[0], vv[2], false, false);
            auto c = __builtin_amdgcn_permlane32_swap(vv[1], vv[3], false, false);
            u32x4 af = {a[0], c[0], a[1], c[1]};
            u32x4 bf = {p[4 * st], p[4 * st + 1], p[4 * st + 2], p[4 * st + 3]};
            acc = __builtin_amdgcn_mfma_f32_32x32x16_bf16(asbf(af), asbf(bf), acc, 0, 0, 0);
        }
    }
#pragma unroll
    for (int i = 0; i < 8; ++i) o[i] = acc[i];
    lsum = acc[8];
}

typedef u32 u32x16 __attribute__((ext_vector_type(16)));
typedef u32 u32x8 __attribute__((ext_vector_type(8)));
__global__ void __launch_bounds__(512, 2)
fused_kernel(const u16* __restrict__ x_, const u16* __restrict__ wq_, const u16* __restrict__ wk_, const u16* __restrict__ wv_,
             const u16* __restrict__ wo_, const u16* __restrict__ wr_, const u16* __restrict__ wg_, const u16* __restrict__ wu_,
             const u16* __restrict__ wd_, const u16* __restrict__ gw_, u16* __restrict__ out_, char* __restrict__ ws_)
{
    u32x16 ka0; u32x8 ka1; u64 T_0, T_1;
    {
        const void* kp = (const void*)__builtin_amdgcn_kernarg_segment_ptr();
        asm volatile("s_memrealtime %2\n\ts_waitcnt lgkmcnt(0)\n\ts_load_dwordx16 %0, %4, 0\n\ts_load_dwordx8 %1, %4, 64\n\ts_waitcnt lgkmcnt(0)\n\ts_memrealtime %3\n\ts_waitcnt lgkmcnt(0)"
                     : "=&s"(ka0), "=&s"(ka1), "=&s"(T_0), "=&s"(T_1) : "s"(kp) : "memory");
    }
#define KP(v, i) ((u64)v[2 * (i)] | ((u64)v[2 * (i) + 1] << 32))
    const u16* x = (const u16*)KP(ka0, 0); const u16* wq = (const u16*)KP(ka0, 1); const u16* wk = (const u16*)KP(ka0, 2); const u16* wv = (const u16*)KP(ka0, 3);
    const u16* wo = (const u16*)KP(ka0, 4); const u16* wr = (const u16*)KP(ka0, 5); const u16* wg = (const u16*)KP(ka0, 6); const u16* wu = (const u16*)KP(ka0, 7);
    const u16* wd = (const u16*)KP(ka1, 0); const u16* gw = (const u16*)KP(ka1, 1); u16* out = (u16*)KP(ka1, 2); char* ws = (char*)KP(ka1, 3);
    __shared__ __attribute__((aligned(1024))) char smem[L_END];
    LDSP char* lds = (LDSP char*)smem;
    const u32 lb = (u32)(size_t)lds;
    const int tid = threadIdx.x;
    const int lane = tid & 63;
    const int w = __builtin_amdgcn_readfirstlane(tid >> 6);
    const int b = blockIdx.x;
    const int xcd = b & 7, jj = b >> 3;
    const int tb = xcd * 4 + (jj >> 3), hg = jj & 7;
    const int g = lane >> 4, l15 = lane & 15, h = lane >> 5, l31 = lane & 31;
    const bool isK = hg < 4;
    const bool ffn = jj < 28;
    const int col0 = (xcd * 28 + jj) * 8;
    STAMP(1); STAMPC(18);

    u32 epo;
    { u32 z = 0; GLD32C(epo, ws, z, 0); }
    const int T0 = tb * 64 + (hg & 3) * 16;
    u32x4 xf0, xf1, vf0, vf1;
    {
        const u16* wsrc = isK ? wk : wv;
#pragma unroll
        for (int i = 0; i < 2; ++i) {
            u32 slot = 64 * i + lane, pos = slot >> 3, B = pos ^ ((pos >> 2) & 1);
            u32 row = 4 * B + ((slot >> 1) & 3);
            dma16(wsrc, (64 * w + row) * 32 + (slot & 1) * 16, lb + L_W + w * 2048 + i * 1024);
        }
        u32 xo = (T0 + l15) * 1024 + (64 * w + 16 * g) * 2;
        GLD128(xf0, x, xo, 0);
        GLD128(xf1, x, xo, 16);
        const u16* vec = isK ? wr : gw;
        u32 vo = (64 * w + 16 * g) * 2;
        GLD128(vf0, vec, vo, 0);
        GLD128(vf1, vec, vo, 16);
    }
    if (w < 7) {
        for (int p = w; p < 128; p += 7) {
            if (p < 64) {
                dma16(x, (tb * 64 + p) * 1024 + ((lane ^ (p & 15)) << 4), lb + L_K + p * 1024);
            } else {
                int G = p - 64;
                dma16(wq, (8 * G + ((lane >> 2) & 7)) * 1024 + hg * 128 + (lane >> 5) * 64 + (lane & 3) * 16, lb + L_V + G * 1024);
            }
        }
    }
    if (tid < 256) { u32 o1 = (tid & 15) == 0 ? 0x3f803f80u : 0u; ST128(L_ONE + tid * 16, ((u32x4){o1, o1, o1, o1})); }
    asm volatile("s_cmp_eq_u32 %[w], 7\n\ts_cbranch_scc1 1f\n\ts_waitcnt vmcnt(18)\n\ts_branch 2f\n1:\n\ts_waitcnt vmcnt(0)\n2:"
                 : "+v"(xf0), "+v"(xf1), "+v"(vf0), "+v"(vf1), "+v"(epo) : [w] "s"(w) : "memory", "scc");
    STAMP(2);
    {
        const u32 ta = L_W + w * 2048 + l15 * 8;
        const u32 gx = g & 1;
        u32x4 wf0 = cat(trread(lds, ta + ((4 * g + 0) ^ gx) * 128), trread(lds, ta + ((4 * g + 1) ^ gx) * 128));
        u32x4 wf1 = cat(trread(lds, ta + ((4 * g + 2) ^ gx) * 128), trread(lds, ta + ((4 * g + 3) ^ gx) * 128));
        f32x4 acc = {0, 0, 0, 0}, dacc = {0, 0, 0, 0};
        if (isK) {
            acc = __builtin_amdgcn_mfma_f32_16x16x32_bf16(asbf(wf0), asbf(xf0), acc, 0, 0, 0);
            acc = __builtin_amdgcn_mfma_f32_16x16x32_bf16(asbf(wf1), asbf(xf1), acc, 0, 0, 0);
        } else {
            acc = __builtin_amdgcn_mfma_f32_16x16x32_bf16(asbf(xf0), asbf(wf0), acc, 0, 0, 0);
            acc = __builtin_amdgcn_mfma_f32_16x16x32_bf16(asbf(xf1), asbf(wf1), acc, 0, 0, 0);
        }
        dacc = __builtin_amdgcn_mfma_f32_16x16x32_bf16(asbf(vf0), asbf(xf0), dacc, 0, 0, 0);
        dacc = __builtin_amdgcn_mfma_f32_16x16x32_bf16(asbf(vf1), asbf(xf1), dacc, 0, 0, 0);
        ST128(L_P1 + w * 1024 + lane * 16, __builtin_bit_cast(u32x4, acc));
        if (g == 0) STF(L_PD + w * 64 + l15 * 4, dacc[0]);
    }
    BAR();
    STAMP(3);
    u32 epoch = 0;
    if (w == 7) {
        epoch = epo + 1;
        f32x4 s = __builtin_bit_cast(f32x4, LD128(L_P1 + lane * 16));
        float d = LDF(L_PD + l15 * 4);
#pragma unroll
        for (int i = 1; i < 8; ++i) {
            s += __builtin_bit_cast(f32x4, LD128(L_P1 + i * 1024 + lane * 16));
            d += LDF(L_PD + i * 64 + l15 * 4);
        }
        u32x2 pv = {pk(s[0], s[1]), pk(s[2], s[3])};
        const int tile = T0 >> 5, sub = (T0 >> 4) & 1;
        u32 key = 0;
        if (isK) {
            u32 off = WS_KV + tile * 2048 + ((g & 1) * 32 + sub * 16 + l15) * 16 + (g >> 1) * 8;
            GST64C(ws, off, pv);
            u32 rb = pk(d, d) & 0xffffu;
            u32 ord = (rb & 0x8000u) ? (~rb & 0xffffu) : (rb | 0x8000u);
            key = wave_max_u32((ord << 16) | (0xffffu - (u32)(T0 + l15)));
        } else {
            u32 off = WS_KV + tile * 2048 + 1024 + sub * 512 + (g >> 1) * 256 + l15 * 16 + (g & 1) * 8;
            GST64C(ws, off, pv);
            if (g == 0) { u32 go = WS_G + (T0 + l15) * 4; u32 dv = __float_as_uint(d); GST32C(ws, go, dv); }
        }
        VMWAIT(0);
        if (lane < 8) { u32x2 fl = {key, epoch}; u32 fo = WS_F1 + lane * 2048 + b * 8; GST64C(ws, fo, fl); }
        STAMP(4);
    } else {
        VMWAIT(0);
    }
    BAR();
    STAMP(5);

    u32x4 qx, qy;
    if (w < 4) {
        const int th = w & 1, hp = w >> 1;
        f32x16 acc = {0};
        const int tok = th * 32 + l31;
        const u32 abase = L_V + h * 1024 + hp * 512 + ((l15 >> 2) * 64) + ((lane >> 4) & 1) * 32 + (l15 & 3) * 8;
        const u32 bbase = L_K + tok * 1024;
#pragma unroll
        for (int s = 0; s < 32; ++s) {
            u32x4 af = cat(trread(lds, abase + s * 2048), trread(lds, abase + s * 2048 + 256));
            u32x4 bf = LD128(bbase + (s >> 3) * 256 + ((((2 * s + h) & 15) ^ (tok & 15)) << 4));
            acc = __builtin_amdgcn_mfma_f32_32x32x16_bf16(asbf(af), asbf(bf), acc, 0, 0, 0);
        }
        const float qs = 0.25f * 1.4426950408889634f;
#pragma unroll
        for (int i = 0; i < 4; ++i) {
            u32 a = pk(acc[2 * i], acc[2 * i + 1]), c = pk(acc[8 + 2 * i], acc[8 + 2 * i + 1]);
            qx[i] = pk(lo16(a) * qs, hi16(a) * qs);
            qy[i] = pk(lo16(c) * qs, hi16(c) * qs);
        }
        ST128(L_P1 + w * 2048 + lane * 16, qx);
        ST128(L_P1 + w * 2048 + 1024 + lane * 16, qy);
        STAMP(6);
    } else {
        if (w < 6 && ffn) {
#pragma unroll
            for (int i = 0; i < 8; ++i) {
                int pc = (w - 4) * 8 + i;
                u32 slot = 64 * (pc & 1) + lane, pos = slot >> 3, B = pos ^ ((pos >> 2) & 1);
                u32 row = 64 * (pc >> 1) + 4 * B + ((slot >> 1) & 3);
                u64 src = (u64)((slot & 1) ? wu : wg) + (u64)row * 3584 + col0 * 2;
                asm volatile("s_mov_b32 m0, %0\n\ts_nop 0\n\tglobal_load_lds_dwordx4 %1, off" :: "s"(lb + L_W + pc * 1024), "v"(src) : "memory");
            }
        }
        if (w == 7) {
            u32 fo = WS_F1 + xcd * 2048 + lane * 32;
            u32x4 f0, f1;
            u32 spins = 0;
            while (true) {
                GLD128C(f0, ws, fo, 0);
                GLD128C(f1, ws, fo, 16);
                asm volatile("s_waitcnt vmcnt(0)" : "+v"(f0), "+v"(f1) :: "memory");
                bool ok = (f0[1] == epoch) & (f0[3] == epoch) & (f1[1] == epoch) & (f1[3] == epoch);
                if (__all(ok) || ++spins > SPIN_MAX) break;
                __builtin_amdgcn_s_sleep(1);
            }
            u32 key = wave_max_u32(max(max(f0[0], f0[2]), max(f1[0], f1[2])));
            if (lane == 0) {
                ST32(L_CT, key); ST32(L_CT + 4, epoch);
                if (b == 0) { u32 z = 0; GST32C(ws, z, epoch); }
            }
            STAMP(7);
        }
        if (w < 6) VMWAIT(0);
    }
    BAR();
    STAMP(8);
    const u32 key = __builtin_amdgcn_readfirstlane(LD32(L_CT));
    epoch = __builtin_amdgcn_readfirstlane(LD32(L_CT + 4));
    const int sel = 0xffff - (key & 0xffff);
    const u32 rord = key >> 16;
    const float rsel = __uint_as_float(((rord & 0x8000u) ? (rord & 0x7fffu) : (~rord & 0xffffu)) << 16);

    u32x4 xs0, xs1;
    {
        u32 so = (sel & 2047) * 1024 + (64 * w + 16 * g) * 2;
        GLD128(xs0, x, so, 0);
        GLD128(xs1, x, so, 16);
    }
    const int hf = w >> 2, jw = w & 3;
    {
        const char* kvb = ws + WS_KV;
#pragma unroll
        for (int r = 0; r < 16; ++r) {
            int m = 4 * r + jw;
            int isV = ((m & 1) == 0 && m != 0) || m == 63;
            int t = m == 63 ? 31 : (isV ? (m >> 1) - 1 : (m + 1) >> 1);
            t += 32 * hf;
            dma16c(kvb, t * 2048 + isV * 1024 + lane * 16, lb + (isV ? L_V : L_K) + t * 1024);
        }
    }
    u32 wd0, wd1, wd2, wd3, wd4, wd5, wd6, wd7;
    {
        const u32 wdo = (xcd * 28 + min(jj, 27)) * 8192 + (64 * w + lane) * 2, o2 = wdo + 4096;
        GLD16(wd0, wd, wdo, 0); GLD16(wd1, wd, wdo, 1024); GLD16(wd2, wd, wdo, 2048); GLD16(wd3, wd, wdo, 3072);
        GLD16(wd4, wd, o2, 0); GLD16(wd5, wd, o2, 1024); GLD16(wd6, wd, o2, 2048); GLD16(wd7, wd, o2, 3072);
    }
    if (w >= 4) {
        qx = LD128(L_P1 + (w - 4) * 2048 + lane * 16);
        qy = LD128(L_P1 + (w - 4) * 2048 + 1024 + lane * 16);
    }
    asm volatile("s_waitcnt vmcnt(21)" : "+v"(xs0), "+v"(xs1) :: "memory");
    STAMP(9);
    if (ffn) {
        const u32 ta = L_W + w * 2048 + l15 * 8;
        const u32 gx = g & 1;
        u32x4 wf0 = cat(trread(lds, ta + ((4 * g + 0) ^ gx) * 128), trread(lds, ta + ((4 * g + 1) ^ gx) * 128));
        u32x4 wf1 = cat(trread(lds, ta + ((4 * g + 2) ^ gx) * 128), trread(lds, ta + ((4 * g + 3) ^ gx) * 128));
        f32x4 acc = {0, 0, 0, 0};
        acc = __builtin_amdgcn_mfma_f32_16x16x32_bf16(asbf(wf0), asbf(xs0), acc, 0, 0, 0);
        acc = __builtin_amdgcn_mfma_f32_16x16x32_bf16(asbf(wf1), asbf(xs1), acc, 0, 0, 0);
        if (l15 == 0) ST128(L_FP + w * 64 + g * 16, __builtin_bit_cast(u32x4, acc));
    }
    BAR();
    STAMP(10); STAMPC(19);

    f32x16 SX0, SX1, SY0, SY1, OX, OY;
    u32x4 K0, K1, V00, V01, V10, V11;
    u32 idx = 0xDDDDu;
    u32 ka = lb + L_K + hf * 32768 + lane * 16;
    const bool vlane = l31 < 16;
    u32 va = vlane ? (lb + L_V + hf * 32768 + h * 256 + l15 * 16) : (lb + L_ONE - 1024 + h * 256 + l15 * 16);
    u32 vinc = vlane ? 4096u : 0u;
    {
        u32 va0 = vlane ? va : va + 1024;
        asm volatile(
            "ds_read_b128 v[168:171], v193\n"
            "ds_read_b128 v[172:175], v193 offset:1024\n"
            "ds_read_b128 v[176:179], %[va0]\n"
            "ds_read_b128 v[180:183], %[va0] offset:512\n"
            "v_mov_b32 v128, 0\n v_mov_b32 v129, 0\n v_mov_b32 v130, 0\n v_mov_b32 v131, 0\n"
            "v_mov_b32 v132, 0\n v_mov_b32 v133, 0\n v_mov_b32 v134, 0\n v_mov_b32 v135, 0\n"
            "v_mov_b32 v136, 0\n v_mov_b32 v137, 0\n v_mov_b32 v138, 0\n v_mov_b32 v139, 0\n"
            "v_mov_b32 v140, 0\n v_mov_b32 v141, 0\n v_mov_b32 v142, 0\n v_mov_b32 v143, 0\n"
            "v_mov_b32 v144, 0\n v_mov_b32 v145, 0\n v_mov_b32 v146, 0\n v_mov_b32 v147, 0\n"
            "v_mov_b32 v148, 0\n v_mov_b32 v149, 0\n v_mov_b32 v150, 0\n v_mov_b32 v151, 0\n"
            "v_mov_b32 v152, 0\n v_mov_b32 v153, 0\n v_mov_b32 v154, 0\n v_mov_b32 v155, 0\n"
            "v_mov_b32 v156, 0\n v_mov_b32 v157, 0\n v_mov_b32 v158, 0\n v_mov_b32 v159, 0\n"
            "v_mov_b32 v120, 0\n v_mov_b32 v121, 0\n v_mov_b32 v122, 0\n v_mov_b32 v123, 0\n"
            "v_mov_b32 v124, 0\n v_mov_b32 v125, 0\n v_mov_b32 v126, 0\n v_mov_b32 v127, 0\n"
            "v_mov_b32 v188, 0\n v_mov_b32 v189, 0\n v_mov_b32 v190, 0\n v_mov_b32 v191, 0\n"
            "s_waitcnt lgkmcnt(0)\n"
            "v_mfma_f32_32x32x16_bf16 v[64:79], v[168:171], v[160:163], 0\n"
            "v_mfma_f32_32x32x16_bf16 v[96:111], v[168:171], v[164:167], 0\n"
            "s_nop 7\n s_nop 7\n"
            : "={v[64:79]}"(SX0), "={v[80:95]}"(SX1), "={v[96:111]}"(SY0), "={v[112:127]}"(SY1),
              "={v[128:143]}"(OX), "={v[144:159]}"(OY),
              "={v[168:171]}"(K0), "={v[172:175]}"(K1), "={v[176:179]}"(V00), "={v[180:183]}"(V01),
              "={v[184:187]}"(V10), "={v[188:191]}"(V11)
            : "{v[160:163]}"(qx), "{v[164:167]}"(qy), "{v193}"(ka), [va0] "v"(va0)
            : "memory");
    }
#define ATT_RUN(N) do { u32 cnt_; asm volatile("s_mov_b32 %[cnt], " #N "\n1:\n" ATT_BODY4 "s_sub_u32 %[cnt], %[cnt], 1\ns_cmp_lg_u32 %[cnt], 0\ns_cbranch_scc1 1b\n" \
            : "+{v[64:79]}"(SX0), "+{v[80:95]}"(SX1), "+{v[96:111]}"(SY0), "+{v[112:127]}"(SY1), \
              "+{v[128:143]}"(OX), "+{v[144:159]}"(OY), \
              "+{v[168:171]}"(K0), "+{v[172:175]}"(K1), "+{v[176:179]}"(V00), "+{v[180:183]}"(V01), \
              "+{v[184:187]}"(V10), "+{v[188:191]}"(V11), "+{v193}"(ka), "+{v194}"(va), [cnt] "=&s"(cnt_) \
            : "{v[160:163]}"(qx), "{v[164:167]}"(qy), "{v192}"(idx), "{v195}"(vinc) \
            : "memory", "scc"); } while (0)
    ATT_RUN(1);
    VMWAIT(19); BAR();
    ATT_RUN(1);
    asm volatile("s_waitcnt vmcnt(0)" : "+v"(wd0), "+v"(wd1), "+v"(wd2), "+v"(wd3), "+v"(wd4), "+v"(wd5), "+v"(wd6), "+v"(wd7) :: "memory");
    BAR();
    if (ffn) {
        float sum = LDF(L_FP + l15 * 4);
#pragma unroll
        for (int i = 1; i < 8; ++i) sum += LDF(L_FP + i * 64 + l15 * 4);
        sum = bfr(sum);
        float up = __shfl(sum, (lane & 7) + 8);
        float gt = __shfl(sum, lane & 7);
        float hv = bfr(bfr(gt * sigm(gt)) * up);
        u32 hb = __float_as_uint(hv);
        u32 h01 = ((u32)__builtin_amdgcn_readlane(hb, 0) >> 16) | ((u32)__builtin_amdgcn_readlane(hb, 1) & 0xffff0000u);
        u32 h23 = ((u32)__builtin_amdgcn_readlane(hb, 2) >> 16) | ((u32)__builtin_amdgcn_readlane(hb, 3) & 0xffff0000u);
        u32 h45 = ((u32)__builtin_amdgcn_readlane(hb, 4) >> 16) | ((u32)__builtin_amdgcn_readlane(hb, 5) & 0xffff0000u);
        u32 h67 = ((u32)__builtin_amdgcn_readlane(hb, 6) >> 16) | ((u32)__builtin_amdgcn_readlane(hb, 7) & 0xffff0000u);
        float y = lo16(h01) * lo16(wd0) + hi16(h01) * lo16(wd1) + lo16(h23) * lo16(wd2) + hi16(h23) * lo16(wd3)
                + lo16(h45) * lo16(wd4) + hi16(h45) * lo16(wd5) + lo16(h67) * lo16(wd6) + hi16(h67) * lo16(wd7);
        u32x2 gr = {__float_as_uint(y), epoch};
        u32 yo = WS_Y + (xcd * 28 + jj) * 4096 + (64 * w + lane) * 8;
        GST64C(ws, yo, gr);
    }
    ATT_RUN(2);
    BAR();
#pragma unroll
    for (int i = 0; i < 8; ++i) {
        int q8 = w * 8 + i;
        u32 row = 8 * q8 + (lane >> 3), slot = lane & 7;
        u32 f = ((row >> 1) & 1) | (((row >> 3) & 1) << 1);
        u32 c = (((slot >> 1) ^ f) << 1) | (slot & 1);
        int reg = q8 >> 4;
        u32 dst = ((reg & 2) ? L_V : L_K) + (reg & 1) * 32768 + (q8 & 15) * 1024;
        dma16(wo, row * 1024 + hg * 128 + c * 16, lb + dst);
    }
    ATT_RUN(4);
    asm volatile(
        "s_nop 1\n"
        "v_smfmac_f32_32x32x32_bf16 v[144:159], v[188:191], v[120:127], v192\n"
        "s_nop 7\n s_nop 7\n s_nop 3\n s_waitcnt lgkmcnt(0)\n"
        : "+{v[144:159]}"(OY), "+{v[128:143]}"(OX), "+{v[112:127]}"(SY1), "+{v[188:191]}"(V11)
        : "{v192}"(idx) : "memory");
    STAMP(12); STAMPC(20);

    const int th = w & 1, hp = (w >> 1) & 1, blk = w >> 2;
    {
        f32x16& mine = blk ? OY : OX;
        f32x16& give = blk ? OX : OY;
        const u32 cw = L_W + w * 2304, cr = L_W + (w ^ 4) * 2304;
        ST128(cw + lane * 16, ((u32x4){__float_as_uint(give[0]), __float_as_uint(give[1]), __float_as_uint(give[2]), __float_as_uint(give[3])}));
        ST128(cw + 1024 + lane * 16, ((u32x4){__float_as_uint(give[4]), __float_as_uint(give[5]), __float_as_uint(give[6]), __float_as_uint(give[7])}));
        STF(cw + 2048 + lane * 4, give[8]);
        BAR();
        f32x4 p0 = __builtin_bit_cast(f32x4, LD128(cr + lane * 16));
        f32x4 p1 = __builtin_bit_cast(f32x4, LD128(cr + 1024 + lane * 16));
        float l = LDF(cr + 2048 + lane * 4) + mine[8];
        float o[8];
#pragma unroll
        for (int i = 0; i < 4; ++i) { o[i] = mine[i] + p0[i]; o[4 + i] = mine[4 + i] + p1[i]; }
        {
            auto sw = __builtin_amdgcn_permlane32_swap(__float_as_uint(l), __float_as_uint(l), false, false);
            l = __uint_as_float(sw[0]);
        }
        float amax = fmaxf(fmaxf(fmaxf(fabsf(o[0]), fabsf(o[1])), fmaxf(fabsf(o[2]), fabsf(o[3]))),
                           fmaxf(fmaxf(fabsf(o[4]), fabsf(o[5])), fmaxf(fabsf(o[6]), fabsf(o[7]))));
        bool safe = (l >= 8.6736174e-19f) && (l <= 1.2676506e30f) && (amax <= 3.0e38f);
        if (!__all(safe)) {
            slow_block(ws + WS_KV, blk ? qy : qx, lane, o, l);
            auto sw = __builtin_amdgcn_permlane32_swap(__float_as_uint(l), __float_as_uint(l), false, false);
            l = __uint_as_float(sw[0]);
        }
        const float ri = __builtin_amdgcn_rcpf(l);
        u32 A0 = pk(o[0] * ri, o[1] * ri), A1 = pk(o[2] * ri, o[3] * ri);
        u32 B0 = pk(o[4] * ri, o[5] * ri), B1 = pk(o[6] * ri, o[7] * ri);
        auto s0 = __builtin_amdgcn_permlane32_swap(A0, B0, false, false);
        auto s1 = __builtin_amdgcn_permlane32_swap(A1, B1, false, false);
        u32x4 ov = {s0[0], s1[0], s0[1], s1[1]};
        const int head = hg * 4 + hp * 2 + blk;
        const int tl = th * 32 + l31;
        u32 oo = WS_O + ((tb * 4 + (tl >> 4)) * 16 + (head >> 1)) * 1024 + (((head & 1) * 2 + h) * 16 + (tl & 15)) * 16;
        GST128C(ws, oo, ov);
        VMWAIT(0);
    }
    BAR();
    if (tid == 0) { u32x2 fl = {0u, epoch}; u32 fo = WS_F2 + tb * 128 + hg * 8; GST64C(ws, fo, fl); }
    STAMP(13);

    const int tq = w & 3, kh = w >> 2;
    const int tokg = tb * 64 + tq * 16 + l15;
    const int ncol = hg * 64 + kh * 32 + (g & 1) * 16 + (g >> 1) * 8;
    const bool owner = (sel >> 6) == tb;
    if (owner) {
        u32x2 gr[28];
        const u32 yo = WS_Y + (28 * w) * 4096 + (hg * 64 + lane) * 8;
        u32 spins = 0;
        while (true) {
#pragma unroll
            for (int i = 0; i < 28; ++i) { u32 a = yo + i * 4096; GLD64C(gr[i], ws, a, 0); }
            asm volatile("s_waitcnt vmcnt(0)" : "+v"(gr[0]), "+v"(gr[1]), "+v"(gr[2]), "+v"(gr[3]), "+v"(gr[4]), "+v"(gr[5]), "+v"(gr[6]), "+v"(gr[7]), "+v"(gr[8]), "+v"(gr[9]), "+v"(gr[10]), "+v"(gr[11]), "+v"(gr[12]), "+v"(gr[13]), "+v"(gr[14]), "+v"(gr[15]), "+v"(gr[16]), "+v"(gr[17]), "+v"(gr[18]), "+v"(gr[19]), "+v"(gr[20]), "+v"(gr[21]), "+v"(gr[22]), "+v"(gr[23]), "+v"(gr[24]), "+v"(gr[25]), "+v"(gr[26]), "+v"(gr[27]) :: "memory");
            bool ok = true;
#pragma unroll
            for (int i = 0; i < 28; ++i) ok &= (gr[i][1] == epoch);
            if (__all(ok) || ++spins > SPIN_MAX) break;
        }
        float ys = 0.f;
#pragma unroll
        for (int i = 0; i < 28; ++i) ys += __uint_as_float(gr[i][0]);
        STF(L_YP + w * 256 + lane * 4, ys);
    }
    u32x4 af[4][8];
    {
        const u32 f = ((l15 >> 3) & 1) | ((g & 1) << 1);
        const u32 rb = (kh ? L_V : L_K) + (8 * g + (l15 >> 2)) * 128 + (l15 & 3) * 8;
#pragma unroll
        for (int nb = 0; nb < 4; ++nb) {
            const u32 a = rb + ((nb ^ f) * 32);
#pragma unroll
            for (int st = 0; st < 8; ++st) {
                const u32 o_ = (st >> 2) * 32768 + (st & 3) * 4096;
                af[nb][st] = cat(trread(lds, a + o_), trread(lds, a + o_ + 512));
            }
        }
    }
    STAMP(14);
    {
        u32 fo = WS_F2 + tb * 128 + (kh * 4 + (lane & 3)) * 8;
        u32x2 fl; u32 spins = 0;
        while (true) {
            GLD64C(fl, ws, fo, 0);
            asm volatile("s_waitcnt vmcnt(0)" : "+v"(fl) :: "memory");
            if (__all(fl[1] == epoch) || ++spins > SPIN_MAX) break;
            __builtin_amdgcn_s_sleep(1);
        }
    }
    STAMP(15);
    u32x4 xe; u32 gl;
    {
        u32 xo = tokg * 1024 + ncol * 2;
        GLD128(xe, x, xo, 0);
        u32 go = WS_G + tokg * 4;
        GLD32C(gl, ws, go, 0);
    }
    u32x4 of0, of1, of2, of3, of4, of5, of6, of7;
    {
        u32 oo = WS_O + ((tb * 4 + tq) * 16 + 8 * kh) * 1024 + lane * 16;
        GLD128C(of0, ws, oo, 0); GLD128C(of1, ws, oo, 1024); GLD128C(of2, ws, oo, 2048); GLD128C(of3, ws, oo, 3072);
        u32 o2 = oo + 4096;
        GLD128C(of4, ws, o2, 0); GLD128C(of5, ws, o2, 1024); GLD128C(of6, ws, o2, 2048); GLD128C(of7, ws, o2, 3072);
    }
    f32x4 acc[4] = {{0, 0, 0, 0}, {0, 0, 0, 0}, {0, 0, 0, 0}, {0, 0, 0, 0}};
#define OSTEP(N, OF, ST) \
    asm volatile("s_waitcnt vmcnt(" #N ")" : "+v"(OF) :: "memory"); \
    _Pragma("unroll") for (int nb = 0; nb < 4; ++nb) acc[nb] = __builtin_amdgcn_mfma_f32_16x16x32_bf16(asbf(af[nb][ST]), asbf(OF), acc[nb], 0, 0, 0);
    asm volatile("s_waitcnt vmcnt(8)" : "+v"(xe), "+v"(gl) :: "memory");
    OSTEP(7, of0, 0) OSTEP(6, of1, 1) OSTEP(5, of2, 2) OSTEP(4, of3, 3)
    OSTEP(3, of4, 4) OSTEP(2, of5, 5) OSTEP(1, of6, 6) OSTEP(0, of7, 7)
    STAMP(16);
    {
        f32x4 k0 = kh ? acc[2] : acc[0], k1 = kh ? acc[3] : acc[1];
        f32x4 g0 = kh ? acc[0] : acc[2], g1 = kh ? acc[1] : acc[3];
        ST128(L_W + w * 2048 + lane * 16, __builtin_bit_cast(u32x4, g0));
        ST128(L_W + w * 2048 + 1024 + lane * 16, __builtin_bit_cast(u32x4, g1));
        BAR();
        k0 += __builtin_bit_cast(f32x4, LD128(L_W + (w ^ 4) * 2048 + lane * 16));
        k1 += __builtin_bit_cast(f32x4, LD128(L_W + (w ^ 4) * 2048 + 1024 + lane * 16));
        float a[8];
#pragma unroll
        for (int i = 0; i < 4; ++i) {
            auto sw = __builtin_amdgcn_permlane16_swap(__float_as_uint(k0[i]), __float_as_uint(k1[i]), false, false);
            a[i] = __uint_as_float(sw[0]); a[4 + i] = __uint_as_float(sw[1]);
        }
        const float gt = bfr(sigm(bfr(__uint_as_float(gl))));
        const float og = bfr(1.0f - gt);
        float bb[8];
#pragma unroll
        for (int i = 0; i < 4; ++i) { bb[2 * i] = lo16(xe[i]); bb[2 * i + 1] = hi16(xe[i]); }
        if (owner && tokg == sel) {
            const float sg = bfr(sigm(rsel));
            const int c0 = kh * 32 + (g & 1) * 16 + (g >> 1) * 8;
#pragma unroll
            for (int i = 0; i < 8; ++i) {
                float y = 0.f;
#pragma unroll
                for (int p = 0; p < 8; ++p) y += LDF(L_YP + p * 256 + (c0 + i) * 4);
                bb[i] = bfr(bb[i] + bfr(bfr(y) * sg));
            }
        }
        u32x4 ov;
#pragma unroll
        for (int i = 0; i < 4; ++i)
            ov[i] = pk(bfr(gt * bfr(a[2 * i])) + bfr(og * bb[2 * i]), bfr(gt * bfr(a[2 * i + 1])) + bfr(og * bb[2 * i + 1]));
        { u32 oo_ = tokg * 1024 + ncol * 2; asm volatile("global_store_dwordx4 %0, %1, %2\n\ts_nop 1" :: "v"(oo_), "v"(ov), "s"(out) : "memory"); }
    }
    STAMP(17); STAMPC(21); STAMPDUMP();
}

extern "C" void kernel_launch(void* const* d_in, const int* in_sizes, int n_in, void* d_out, int out_size,
                              void* d_ws, size_t ws_size, hipStream_t stream) {
    (void)in_sizes; (void)n_in; (void)out_size; (void)ws_size;
    fused_kernel<<<256, 512, 0, stream>>>((const u16*)d_in[0], (const u16*)d_in[1], (const u16*)d_in[2], (const u16*)d_in[3],
                                          (const u16*)d_in[4], (const u16*)d_in[6], (const u16*)d_in[7], (const u16*)d_in[8],
                                          (const u16*)d_in[9], (const u16*)d_in[10], (u16*)d_out, (char*)d_ws);
}
```

```cpp
#include <hip/hip_runtime.h>
#include <stdint.h>

typedef unsigned int u32;
typedef unsigned long long u64;
typedef unsigned short u16;
typedef u32 u32x2 __attribute__((ext_vector_type(2)));
typedef u32 u32x4 __attribute__((ext_vector_type(4)));
typedef u32 u32x8 __attribute__((ext_vector_type(8)));
typedef u32 u32x16 __attribute__((ext_vector_type(16)));
typedef float f32x2 __attribute__((ext_vector_type(2)));
typedef float f32x4 __attribute__((ext_vector_type(4)));
typedef float f32x16 __attribute__((ext_vector_type(16)));
typedef __bf16 bf16x2 __attribute__((ext_vector_type(2)));
typedef __bf16 bf16x8 __attribute__((ext_vector_type(8)));
#define LDSP __attribute__((address_space(3)))

constexpr u32 WS_E   = 0;
constexpr u32 WS_F1  = 4096;
constexpr u32 WS_G   = 24576;
constexpr u32 WS_K   = 65536;
constexpr u32 WS_V   = 131072;
constexpr u32 WS_O   = 262144;
constexpr u32 WS_Y   = 2621440;
constexpr u32 WS_X   = 3145728;
constexpr u32 WS_DBG = 4194304;

constexpr u32 L_K   = 0;
constexpr u32 L_V   = 65536;
constexpr u32 L_ONE = 131072;
constexpr u32 L_W   = 135168;
constexpr u32 L_M   = 151552;
constexpr u32 L_P1  = L_M;
constexpr u32 L_PD  = L_M + 8192;
constexpr u32 L_FP  = L_M + 8704;
constexpr u32 L_YP  = L_M + 9216;
constexpr u32 L_CT  = L_M + 11264;
constexpr u32 L_END = 163840;

constexpr u32 SPIN_MAX = 1u << 22;
constexpr u32 POISON = 0xffffffffu;

#define LD128(a) (*(const LDSP u32x4*)(lds + (a)))
#define ST128(a, v) (*(LDSP u32x4*)(lds + (a)) = (v))
#define LD32(a) (*(const LDSP u32*)(lds + (a)))
#define ST32(a, v) (*(LDSP u32*)(lds + (a)) = (v))
#define LDF(a) (*(const LDSP float*)(lds + (a)))
#define STF(a, v) (*(LDSP float*)(lds + (a)) = (v))

__device__ __forceinline__ u32x2 trread(LDSP char* lds, u32 a) {
    typedef short v4s __attribute__((ext_vector_type(4)));
    v4s r = __builtin_amdgcn_ds_read_tr16_b64_v4i16((LDSP v4s*)(lds + a));
    return __builtin_bit_cast(u32x2, r);
}
__device__ __forceinline__ bf16x8 asbf(u32x4 v) { return __builtin_bit_cast(bf16x8, v); }
__device__ __forceinline__ u32x4 cat(u32x2 a, u32x2 b) { u32x4 r = {a[0], a[1], b[0], b[1]}; return r; }
__device__ __forceinline__ u32 pk(float a, float b) {
    f32x2 f = {a, b};
    return __builtin_bit_cast(u32, __builtin_convertvector(f, bf16x2));
}
__device__ __forceinline__ float bfr(float a) {
    return __uint_as_float(pk(a, a) & 0xffff0000u);
}
__device__ __forceinline__ float lo16(u32 v) { return __uint_as_float(v << 16); }
__device__ __forceinline__ float hi16(u32 v) { return __uint_as_float(v & 0xffff0000u); }
__device__ __forceinline__ float sigm(float z) { return __builtin_amdgcn_rcpf(1.0f + __builtin_amdgcn_exp2f(-1.4426950408889634f * z)); }

__device__ __forceinline__ void dma16(const void* base, u32 voff, u32 ldsaddr) {
    asm volatile("s_mov_b32 m0, %0\n\ts_nop 0\n\tglobal_load_lds_dwordx4 %1, %2" :: "s"(ldsaddr), "v"(voff), "s"(base) : "memory");
}
__device__ __forceinline__ void dma16c4(const void* base, u32 voff, u32 ldsaddr) {
    asm volatile("s_mov_b32 m0, %0\n\ts_nop 0\n\tglobal_load_lds_dwordx4 %1, %2 sc1\n\tglobal_load_lds_dwordx4 %1, %2 offset:1024 sc1\n\t"
                 "global_load_lds_dwordx4 %1, %2 offset:2048 sc1\n\tglobal_load_lds_dwordx4 %1, %2 offset:3072 sc1"
                 :: "s"(ldsaddr), "v"(voff), "s"(base) : "memory");
}
__device__ __forceinline__ void dma16c3(const void* base, u32 voff, u32 ldsaddr) {
    asm volatile("s_mov_b32 m0, %0\n\ts_nop 0\n\tglobal_load_lds_dwordx4 %1, %2 sc1\n\tglobal_load_lds_dwordx4 %1, %2 offset:1024 sc1\n\t"
                 "global_load_lds_dwordx4 %1, %2 offset:2048 sc1"
                 :: "s"(ldsaddr), "v"(voff), "s"(base) : "memory");
}
__device__ __forceinline__ void dma16c2(const void* base, u32 voff, u32 ldsaddr) {
    asm volatile("s_mov_b32 m0, %0\n\ts_nop 0\n\tglobal_load_lds_dwordx4 %1, %2 sc1\n\tglobal_load_lds_dwordx4 %1, %2 offset:1024 sc1"
                 :: "s"(ldsaddr), "v"(voff), "s"(base) : "memory");
}
__device__ __forceinline__ void dma16c(const void* base, u32 voff, u32 ldsaddr) {
    asm volatile("s_mov_b32 m0, %0\n\ts_nop 0\n\tglobal_load_lds_dwordx4 %1, %2 sc1" :: "s"(ldsaddr), "v"(voff), "s"(base) : "memory");
}
#define GLD128(dst, base, voff, OFF)  asm volatile("global_load_dwordx4 %0, %1, %2 offset:" #OFF : "=v"(dst) : "v"(voff), "s"(base) : "memory")
#define GLD128C(dst, base, voff, OFF) asm volatile("global_load_dwordx4 %0, %1, %2 offset:" #OFF " sc1" : "=v"(dst) : "v"(voff), "s"(base) : "memory")
#define GLD64C(dst, base, voff, OFF)  asm volatile("global_load_dwordx2 %0, %1, %2 offset:" #OFF " sc1" : "=v"(dst) : "v"(voff), "s"(base) : "memory")
#define GLD32C(dst, base, voff, OFF)  asm volatile("global_load_dword %0, %1, %2 offset:" #OFF " sc1" : "=v"(dst) : "v"(voff), "s"(base) : "memory")
#define GLD32(dst, base, voff, OFF)   asm volatile("global_load_dword %0, %1, %2 offset:" #OFF : "=v"(dst) : "v"(voff), "s"(base) : "memory")
#define GLD16(dst, base, voff, OFF)   asm volatile("global_load_ushort %0, %1, %2 offset:" #OFF : "=v"(dst) : "v"(voff), "s"(base) : "memory")
#define GST128(base, voff, val)  asm volatile("global_store_dwordx4 %0, %1, %2\n\ts_nop 1" :: "v"(voff), "v"(val), "s"(base) : "memory")
#define GST128C(base, voff, val) asm volatile("global_store_dwordx4 %0, %1, %2 sc1\n\ts_nop 1" :: "v"(voff), "v"(val), "s"(base) : "memory")
#define GST64C(base, voff, val)  asm volatile("global_store_dwordx2 %0, %1, %2 sc1\n\ts_nop 0" :: "v"(voff), "v"(val), "s"(base) : "memory")
#define GST32C(base, voff, val)  asm volatile("global_store_dword %0, %1, %2 sc1" :: "v"(voff), "v"(val), "s"(base) : "memory")
#define VMWAIT(N) asm volatile("s_waitcnt vmcnt(" #N ")" ::: "memory")
#define BAR() asm volatile("s_waitcnt lgkmcnt(0)\n\ts_barrier" ::: "memory")

#define STAMP(i)
#define STAMPC(i)
#define STAMPDUMP()
#define FSTAMP(i) STAMP(i)
#define LSTAMP(i)

__device__ __forceinline__ u32 wave_max_u32(u32 v) {
    v = max(v, (u32)__builtin_amdgcn_update_dpp(0, (int)v, 0x111, 0xf, 0xf, false));
    v = max(v, (u32)__builtin_amdgcn_update_dpp(0, (int)v, 0x112, 0xf, 0xf, false));
    v = max(v, (u32)__builtin_amdgcn_update_dpp(0, (int)v, 0x114, 0xf, 0xf, false));
    v = max(v, (u32)__builtin_amdgcn_update_dpp(0, (int)v, 0x118, 0xf, 0xf, false));
    v = max(v, (u32)__builtin_amdgcn_update_dpp(0, (int)v, 0x142, 0xa, 0xf, false));
    v = max(v, (u32)__builtin_amdgcn_update_dpp(0, (int)v, 0x143, 0xc, 0xf, false));
    return (u32)__builtin_amdgcn_readlane((int)v, 63);
}
#define ATT_A \
    "s_mov_b32 %[cnt], 2\n" \
    "1:\n" \
    "v_exp_f32 v64, v64\n" \
    "v_exp_f32 v65, v65\n" \
    "v_smfmac_f32_32x32x32_bf16 v[144:159], v[188:191], v[120:127], v192\n" \
    "ds_read_b128 v[184:187], v194 offset:1024\n" \
    "v_exp_f32 v66, v66\n" \
    "v_exp_f32 v67, v67\n" \
    "v_exp_f32 v68, v68\n" \
    "v_exp_f32 v69, v69\n" \
    "s_waitcnt lgkmcnt(1)\n" \
    "v_mfma_f32_32x32x16_bf16 v[80:95], v[172:175], v[160:163], 0\n" \
    "ds_read_b128 v[188:191], v194 offset:1536\n" \
    "v_exp_f32 v70, v70\n" \
    "v_exp_f32 v71, v71\n" \
    "v_exp_f32 v72, v72\n" \
    "v_exp_f32 v73, v73\n" \
    "v_smfmac_f32_32x32x32_bf16 v[128:143], v[176:179], v[64:71], v192\n" \
    "v_exp_f32 v74, v74\n" \
    "s_mov_b32 m0, %[skv]\n" \
    "v_exp_f32 v75, v75\n" \
    "v_exp_f32 v76, v76\n" \
    "global_load_lds_dwordx4 v196, %[kvb] offset:0 sc1\n" \
    "v_exp_f32 v77, v77\n" \
    "v_exp_f32 v78, v78\n" \
    "v_exp_f32 v79, v79\n" \
    "v_exp_f32 v96, v96\n" \
    "v_exp_f32 v97, v97\n" \
    "v_smfmac_f32_32x32x32_bf16 v[128:143], v[180:183], v[72:79], v192\n" \
    "v_exp_f32 v98, v98\n" \
    "v_exp_f32 v99, v99\n" \
    "v_exp_f32 v100, v100\n" \
    "v_exp_f32 v101, v101\n" \
    "v_mfma_f32_32x32x16_bf16 v[112:127], v[172:175], v[164:167], 0\n" \
    "ds_read_b128 v[168:171], v193 offset:2048\n" \
    "v_exp_f32 v102, v102\n" \
    "v_exp_f32 v103, v103\n" \
    "v_exp_f32 v104, v104\n" \
    "v_exp_f32 v105, v105\n" \
    "v_smfmac_f32_32x32x32_bf16 v[144:159], v[176:179], v[96:103], v192\n" \
    "v_exp_f32 v106, v106\n" \
    "v_exp_f32 v107, v107\n" \
    "v_exp_f32 v108, v108\n" \
    "v_exp_f32 v109, v109\n" \
    "v_exp_f32 v110, v110\n" \
    "v_exp_f32 v111, v111\n" \
    "v_exp_f32 v80, v80\n" \
    "v_exp_f32 v81, v81\n" \
    "v_smfmac_f32_32x32x32_bf16 v[144:159], v[180:183], v[104:111], v192\n" \
    "ds_read_b128 v[176:179], v194 offset:2048\n" \
    "v_exp_f32 v82, v82\n" \
    "v_exp_f32 v83, v83\n" \
    "v_exp_f32 v84, v84\n" \
    "v_exp_f32 v85, v85\n" \
    "s_waitcnt lgkmcnt(1)\n" \
    "v_mfma_f32_32x32x16_bf16 v[64:79], v[168:171], v[160:163], 0\n" \
    "ds_read_b128 v[180:183], v194 offset:2560\n" \
    "v_exp_f32 v86, v86\n" \
    "v_exp_f32 v87, v87\n" \
    "v_exp_f32 v88, v88\n" \
    "v_exp_f32 v89, v89\n" \
    "v_smfmac_f32_32x32x32_bf16 v[128:143], v[184:187], v[80:87], v192\n" \
    "v_exp_f32 v90, v90\n" \
    "s_mov_b32 m0, %[skv]\n" \
    "v_exp_f32 v91, v91\n" \
    "v_exp_f32 v92, v92\n" \
    "global_load_lds_dwordx4 v196, %[kvb] offset:1024 sc1\n" \
    "v_exp_f32 v93, v93\n" \
    "v_exp_f32 v94, v94\n" \
    "v_exp_f32 v95, v95\n" \
    "v_exp_f32 v112, v112\n" \
    "v_exp_f32 v113, v113\n" \
    "v_smfmac_f32_32x32x32_bf16 v[128:143], v[188:191], v[88:95], v192\n" \
    "v_exp_f32 v114, v114\n" \
    "v_exp_f32 v115, v115\n" \
    "v_exp_f32 v116, v116\n" \
    "v_exp_f32 v117, v117\n" \
    "v_mfma_f32_32x32x16_bf16 v[96:111], v[168:171], v[164:167], 0\n" \
    "ds_read_b128 v[172:175], v193 offset:3072\n" \
    "v_exp_f32 v118, v118\n" \
    "v_exp_f32 v119, v119\n" \
    "v_exp_f32 v120, v120\n" \
    "v_exp_f32 v121, v121\n" \
    "v_smfmac_f32_32x32x32_bf16 v[144:159], v[184:187], v[112:119], v192\n" \
    "v_exp_f32 v122, v122\n" \
    "s_mov_b32 m0, %[sg]\n" \
    "v_exp_f32 v123, v123\n" \
    "v_exp_f32 v124, v124\n" \
    "global_load_lds_dwordx4 v[198:199], off\n" \
    "v_exp_f32 v125, v125\n" \
    "v_exp_f32 v126, v126\n" \
    "v_exp_f32 v127, v127\n" \
    "s_cmp_eq_u32 %[cnt], 1\n" \
    "s_cbranch_scc0 8f\n" \
    "s_setprio 0\n" \
    "8:\n" \
    "v_exp_f32 v64, v64\n" \
    "v_exp_f32 v65, v65\n" \
    "v_smfmac_f32_32x32x32_bf16 v[144:159], v[188:191], v[120:127], v192\n" \
    "ds_read_b128 v[184:187], v194 offset:3072\n" \
    "v_exp_f32 v66, v66\n" \
    "v_exp_f32 v67, v67\n" \
    "v_exp_f32 v68, v68\n" \
    "v_exp_f32 v69, v69\n" \
    "s_waitcnt lgkmcnt(1)\n" \
    "v_mfma_f32_32x32x16_bf16 v[80:95], v[172:175], v[160:163], 0\n" \
    "ds_read_b128 v[188:191], v194 offset:3584\n" \
    "v_exp_f32 v70, v70\n" \
    "v_exp_f32 v71, v71\n" \
    "v_exp_f32 v72, v72\n" \
    "v_exp_f32 v73, v73\n" \
    "v_smfmac_f32_32x32x32_bf16 v[128:143], v[176:179], v[64:71], v192\n" \
    "v_exp_f32 v74, v74\n" \
    "s_mov_b32 m0, %[skv]\n" \
    "v_exp_f32 v75, v75\n" \
    "v_exp_f32 v76, v76\n" \
    "global_load_lds_dwordx4 v196, %[kvb] offset:2048 sc1\n" \
    "v_exp_f32 v77, v77\n" \
    "v_exp_f32 v78, v78\n" \
    "v_exp_f32 v79, v79\n" \
    "v_exp_f32 v96, v96\n" \
    "v_exp_f32 v97, v97\n" \
    "v_smfmac_f32_32x32x32_bf16 v[128:143], v[180:183], v[72:79], v192\n" \
    "v_exp_f32 v98, v98\n" \
    "v_exp_f32 v99, v99\n" \
    "v_exp_f32 v100, v100\n" \
    "v_exp_f32 v101, v101\n" \
    "v_mfma_f32_32x32x16_bf16 v[112:127], v[172:175], v[164:167], 0\n" \
    "ds_read_b128 v[168:171], v193 offset:4096\n" \
    "v_exp_f32 v102, v102\n" \
    "v_exp_f32 v103, v103\n" \
    "v_exp_f32 v104, v104\n" \
    "v_exp_f32 v105, v105\n" \
    "v_smfmac_f32_32x32x32_bf16 v[144:159], v[176:179], v[96:103], v192\n" \
    "v_exp_f32 v106, v106\n" \
    "v_exp_f32 v107, v107\n" \
    "v_exp_f32 v108, v108\n" \
    "v_exp_f32 v109, v109\n" \
    "v_exp_f32 v110, v110\n" \
    "v_exp_f32 v111, v111\n" \
    "v_exp_f32 v80, v80\n" \
    "v_exp_f32 v81, v81\n" \
    "v_smfmac_f32_32x32x32_bf16 v[144:159], v[180:183], v[104:111], v192\n" \
    "ds_read_b128 v[176:179], v194 offset:4096\n" \
    "v_exp_f32 v82, v82\n" \
    "v_exp_f32 v83, v83\n" \
    "v_exp_f32 v84, v84\n" \
    "v_exp_f32 v85, v85\n" \
    "s_waitcnt lgkmcnt(1)\n" \
    "v_mfma_f32_32x32x16_bf16 v[64:79], v[168:171], v[160:163], 0\n" \
    "ds_read_b128 v[180:183], v194 offset:4608\n" \
    "v_exp_f32 v86, v86\n" \
    "v_exp_f32 v87, v87\n" \
    "v_exp_f32 v88, v88\n" \
    "v_exp_f32 v89, v89\n" \
    "v_smfmac_f32_32x32x32_bf16 v[128:143], v[184:187], v[80:87], v192\n" \
    "v_exp_f32 v90, v90\n" \
    "s_mov_b32 m0, %[skv]\n" \
    "v_exp_f32 v91, v91\n" \
    "v_exp_f32 v92, v92\n" \
    "global_load_lds_dwordx4 v196, %[kvb] offset:3072 sc1\n" \
    "v_exp_f32 v93, v93\n" \
    "v_exp_f32 v94, v94\n" \
    "v_exp_f32 v95, v95\n" \
    "v_exp_f32 v112, v112\n" \
    "v_exp_f32 v113, v113\n" \
    "v_smfmac_f32_32x32x32_bf16 v[128:143], v[188:191], v[88:95], v192\n" \
    "v_exp_f32 v114, v114\n" \
    "v_exp_f32 v115, v115\n" \
    "v_exp_f32 v116, v116\n" \
    "v_exp_f32 v117, v117\n" \
    "v_mfma_f32_32x32x16_bf16 v[96:111], v[168:171], v[164:167], 0\n" \
    "ds_read_b128 v[172:175], v193 offset:5120\n" \
    "v_exp_f32 v118, v118\n" \
    "v_exp_f32 v119, v119\n" \
    "v_exp_f32 v120, v120\n" \
    "v_exp_f32 v121, v121\n" \
    "v_smfmac_f32_32x32x32_bf16 v[144:159], v[184:187], v[112:119], v192\n" \
    "v_exp_f32 v122, v122\n" \
    "v_exp_f32 v123, v123\n" \
    "v_exp_f32 v124, v124\n" \
    "v_exp_f32 v125, v125\n" \
    "v_exp_f32 v126, v126\n" \
    "v_exp_f32 v127, v127\n" \
    "v_add_u32 v196, 0x2000, v196\n" \
    "v_mov_b32 v198, v200\n" \
    "v_mov_b32 v199, v201\n" \
    "s_add_u32 %[skv], %[skv], 0x2000\n" \
    "s_add_u32 %[sg], %[sg], 0x400\n" \
    "v_add_u32 v193, 4096, v193\n" \
    "v_add_u32 v194, v195, v194\n" \
    "s_sub_u32 %[cnt], %[cnt], 1\n" \
    "s_cmp_lg_u32 %[cnt], 0\n" \
    "s_cbranch_scc1 1b\n" \
    "s_waitcnt vmcnt(5)\n"

#define ATT_B \
    "v_exp_f32 v64, v64\n" \
    "v_exp_f32 v65, v65\n" \
    "v_smfmac_f32_32x32x32_bf16 v[144:159], v[188:191], v[120:127], v192\n" \
    "ds_read_b128 v[184:187], v194 offset:1024\n" \
    "v_exp_f32 v66, v66\n" \
    "v_exp_f32 v67, v67\n" \
    "v_exp_f32 v68, v68\n" \
    "v_exp_f32 v69, v69\n" \
    "s_waitcnt lgkmcnt(1)\n" \
    "v_mfma_f32_32x32x16_bf16 v[80:95], v[172:175], v[160:163], 0\n" \
    "ds_read_b128 v[188:191], v194 offset:1536\n" \
    "v_exp_f32 v70, v70\n" \
    "v_exp_f32 v71, v71\n" \
    "v_exp_f32 v72, v72\n" \
    "v_exp_f32 v73, v73\n" \
    "v_smfmac_f32_32x32x32_bf16 v[128:143], v[176:179], v[64:71], v192\n" \
    "v_exp_f32 v74, v74\n" \
    "s_mov_b32 m0, %[skv3]\n" \
    "v_exp_f32 v75, v75\n" \
    "v_exp_f32 v76, v76\n" \
    "global_load_dwordx4 v[204:207], v202, %[xb]\n" \
    "v_exp_f32 v77, v77\n" \
    "v_exp_f32 v78, v78\n" \
    "v_exp_f32 v79, v79\n" \
    "v_exp_f32 v96, v96\n" \
    "v_exp_f32 v97, v97\n" \
    "v_smfmac_f32_32x32x32_bf16 v[128:143], v[180:183], v[72:79], v192\n" \
    "v_exp_f32 v98, v98\n" \
    "v_exp_f32 v99, v99\n" \
    "v_exp_f32 v100, v100\n" \
    "v_exp_f32 v101, v101\n" \
    "v_mfma_f32_32x32x16_bf16 v[112:127], v[172:175], v[164:167], 0\n" \
    "ds_read_b128 v[168:171], v193 offset:2048\n" \
    "v_exp_f32 v102, v102\n" \
    "v_exp_f32 v103, v103\n" \
    "v_exp_f32 v104, v104\n" \
    "v_exp_f32 v105, v105\n" \
    "v_smfmac_f32_32x32x32_bf16 v[144:159], v[176:179], v[96:103], v192\n" \
    "v_exp_f32 v106, v106\n" \
    "v_exp_f32 v107, v107\n" \
    "v_exp_f32 v108, v108\n" \
    "global_load_dwordx4 v[208:211], v202, %[xb] offset:16\n" \
    "v_exp_f32 v109, v109\n" \
    "v_exp_f32 v110, v110\n" \
    "v_exp_f32 v111, v111\n" \
    "v_exp_f32 v80, v80\n" \
    "v_exp_f32 v81, v81\n" \
    "v_smfmac_f32_32x32x32_bf16 v[144:159], v[180:183], v[104:111], v192\n" \
    "ds_read_b128 v[176:179], v194 offset:2048\n" \
    "v_exp_f32 v82, v82\n" \
    "v_exp_f32 v83, v83\n" \
    "v_exp_f32 v84, v84\n" \
    "v_exp_f32 v85, v85\n" \
    "s_waitcnt lgkmcnt(1)\n" \
    "v_mfma_f32_32x32x16_bf16 v[64:79], v[168:171], v[160:163], 0\n" \
    "ds_read_b128 v[180:183], v194 offset:2560\n" \
    "v_exp_f32 v86, v86\n" \
    "v_exp_f32 v87, v87\n" \
    "v_exp_f32 v88, v88\n" \
    "v_exp_f32 v89, v89\n" \
    "v_smfmac_f32_32x32x32_bf16 v[128:143], v[184:187], v[80:87], v192\n" \
    "v_exp_f32 v90, v90\n" \
    "v_exp_f32 v91, v91\n" \
    "v_exp_f32 v92, v92\n" \
    "global_load_lds_dwordx4 v197, %[kvb] offset:0 sc1\n" \
    "v_exp_f32 v93, v93\n" \
    "v_exp_f32 v94, v94\n" \
    "v_exp_f32 v95, v95\n" \
    "v_exp_f32 v112, v112\n" \
    "v_exp_f32 v113, v113\n" \
    "v_smfmac_f32_32x32x32_bf16 v[128:143], v[188:191], v[88:95], v192\n" \
    "v_exp_f32 v114, v114\n" \
    "v_exp_f32 v115, v115\n" \
    "v_exp_f32 v116, v116\n" \
    "v_exp_f32 v117, v117\n" \
    "v_mfma_f32_32x32x16_bf16 v[96:111], v[168:171], v[164:167], 0\n" \
    "ds_read_b128 v[172:175], v193 offset:3072\n" \
    "v_exp_f32 v118, v118\n" \
    "v_exp_f32 v119, v119\n" \
    "v_exp_f32 v120, v120\n" \
    "v_exp_f32 v121, v121\n" \
    "v_smfmac_f32_32x32x32_bf16 v[144:159], v[184:187], v[112:119], v192\n" \
    "v_exp_f32 v122, v122\n" \
    "v_exp_f32 v123, v123\n" \
    "v_exp_f32 v124, v124\n" \
    "global_load_lds_dwordx4 v197, %[kvb] offset:1024 sc1\n" \
    "v_exp_f32 v125, v125\n" \
    "v_exp_f32 v126, v126\n" \
    "v_exp_f32 v127, v127\n" \
    "v_exp_f32 v64, v64\n" \
    "v_exp_f32 v65, v65\n" \
    "v_smfmac_f32_32x32x32_bf16 v[144:159], v[188:191], v[120:127], v192\n" \
    "ds_read_b128 v[184:187], v194 offset:3072\n" \
    "v_exp_f32 v66, v66\n" \
    "v_exp_f32 v67, v67\n" \
    "v_exp_f32 v68, v68\n" \
    "v_exp_f32 v69, v69\n" \
    "s_waitcnt lgkmcnt(1)\n" \
    "v_mfma_f32_32x32x16_bf16 v[80:95], v[172:175], v[160:163], 0\n" \
    "ds_read_b128 v[188:191], v194 offset:3584\n" \
    "v_exp_f32 v70, v70\n" \
    "v_exp_f32 v71, v71\n" \
    "v_exp_f32 v72, v72\n" \
    "v_exp_f32 v73, v73\n" \
    "v_smfmac_f32_32x32x32_bf16 v[128:143], v[176:179], v[64:71], v192\n" \
    "v_exp_f32 v74, v74\n" \
    "v_exp_f32 v75, v75\n" \
    "v_exp_f32 v76, v76\n" \
    "global_load_lds_dwordx4 v197, %[kvb] offset:2048 sc1\n" \
    "v_exp_f32 v77, v77\n" \
    "v_exp_f32 v78, v78\n" \
    "v_exp_f32 v79, v79\n" \
    "v_exp_f32 v96, v96\n" \
    "v_exp_f32 v97, v97\n" \
    "v_smfmac_f32_32x32x32_bf16 v[128:143], v[180:183], v[72:79], v192\n" \
    "v_exp_f32 v98, v98\n" \
    "v_exp_f32 v99, v99\n" \
    "v_exp_f32 v100, v100\n" \
    "v_exp_f32 v101, v101\n" \
    "v_mfma_f32_32x32x16_bf16 v[112:127], v[172:175], v[164:167], 0\n" \
    "ds_read_b128 v[168:171], v193 offset:4096\n" \
    "v_exp_f32 v102, v102\n" \
    "v_exp_f32 v103, v103\n" \
    "v_exp_f32 v104, v104\n" \
    "v_exp_f32 v105, v105\n" \
    "v_smfmac_f32_32x32x32_bf16 v[144:159], v[176:179], v[96:103], v192\n" \
    "v_exp_f32 v106, v106\n" \
    "v_exp_f32 v107, v107\n" \
    "v_exp_f32 v108, v108\n" \
    "global_load_lds_dwordx4 v197, %[kvb] offset:3072 sc1\n" \
    "v_exp_f32 v109, v109\n" \
    "v_exp_f32 v110, v110\n" \
    "v_exp_f32 v111, v111\n" \
    "v_exp_f32 v80, v80\n" \
    "v_exp_f32 v81, v81\n" \
    "v_smfmac_f32_32x32x32_bf16 v[144:159], v[180:183], v[104:111], v192\n" \
    "ds_read_b128 v[176:179], v194 offset:4096\n" \
    "v_exp_f32 v82, v82\n" \
    "v_exp_f32 v83, v83\n" \
    "v_exp_f32 v84, v84\n" \
    "v_exp_f32 v85, v85\n" \
    "s_waitcnt lgkmcnt(1)\n" \
    "v_mfma_f32_32x32x16_bf16 v[64:79], v[168:171], v[160:163], 0\n" \
    "ds_read_b128 v[180:183], v194 offset:4608\n" \
    "v_exp_f32 v86, v86\n" \
    "v_exp_f32 v87, v87\n" \
    "v_exp_f32 v88, v88\n" \
    "v_exp_f32 v89, v89\n" \
    "v_smfmac_f32_32x32x32_bf16 v[128:143], v[184:187], v[80:87], v192\n" \
    "v_exp_f32 v90, v90\n" \
    "v_exp_f32 v91, v91\n" \
    "v_exp_f32 v92, v92\n" \
    "global_load_dword v212, v203, %[wdb] offset:0\n" \
    "global_load_dword v213, v203, %[wdb] offset:1024\n" \
    "v_exp_f32 v93, v93\n" \
    "v_exp_f32 v94, v94\n" \
    "v_exp_f32 v95, v95\n" \
    "v_exp_f32 v112, v112\n" \
    "v_exp_f32 v113, v113\n" \
    "v_smfmac_f32_32x32x32_bf16 v[128:143], v[188:191], v[88:95], v192\n" \
    "v_exp_f32 v114, v114\n" \
    "v_exp_f32 v115, v115\n" \
    "v_exp_f32 v116, v116\n" \
    "v_exp_f32 v117, v117\n" \
    "v_mfma_f32_32x32x16_bf16 v[96:111], v[168:171], v[164:167], 0\n" \
    "ds_read_b128 v[172:175], v193 offset:5120\n" \
    "v_exp_f32 v118, v118\n" \
    "v_exp_f32 v119, v119\n" \
    "v_exp_f32 v120, v120\n" \
    "v_exp_f32 v121, v121\n" \
    "v_smfmac_f32_32x32x32_bf16 v[144:159], v[184:187], v[112:119], v192\n" \
    "v_exp_f32 v122, v122\n" \
    "v_exp_f32 v123, v123\n" \
    "v_exp_f32 v124, v124\n" \
    "global_load_dword v214, v203, %[wdb] offset:2048\n" \
    "global_load_dword v215, v203, %[wdb] offset:3072\n" \
    "v_exp_f32 v125, v125\n" \
    "v_exp_f32 v126, v126\n" \
    "v_exp_f32 v127, v127\n" \
    "v_add_u32 v203, 0x1000, v203\n" \
    "v_add_u32 v193, 4096, v193\n" \
    "v_add_u32 v194, v195, v194\n" \
    "v_exp_f32 v64, v64\n" \
    "v_exp_f32 v65, v65\n" \
    "v_smfmac_f32_32x32x32_bf16 v[144:159], v[188:191], v[120:127], v192\n" \
    "ds_read_b128 v[184:187], v194 offset:1024\n" \
    "v_exp_f32 v66, v66\n" \
    "v_exp_f32 v67, v67\n" \
    "v_exp_f32 v68, v68\n" \
    "v_exp_f32 v69, v69\n" \
    "s_waitcnt lgkmcnt(1)\n" \
    "v_mfma_f32_32x32x16_bf16 v[80:95], v[172:175], v[160:163], 0\n" \
    "ds_read_b128 v[188:191], v194 offset:1536\n" \
    "v_exp_f32 v70, v70\n" \
    "v_exp_f32 v71, v71\n" \
    "v_exp_f32 v72, v72\n" \
    "v_exp_f32 v73, v73\n" \
    "v_smfmac_f32_32x32x32_bf16 v[128:143], v[176:179], v[64:71], v192\n" \
    "v_exp_f32 v74, v74\n" \
    "v_exp_f32 v75, v75\n" \
    "v_exp_f32 v76, v76\n" \
    "global_load_dword v216, v203, %[wdb] offset:0\n" \
    "global_load_dword v217, v203, %[wdb] offset:1024\n" \
    "v_exp_f32 v77, v77\n" \
    "v_exp_f32 v78, v78\n" \
    "v_exp_f32 v79, v79\n" \
    "v_exp_f32 v96, v96\n" \
    "v_exp_f32 v97, v97\n" \
    "v_smfmac_f32_32x32x32_bf16 v[128:143], v[180:183], v[72:79], v192\n" \
    "v_exp_f32 v98, v98\n" \
    "v_exp_f32 v99, v99\n" \
    "v_exp_f32 v100, v100\n" \
    "v_exp_f32 v101, v101\n" \
    "v_mfma_f32_32x32x16_bf16 v[112:127], v[172:175], v[164:167], 0\n" \
    "ds_read_b128 v[168:171], v193 offset:2048\n" \
    "v_exp_f32 v102, v102\n" \
    "v_exp_f32 v103, v103\n" \
    "v_exp_f32 v104, v104\n" \
    "v_exp_f32 v105, v105\n" \
    "v_smfmac_f32_32x32x32_bf16 v[144:159], v[176:179], v[96:103], v192\n" \
    "v_exp_f32 v106, v106\n" \
    "v_exp_f32 v107, v107\n" \
    "v_exp_f32 v108, v108\n" \
    "global_load_dword v218, v203, %[wdb] offset:2048\n" \
    "global_load_dword v219, v203, %[wdb] offset:3072\n" \
    "v_exp_f32 v109, v109\n" \
    "v_exp_f32 v110, v110\n" \
    "v_exp_f32 v111, v111\n" \
    "v_exp_f32 v80, v80\n" \
    "v_exp_f32 v81, v81\n" \
    "v_smfmac_f32_32x32x32_bf16 v[144:159], v[180:183], v[104:111], v192\n" \
    "s_waitcnt vmcnt(12)\n" \
    "ds_read_b64_tr_b16 v[220:221], v228\n" \
    "ds_read_b64_tr_b16 v[222:223], v229\n" \
    "ds_read_b64_tr_b16 v[224:225], v228 offset:256\n" \
    "ds_read_b64_tr_b16 v[226:227], v229 offset:256\n" \
    "ds_read_b128 v[176:179], v194 offset:2048\n" \
    "v_exp_f32 v82, v82\n" \
    "v_exp_f32 v83, v83\n" \
    "v_exp_f32 v84, v84\n" \
    "v_exp_f32 v85, v85\n" \
    "s_waitcnt lgkmcnt(1)\n" \
    "v_mfma_f32_32x32x16_bf16 v[64:79], v[168:171], v[160:163], 0\n" \
    "ds_read_b128 v[180:183], v194 offset:2560\n" \
    "v_exp_f32 v86, v86\n" \
    "v_exp_f32 v87, v87\n" \
    "v_exp_f32 v88, v88\n" \
    "v_exp_f32 v89, v89\n" \
    "v_smfmac_f32_32x32x32_bf16 v[128:143], v[184:187], v[80:87], v192\n" \
    "v_exp_f32 v90, v90\n" \
    "v_exp_f32 v91, v91\n" \
    "v_exp_f32 v92, v92\n" \
    "v_mfma_f32_16x16x32_bf16 v[232:235], v[220:223], v[204:207], 0\n" \
    "v_exp_f32 v93, v93\n" \
    "v_exp_f32 v94, v94\n" \
    "v_exp_f32 v95, v95\n" \
    "v_exp_f32 v112, v112\n" \
    "v_exp_f32 v113, v113\n" \
    "v_smfmac_f32_32x32x32_bf16 v[128:143], v[188:191], v[88:95], v192\n" \
    "v_exp_f32 v114, v114\n" \
    "v_exp_f32 v115, v115\n" \
    "v_exp_f32 v116, v116\n" \
    "v_exp_f32 v117, v117\n" \
    "v_mfma_f32_32x32x16_bf16 v[96:111], v[168:171], v[164:167], 0\n" \
    "ds_read_b128 v[172:175], v193 offset:3072\n" \
    "v_exp_f32 v118, v118\n" \
    "v_exp_f32 v119, v119\n" \
    "v_exp_f32 v120, v120\n" \
    "v_exp_f32 v121, v121\n" \
    "v_smfmac_f32_32x32x32_bf16 v[144:159], v[184:187], v[112:119], v192\n" \
    "v_exp_f32 v122, v122\n" \
    "v_exp_f32 v123, v123\n" \
    "v_exp_f32 v124, v124\n" \
    "v_exp_f32 v125, v125\n" \
    "v_exp_f32 v126, v126\n" \
    "v_exp_f32 v127, v127\n" \
    "s_setprio 0\n" \
    "v_exp_f32 v64, v64\n" \
    "v_exp_f32 v65, v65\n" \
    "v_smfmac_f32_32x32x32_bf16 v[144:159], v[188:191], v[120:127], v192\n" \
    "ds_read_b128 v[184:187], v194 offset:3072\n" \
    "v_exp_f32 v66, v66\n" \
    "v_exp_f32 v67, v67\n" \
    "v_exp_f32 v68, v68\n" \
    "v_exp_f32 v69, v69\n" \
    "s_waitcnt lgkmcnt(1)\n" \
    "v_mfma_f32_32x32x16_bf16 v[80:95], v[172:175], v[160:163], 0\n" \
    "ds_read_b128 v[188:191], v194 offset:3584\n" \
    "v_exp_f32 v70, v70\n" \
    "v_exp_f32 v71, v71\n" \
    "v_exp_f32 v72, v72\n" \
    "v_exp_f32 v73, v73\n" \
    "v_smfmac_f32_32x32x32_bf16 v[128:143], v[176:179], v[64:71], v192\n" \
    "v_exp_f32 v74, v74\n" \
    "v_exp_f32 v75, v75\n" \
    "v_exp_f32 v76, v76\n" \
    "v_mfma_f32_16x16x32_bf16 v[232:235], v[224:227], v[208:211], v[232:235]\n" \
    "v_exp_f32 v77, v77\n" \
    "v_exp_f32 v78, v78\n" \
    "v_exp_f32 v79, v79\n" \
    "v_exp_f32 v96, v96\n" \
    "v_exp_f32 v97, v97\n" \
    "v_smfmac_f32_32x32x32_bf16 v[128:143], v[180:183], v[72:79], v192\n" \
    "v_exp_f32 v98, v98\n" \
    "v_exp_f32 v99, v99\n" \
    "v_exp_f32 v100, v100\n" \
    "v_exp_f32 v101, v101\n" \
    "v_mfma_f32_32x32x16_bf16 v[112:127], v[172:175], v[164:167], 0\n" \
    "ds_read_b128 v[168:171], v193 offset:4096\n" \
    "v_exp_f32 v102, v102\n" \
    "v_exp_f32 v103, v103\n" \
    "v_exp_f32 v104, v104\n" \
    "v_exp_f32 v105, v105\n" \
    "v_smfmac_f32_32x32x32_bf16 v[144:159], v[176:179], v[96:103], v192\n" \
    "v_exp_f32 v106, v106\n" \
    "v_exp_f32 v107, v107\n" \
    "v_exp_f32 v108, v108\n" \
    "v_exp_f32 v109, v109\n" \
    "v_exp_f32 v110, v110\n" \
    "v_exp_f32 v111, v111\n" \
    "v_exp_f32 v80, v80\n" \
    "v_exp_f32 v81, v81\n" \
    "v_smfmac_f32_32x32x32_bf16 v[144:159], v[180:183], v[104:111], v192\n" \
    "ds_write_b128 v230, v[232:235]\n" \
    "ds_read_b128 v[176:179], v194 offset:4096\n" \
    "v_exp_f32 v82, v82\n" \
    "v_exp_f32 v83, v83\n" \
    "v_exp_f32 v84, v84\n" \
    "v_exp_f32 v85, v85\n" \
    "s_waitcnt lgkmcnt(1)\n" \
    "v_mfma_f32_32x32x16_bf16 v[64:79], v[168:171], v[160:163], 0\n" \
    "ds_read_b128 v[180:183], v194 offset:4608\n" \
    "v_exp_f32 v86, v86\n" \
    "v_exp_f32 v87, v87\n" \
    "v_exp_f32 v88, v88\n" \
    "v_exp_f32 v89, v89\n" \
    "v_smfmac_f32_32x32x32_bf16 v[128:143], v[184:187], v[80:87], v192\n" \
    "v_exp_f32 v90, v90\n" \
    "v_exp_f32 v91, v91\n" \
    "v_exp_f32 v92, v92\n" \
    "v_exp_f32 v93, v93\n" \
    "v_exp_f32 v94, v94\n" \
    "v_exp_f32 v95, v95\n" \
    "v_exp_f32 v112, v112\n" \
    "v_exp_f32 v113, v113\n" \
    "v_smfmac_f32_32x32x32_bf16 v[128:143], v[188:191], v[88:95], v192\n" \
    "v_exp_f32 v114, v114\n" \
    "v_exp_f32 v115, v115\n" \
    "v_exp_f32 v116, v116\n" \
    "v_exp_f32 v117, v117\n" \
    "v_mfma_f32_32x32x16_bf16 v[96:111], v[168:171], v[164:167], 0\n" \
    "ds_read_b128 v[172:175], v193 offset:5120\n" \
    "v_exp_f32 v118, v118\n" \
    "v_exp_f32 v119, v119\n" \
    "v_exp_f32 v120, v120\n" \
    "v_exp_f32 v121, v121\n" \
    "v_smfmac_f32_32x32x32_bf16 v[144:159], v[184:187], v[112:119], v192\n" \
    "v_exp_f32 v122, v122\n" \
    "v_exp_f32 v123, v123\n" \
    "v_exp_f32 v124, v124\n" \
    "v_exp_f32 v125, v125\n" \
    "v_exp_f32 v126, v126\n" \
    "v_exp_f32 v127, v127\n" \
    "v_add_u32 v193, 4096, v193\n" \
    "v_add_u32 v194, v195, v194\n" \
    "s_waitcnt vmcnt(0)\n"

#define ATT_C \
    "s_mov_b32 %[cnt], 2\n" \
    "1:\n" \
    "v_exp_f32 v64, v64\n" \
    "v_exp_f32 v65, v65\n" \
    "v_smfmac_f32_32x32x32_bf16 v[144:159], v[188:191], v[120:127], v192\n" \
    "ds_read_b128 v[184:187], v194 offset:1024\n" \
    "v_exp_f32 v66, v66\n" \
    "v_exp_f32 v67, v67\n" \
    "v_exp_f32 v68, v68\n" \
    "v_exp_f32 v69, v69\n" \
    "s_waitcnt lgkmcnt(1)\n" \
    "v_mfma_f32_32x32x16_bf16 v[80:95], v[172:175], v[160:163], 0\n" \
    "ds_read_b128 v[188:191], v194 offset:1536\n" \
    "v_exp_f32 v70, v70\n" \
    "v_exp_f32 v71, v71\n" \
    "v_exp_f32 v72, v72\n" \
    "v_exp_f32 v73, v73\n" \
    "v_smfmac_f32_32x32x32_bf16 v[128:143], v[176:179], v[64:71], v192\n" \
    "v_exp_f32 v74, v74\n" \
    "s_mov_b32 m0, %[swo]\n" \
    "v_exp_f32 v75, v75\n" \
    "v_exp_f32 v76, v76\n" \
    "global_load_lds_dwordx4 v236, %[wob] offset:0\n" \
    "v_exp_f32 v77, v77\n" \
    "v_exp_f32 v78, v78\n" \
    "v_exp_f32 v79, v79\n" \
    "v_exp_f32 v96, v96\n" \
    "v_exp_f32 v97, v97\n" \
    "v_smfmac_f32_32x32x32_bf16 v[128:143], v[180:183], v[72:79], v192\n" \
    "v_exp_f32 v98, v98\n" \
    "v_exp_f32 v99, v99\n" \
    "v_exp_f32 v100, v100\n" \
    "v_exp_f32 v101, v101\n" \
    "v_mfma_f32_32x32x16_bf16 v[112:127], v[172:175], v[164:167], 0\n" \
    "ds_read_b128 v[168:171], v193 offset:2048\n" \
    "v_exp_f32 v102, v102\n" \
    "v_exp_f32 v103, v103\n" \
    "v_exp_f32 v104, v104\n" \
    "v_exp_f32 v105, v105\n" \
    "v_smfmac_f32_32x32x32_bf16 v[144:159], v[176:179], v[96:103], v192\n" \
    "v_exp_f32 v106, v106\n" \
    "v_exp_f32 v107, v107\n" \
    "v_exp_f32 v108, v108\n" \
    "v_exp_f32 v109, v109\n" \
    "v_exp_f32 v110, v110\n" \
    "v_exp_f32 v111, v111\n" \
    "v_exp_f32 v80, v80\n" \
    "v_exp_f32 v81, v81\n" \
    "v_smfmac_f32_32x32x32_bf16 v[144:159], v[180:183], v[104:111], v192\n" \
    "ds_read_b128 v[176:179], v194 offset:2048\n" \
    "v_exp_f32 v82, v82\n" \
    "v_exp_f32 v83, v83\n" \
    "v_exp_f32 v84, v84\n" \
    "v_exp_f32 v85, v85\n" \
    "s_waitcnt lgkmcnt(1)\n" \
    "v_mfma_f32_32x32x16_bf16 v[64:79], v[168:171], v[160:163], 0\n" \
    "ds_read_b128 v[180:183], v194 offset:2560\n" \
    "v_exp_f32 v86, v86\n" \
    "v_exp_f32 v87, v87\n" \
    "v_exp_f32 v88, v88\n" \
    "v_exp_f32 v89, v89\n" \
    "v_smfmac_f32_32x32x32_bf16 v[128:143], v[184:187], v[80:87], v192\n" \
    "v_exp_f32 v90, v90\n" \
    "v_exp_f32 v91, v91\n" \
    "v_exp_f32 v92, v92\n" \
    "global_load_lds_dwordx4 v237, %[wob] offset:1024\n" \
    "v_exp_f32 v93, v93\n" \
    "v_exp_f32 v94, v94\n" \
    "v_exp_f32 v95, v95\n" \
    "v_exp_f32 v112, v112\n" \
    "v_exp_f32 v113, v113\n" \
    "v_smfmac_f32_32x32x32_bf16 v[128:143], v[188:191], v[88:95], v192\n" \
    "v_exp_f32 v114, v114\n" \
    "v_exp_f32 v115, v115\n" \
    "v_exp_f32 v116, v116\n" \
    "v_exp_f32 v117, v117\n" \
    "v_mfma_f32_32x32x16_bf16 v[96:111], v[168:171], v[164:167], 0\n" \
    "ds_read_b128 v[172:175], v193 offset:3072\n" \
    "v_exp_f32 v118, v118\n" \
    "v_exp_f32 v119, v119\n" \
    "v_exp_f32 v120, v120\n" \
    "v_exp_f32 v121, v121\n" \
    "v_smfmac_f32_32x32x32_bf16 v[144:159], v[184:187], v[112:119], v192\n" \
    "v_exp_f32 v122, v122\n" \
    "v_exp_f32 v123, v123\n" \
    "v_exp_f32 v124, v124\n" \
    "v_exp_f32 v125, v125\n" \
    "v_exp_f32 v126, v126\n" \
    "v_exp_f32 v127, v127\n" \
    "v_exp_f32 v64, v64\n" \
    "v_exp_f32 v65, v65\n" \
    "v_smfmac_f32_32x32x32_bf16 v[144:159], v[188:191], v[120:127], v192\n" \
    "ds_read_b128 v[184:187], v194 offset:3072\n" \
    "v_exp_f32 v66, v66\n" \
    "v_exp_f32 v67, v67\n" \
    "v_exp_f32 v68, v68\n" \
    "v_exp_f32 v69, v69\n" \
    "s_waitcnt lgkmcnt(1)\n" \
    "v_mfma_f32_32x32x16_bf16 v[80:95], v[172:175], v[160:163], 0\n" \
    "ds_read_b128 v[188:191], v194 offset:3584\n" \
    "v_exp_f32 v70, v70\n" \
    "v_exp_f32 v71, v71\n" \
    "v_exp_f32 v72, v72\n" \
    "v_exp_f32 v73, v73\n" \
    "v_smfmac_f32_32x32x32_bf16 v[128:143], v[176:179], v[64:71], v192\n" \
    "v_exp_f32 v74, v74\n" \
    "v_exp_f32 v75, v75\n" \
    "v_exp_f32 v76, v76\n" \
    "global_load_lds_dwordx4 v238, %[wob] offset:2048\n" \
    "v_exp_f32 v77, v77\n" \
    "v_exp_f32 v78, v78\n" \
    "v_exp_f32 v79, v79\n" \
    "v_exp_f32 v96, v96\n" \
    "v_exp_f32 v97, v97\n" \
    "v_smfmac_f32_32x32x32_bf16 v[128:143], v[180:183], v[72:79], v192\n" \
    "v_exp_f32 v98, v98\n" \
    "v_exp_f32 v99, v99\n" \
    "v_exp_f32 v100, v100\n" \
    "v_exp_f32 v101, v101\n" \
    "v_mfma_f32_32x32x16_bf16 v[112:127], v[172:175], v[164:167], 0\n" \
    "ds_read_b128 v[168:171], v193 offset:4096\n" \
    "v_exp_f32 v102, v102\n" \
    "v_exp_f32 v103, v103\n" \
    "v_exp_f32 v104, v104\n" \
    "v_exp_f32 v105, v105\n" \
    "v_smfmac_f32_32x32x32_bf16 v[144:159], v[176:179], v[96:103], v192\n" \
    "v_exp_f32 v106, v106\n" \
    "v_exp_f32 v107, v107\n" \
    "v_exp_f32 v108, v108\n" \
    "v_exp_f32 v109, v109\n" \
    "v_exp_f32 v110, v110\n" \
    "v_exp_f32 v111, v111\n" \
    "v_exp_f32 v80, v80\n" \
    "v_exp_f32 v81, v81\n" \
    "v_smfmac_f32_32x32x32_bf16 v[144:159], v[180:183], v[104:111], v192\n" \
    "ds_read_b128 v[176:179], v194 offset:4096\n" \
    "v_exp_f32 v82, v82\n" \
    "v_exp_f32 v83, v83\n" \
    "v_exp_f32 v84, v84\n" \
    "v_exp_f32 v85, v85\n" \
    "s_waitcnt lgkmcnt(1)\n" \
    "v_mfma_f32_32x32x16_bf16 v[64:79], v[168:171], v[160:163], 0\n" \
    "ds_read_b128 v[180:183], v194 offset:4608\n" \
    "v_exp_f32 v86, v86\n" \
    "v_exp_f32 v87, v87\n" \
    "v_exp_f32 v88, v88\n" \
    "v_exp_f32 v89, v89\n" \
    "v_smfmac_f32_32x32x32_bf16 v[128:143], v[184:187], v[80:87], v192\n" \
    "v_exp_f32 v90, v90\n" \
    "v_exp_f32 v91, v91\n" \
    "v_exp_f32 v92, v92\n" \
    "global_load_lds_dwordx4 v239, %[wob] offset:3072\n" \
    "v_exp_f32 v93, v93\n" \
    "v_exp_f32 v94, v94\n" \
    "v_exp_f32 v95, v95\n" \
    "v_exp_f32 v112, v112\n" \
    "v_exp_f32 v113, v113\n" \
    "v_smfmac_f32_32x32x32_bf16 v[128:143], v[188:191], v[88:95], v192\n" \
    "v_exp_f32 v114, v114\n" \
    "v_exp_f32 v115, v115\n" \
    "v_exp_f32 v116, v116\n" \
    "v_exp_f32 v117, v117\n" \
    "v_mfma_f32_32x32x16_bf16 v[96:111], v[168:171], v[164:167], 0\n" \
    "ds_read_b128 v[172:175], v193 offset:5120\n" \
    "v_exp_f32 v118, v118\n" \
    "v_exp_f32 v119, v119\n" \
    "v_exp_f32 v120, v120\n" \
    "v_exp_f32 v121, v121\n" \
    "v_smfmac_f32_32x32x32_bf16 v[144:159], v[184:187], v[112:119], v192\n" \
    "v_exp_f32 v122, v122\n" \
    "v_exp_f32 v123, v123\n" \
    "v_exp_f32 v124, v124\n" \
    "v_exp_f32 v125, v125\n" \
    "v_exp_f32 v126, v126\n" \
    "v_exp_f32 v127, v127\n" \
    "v_add_u32 v236, 0x8000, v236\n" \
    "v_add_u32 v237, 0x8000, v237\n" \
    "v_add_u32 v238, 0x8000, v238\n" \
    "v_add_u32 v239, 0x8000, v239\n" \
    "s_add_u32 %[swo], %[swo], 0x1000\n" \
    "v_add_u32 v193, 4096, v193\n" \
    "v_add_u32 v194, v195, v194\n" \
    "s_sub_u32 %[cnt], %[cnt], 1\n" \
    "s_cmp_lg_u32 %[cnt], 0\n" \
    "s_cbranch_scc1 1b\n" \
    "s_mov_b32 %[cnt], 2\n" \
    "2:\n" \
    "v_exp_f32 v64, v64\n" \
    "v_exp_f32 v65, v65\n" \
    "v_smfmac_f32_32x32x32_bf16 v[144:159], v[188:191], v[120:127], v192\n" \
    "ds_read_b128 v[184:187], v194 offset:1024\n" \
    "v_exp_f32 v66, v66\n" \
    "v_exp_f32 v67, v67\n" \
    "v_exp_f32 v68, v68\n" \
    "v_exp_f32 v69, v69\n" \
    "s_waitcnt lgkmcnt(1)\n" \
    "v_mfma_f32_32x32x16_bf16 v[80:95], v[172:175], v[160:163], 0\n" \
    "ds_read_b128 v[188:191], v194 offset:1536\n" \
    "v_exp_f32 v70, v70\n" \
    "v_exp_f32 v71, v71\n" \
    "v_exp_f32 v72, v72\n" \
    "v_exp_f32 v73, v73\n" \
    "v_smfmac_f32_32x32x32_bf16 v[128:143], v[176:179], v[64:71], v192\n" \
    "v_exp_f32 v74, v74\n" \
    "v_exp_f32 v75, v75\n" \
    "v_exp_f32 v76, v76\n" \
    "v_exp_f32 v77, v77\n" \
    "v_exp_f32 v78, v78\n" \
    "v_exp_f32 v79, v79\n" \
    "v_exp_f32 v96, v96\n" \
    "v_exp_f32 v97, v97\n" \
    "v_smfmac_f32_32x32x32_bf16 v[128:143], v[180:183], v[72:79], v192\n" \
    "v_exp_f32 v98, v98\n" \
    "v_exp_f32 v99, v99\n" \
    "v_exp_f32 v100, v100\n" \
    "v_exp_f32 v101, v101\n" \
    "v_mfma_f32_32x32x16_bf16 v[112:127], v[172:175], v[164:167], 0\n" \
    "ds_read_b128 v[168:171], v193 offset:2048\n" \
    "v_exp_f32 v102, v102\n" \
    "v_exp_f32 v103, v103\n" \
    "v_exp_f32 v104, v104\n" \
    "v_exp_f32 v105, v105\n" \
    "v_smfmac_f32_32x32x32_bf16 v[144:159], v[176:179], v[96:103], v192\n" \
    "v_exp_f32 v106, v106\n" \
    "v_exp_f32 v107, v107\n" \
    "v_exp_f32 v108, v108\n" \
    "v_exp_f32 v109, v109\n" \
    "v_exp_f32 v110, v110\n" \
    "v_exp_f32 v111, v111\n" \
    "v_exp_f32 v80, v80\n" \
    "v_exp_f32 v81, v81\n" \
    "v_smfmac_f32_32x32x32_bf16 v[144:159], v[180:183], v[104:111], v192\n" \
    "ds_read_b128 v[176:179], v194 offset:2048\n" \
    "v_exp_f32 v82, v82\n" \
    "v_exp_f32 v83, v83\n" \
    "v_exp_f32 v84, v84\n" \
    "v_exp_f32 v85, v85\n" \
    "s_waitcnt lgkmcnt(1)\n" \
    "v_mfma_f32_32x32x16_bf16 v[64:79], v[168:171], v[160:163], 0\n" \
    "ds_read_b128 v[180:183], v194 offset:2560\n" \
    "v_exp_f32 v86, v86\n" \
    "v_exp_f32 v87, v87\n" \
    "v_exp_f32 v88, v88\n" \
    "v_exp_f32 v89, v89\n" \
    "v_smfmac_f32_32x32x32_bf16 v[128:143], v[184:187], v[80:87], v192\n" \
    "v_exp_f32 v90, v90\n" \
    "v_exp_f32 v91, v91\n" \
    "v_exp_f32 v92, v92\n" \
    "v_exp_f32 v93, v93\n" \
    "v_exp_f32 v94, v94\n" \
    "v_exp_f32 v95, v95\n" \
    "v_exp_f32 v112, v112\n" \
    "v_exp_f32 v113, v113\n" \
    "v_smfmac_f32_32x32x32_bf16 v[128:143], v[188:191], v[88:95], v192\n" \
    "v_exp_f32 v114, v114\n" \
    "v_exp_f32 v115, v115\n" \
    "v_exp_f32 v116, v116\n" \
    "v_exp_f32 v117, v117\n" \
    "v_mfma_f32_32x32x16_bf16 v[96:111], v[168:171], v[164:167], 0\n" \
    "ds_read_b128 v[172:175], v193 offset:3072\n" \
    "v_exp_f32 v118, v118\n" \
    "v_exp_f32 v119, v119\n" \
    "v_exp_f32 v120, v120\n" \
    "v_exp_f32 v121, v121\n" \
    "v_smfmac_f32_32x32x32_bf16 v[144:159], v[184:187], v[112:119], v192\n" \
    "v_exp_f32 v122, v122\n" \
    "v_exp_f32 v123, v123\n" \
    "v_exp_f32 v124, v124\n" \
    "v_exp_f32 v125, v125\n" \
    "v_exp_f32 v126, v126\n" \
    "v_exp_f32 v127, v127\n" \
    "v_exp_f32 v64, v64\n" \
    "v_exp_f32 v65, v65\n" \
    "v_smfmac_f32_32x32x32_bf16 v[144:159], v[188:191], v[120:127], v192\n" \
    "ds_read_b128 v[184:187], v194 offset:3072\n" \
    "v_exp_f32 v66, v66\n" \
    "v_exp_f32 v67, v67\n" \
    "v_exp_f32 v68, v68\n" \
    "v_exp_f32 v69, v69\n" \
    "s_waitcnt lgkmcnt(1)\n" \
    "v_mfma_f32_32x32x16_bf16 v[80:95], v[172:175], v[160:163], 0\n" \
    "ds_read_b128 v[188:191], v194 offset:3584\n" \
    "v_exp_f32 v70, v70\n" \
    "v_exp_f32 v71, v71\n" \
    "v_exp_f32 v72, v72\n" \
    "v_exp_f32 v73, v73\n" \
    "v_smfmac_f32_32x32x32_bf16 v[128:143], v[176:179], v[64:71], v192\n" \
    "v_exp_f32 v74, v74\n" \
    "v_exp_f32 v75, v75\n" \
    "v_exp_f32 v76, v76\n" \
    "v_exp_f32 v77, v77\n" \
    "v_exp_f32 v78, v78\n" \
    "v_exp_f32 v79, v79\n" \
    "v_exp_f32 v96, v96\n" \
    "v_exp_f32 v97, v97\n" \
    "v_smfmac_f32_32x32x32_bf16 v[128:143], v[180:183], v[72:79], v192\n" \
    "v_exp_f32 v98, v98\n" \
    "v_exp_f32 v99, v99\n" \
    "v_exp_f32 v100, v100\n" \
    "v_exp_f32 v101, v101\n" \
    "v_mfma_f32_32x32x16_bf16 v[112:127], v[172:175], v[164:167], 0\n" \
    "ds_read_b128 v[168:171], v193 offset:4096\n" \
    "v_exp_f32 v102, v102\n" \
    "v_exp_f32 v103, v103\n" \
    "v_exp_f32 v104, v104\n" \
    "v_exp_f32 v105, v105\n" \
    "v_smfmac_f32_32x32x32_bf16 v[144:159], v[176:179], v[96:103], v192\n" \
    "v_exp_f32 v106, v106\n" \
    "v_exp_f32 v107, v107\n" \
    "v_exp_f32 v108, v108\n" \
    "v_exp_f32 v109, v109\n" \
    "v_exp_f32 v110, v110\n" \
    "v_exp_f32 v111, v111\n" \
    "v_exp_f32 v80, v80\n" \
    "v_exp_f32 v81, v81\n" \
    "v_smfmac_f32_32x32x32_bf16 v[144:159], v[180:183], v[104:111], v192\n" \
    "ds_read_b128 v[176:179], v194 offset:4096\n" \
    "v_exp_f32 v82, v82\n" \
    "v_exp_f32 v83, v83\n" \
    "v_exp_f32 v84, v84\n" \
    "v_exp_f32 v85, v85\n" \
    "s_waitcnt lgkmcnt(1)\n" \
    "v_mfma_f32_32x32x16_bf16 v[64:79], v[168:171], v[160:163], 0\n" \
    "ds_read_b128 v[180:183], v194 offset:4608\n" \
    "v_exp_f32 v86, v86\n" \
    "v_exp_f32 v87, v87\n" \
    "v_exp_f32 v88, v88\n" \
    "v_exp_f32 v89, v89\n" \
    "v_smfmac_f32_32x32x32_bf16 v[128:143], v[184:187], v[80:87], v192\n" \
    "v_exp_f32 v90, v90\n" \
    "v_exp_f32 v91, v91\n" \
    "v_exp_f32 v92, v92\n" \
    "v_exp_f32 v93, v93\n" \
    "v_exp_f32 v94, v94\n" \
    "v_exp_f32 v95, v95\n" \
    "v_exp_f32 v112, v112\n" \
    "v_exp_f32 v113, v113\n" \
    "v_smfmac_f32_32x32x32_bf16 v[128:143], v[188:191], v[88:95], v192\n" \
    "v_exp_f32 v114, v114\n" \
    "v_exp_f32 v115, v115\n" \
    "v_exp_f32 v116, v116\n" \
    "v_exp_f32 v117, v117\n" \
    "v_mfma_f32_32x32x16_bf16 v[96:111], v[168:171], v[164:167], 0\n" \
    "ds_read_b128 v[172:175], v193 offset:5120\n" \
    "v_exp_f32 v118, v118\n" \
    "v_exp_f32 v119, v119\n" \
    "v_exp_f32 v120, v120\n" \
    "v_exp_f32 v121, v121\n" \
    "v_smfmac_f32_32x32x32_bf16 v[144:159], v[184:187], v[112:119], v192\n" \
    "v_exp_f32 v122, v122\n" \
    "v_exp_f32 v123, v123\n" \
    "v_exp_f32 v124, v124\n" \
    "v_exp_f32 v125, v125\n" \
    "v_exp_f32 v126, v126\n" \
    "v_exp_f32 v127, v127\n" \
    "s_cmp_eq_u32 %[cnt], 2\n" \
    "s_cbranch_scc0 8f\n" \
    "s_setprio 0\n" \
    "8:\n" \
    "v_add_u32 v193, 4096, v193\n" \
    "v_add_u32 v194, v195, v194\n" \
    "s_sub_u32 %[cnt], %[cnt], 1\n" \
    "s_cmp_lg_u32 %[cnt], 0\n" \
    "s_cbranch_scc1 2b\n" \
    "s_nop 1\n" \
    "v_smfmac_f32_32x32x32_bf16 v[144:159], v[188:191], v[120:127], v192\n" \
    "s_setprio 0\n" \
    "s_waitcnt vmcnt(0)\n" \
    "s_nop 7\n" \
    "s_nop 7\n" \
    "s_nop 3\n" \
    "s_waitcnt lgkmcnt(0)\n"

__device__ __forceinline__ void slow_block(const char* ws, u32x4 q, int lane, float* o, float& lsum) {
    const u32 koff = WS_K + lane * 16;
    const u32 r32 = lane & 31;
    float m = -3.0e38f;
    for (int t = 0; t < 64; ++t) {
        u32x4 kf;
        GLD128C(kf, ws, koff + t * 1024, 0);
        asm volatile("s_waitcnt vmcnt(0)" : "+v"(kf) :: "memory");
        f32x16 s = {0};
        s = __builtin_amdgcn_mfma_f32_32x32x16_bf16(asbf(kf), asbf(q), s, 0, 0, 0);
#pragma unroll
        for (int i = 0; i < 16; ++i) m = fmaxf(m, s[i]);
    }
    {
        auto sw = __builtin_amdgcn_permlane32_swap(__float_as_uint(m), __float_as_uint(m), false, false);
        m = fmaxf(__uint_as_float(sw[0]), __uint_as_float(sw[1]));
    }
    f32x16 acc = {0};
    const u32 voff = WS_V + (lane >> 5) * 256 + (lane & 15) * 16;
    for (int t = 0; t < 64; ++t) {
        u32x4 kf, v0, v1;
        GLD128C(kf, ws, koff + t * 1024, 0);
        GLD128C(v0, ws, voff + t * 1024, 0);
        GLD128C(v1, ws, voff + t * 1024, 512);
        asm volatile("s_waitcnt vmcnt(0)" : "+v"(kf), "+v"(v0), "+v"(v1) :: "memory");
        if (r32 >= 16) { v0 = (u32x4){0x3f803f80u, 0x3f803f80u, 0x3f803f80u, 0x3f803f80u}; v1 = v0; }
        f32x16 s = {0};
        s = __builtin_amdgcn_mfma_f32_32x32x16_bf16(asbf(kf), asbf(q), s, 0, 0, 0);
        u32 p[8];
#pragma unroll
        for (int i = 0; i < 8; ++i)
            p[i] = pk(__builtin_amdgcn_exp2f(s[2 * i] - m), __builtin_amdgcn_exp2f(s[2 * i + 1] - m));
#pragma unroll
        for (int st = 0; st < 2; ++st) {
            u32x4 vv = st ? v1 : v0;
            auto a = __builtin_amdgcn_permlane32_swap(vv[0], vv[2], false, false);
            auto c = __builtin_amdgcn_permlane32_swap(vv[1], vv[3], false, false);
            u32x4 af = {a[0], c[0], a[1], c[1]};
            u32x4 bf = {p[4 * st], p[4 * st + 1], p[4 * st + 2], p[4 * st + 3]};
            acc = __builtin_amdgcn_mfma_f32_32x32x16_bf16(asbf(af), asbf(bf), acc, 0, 0, 0);
        }
    }
#pragma unroll
    for (int i = 0; i < 8; ++i) o[i] = acc[i];
    lsum = acc[8];
}

#define PRIO_MODE 1
#define PRIO_START() do { if (w >= 4) __builtin_amdgcn_s_setprio(1); } while (0)
#define PRIO_END() __builtin_amdgcn_s_setprio(0)
__global__ void __launch_bounds__(512, 2)
fused_kernel(const u16* x_, const u16* wq_, const u16* wk_, const u16* wv_, const u16* wo_, const u16* wr_, const u16* wg_,
             const u16* wu_, const u16* wd_, const u16* gw_, u16* out_, char* ws_)
{
    u32x16 ka0; u32x8 ka1;
    {
        const void* kp = (const void*)__builtin_amdgcn_kernarg_segment_ptr();
        asm volatile("s_load_dwordx16 %0, %2, 0\n\ts_load_dwordx8 %1, %2, 64\n\ts_waitcnt lgkmcnt(0)"
                     : "=&s"(ka0), "=&s"(ka1) : "s"(kp) : "memory");
    }
#define KP(v, i) ((u64)v[2 * (i)] | ((u64)v[2 * (i) + 1] << 32))
    const u16* x  = (const u16*)KP(ka0, 0); const u16* wq = (const u16*)KP(ka0, 1);
    const u16* wk = (const u16*)KP(ka0, 2); const u16* wv = (const u16*)KP(ka0, 3);
    const u16* wo = (const u16*)KP(ka0, 4); const u16* wr = (const u16*)KP(ka0, 5);
    const u16* wg = (const u16*)KP(ka0, 6); const u16* wu = (const u16*)KP(ka0, 7);
    const u16* wd = (const u16*)KP(ka1, 0); const u16* gw = (const u16*)KP(ka1, 1);
    u16* out = (u16*)KP(ka1, 2); char* ws = (char*)KP(ka1, 3);

    __shared__ __attribute__((aligned(1024))) char smem[L_END];
    LDSP char* lds = (LDSP char*)smem;
    const u32 lb = (u32)(size_t)lds;
    const int tid = threadIdx.x;
    const int lane = tid & 63;
    const int w = __builtin_amdgcn_readfirstlane(tid >> 6);
    const int b = blockIdx.x;
    const int xcd = b & 7, jj = b >> 3;
    const int tb = xcd * 4 + (jj >> 3), hg = jj & 7;
    const int g = lane >> 4, l15 = lane & 15, h = lane >> 5, l31 = lane & 31;
    const bool isK = hg < 4;
    const bool ffn = jj < 28;
    const int fj = xcd * 28 + min(jj, 27);
    STAMP(1); STAMPC(18);

    const int th = w & 1, hp = (w >> 1) & 1, blk = w >> 2;
    const int T0 = tb * 64 + (hg & 3) * 16;
    u32x4 xf0, xf1, vf0, vf1;
    {
        const u16* wsrc = isK ? wk : wv;
#pragma unroll
        for (int i = 0; i < 2; ++i) {
            u32 slot = 64 * i + lane, pos = slot >> 3, B = pos ^ ((pos >> 2) & 1);
            u32 row = 4 * B + ((slot >> 1) & 3);
            dma16(wsrc, (64 * w + row) * 32 + (slot & 1) * 16, lb + L_W + w * 2048 + i * 1024);
        }
        u32 xo = (T0 + l15) * 1024 + (64 * w + 16 * g) * 2;
        GLD128(xf0, x, xo, 0);
        GLD128(xf1, x, xo, 16);
        const u16* vec = isK ? wr : gw;
        u32 vo = (64 * w + 16 * g) * 2;
        GLD128(vf0, vec, vo, 0);
        GLD128(vf1, vec, vo, 16);
    }
    u32 epo;
    { u32 z = 0; GLD32C(epo, ws, z, 0); }
    asm volatile("s_barrier" ::: "memory");
    if (w < 7) {
#pragma unroll
        for (int i = 0; i < 19; ++i) {
            const int p = i < 18 ? w + 7 * i : __builtin_amdgcn_readfirstlane(min(w + 7 * i, 127));
            if (i < 9 || (i == 9 && w == 0)) {
                dma16(x, (tb * 64 + p) * 1024 + ((lane ^ (p & 15)) << 4), lb + L_K + p * 1024);
            } else {
                const int G = p - 64;
                dma16(wq, (8 * G + ((lane >> 2) & 7)) * 1024 + hg * 128 + (lane >> 5) * 64 + (lane & 3) * 16, lb + L_V + G * 1024);
            }
        }
    } else {
        const u32x4 pz = {POISON, POISON, POISON, POISON};
#pragma unroll
        for (int i = 0; i < 8; ++i) {
            u32 po = WS_O + ((tb * 4 + (i >> 1)) * 16 + hg * 2 + (i & 1)) * 1024 + lane * 16;
            GST128C(ws, po, pz);
        }
        u32 yo = (ffn ? WS_Y + fj * 2048 : WS_X + b * 2048) + lane * 16;
        GST128C(ws, yo, pz);
        yo += 1024;
        GST128C(ws, yo, pz);
    }
    if (tid < 256) { u32 o1 = (tid & 15) == 0 ? 0x3f803f80u : 0u; ST128(L_ONE + tid * 16, ((u32x4){o1, o1, o1, o1})); }
    asm volatile("s_cmp_eq_u32 %[w], 7\n\ts_cbranch_scc1 1f\n\ts_waitcnt vmcnt(20)\n\ts_branch 2f\n1:\n\ts_waitcnt vmcnt(11)\n2:"
                 : "+v"(xf0), "+v"(xf1), "+v"(vf0), "+v"(vf1) : [w] "s"(w) : "memory", "scc");
    FSTAMP(2);
    {
        const u32 ta = L_W + w * 2048 + l15 * 8;
        const u32 gx = g & 1;
        u32x4 wf0 = cat(trread(lds, ta + ((4 * g + 0) ^ gx) * 128), trread(lds, ta + ((4 * g + 1) ^ gx) * 128));
        u32x4 wf1 = cat(trread(lds, ta + ((4 * g + 2) ^ gx) * 128), trread(lds, ta + ((4 * g + 3) ^ gx) * 128));
        f32x4 acc = {0, 0, 0, 0}, dacc = {0, 0, 0, 0};
        if (isK) {
            acc = __builtin_amdgcn_mfma_f32_16x16x32_bf16(asbf(wf0), asbf(xf0), acc, 0, 0, 0);
            acc = __builtin_amdgcn_mfma_f32_16x16x32_bf16(asbf(wf1), asbf(xf1), acc, 0, 0, 0);
        } else {
            acc = __builtin_amdgcn_mfma_f32_16x16x32_bf16(asbf(xf0), asbf(wf0), acc, 0, 0, 0);
            acc = __builtin_amdgcn_mfma_f32_16x16x32_bf16(asbf(xf1), asbf(wf1), acc, 0, 0, 0);
        }
        dacc = __builtin_amdgcn_mfma_f32_16x16x32_bf16(asbf(vf0), asbf(xf0), dacc, 0, 0, 0);
        dacc = __builtin_amdgcn_mfma_f32_16x16x32_bf16(asbf(vf1), asbf(xf1), dacc, 0, 0, 0);
        ST128(L_P1 + w * 1024 + lane * 16, __builtin_bit_cast(u32x4, acc));
        if (g == 0) STF(L_PD + w * 64 + l15 * 4, dacc[0]);
    }
    BAR();
    FSTAMP(3);
    asm volatile("s_cmp_eq_u32 %[w], 7\n\ts_cbranch_scc1 1f\n\ts_waitcnt vmcnt(19)\n\ts_branch 2f\n1:\n\ts_waitcnt vmcnt(10)\n2:"
                 : "+v"(epo) : [w] "s"(w) : "memory", "scc");
    const u32 epoch = (u32)__builtin_amdgcn_readfirstlane((int)epo) + 1u;
    if (w == 7) {
        f32x4 ps[8]; float pd[8];
#pragma unroll
        for (int i = 0; i < 8; ++i) {
            ps[i] = __builtin_bit_cast(f32x4, LD128(L_P1 + i * 1024 + lane * 16));
            pd[i] = LDF(L_PD + i * 64 + l15 * 4);
        }
        __builtin_amdgcn_sched_barrier(0);
        const f32x4 s = ((ps[0] + ps[1]) + (ps[2] + ps[3])) + ((ps[4] + ps[5]) + (ps[6] + ps[7]));
        const float d = ((pd[0] + pd[1]) + (pd[2] + pd[3])) + ((pd[4] + pd[5]) + (pd[6] + pd[7]));
        u32x2 pv = {pk(s[0], s[1]), pk(s[2], s[3])};
        const int tile = T0 >> 5, sub = (T0 >> 4) & 1;
        u32 key = 0;
        if (isK) {
            u32 off = WS_K + tile * 1024 + ((g & 1) * 32 + sub * 16 + l15) * 16 + (g >> 1) * 8;
            GST64C(ws, off, pv);
            u32 rb = pk(d, d) & 0xffffu;
            u32 ord = (rb & 0x8000u) ? (~rb & 0xffffu) : (rb | 0x8000u);
            key = wave_max_u32((ord << 16) | (0xffffu - (u32)(T0 + l15)));
        } else {
            u32 off = WS_V + tile * 1024 + sub * 512 + (g >> 1) * 256 + l15 * 16 + (g & 1) * 8;
            GST64C(ws, off, pv);
            if (g == 0) { u32 go = WS_G + (T0 + l15) * 4; u32 dv = __float_as_uint(d); GST32C(ws, go, dv); }
        }
        VMWAIT(0);
        if (lane < 8) { u32x2 fl = {key, epoch}; u32 fo = WS_F1 + lane * 2048 + (tile * 4 + (isK ? 0 : 2) + sub) * 8; GST64C(ws, fo, fl); }
        FSTAMP(4);
    } else {
        VMWAIT(0);
    }
    BAR();
    FSTAMP(5);

    f32x16 qacc = {0};
    if (w < 4) {
        const int qh = w >> 1;
        const int tok = th * 32 + l31;
        u32 abase = L_V + h * 1024 + qh * 512 + ((l15 >> 2) * 64) + ((lane >> 4) & 1) * 32 + (l15 & 3) * 8;
        asm volatile("" : "+v"(abase));
        u32 bb[8];
#pragma unroll
        for (int j = 0; j < 8; ++j) { bb[j] = L_K + tok * 1024 + (((2 * j + h) ^ (tok & 15)) << 4); asm volatile("" : "+v"(bb[j])); }
#pragma unroll
        for (int s = 0; s < 32; ++s) {
            u32x4 af = cat(trread(lds, abase + s * 2048), trread(lds, abase + s * 2048 + 256));
            u32x4 bf = LD128(bb[s & 7] + (s >> 3) * 256);
            qacc = __builtin_amdgcn_mfma_f32_32x32x16_bf16(asbf(af), asbf(bf), qacc, 0, 0, 0);
        }
        FSTAMP(6);
    } else {
        const int j = w - 4;
        const u32 fo = WS_F1 + xcd * 2048 + (j * 64 + l31 * 2) * 8;
        u32x4 f;
        u32 spins = 0;
        while (true) {
            asm volatile("global_load_dwordx4 %0, %1, %2 sc1\n\ts_waitcnt vmcnt(0)" : "=&v"(f) : "v"(fo), "s"(ws) : "memory");
            if (__all((f[1] == epoch) & (f[3] == epoch)) || ++spins > SPIN_MAX) break;
        }
        const u32 key = wave_max_u32(max(f[0], f[2]));
        if (lane == 0) ST32(L_CT + j * 4, key);
        FSTAMP(7);
    }
    BAR();
    FSTAMP(9);
    u32 kvo = (w & 1) * 65536 + ((w >> 1) & 1) * 32768;
    if (w >= 4) {
        dma16c4(ws + WS_K, kvo + lane * 16, lb + L_K + kvo);
        dma16c4(ws + WS_K, kvo + 4096 + lane * 16, lb + L_K + kvo + 4096);
        dma16c2(ws + WS_K, kvo + 8192 + lane * 16, lb + L_K + kvo + 8192);
    } else {
        const float qs = 0.25f * 1.4426950408889634f;
        u32x4 qx, qy;
#pragma unroll
        for (int i = 0; i < 4; ++i) {
            u32 a = pk(qacc[2 * i], qacc[2 * i + 1]), c = pk(qacc[8 + 2 * i], qacc[8 + 2 * i + 1]);
            qx[i] = pk(lo16(a) * qs, hi16(a) * qs);
            qy[i] = pk(lo16(c) * qs, hi16(c) * qs);
        }
        ST128(L_P1 + w * 2048 + lane * 16, qx);
        ST128(L_P1 + w * 2048 + 1024 + lane * 16, qy);
    }
    const u32 key = max(max(LD32(L_CT), LD32(L_CT + 4)), max(LD32(L_CT + 8), LD32(L_CT + 12)));
    const int sel = __builtin_amdgcn_readfirstlane(0xffff - (key & 0xffff)) & 2047;
    u32x8 EXA; u32x4 EXB, WOA;
    u32 skv = lb + L_K + kvo + (10 + 4 * (w >> 2)) * 1024, sg = lb + L_W + w * 2048;
    const u32 skv3 = lb + L_K + kvo + (26 + 2 * (w >> 2)) * 1024;
    u32 swo = lb + ((w & 4) ? L_V : L_K) + ((w >> 1) & 1) * 32768 + (w & 1) * 8192;
    {
        EXA[0] = kvo + (10 + 4 * (w >> 2)) * 1024 + lane * 16;
        EXA[1] = kvo + (26 + 2 * (w >> 2)) * 1024 + lane * 16;
        const u32 pos = lane >> 3, B = pos ^ ((pos >> 2) & 1);
        const u32 row = 64 * w + 4 * B + ((lane >> 1) & 3);
        const u64 g0 = (u64)((lane & 1) ? wu : wg) + (u64)(row * 3584 + fj * 16), g1 = g0 + 32 * 3584;
        EXA[2] = (u32)g0; EXA[3] = (u32)(g0 >> 32); EXA[4] = (u32)g1; EXA[5] = (u32)(g1 >> 32);
        EXA[6] = sel * 1024 + (64 * w + 16 * g) * 2;
        EXA[7] = fj * 8192 + (128 * (w & 3) + 2 * lane) * 2;
        const u32 ta = lb + L_W + w * 2048 + l15 * 8, gx = g & 1;
        EXB[0] = ta + (4 * g + gx) * 128;
        EXB[1] = ta + (4 * g + (gx ^ 1)) * 128;
        EXB[2] = lb + (l15 == 0 ? L_FP + w * 64 + g * 16 : L_P1 + w * 1024 + lane * 16);
        EXB[3] = 0;
#pragma unroll
        for (int i = 0; i < 4; ++i) {
            const u32 r = 64 * w + 8 * i + (lane >> 3), slot = lane & 7;
            const u32 f = ((r >> 1) & 1) | ((i & 1) << 1);
            const u32 c = (((slot >> 1) ^ f) << 1) | (slot & 1);
            WOA[i] = r * 1024 + hg * 128 + c * 16 - i * 1024;
        }
    }
    const int hf = w >> 2;
    u32 ka = lb + L_K + hf * 32768 + lane * 16;
    const bool vlane = l31 < 16;
    u32 va = vlane ? (lb + L_V + hf * 32768 + h * 256 + l15 * 16) : (lb + L_ONE - 1024 + h * 256 + l15 * 16);
    u32 vinc = vlane ? 4096u : 0u;
    u32 va0 = vlane ? va : va + 1024;
    u32 qa = lb + L_P1 + (w & 3) * 2048 + lane * 16;
    asm volatile("" : "+{v[196:203]}"(EXA), "+{v[228:231]}"(EXB), "+{v[236:239]}"(WOA), "+{v193}"(ka), "+{v194}"(va), "+{v195}"(vinc), "+v"(va0), "+v"(qa),
                 "+s"(skv), "+s"(sg), "+s"(swo));
    if (w >= 4) VMWAIT(0);
    BAR();
    STAMP(10); STAMPC(19);
    if (b == 0 && tid == 256) { u32 z = 0; GST32C(ws, z, epoch); }

    u32x4 qx, qy;
    f32x16 SX0, SX1, SY0, SY1, OX, OY;
    u32x4 K0, K1, V00, V01, V10, V11;
    u32 idx = 0xDDDDu;
    {
        asm volatile(
            "ds_read_b128 v[160:163], %[qa]\n"
            "ds_read_b128 v[164:167], %[qa] offset:1024\n"
            "ds_read_b128 v[168:171], v193\n"
            "ds_read_b128 v[172:175], v193 offset:1024\n"
            "ds_read_b128 v[176:179], %[va0]\n"
            "ds_read_b128 v[180:183], %[va0] offset:512\n"
            "v_mov_b32 v128, 0\n v_mov_b32 v129, 0\n v_mov_b32 v130, 0\n v_mov_b32 v131, 0\n"
            "v_mov_b32 v132, 0\n v_mov_b32 v133, 0\n v_mov_b32 v134, 0\n v_mov_b32 v135, 0\n"
            "v_mov_b32 v136, 0\n v_mov_b32 v137, 0\n v_mov_b32 v138, 0\n v_mov_b32 v139, 0\n"
            "v_mov_b32 v140, 0\n v_mov_b32 v141, 0\n v_mov_b32 v142, 0\n v_mov_b32 v143, 0\n"
            "v_mov_b32 v144, 0\n v_mov_b32 v145, 0\n v_mov_b32 v146, 0\n v_mov_b32 v147, 0\n"
            "v_mov_b32 v148, 0\n v_mov_b32 v149, 0\n v_mov_b32 v150, 0\n v_mov_b32 v151, 0\n"
            "v_mov_b32 v152, 0\n v_mov_b32 v153, 0\n v_mov_b32 v154, 0\n v_mov_b32 v155, 0\n"
            "v_mov_b32 v156, 0\n v_mov_b32 v157, 0\n v_mov_b32 v158, 0\n v_mov_b32 v159, 0\n"
            "v_mov_b32 v120, 0\n v_mov_b32 v121, 0\n v_mov_b32 v122, 0\n v_mov_b32 v123, 0\n"
            "v_mov_b32 v124, 0\n v_mov_b32 v125, 0\n v_mov_b32 v126, 0\n v_mov_b32 v127, 0\n"
            "v_mov_b32 v188, 0\n v_mov_b32 v189, 0\n v_mov_b32 v190, 0\n v_mov_b32 v191, 0\n"
            "s_waitcnt lgkmcnt(0)\n"
            "v_mfma_f32_32x32x16_bf16 v[64:79], v[168:171], v[160:163], 0\n"
            "v_mfma_f32_32x32x16_bf16 v[96:111], v[168:171], v[164:167], 0\n"
            "s_nop 7\n s_nop 7\n"
            : "={v[64:79]}"(SX0), "={v[80:95]}"(SX1), "={v[96:111]}"(SY0), "={v[112:127]}"(SY1),
              "={v[128:143]}"(OX), "={v[144:159]}"(OY),
              "={v[168:171]}"(K0), "={v[172:175]}"(K1), "={v[176:179]}"(V00), "={v[180:183]}"(V01),
              "={v[184:187]}"(V10), "={v[188:191]}"(V11), "=&{v[160:163]}"(qx), "=&{v[164:167]}"(qy)
            : "{v193}"(ka), [va0] "v"(va0), [qa] "v"(qa)
            : "memory");
    }
#define ATT_OPS \
            : "+{v[64:79]}"(SX0), "+{v[80:95]}"(SX1), "+{v[96:111]}"(SY0), "+{v[112:127]}"(SY1), \
              "+{v[128:143]}"(OX), "+{v[144:159]}"(OY), \
              "+{v[168:171]}"(K0), "+{v[172:175]}"(K1), "+{v[176:179]}"(V00), "+{v[180:183]}"(V01), \
              "+{v[184:187]}"(V10), "+{v[188:191]}"(V11), "+{v193}"(ka), "+{v194}"(va), [cnt] "=&s"(cnt_)
#define ATT_INS : "{v[160:163]}"(qx), "{v[164:167]}"(qy), "{v192}"(idx), "{v195}"(vinc)
    u32 cnt_;
    const char* kvb = ws + WS_K;
    PRIO_START();
    asm volatile(ATT_A ATT_OPS, "+{v[196:203]}"(EXA), "+{v[228:231]}"(EXB), "+{v[236:239]}"(WOA), [skv] "+s"(skv), [sg] "+s"(sg)
                 ATT_INS, [kvb] "s"(kvb) : "memory", "scc");
    LSTAMP(4);
    BAR();
    LSTAMP(8);
    PRIO_START();
    u32x16 EXD;
    asm volatile(ATT_B ATT_OPS, "+{v[196:203]}"(EXA), "=&{v[204:219]}"(EXD), "+{v[236:239]}"(WOA)
                 ATT_INS, "{v[228:231]}"(EXB), [kvb] "s"(kvb), [skv3] "s"(skv3), [xb] "s"(x), [wdb] "s"(wd)
                 : "memory", "scc", "v220", "v221", "v222", "v223", "v224", "v225", "v226", "v227", "v232", "v233", "v234", "v235");
    LSTAMP(9);
    BAR();
    LSTAMP(11);
    if (ffn && w < 4) {
        float sum = LDF(L_FP + l15 * 4);
#pragma unroll
        for (int i = 1; i < 8; ++i) sum += LDF(L_FP + i * 64 + l15 * 4);
        sum = bfr(sum);
        float up = __shfl(sum, (lane & 7) + 8);
        float gt = __shfl(sum, lane & 7);
        float hv = bfr(bfr(gt * sigm(gt)) * up);
        u32 hb = __float_as_uint(hv);
        float y0 = 0.f, y1 = 0.f;
#pragma unroll
        for (int i = 0; i < 8; ++i) {
            const float hs = __uint_as_float((u32)__builtin_amdgcn_readlane(hb, i));
            y0 += hs * lo16(EXD[8 + i]);
            y1 += hs * hi16(EXD[8 + i]);
        }
        u32x2 gr = {__float_as_uint(y0), __float_as_uint(y1)};
        gr[0] = gr[0] == POISON ? 0x7fc00000u : gr[0];
        gr[1] = gr[1] == POISON ? 0x7fc00000u : gr[1];
        u32 yo = WS_Y + fj * 2048 + (128 * w + 2 * lane) * 4;
        GST64C(ws, yo, gr);
    }
    LSTAMP(22);
    PRIO_START();
    asm volatile(ATT_C ATT_OPS, "+{v[236:239]}"(WOA), [swo] "+s"(swo) ATT_INS, [wob] "s"(wo) : "memory", "scc");
    const int tq = w & 3, kh = w >> 2;
    const bool owner = (sel >> 6) == tb;
    int lane_b = lane;
    asm volatile("" : "+v"(lane_b));
    {
    const int lane = lane_b;
    const int g = lane >> 4, l15 = lane & 15, h = lane >> 5, l31 = lane & 31;
    const int tokg = tb * 64 + tq * 16 + l15;
    const int ncol = hg * 64 + kh * 32 + (g & 1) * 16 + (g >> 1) * 8;
    u32 oslot;
    {
        const int head = hg * 4 + hp * 2 + blk, tl = th * 32 + l31;
        oslot = WS_O + ((tb * 4 + (tl >> 4)) * 16 + (head >> 1)) * 1024 + (((head & 1) * 2 + h) * 16 + (tl & 15)) * 16;
    }
    STAMP(12); STAMPC(20);
    u32x4 xe, Y0, Y1, Y2, Y3, Y4, Y5, Y6; u32 gl;
    const u32 yo0 = WS_Y + (28 * w + g) * 2048 + hg * 256 + l15 * 16;
    {
        u32 xo = tokg * 1024 + ncol * 2;
        GLD128(xe, x, xo, 0);
        u32 go = WS_G + tokg * 4;
        GLD32C(gl, ws, go, 0);
        u32 yo = yo0;
        asm volatile("s_cmp_lg_u32 %[stb], %[tb]\n\ts_cbranch_scc1 3f\n"
                     "global_load_dwordx4 %0, %[yo], %[ws] sc1\n v_add_u32 %[yo], 0x2000, %[yo]\n"
                     "global_load_dwordx4 %1, %[yo], %[ws] sc1\n v_add_u32 %[yo], 0x2000, %[yo]\n"
                     "global_load_dwordx4 %2, %[yo], %[ws] sc1\n v_add_u32 %[yo], 0x2000, %[yo]\n"
                     "global_load_dwordx4 %3, %[yo], %[ws] sc1\n v_add_u32 %[yo], 0x2000, %[yo]\n"
                     "global_load_dwordx4 %4, %[yo], %[ws] sc1\n v_add_u32 %[yo], 0x2000, %[yo]\n"
                     "global_load_dwordx4 %5, %[yo], %[ws] sc1\n v_add_u32 %[yo], 0x2000, %[yo]\n"
                     "global_load_dwordx4 %6, %[yo], %[ws] sc1\n3:"
                     : "=&v"(Y0), "=&v"(Y1), "=&v"(Y2), "=&v"(Y3), "=&v"(Y4), "=&v"(Y5), "=&v"(Y6), [yo] "+v"(yo)
                     : [stb] "s"(sel >> 6), [tb] "s"(tb), [ws] "s"(ws) : "memory", "scc");
    }

    {
        f32x16& mine = blk ? OY : OX;
        f32x16& give = blk ? OX : OY;
        const u32 cw = L_W + w * 2304, cr = L_W + (w ^ 4) * 2304;
        ST128(cw + lane * 16, ((u32x4){__float_as_uint(give[0]), __float_as_uint(give[1]), __float_as_uint(give[2]), __float_as_uint(give[3])}));
        ST128(cw + 1024 + lane * 16, ((u32x4){__float_as_uint(give[4]), __float_as_uint(give[5]), __float_as_uint(give[6]), __float_as_uint(give[7])}));
        STF(cw + 2048 + lane * 4, give[8]);
        BAR();
        f32x4 p0 = __builtin_bit_cast(f32x4, LD128(cr + lane * 16));
        f32x4 p1 = __builtin_bit_cast(f32x4, LD128(cr + 1024 + lane * 16));
        float l = LDF(cr + 2048 + lane * 4) + mine[8];
        float o[8];
#pragma unroll
        for (int i = 0; i < 4; ++i) { o[i] = mine[i] + p0[i]; o[4 + i] = mine[4 + i] + p1[i]; }
        {
            auto sw = __builtin_amdgcn_permlane32_swap(__float_as_uint(l), __float_as_uint(l), false, false);
            l = __uint_as_float(sw[0]);
        }
        float amax = fmaxf(fmaxf(fmaxf(fabsf(o[0]), fabsf(o[1])), fmaxf(fabsf(o[2]), fabsf(o[3]))),
                           fmaxf(fmaxf(fabsf(o[4]), fabsf(o[5])), fmaxf(fabsf(o[6]), fabsf(o[7]))));
        bool safe = (l >= 8.6736174e-19f) && (l <= 1.2676506e30f) && (amax <= 3.0e38f);
        if (!__all(safe)) {
            slow_block(ws, blk ? qy : qx, lane, o, l);
            auto sw = __builtin_amdgcn_permlane32_swap(__float_as_uint(l), __float_as_uint(l), false, false);
            l = __uint_as_float(sw[0]);
        }
        const float ri = __builtin_amdgcn_rcpf(l);
        u32 A0 = pk(o[0] * ri, o[1] * ri), A1 = pk(o[2] * ri, o[3] * ri);
        u32 B0 = pk(o[4] * ri, o[5] * ri), B1 = pk(o[6] * ri, o[7] * ri);
        auto s0 = __builtin_amdgcn_permlane32_swap(A0, B0, false, false);
        auto s1 = __builtin_amdgcn_permlane32_swap(A1, B1, false, false);
        u32x4 ov = {s0[0], s1[0], s0[1], s1[1]};
#pragma unroll
        for (int i = 0; i < 4; ++i) ov[i] = ov[i] == POISON ? 0x7fc07fc0u : ov[i];
        GST128C(ws, oslot, ov);
    }
    STAMP(13);

    u32x4 af[4][8];
    {
        const u32 f = ((l15 >> 3) & 1) | ((g & 1) << 1);
        const u32 rb = (kh ? L_V : L_K) + (8 * g + (l15 >> 2)) * 128 + (l15 & 3) * 8;
#pragma unroll
        for (int nb = 0; nb < 4; ++nb) {
            u32 a = rb + ((nb ^ f) * 32);
            asm volatile("" : "+v"(a));
#pragma unroll
            for (int st = 0; st < 8; ++st) {
                const u32 o_ = (st >> 2) * 32768 + (st & 3) * 4096;
                af[nb][st] = cat(trread(lds, a + o_), trread(lds, a + o_ + 512));
            }
        }
    }
    STAMP(14);
    asm volatile("s_waitcnt vmcnt(1)" : "+v"(xe), "+v"(gl), "+v"(Y0), "+v"(Y1), "+v"(Y2), "+v"(Y3), "+v"(Y4), "+v"(Y5), "+v"(Y6) :: "memory");
    if (owner) {
        u32 spins = 0;
#define YMX(v) max(max(v[0], v[1]), max(v[2], v[3]))
        while (!__all(max(max(max(YMX(Y0), YMX(Y1)), max(YMX(Y2), YMX(Y3))), max(max(YMX(Y4), YMX(Y5)), YMX(Y6))) != POISON) && ++spins <= SPIN_MAX) {
            u32 yo = yo0;
            asm volatile("global_load_dwordx4 %0, %[yo], %[ws] sc1\n v_add_u32 %[yo], 0x2000, %[yo]\n"
                         "global_load_dwordx4 %1, %[yo], %[ws] sc1\n v_add_u32 %[yo], 0x2000, %[yo]\n"
                         "global_load_dwordx4 %2, %[yo], %[ws] sc1\n v_add_u32 %[yo], 0x2000, %[yo]\n"
                         "global_load_dwordx4 %3, %[yo], %[ws] sc1\n v_add_u32 %[yo], 0x2000, %[yo]\n"
                         "global_load_dwordx4 %4, %[yo], %[ws] sc1\n v_add_u32 %[yo], 0x2000, %[yo]\n"
                         "global_load_dwordx4 %5, %[yo], %[ws] sc1\n v_add_u32 %[yo], 0x2000, %[yo]\n"
                         "global_load_dwordx4 %6, %[yo], %[ws] sc1\n s_waitcnt vmcnt(0)"
                         : "=&v"(Y0), "=&v"(Y1), "=&v"(Y2), "=&v"(Y3), "=&v"(Y4), "=&v"(Y5), "=&v"(Y6), [yo] "+v"(yo)
                         : [ws] "s"(ws) : "memory");
        }
        f32x4 ys;
#pragma unroll
        for (int i = 0; i < 4; ++i) {
            float v = ((__uint_as_float(Y0[i]) + __uint_as_float(Y1[i])) + (__uint_as_float(Y2[i]) + __uint_as_float(Y3[i])))
                    + ((__uint_as_float(Y4[i]) + __uint_as_float(Y5[i])) + __uint_as_float(Y6[i]));
            auto a = __builtin_amdgcn_permlane32_swap(__float_as_uint(v), __float_as_uint(v), false, false);
            v = __uint_as_float(a[0]) + __uint_as_float(a[1]);
            auto c = __builtin_amdgcn_permlane16_swap(__float_as_uint(v), __float_as_uint(v), false, false);
            ys[i] = __uint_as_float(c[0]) + __uint_as_float(c[1]);
        }
        if (g == 0) ST128(L_YP + w * 256 + l15 * 16, __builtin_bit_cast(u32x4, ys));
    }
    u32x4 of0, of1, of2, of3, of4, of5, of6, of7;
    {
        const u32 oo = WS_O + ((tb * 4 + tq) * 16 + 8 * kh) * 1024 + lane * 16, o2 = oo + 4096;
        u32 spins = 0;
        while (true) {
            asm volatile("global_load_dwordx4 %0, %8, %10 sc1\n\tglobal_load_dwordx4 %1, %8, %10 offset:1024 sc1\n\t"
                         "global_load_dwordx4 %2, %8, %10 offset:2048 sc1\n\tglobal_load_dwordx4 %3, %8, %10 offset:3072 sc1\n\t"
                         "global_load_dwordx4 %4, %9, %10 sc1\n\tglobal_load_dwordx4 %5, %9, %10 offset:1024 sc1\n\t"
                         "global_load_dwordx4 %6, %9, %10 offset:2048 sc1\n\tglobal_load_dwordx4 %7, %9, %10 offset:3072 sc1\n\t"
                         "s_waitcnt vmcnt(0)"
                         : "=&v"(of0), "=&v"(of1), "=&v"(of2), "=&v"(of3), "=&v"(of4), "=&v"(of5), "=&v"(of6), "=&v"(of7)
                         : "v"(oo), "v"(o2), "s"(ws) : "memory");
            u32 mx = 0;
#define OMX(v) mx = max(max(mx, max(v[0], v[1])), max(v[2], v[3]));
            OMX(of0) OMX(of1) OMX(of2) OMX(of3) OMX(of4) OMX(of5) OMX(of6) OMX(of7)
            if (__all(mx != POISON) || ++spins > SPIN_MAX) break;
        }
    }
    STAMP(15);
    f32x4 acc[4] = {{0, 0, 0, 0}, {0, 0, 0, 0}, {0, 0, 0, 0}, {0, 0, 0, 0}};
#define OSTEP(OF, ST) \
    _Pragma("unroll") for (int nb = 0; nb < 4; ++nb) acc[nb] = __builtin_amdgcn_mfma_f32_16x16x32_bf16(asbf(af[nb][ST]), asbf(OF), acc[nb], 0, 0, 0);
    OSTEP(of0, 0) OSTEP(of1, 1) OSTEP(of2, 2) OSTEP(of3, 3) OSTEP(of4, 4) OSTEP(of5, 5) OSTEP(of6, 6) OSTEP(of7, 7)
    STAMP(16);
    {
        f32x4 k0 = kh ? acc[2] : acc[0], k1 = kh ? acc[3] : acc[1];
        f32x4 g0 = kh ? acc[0] : acc[2], g1 = kh ? acc[1] : acc[3];
        ST128(L_K + 16384 + w * 2048 + lane * 16, __builtin_bit_cast(u32x4, g0));
        ST128(L_K + 16384 + w * 2048 + 1024 + lane * 16, __builtin_bit_cast(u32x4, g1));
        BAR();
        k0 += __builtin_bit_cast(f32x4, LD128(L_K + 16384 + (w ^ 4) * 2048 + lane * 16));
        k1 += __builtin_bit_cast(f32x4, LD128(L_K + 16384 + (w ^ 4) * 2048 + 1024 + lane * 16));
        float a[8];
#pragma unroll
        for (int i = 0; i < 4; ++i) {
            auto sw = __builtin_amdgcn_permlane16_swap(__float_as_uint(k0[i]), __float_as_uint(k1[i]), false, false);
            a[i] = __uint_as_float(sw[0]); a[4 + i] = __uint_as_float(sw[1]);
        }
        const float gt = bfr(sigm(bfr(__uint_as_float(gl))));
        const float og = bfr(1.0f - gt);
        float bb[8];
#pragma unroll
        for (int i = 0; i < 4; ++i) { bb[2 * i] = lo16(xe[i]); bb[2 * i + 1] = hi16(xe[i]); }
        if (owner && tokg == sel) {
            const u32 rord = key >> 16;
            const float rsel = __uint_as_float(((rord & 0x8000u) ? (rord & 0x7fffu) : (~rord & 0xffffu)) << 16);
            const float sg = bfr(sigm(rsel));
            const int c0 = kh * 32 + (g & 1) * 16 + (g >> 1) * 8;
#pragma unroll
            for (int i = 0; i < 8; ++i) {
                float y = 0.f;
#pragma unroll
                for (int p = 0; p < 8; ++p) y += LDF(L_YP + p * 256 + (c0 + i) * 4);
                bb[i] = bfr(bb[i] + bfr(bfr(y) * sg));
            }
        }
        u32x4 ov;
#pragma unroll
        for (int i = 0; i < 4; ++i)
            ov[i] = pk(bfr(gt * bfr(a[2 * i])) + bfr(og * bb[2 * i]), bfr(gt * bfr(a[2 * i + 1])) + bfr(og * bb[2 * i + 1]));
        u32 oo_ = tokg * 1024 + ncol * 2;
        GST128(out, oo_, ov);
    }
    STAMP(17); STAMPC(21); STAMPDUMP();
    }
}

extern "C" void kernel_launch(void* const* d_in, const int* in_sizes, int n_in, void* d_out, int out_size,
                              void* d_ws, size_t ws_size, hipStream_t stream) {
    (void)in_sizes; (void)n_in; (void)out_size; (void)ws_size;
    fused_kernel<<<256, 512, 0, stream>>>((const u16*)d_in[0], (const u16*)d_in[1], (const u16*)d_in[2], (const u16*)d_in[3],
                                          (const u16*)d_in[4], (const u16*)d_in[6], (const u16*)d_in[7], (const u16*)d_in[8],
                                          (const u16*)d_in[9], (const u16*)d_in[10], (u16*)d_out, (char*)d_ws);
}
```

```cpp
#include <hip/hip_runtime.h>
#include <stdint.h>
#include <stdio.h>
#define DBG_STAMP
#define V2N

typedef unsigned int u32;
typedef unsigned long long u64;
typedef unsigned short u16;
typedef u32 u32x2 __attribute__((ext_vector_type(2)));
typedef u32 u32x4 __attribute__((ext_vector_type(4)));
typedef u32 u32x8 __attribute__((ext_vector_type(8)));
typedef u32 u32x16 __attribute__((ext_vector_type(16)));
typedef float f32x2 __attribute__((ext_vector_type(2)));
typedef float f32x4 __attribute__((ext_vector_type(4)));
typedef float f32x16 __attribute__((ext_vector_type(16)));
typedef __bf16 bf16x2 __attribute__((ext_vector_type(2)));
typedef __bf16 bf16x8 __attribute__((ext_vector_type(8)));
#define LDSP __attribute__((address_space(3)))

constexpr u32 WS_E   = 0;
constexpr u32 WS_F1  = 4096;
constexpr u32 WS_G   = 24576;
constexpr u32 WS_K   = 65536;
constexpr u32 WS_V   = 131072;
constexpr u32 WS_O   = 262144;
constexpr u32 WS_Y   = 2621440;
constexpr u32 WS_X   = 3145728;
constexpr u32 WS_DBG = 4194304;

constexpr u32 L_K   = 0;
constexpr u32 L_V   = 65536;
constexpr u32 L_ONE = 131072;
constexpr u32 L_W   = 135168;
constexpr u32 L_M   = 151552;
constexpr u32 L_P1  = L_M;
constexpr u32 L_PD  = L_M + 8192;
constexpr u32 L_FP  = L_M + 8704;
constexpr u32 L_YP  = L_M + 9216;
constexpr u32 L_CT  = L_M + 11264;
constexpr u32 L_END = 163840;

constexpr u32 SPIN_MAX = 1u << 22;
constexpr u32 POISON = 0xffffffffu;

#define LD128(a) (*(const LDSP u32x4*)(lds + (a)))
#define ST128(a, v) (*(LDSP u32x4*)(lds + (a)) = (v))
#define LD32(a) (*(const LDSP u32*)(lds + (a)))
#define ST32(a, v) (*(LDSP u32*)(lds + (a)) = (v))
#define LDF(a) (*(const LDSP float*)(lds + (a)))
#define STF(a, v) (*(LDSP float*)(lds + (a)) = (v))

__device__ __forceinline__ u32x2 trread(LDSP char* lds, u32 a) {
    typedef short v4s __attribute__((ext_vector_type(4)));
    v4s r = __builtin_amdgcn_ds_read_tr16_b64_v4i16((LDSP v4s*)(lds + a));
    return __builtin_bit_cast(u32x2, r);
}
__device__ __forceinline__ bf16x8 asbf(u32x4 v) { return __builtin_bit_cast(bf16x8, v); }
__device__ __forceinline__ u32x4 cat(u32x2 a, u32x2 b) { u32x4 r = {a[0], a[1], b[0], b[1]}; return r; }
__device__ __forceinline__ u32 pk(float a, float b) {
    f32x2 f = {a, b};
    return __builtin_bit_cast(u32, __builtin_convertvector(f, bf16x2));
}
__device__ __forceinline__ float bfr(float a) {
    return __uint_as_float(pk(a, a) & 0xffff0000u);
}
__device__ __forceinline__ float lo16(u32 v) { return __uint_as_float(v << 16); }
__device__ __forceinline__ float hi16(u32 v) { return __uint_as_float(v & 0xffff0000u); }
__device__ __forceinline__ float sigm(float z) { return __builtin_amdgcn_rcpf(1.0f + __builtin_amdgcn_exp2f(-1.4426950408889634f * z)); }

__device__ __forceinline__ void dma16(const void* base, u32 voff, u32 ldsaddr) {
    asm volatile("s_mov_b32 m0, %0\n\ts_nop 0\n\tglobal_load_lds_dwordx4 %1, %2" :: "s"(ldsaddr), "v"(voff), "s"(base) : "memory");
}
__device__ __forceinline__ void dma16c4(const void* base, u32 voff, u32 ldsaddr) {
    asm volatile("s_mov_b32 m0, %0\n\ts_nop 0\n\tglobal_load_lds_dwordx4 %1, %2 sc1\n\tglobal_load_lds_dwordx4 %1, %2 offset:1024 sc1\n\t"
                 "global_load_lds_dwordx4 %1, %2 offset:2048 sc1\n\tglobal_load_lds_dwordx4 %1, %2 offset:3072 sc1"
                 :: "s"(ldsaddr), "v"(voff), "s"(base) : "memory");
}
__device__ __forceinline__ void dma16c3(const void* base, u32 voff, u32 ldsaddr) {
    asm volatile("s_mov_b32 m0, %0\n\ts_nop 0\n\tglobal_load_lds_dwordx4 %1, %2 sc1\n\tglobal_load_lds_dwordx4 %1, %2 offset:1024 sc1\n\t"
                 "global_load_lds_dwordx4 %1, %2 offset:2048 sc1"
                 :: "s"(ldsaddr), "v"(voff), "s"(base) : "memory");
}
__device__ __forceinline__ void dma16c2(const void* base, u32 voff, u32 ldsaddr) {
    asm volatile("s_mov_b32 m0, %0\n\ts_nop 0\n\tglobal_load_lds_dwordx4 %1, %2 sc1\n\tglobal_load_lds_dwordx4 %1, %2 offset:1024 sc1"
                 :: "s"(ldsaddr), "v"(voff), "s"(base) : "memory");
}
__device__ __forceinline__ void dma16c(const void* base, u32 voff, u32 ldsaddr) {
    asm volatile("s_mov_b32 m0, %0\n\ts_nop 0\n\tglobal_load_lds_dwordx4 %1, %2 sc1" :: "s"(ldsaddr), "v"(voff), "s"(base) : "memory");
}
#define GLD128(dst, base, voff, OFF)  asm volatile("global_load_dwordx4 %0, %1, %2 offset:" #OFF : "=v"(dst) : "v"(voff), "s"(base) : "memory")
#define GLD128C(dst, base, voff, OFF) asm volatile("global_load_dwordx4 %0, %1, %2 offset:" #OFF " sc1" : "=v"(dst) : "v"(voff), "s"(base) : "memory")
#define GLD64C(dst, base, voff, OFF)  asm volatile("global_load_dwordx2 %0, %1, %2 offset:" #OFF " sc1" : "=v"(dst) : "v"(voff), "s"(base) : "memory")
#define GLD32C(dst, base, voff, OFF)  asm volatile("global_load_dword %0, %1, %2 offset:" #OFF " sc1" : "=v"(dst) : "v"(voff), "s"(base) : "memory")
#define GLD32(dst, base, voff, OFF)   asm volatile("global_load_dword %0, %1, %2 offset:" #OFF : "=v"(dst) : "v"(voff), "s"(base) : "memory")
#define GLD16(dst, base, voff, OFF)   asm volatile("global_load_ushort %0, %1, %2 offset:" #OFF : "=v"(dst) : "v"(voff), "s"(base) : "memory")
#define GST128(base, voff, val)  asm volatile("global_store_dwordx4 %0, %1, %2\n\ts_nop 1" :: "v"(voff), "v"(val), "s"(base) : "memory")
#define GST128C(base, voff, val) asm volatile("global_store_dwordx4 %0, %1, %2 sc1\n\ts_nop 1" :: "v"(voff), "v"(val), "s"(base) : "memory")
#define GST64C(base, voff, val)  asm volatile("global_store_dwordx2 %0, %1, %2 sc1\n\ts_nop 0" :: "v"(voff), "v"(val), "s"(base) : "memory")
#define GST32C(base, voff, val)  asm volatile("global_store_dword %0, %1, %2 sc1" :: "v"(voff), "v"(val), "s"(base) : "memory")
#define VMWAIT(N) asm volatile("s_waitcnt vmcnt(" #N ")" ::: "memory")
#define BAR() asm volatile("s_waitcnt lgkmcnt(0)\n\ts_barrier" ::: "memory")

#define STAMP(i) do { u32 t_ = (u32)__builtin_amdgcn_s_memrealtime(); if (lane == 0) ST32(L_CT + 64 + (w * 24 + (i)) * 4, t_); } while (0)
#define STAMPC(i) do { u32 t_ = (u32)__builtin_amdgcn_s_memtime(); if (lane == 0) ST32(L_CT + 64 + (w * 24 + (i)) * 4, t_); } while (0)
#define STAMPDUMP() do { if (lane < 24) ((u32*)(ws + WS_DBG))[(blockIdx.x * 8 + w) * 32 + lane] = LD32(L_CT + 64 + (w * 24 + lane) * 4); } while (0)
#define FSTAMP(i) STAMP(i)
#define LSTAMP(i)

__device__ __forceinline__ u32 wave_max_u32(u32 v) {
    v = max(v, (u32)__builtin_amdgcn_update_dpp(0, (int)v, 0x111, 0xf, 0xf, false));
    v = max(v, (u32)__builtin_amdgcn_update_dpp(0, (int)v, 0x112, 0xf, 0xf, false));
    v = max(v, (u32)__builtin_amdgcn_update_dpp(0, (int)v, 0x114, 0xf, 0xf, false));
    v = max(v, (u32)__builtin_amdgcn_update_dpp(0, (int)v, 0x118, 0xf, 0xf, false));
    v = max(v, (u32)__builtin_amdgcn_update_dpp(0, (int)v, 0x142, 0xa, 0xf, false));
    v = max(v, (u32)__builtin_amdgcn_update_dpp(0, (int)v, 0x143, 0xc, 0xf, false));
    return (u32)__builtin_amdgcn_readlane((int)v, 63);
}
#define ATT_A \
    "s_mov_b32 %[cnt], 2\n" \
    "1:\n" \
    "v_exp_f32 v64, v64\n" \
    "v_exp_f32 v65, v65\n" \
    "v_smfmac_f32_32x32x32_bf16 v[144:159], v[188:191], v[120:127], v192\n" \
    "ds_read_b128 v[184:187], v194 offset:1024\n" \
    "v_exp_f32 v66, v66\n" \
    "v_exp_f32 v67, v67\n" \
    "v_exp_f32 v68, v68\n" \
    "v_exp_f32 v69, v69\n" \
    "s_waitcnt lgkmcnt(1)\n" \
    "v_mfma_f32_32x32x16_bf16 v[80:95], v[172:175], v[160:163], 0\n" \
    "ds_read_b128 v[188:191], v194 offset:1536\n" \
    "v_exp_f32 v70, v70\n" \
    "v_exp_f32 v71, v71\n" \
    "v_exp_f32 v72, v72\n" \
    "v_exp_f32 v73, v73\n" \
    "v_smfmac_f32_32x32x32_bf16 v[128:143], v[176:179], v[64:71], v192\n" \
    "v_exp_f32 v74, v74\n" \
    "s_mov_b32 m0, %[skv]\n" \
    "v_exp_f32 v75, v75\n" \
    "v_exp_f32 v76, v76\n" \
    "global_load_lds_dwordx4 v196, %[kvb] offset:0 sc1\n" \
    "v_exp_f32 v77, v77\n" \
    "v_exp_f32 v78, v78\n" \
    "v_exp_f32 v79, v79\n" \
    "v_exp_f32 v96, v96\n" \
    "v_exp_f32 v97, v97\n" \
    "v_smfmac_f32_32x32x32_bf16 v[128:143], v[180:183], v[72:79], v192\n" \
    "v_exp_f32 v98, v98\n" \
    "v_exp_f32 v99, v99\n" \
    "v_exp_f32 v100, v100\n" \
    "v_exp_f32 v101, v101\n" \
    "v_mfma_f32_32x32x16_bf16 v[112:127], v[172:175], v[164:167], 0\n" \
    "ds_read_b128 v[168:171], v193 offset:2048\n" \
    "v_exp_f32 v102, v102\n" \
    "v_exp_f32 v103, v103\n" \
    "v_exp_f32 v104, v104\n" \
    "v_exp_f32 v105, v105\n" \
    "v_smfmac_f32_32x32x32_bf16 v[144:159], v[176:179], v[96:103], v192\n" \
    "v_exp_f32 v106, v106\n" \
    "v_exp_f32 v107, v107\n" \
    "v_exp_f32 v108, v108\n" \
    "v_exp_f32 v109, v109\n" \
    "v_exp_f32 v110, v110\n" \
    "v_exp_f32 v111, v111\n" \
    "v_exp_f32 v80, v80\n" \
    "v_exp_f32 v81, v81\n" \
    "v_smfmac_f32_32x32x32_bf16 v[144:159], v[180:183], v[104:111], v192\n" \
    "ds_read_b128 v[176:179], v194 offset:2048\n" \
    "v_exp_f32 v82, v82\n" \
    "v_exp_f32 v83, v83\n" \
    "v_exp_f32 v84, v84\n" \
    "v_exp_f32 v85, v85\n" \
    "s_waitcnt lgkmcnt(1)\n" \
    "v_mfma_f32_32x32x16_bf16 v[64:79], v[168:171], v[160:163], 0\n" \
    "ds_read_b128 v[180:183], v194 offset:2560\n" \
    "v_exp_f32 v86, v86\n" \
    "v_exp_f32 v87, v87\n" \
    "v_exp_f32 v88, v88\n" \
    "v_exp_f32 v89, v89\n" \
    "v_smfmac_f32_32x32x32_bf16 v[128:143], v[184:187], v[80:87], v192\n" \
    "v_exp_f32 v90, v90\n" \
    "s_mov_b32 m0, %[skv]\n" \
    "v_exp_f32 v91, v91\n" \
    "v_exp_f32 v92, v92\n" \
    "global_load_lds_dwordx4 v196, %[kvb] offset:1024 sc1\n" \
    "v_exp_f32 v93, v93\n" \
    "v_exp_f32 v94, v94\n" \
    "v_exp_f32 v95, v95\n" \
    "v_exp_f32 v112, v112\n" \
    "v_exp_f32 v113, v113\n" \
    "v_smfmac_f32_32x32x32_bf16 v[128:143], v[188:191], v[88:95], v192\n" \
    "v_exp_f32 v114, v114\n" \
    "v_exp_f32 v115, v115\n" \
    "v_exp_f32 v116, v116\n" \
    "v_exp_f32 v117, v117\n" \
    "v_mfma_f32_32x32x16_bf16 v[96:111], v[168:171], v[164:167], 0\n" \
    "ds_read_b128 v[172:175], v193 offset:3072\n" \
    "v_exp_f32 v118, v118\n" \
    "v_exp_f32 v119, v119\n" \
    "v_exp_f32 v120, v120\n" \
    "v_exp_f32 v121, v121\n" \
    "v_smfmac_f32_32x32x32_bf16 v[144:159], v[184:187], v[112:119], v192\n" \
    "v_exp_f32 v122, v122\n" \
    "s_mov_b32 m0, %[sg]\n" \
    "v_exp_f32 v123, v123\n" \
    "v_exp_f32 v124, v124\n" \
    "global_load_lds_dwordx4 v[198:199], off\n" \
    "v_exp_f32 v125, v125\n" \
    "v_exp_f32 v126, v126\n" \
    "v_exp_f32 v127, v127\n" \
    "s_cmp_eq_u32 %[cnt], 1\n" \
    "s_cbranch_scc0 8f\n" \
    "s_setprio 0\n" \
    "8:\n" \
    "v_exp_f32 v64, v64\n" \
    "v_exp_f32 v65, v65\n" \
    "v_smfmac_f32_32x32x32_bf16 v[144:159], v[188:191], v[120:127], v192\n" \
    "ds_read_b128 v[184:187], v194 offset:3072\n" \
    "v_exp_f32 v66, v66\n" \
    "v_exp_f32 v67, v67\n" \
    "v_exp_f32 v68, v68\n" \
    "v_exp_f32 v69, v69\n" \
    "s_waitcnt lgkmcnt(1)\n" \
    "v_mfma_f32_32x32x16_bf16 v[80:95], v[172:175], v[160:163], 0\n" \
    "ds_read_b128 v[188:191], v194 offset:3584\n" \
    "v_exp_f32 v70, v70\n" \
    "v_exp_f32 v71, v71\n" \
    "v_exp_f32 v72, v72\n" \
    "v_exp_f32 v73, v73\n" \
    "v_smfmac_f32_32x32x32_bf16 v[128:143], v[176:179], v[64:71], v192\n" \
    "v_exp_f32 v74, v74\n" \
    "s_mov_b32 m0, %[skv]\n" \
    "v_exp_f32 v75, v75\n" \
    "v_exp_f32 v76, v76\n" \
    "global_load_lds_dwordx4 v196, %[kvb] offset:2048 sc1\n" \
    "v_exp_f32 v77, v77\n" \
    "v_exp_f32 v78, v78\n" \
    "v_exp_f32 v79, v79\n" \
    "v_exp_f32 v96, v96\n" \
    "v_exp_f32 v97, v97\n" \
    "v_smfmac_f32_32x32x32_bf16 v[128:143], v[180:183], v[72:79], v192\n" \
    "v_exp_f32 v98, v98\n" \
    "v_exp_f32 v99, v99\n" \
    "v_exp_f32 v100, v100\n" \
    "v_exp_f32 v101, v101\n" \
    "v_mfma_f32_32x32x16_bf16 v[112:127], v[172:175], v[164:167], 0\n" \
    "ds_read_b128 v[168:171], v193 offset:4096\n" \
    "v_exp_f32 v102, v102\n" \
    "v_exp_f32 v103, v103\n" \
    "v_exp_f32 v104, v104\n" \
    "v_exp_f32 v105, v105\n" \
    "v_smfmac_f32_32x32x32_bf16 v[144:159], v[176:179], v[96:103], v192\n" \
    "v_exp_f32 v106, v106\n" \
    "v_exp_f32 v107, v107\n" \
    "v_exp_f32 v108, v108\n" \
    "v_exp_f32 v109, v109\n" \
    "v_exp_f32 v110, v110\n" \
    "v_exp_f32 v111, v111\n" \
    "v_exp_f32 v80, v80\n" \
    "v_exp_f32 v81, v81\n" \
    "v_smfmac_f32_32x32x32_bf16 v[144:159], v[180:183], v[104:111], v192\n" \
    "ds_read_b128 v[176:179], v194 offset:4096\n" \
    "v_exp_f32 v82, v82\n" \
    "v_exp_f32 v83, v83\n" \
    "v_exp_f32 v84, v84\n" \
    "v_exp_f32 v85, v85\n" \
    "s_waitcnt lgkmcnt(1)\n" \
    "v_mfma_f32_32x32x16_bf16 v[64:79], v[168:171], v[160:163], 0\n" \
    "ds_read_b128 v[180:183], v194 offset:4608\n" \
    "v_exp_f32 v86, v86\n" \
    "v_exp_f32 v87, v87\n" \
    "v_exp_f32 v88, v88\n" \
    "v_exp_f32 v89, v89\n" \
    "v_smfmac_f32_32x32x32_bf16 v[128:143], v[184:187], v[80:87], v192\n" \
    "v_exp_f32 v90, v90\n" \
    "s_mov_b32 m0, %[skv]\n" \
    "v_exp_f32 v91, v91\n" \
    "v_exp_f32 v92, v92\n" \
    "global_load_lds_dwordx4 v196, %[kvb] offset:3072 sc1\n" \
    "v_exp_f32 v93, v93\n" \
    "v_exp_f32 v94, v94\n" \
    "v_exp_f32 v95, v95\n" \
    "v_exp_f32 v112, v112\n" \
    "v_exp_f32 v113, v113\n" \
    "v_smfmac_f32_32x32x32_bf16 v[128:143], v[188:191], v[88:95], v192\n" \
    "v_exp_f32 v114, v114\n" \
    "v_exp_f32 v115, v115\n" \
    "v_exp_f32 v116, v116\n" \
    "v_exp_f32 v117, v117\n" \
    "v_mfma_f32_32x32x16_bf16 v[96:111], v[168:171], v[164:167], 0\n" \
    "ds_read_b128 v[172:175], v193 offset:5120\n" \
    "v_exp_f32 v118, v118\n" \
    "v_exp_f32 v119, v119\n" \
    "v_exp_f32 v120, v120\n" \
    "v_exp_f32 v121, v121\n" \
    "v_smfmac_f32_32x32x32_bf16 v[144:159], v[184:187], v[112:119], v192\n" \
    "v_exp_f32 v122, v122\n" \
    "v_exp_f32 v123, v123\n" \
    "v_exp_f32 v124, v124\n" \
    "v_exp_f32 v125, v125\n" \
    "v_exp_f32 v126, v126\n" \
    "v_exp_f32 v127, v127\n" \
    "v_add_u32 v196, 0x2000, v196\n" \
    "v_mov_b32 v198, v200\n" \
    "v_mov_b32 v199, v201\n" \
    "s_add_u32 %[skv], %[skv], 0x2000\n" \
    "s_add_u32 %[sg], %[sg], 0x400\n" \
    "v_add_u32 v193, 4096, v193\n" \
    "v_add_u32 v194, v195, v194\n" \
    "s_sub_u32 %[cnt], %[cnt], 1\n" \
    "s_cmp_lg_u32 %[cnt], 0\n" \
    "s_cbranch_scc1 1b\n" \
    "s_waitcnt vmcnt(5)\n"

#define ATT_B \
    "v_exp_f32 v64, v64\n" \
    "v_exp_f32 v65, v65\n" \
    "v_smfmac_f32_32x32x32_bf16 v[144:159], v[188:191], v[120:127], v192\n" \
    "ds_read_b128 v[184:187], v194 offset:1024\n" \
    "v_exp_f32 v66, v66\n" \
    "v_exp_f32 v67, v67\n" \
    "v_exp_f32 v68, v68\n" \
    "v_exp_f32 v69, v69\n" \
    "s_waitcnt lgkmcnt(1)\n" \
    "v_mfma_f32_32x32x16_bf16 v[80:95], v[172:175], v[160:163], 0\n" \
    "ds_read_b128 v[188:191], v194 offset:1536\n" \
    "v_exp_f32 v70, v70\n" \
    "v_exp_f32 v71, v71\n" \
    "v_exp_f32 v72, v72\n" \
    "v_exp_f32 v73, v73\n" \
    "v_smfmac_f32_32x32x32_bf16 v[128:143], v[176:179], v[64:71], v192\n" \
    "v_exp_f32 v74, v74\n" \
    "s_mov_b32 m0, %[skv3]\n" \
    "v_exp_f32 v75, v75\n" \
    "v_exp_f32 v76, v76\n" \
    "global_load_dwordx4 v[204:207], v202, %[xb]\n" \
    "v_exp_f32 v77, v77\n" \
    "v_exp_f32 v78, v78\n" \
    "v_exp_f32 v79, v79\n" \
    "v_exp_f32 v96, v96\n" \
    "v_exp_f32 v97, v97\n" \
    "v_smfmac_f32_32x32x32_bf16 v[128:143], v[180:183], v[72:79], v192\n" \
    "v_exp_f32 v98, v98\n" \
    "v_exp_f32 v99, v99\n" \
    "v_exp_f32 v100, v100\n" \
    "v_exp_f32 v101, v101\n" \
    "v_mfma_f32_32x32x16_bf16 v[112:127], v[172:175], v[164:167], 0\n" \
    "ds_read_b128 v[168:171], v193 offset:2048\n" \
    "v_exp_f32 v102, v102\n" \
    "v_exp_f32 v103, v103\n" \
    "v_exp_f32 v104, v104\n" \
    "v_exp_f32 v105, v105\n" \
    "v_smfmac_f32_32x32x32_bf16 v[144:159], v[176:179], v[96:103], v192\n" \
    "v_exp_f32 v106, v106\n" \
    "v_exp_f32 v107, v107\n" \
    "v_exp_f32 v108, v108\n" \
    "global_load_dwordx4 v[208:211], v202, %[xb] offset:16\n" \
    "v_exp_f32 v109, v109\n" \
    "v_exp_f32 v110, v110\n" \
    "v_exp_f32 v111, v111\n" \
    "v_exp_f32 v80, v80\n" \
    "v_exp_f32 v81, v81\n" \
    "v_smfmac_f32_32x32x32_bf16 v[144:159], v[180:183], v[104:111], v192\n" \
    "ds_read_b128 v[176:179], v194 offset:2048\n" \
    "v_exp_f32 v82, v82\n" \
    "v_exp_f32 v83, v83\n" \
    "v_exp_f32 v84, v84\n" \
    "v_exp_f32 v85, v85\n" \
    "s_waitcnt lgkmcnt(1)\n" \
    "v_mfma_f32_32x32x16_bf16 v[64:79], v[168:171], v[160:163], 0\n" \
    "ds_read_b128 v[180:183], v194 offset:2560\n" \
    "v_exp_f32 v86, v86\n" \
    "v_exp_f32 v87, v87\n" \
    "v_exp_f32 v88, v88\n" \
    "v_exp_f32 v89, v89\n" \
    "v_smfmac_f32_32x32x32_bf16 v[128:143], v[184:187], v[80:87], v192\n" \
    "v_exp_f32 v90, v90\n" \
    "v_exp_f32 v91, v91\n" \
    "v_exp_f32 v92, v92\n" \
    "global_load_lds_dwordx4 v197, %[kvb] offset:0 sc1\n" \
    "v_exp_f32 v93, v93\n" \
    "v_exp_f32 v94, v94\n" \
    "v_exp_f32 v95, v95\n" \
    "v_exp_f32 v112, v112\n" \
    "v_exp_f32 v113, v113\n" \
    "v_smfmac_f32_32x32x32_bf16 v[128:143], v[188:191], v[88:95], v192\n" \
    "v_exp_f32 v114, v114\n" \
    "v_exp_f32 v115, v115\n" \
    "v_exp_f32 v116, v116\n" \
    "v_exp_f32 v117, v117\n" \
    "v_mfma_f32_32x32x16_bf16 v[96:111], v[168:171], v[164:167], 0\n" \
    "ds_read_b128 v[172:175], v193 offset:3072\n" \
    "v_exp_f32 v118, v118\n" \
    "v_exp_f32 v119, v119\n" \
    "v_exp_f32 v120, v120\n" \
    "v_exp_f32 v121, v121\n" \
    "v_smfmac_f32_32x32x32_bf16 v[144:159], v[184:187], v[112:119], v192\n" \
    "v_exp_f32 v122, v122\n" \
    "v_exp_f32 v123, v123\n" \
    "v_exp_f32 v124, v124\n" \
    "global_load_lds_dwordx4 v197, %[kvb] offset:1024 sc1\n" \
    "v_exp_f32 v125, v125\n" \
    "v_exp_f32 v126, v126\n" \
    "v_exp_f32 v127, v127\n" \
    "v_exp_f32 v64, v64\n" \
    "v_exp_f32 v65, v65\n" \
    "v_smfmac_f32_32x32x32_bf16 v[144:159], v[188:191], v[120:127], v192\n" \
    "ds_read_b128 v[184:187], v194 offset:3072\n" \
    "v_exp_f32 v66, v66\n" \
    "v_exp_f32 v67, v67\n" \
    "v_exp_f32 v68, v68\n" \
    "v_exp_f32 v69, v69\n" \
    "s_waitcnt lgkmcnt(1)\n" \
    "v_mfma_f32_32x32x16_bf16 v[80:95], v[172:175], v[160:163], 0\n" \
    "ds_read_b128 v[188:191], v194 offset:3584\n" \
    "v_exp_f32 v70, v70\n" \
    "v_exp_f32 v71, v71\n" \
    "v_exp_f32 v72, v72\n" \
    "v_exp_f32 v73, v73\n" \
    "v_smfmac_f32_32x32x32_bf16 v[128:143], v[176:179], v[64:71], v192\n" \
    "v_exp_f32 v74, v74\n" \
    "v_exp_f32 v75, v75\n" \
    "v_exp_f32 v76, v76\n" \
    "global_load_lds_dwordx4 v197, %[kvb] offset:2048 sc1\n" \
    "v_exp_f32 v77, v77\n" \
    "v_exp_f32 v78, v78\n" \
    "v_exp_f32 v79, v79\n" \
    "v_exp_f32 v96, v96\n" \
    "v_exp_f32 v97, v97\n" \
    "v_smfmac_f32_32x32x32_bf16 v[128:143], v[180:183], v[72:79], v192\n" \
    "v_exp_f32 v98, v98\n" \
    "v_exp_f32 v99, v99\n" \
    "v_exp_f32 v100, v100\n" \
    "v_exp_f32 v101, v101\n" \
    "v_mfma_f32_32x32x16_bf16 v[112:127], v[172:175], v[164:167], 0\n" \
    "ds_read_b128 v[168:171], v193 offset:4096\n" \
    "v_exp_f32 v102, v102\n" \
    "v_exp_f32 v103, v103\n" \
    "v_exp_f32 v104, v104\n" \
    "v_exp_f32 v105, v105\n" \
    "v_smfmac_f32_32x32x32_bf16 v[144:159], v[176:179], v[96:103], v192\n" \
    "v_exp_f32 v106, v106\n" \
    "v_exp_f32 v107, v107\n" \
    "v_exp_f32 v108, v108\n" \
    "global_load_lds_dwordx4 v197, %[kvb] offset:3072 sc1\n" \
    "v_exp_f32 v109, v109\n" \
    "v_exp_f32 v110, v110\n" \
    "v_exp_f32 v111, v111\n" \
    "v_exp_f32 v80, v80\n" \
    "v_exp_f32 v81, v81\n" \
    "v_smfmac_f32_32x32x32_bf16 v[144:159], v[180:183], v[104:111], v192\n" \
    "ds_read_b128 v[176:179], v194 offset:4096\n" \
    "v_exp_f32 v82, v82\n" \
    "v_exp_f32 v83, v83\n" \
    "v_exp_f32 v84, v84\n" \
    "v_exp_f32 v85, v85\n" \
    "s_waitcnt lgkmcnt(1)\n" \
    "v_mfma_f32_32x32x16_bf16 v[64:79], v[168:171], v[160:163], 0\n" \
    "ds_read_b128 v[180:183], v194 offset:4608\n" \
    "v_exp_f32 v86, v86\n" \
    "v_exp_f32 v87, v87\n" \
    "v_exp_f32 v88, v88\n" \
    "v_exp_f32 v89, v89\n" \
    "v_smfmac_f32_32x32x32_bf16 v[128:143], v[184:187], v[80:87], v192\n" \
    "v_exp_f32 v90, v90\n" \
    "v_exp_f32 v91, v91\n" \
    "v_exp_f32 v92, v92\n" \
    "global_load_dword v212, v203, %[wdb] offset:0\n" \
    "global_load_dword v213, v203, %[wdb] offset:1024\n" \
    "v_exp_f32 v93, v93\n" \
    "v_exp_f32 v94, v94\n" \
    "v_exp_f32 v95, v95\n" \
    "v_exp_f32 v112, v112\n" \
    "v_exp_f32 v113, v113\n" \
    "v_smfmac_f32_32x32x32_bf16 v[128:143], v[188:191], v[88:95], v192\n" \
    "v_exp_f32 v114, v114\n" \
    "v_exp_f32 v115, v115\n" \
    "v_exp_f32 v116, v116\n" \
    "v_exp_f32 v117, v117\n" \
    "v_mfma_f32_32x32x16_bf16 v[96:111], v[168:171], v[164:167], 0\n" \
    "ds_read_b128 v[172:175], v193 offset:5120\n" \
    "v_exp_f32 v118, v118\n" \
    "v_exp_f32 v119, v119\n" \
    "v_exp_f32 v120, v120\n" \
    "v_exp_f32 v121, v121\n" \
    "v_smfmac_f32_32x32x32_bf16 v[144:159], v[184:187], v[112:119], v192\n" \
    "v_exp_f32 v122, v122\n" \
    "v_exp_f32 v123, v123\n" \
    "v_exp_f32 v124, v124\n" \
    "global_load_dword v214, v203, %[wdb] offset:2048\n" \
    "global_load_dword v215, v203, %[wdb] offset:3072\n" \
    "v_exp_f32 v125, v125\n" \
    "v_exp_f32 v126, v126\n" \
    "v_exp_f32 v127, v127\n" \
    "v_add_u32 v203, 0x1000, v203\n" \
    "v_add_u32 v193, 4096, v193\n" \
    "v_add_u32 v194, v195, v194\n" \
    "v_exp_f32 v64, v64\n" \
    "v_exp_f32 v65, v65\n" \
    "v_smfmac_f32_32x32x32_bf16 v[144:159], v[188:191], v[120:127], v192\n" \
    "ds_read_b128 v[184:187], v194 offset:1024\n" \
    "v_exp_f32 v66, v66\n" \
    "v_exp_f32 v67, v67\n" \
    "v_exp_f32 v68, v68\n" \
    "v_exp_f32 v69, v69\n" \
    "s_waitcnt lgkmcnt(1)\n" \
    "v_mfma_f32_32x32x16_bf16 v[80:95], v[172:175], v[160:163], 0\n" \
    "ds_read_b128 v[188:191], v194 offset:1536\n" \
    "v_exp_f32 v70, v70\n" \
    "v_exp_f32 v71, v71\n" \
    "v_exp_f32 v72, v72\n" \
    "v_exp_f32 v73, v73\n" \
    "v_smfmac_f32_32x32x32_bf16 v[128:143], v[176:179], v[64:71], v192\n" \
    "v_exp_f32 v74, v74\n" \
    "v_exp_f32 v75, v75\n" \
    "v_exp_f32 v76, v76\n" \
    "global_load_dword v216, v203, %[wdb] offset:0\n" \
    "global_load_dword v217, v203, %[wdb] offset:1024\n" \
    "v_exp_f32 v77, v77\n" \
    "v_exp_f32 v78, v78\n" \
    "v_exp_f32 v79, v79\n" \
    "v_exp_f32 v96, v96\n" \
    "v_exp_f32 v97, v97\n" \
    "v_smfmac_f32_32x32x32_bf16 v[128:143], v[180:183], v[72:79], v192\n" \
    "v_exp_f32 v98, v98\n" \
    "v_exp_f32 v99, v99\n" \
    "v_exp_f32 v100, v100\n" \
    "v_exp_f32 v101, v101\n" \
    "v_mfma_f32_32x32x16_bf16 v[112:127], v[172:175], v[164:167], 0\n" \
    "ds_read_b128 v[168:171], v193 offset:2048\n" \
    "v_exp_f32 v102, v102\n" \
    "v_exp_f32 v103, v103\n" \
    "v_exp_f32 v104, v104\n" \
    "v_exp_f32 v105, v105\n" \
    "v_smfmac_f32_32x32x32_bf16 v[144:159], v[176:179], v[96:103], v192\n" \
    "v_exp_f32 v106, v106\n" \
    "v_exp_f32 v107, v107\n" \
    "v_exp_f32 v108, v108\n" \
    "global_load_dword v218, v203, %[wdb] offset:2048\n" \
    "global_load_dword v219, v203, %[wdb] offset:3072\n" \
    "v_exp_f32 v109, v109\n" \
    "v_exp_f32 v110, v110\n" \
    "v_exp_f32 v111, v111\n" \
    "v_exp_f32 v80, v80\n" \
    "v_exp_f32 v81, v81\n" \
    "v_smfmac_f32_32x32x32_bf16 v[144:159], v[180:183], v[104:111], v192\n" \
    "s_waitcnt vmcnt(12)\n" \
    "ds_read_b64_tr_b16 v[220:221], v228\n" \
    "ds_read_b64_tr_b16 v[222:223], v229\n" \
    "ds_read_b64_tr_b16 v[224:225], v228 offset:256\n" \
    "ds_read_b64_tr_b16 v[226:227], v229 offset:256\n" \
    "ds_read_b128 v[176:179], v194 offset:2048\n" \
    "v_exp_f32 v82, v82\n" \
    "v_exp_f32 v83, v83\n" \
    "v_exp_f32 v84, v84\n" \
    "v_exp_f32 v85, v85\n" \
    "s_waitcnt lgkmcnt(1)\n" \
    "v_mfma_f32_32x32x16_bf16 v[64:79], v[168:171], v[160:163], 0\n" \
    "ds_read_b128 v[180:183], v194 offset:2560\n" \
    "v_exp_f32 v86, v86\n" \
    "v_exp_f32 v87, v87\n" \
    "v_exp_f32 v88, v88\n" \
    "v_exp_f32 v89, v89\n" \
    "v_smfmac_f32_32x32x32_bf16 v[128:143], v[184:187], v[80:87], v192\n" \
    "v_exp_f32 v90, v90\n" \
    "v_exp_f32 v91, v91\n" \
    "v_exp_f32 v92, v92\n" \
    "v_mfma_f32_16x16x32_bf16 v[232:235], v[220:223], v[204:207], 0\n" \
    "v_exp_f32 v93, v93\n" \
    "v_exp_f32 v94, v94\n" \
    "v_exp_f32 v95, v95\n" \
    "v_exp_f32 v112, v112\n" \
    "v_exp_f32 v113, v113\n" \
    "v_smfmac_f32_32x32x32_bf16 v[128:143], v[188:191], v[88:95], v192\n" \
    "v_exp_f32 v114, v114\n" \
    "v_exp_f32 v115, v115\n" \
    "v_exp_f32 v116, v116\n" \
    "v_exp_f32 v117, v117\n" \
    "v_mfma_f32_32x32x16_bf16 v[96:111], v[168:171], v[164:167], 0\n" \
    "ds_read_b128 v[172:175], v193 offset:3072\n" \
    "v_exp_f32 v118, v118\n" \
    "v_exp_f32 v119, v119\n" \
    "v_exp_f32 v120, v120\n" \
    "v_exp_f32 v121, v121\n" \
    "v_smfmac_f32_32x32x32_bf16 v[144:159], v[184:187], v[112:119], v192\n" \
    "v_exp_f32 v122, v122\n" \
    "v_exp_f32 v123, v123\n" \
    "v_exp_f32 v124, v124\n" \
    "v_exp_f32 v125, v125\n" \
    "v_exp_f32 v126, v126\n" \
    "v_exp_f32 v127, v127\n" \
    "s_setprio 0\n" \
    "v_exp_f32 v64, v64\n" \
    "v_exp_f32 v65, v65\n" \
    "v_smfmac_f32_32x32x32_bf16 v[144:159], v[188:191], v[120:127], v192\n" \
    "ds_read_b128 v[184:187], v194 offset:3072\n" \
    "v_exp_f32 v66, v66\n" \
    "v_exp_f32 v67, v67\n" \
    "v_exp_f32 v68, v68\n" \
    "v_exp_f32 v69, v69\n" \
    "s_waitcnt lgkmcnt(1)\n" \
    "v_mfma_f32_32x32x16_bf16 v[80:95], v[172:175], v[160:163], 0\n" \
    "ds_read_b128 v[188:191], v194 offset:3584\n" \
    "v_exp_f32 v70, v70\n" \
    "v_exp_f32 v71, v71\n" \
    "v_exp_f32 v72, v72\n" \
    "v_exp_f32 v73, v73\n" \
    "v_smfmac_f32_32x32x32_bf16 v[128:143], v[176:179], v[64:71], v192\n" \
    "v_exp_f32 v74, v74\n" \
    "v_exp_f32 v75, v75\n" \
    "v_exp_f32 v76, v76\n" \
    "v_mfma_f32_16x16x32_bf16 v[232:235], v[224:227], v[208:211], v[232:235]\n" \
    "v_exp_f32 v77, v77\n" \
    "v_exp_f32 v78, v78\n" \
    "v_exp_f32 v79, v79\n" \
    "v_exp_f32 v96, v96\n" \
    "v_exp_f32 v97, v97\n" \
    "v_smfmac_f32_32x32x32_bf16 v[128:143], v[180:183], v[72:79], v192\n" \
    "v_exp_f32 v98, v98\n" \
    "v_exp_f32 v99, v99\n" \
    "v_exp_f32 v100, v100\n" \
    "v_exp_f32 v101, v101\n" \
    "v_mfma_f32_32x32x16_bf16 v[112:127], v[172:175], v[164:167], 0\n" \
    "ds_read_b128 v[168:171], v193 offset:4096\n" \
    "v_exp_f32 v102, v102\n" \
    "v_exp_f32 v103, v103\n" \
    "v_exp_f32 v104, v104\n" \
    "v_exp_f32 v105, v105\n" \
    "v_smfmac_f32_32x32x32_bf16 v[144:159], v[176:179], v[96:103], v192\n" \
    "v_exp_f32 v106, v106\n" \
    "v_exp_f32 v107, v107\n" \
    "v_exp_f32 v108, v108\n" \
    "v_exp_f32 v109, v109\n" \
    "v_exp_f32 v110, v110\n" \
    "v_exp_f32 v111, v111\n" \
    "v_exp_f32 v80, v80\n" \
    "v_exp_f32 v81, v81\n" \
    "v_smfmac_f32_32x32x32_bf16 v[144:159], v[180:183], v[104:111], v192\n" \
    "ds_write_b128 v230, v[232:235]\n" \
    "ds_read_b128 v[176:179], v194 offset:4096\n" \
    "v_exp_f32 v82, v82\n" \
    "v_exp_f32 v83, v83\n" \
    "v_exp_f32 v84, v84\n" \
    "v_exp_f32 v85, v85\n" \
    "s_waitcnt lgkmcnt(1)\n" \
    "v_mfma_f32_32x32x16_bf16 v[64:79], v[168:171], v[160:163], 0\n" \
    "ds_read_b128 v[180:183], v194 offset:4608\n" \
    "v_exp_f32 v86, v86\n" \
    "v_exp_f32 v87, v87\n" \
    "v_exp_f32 v88, v88\n" \
    "v_exp_f32 v89, v89\n" \
    "v_smfmac_f32_32x32x32_bf16 v[128:143], v[184:187], v[80:87], v192\n" \
    "v_exp_f32 v90, v90\n" \
    "v_exp_f32 v91, v91\n" \
    "v_exp_f32 v92, v92\n" \
    "v_exp_f32 v93, v93\n" \
    "v_exp_f32 v94, v94\n" \
    "v_exp_f32 v95, v95\n" \
    "v_exp_f32 v112, v112\n" \
    "v_exp_f32 v113, v113\n" \
    "v_smfmac_f32_32x32x32_bf16 v[128:143], v[188:191], v[88:95], v192\n" \
    "v_exp_f32 v114, v114\n" \
    "v_exp_f32 v115, v115\n" \
    "v_exp_f32 v116, v116\n" \
    "v_exp_f32 v117, v117\n" \
    "v_mfma_f32_32x32x16_bf16 v[96:111], v[168:171], v[164:167], 0\n" \
    "ds_read_b128 v[172:175], v193 offset:5120\n" \
    "v_exp_f32 v118, v118\n" \
    "v_exp_f32 v119, v119\n" \
    "v_exp_f32 v120, v120\n" \
    "v_exp_f32 v121, v121\n" \
    "v_smfmac_f32_32x32x32_bf16 v[144:159], v[184:187], v[112:119], v192\n" \
    "v_exp_f32 v122, v122\n" \
    "v_exp_f32 v123, v123\n" \
    "v_exp_f32 v124, v124\n" \
    "v_exp_f32 v125, v125\n" \
    "v_exp_f32 v126, v126\n" \
    "v_exp_f32 v127, v127\n" \
    "v_add_u32 v193, 4096, v193\n" \
    "v_add_u32 v194, v195, v194\n" \
    "s_waitcnt vmcnt(0)\n"

#define ATT_C \
    "s_mov_b32 %[cnt], 2\n" \
    "1:\n" \
    "v_exp_f32 v64, v64\n" \
    "v_exp_f32 v65, v65\n" \
    "v_smfmac_f32_32x32x32_bf16 v[144:159], v[188:191], v[120:127], v192\n" \
    "ds_read_b128 v[184:187], v194 offset:1024\n" \
    "v_exp_f32 v66, v66\n" \
    "v_exp_f32 v67, v67\n" \
    "v_exp_f32 v68, v68\n" \
    "v_exp_f32 v69, v69\n" \
    "s_waitcnt lgkmcnt(1)\n" \
    "v_mfma_f32_32x32x16_bf16 v[80:95], v[172:175], v[160:163], 0\n" \
    "ds_read_b128 v[188:191], v194 offset:1536\n" \
    "v_exp_f32 v70, v70\n" \
    "v_exp_f32 v71, v71\n" \
    "v_exp_f32 v72, v72\n" \
    "v_exp_f32 v73, v73\n" \
    "v_smfmac_f32_32x32x32_bf16 v[128:143], v[176:179], v[64:71], v192\n" \
    "v_exp_f32 v74, v74\n" \
    "s_mov_b32 m0, %[swo]\n" \
    "v_exp_f32 v75, v75\n" \
    "v_exp_f32 v76, v76\n" \
    "global_load_lds_dwordx4 v236, %[wob] offset:0\n" \
    "v_exp_f32 v77, v77\n" \
    "v_exp_f32 v78, v78\n" \
    "v_exp_f32 v79, v79\n" \
    "v_exp_f32 v96, v96\n" \
    "v_exp_f32 v97, v97\n" \
    "v_smfmac_f32_32x32x32_bf16 v[128:143], v[180:183], v[72:79], v192\n" \
    "v_exp_f32 v98, v98\n" \
    "v_exp_f32 v99, v99\n" \
    "v_exp_f32 v100, v100\n" \
    "v_exp_f32 v101, v101\n" \
    "v_mfma_f32_32x32x16_bf16 v[112:127], v[172:175], v[164:167], 0\n" \
    "ds_read_b128 v[168:171], v193 offset:2048\n" \
    "v_exp_f32 v102, v102\n" \
    "v_exp_f32 v103, v103\n" \
    "v_exp_f32 v104, v104\n" \
    "v_exp_f32 v105, v105\n" \
    "v_smfmac_f32_32x32x32_bf16 v[144:159], v[176:179], v[96:103], v192\n" \
    "v_exp_f32 v106, v106\n" \
    "v_exp_f32 v107, v107\n" \
    "v_exp_f32 v108, v108\n" \
    "v_exp_f32 v109, v109\n" \
    "v_exp_f32 v110, v110\n" \
    "v_exp_f32 v111, v111\n" \
    "v_exp_f32 v80, v80\n" \
    "v_exp_f32 v81, v81\n" \
    "v_smfmac_f32_32x32x32_bf16 v[144:159], v[180:183], v[104:111], v192\n" \
    "ds_read_b128 v[176:179], v194 offset:2048\n" \
    "v_exp_f32 v82, v82\n" \
    "v_exp_f32 v83, v83\n" \
    "v_exp_f32 v84, v84\n" \
    "v_exp_f32 v85, v85\n" \
    "s_waitcnt lgkmcnt(1)\n" \
    "v_mfma_f32_32x32x16_bf16 v[64:79], v[168:171], v[160:163], 0\n" \
    "ds_read_b128 v[180:183], v194 offset:2560\n" \
    "v_exp_f32 v86, v86\n" \
    "v_exp_f32 v87, v87\n" \
    "v_exp_f32 v88, v88\n" \
    "v_exp_f32 v89, v89\n" \
    "v_smfmac_f32_32x32x32_bf16 v[128:143], v[184:187], v[80:87], v192\n" \
    "v_exp_f32 v90, v90\n" \
    "v_exp_f32 v91, v91\n" \
    "v_exp_f32 v92, v92\n" \
    "global_load_lds_dwordx4 v237, %[wob] offset:1024\n" \
    "v_exp_f32 v93, v93\n" \
    "v_exp_f32 v94, v94\n" \
    "v_exp_f32 v95, v95\n" \
    "v_exp_f32 v112, v112\n" \
    "v_exp_f32 v113, v113\n" \
    "v_smfmac_f32_32x32x32_bf16 v[128:143], v[188:191], v[88:95], v192\n" \
    "v_exp_f32 v114, v114\n" \
    "v_exp_f32 v115, v115\n" \
    "v_exp_f32 v116, v116\n" \
    "v_exp_f32 v117, v117\n" \
    "v_mfma_f32_32x32x16_bf16 v[96:111], v[168:171], v[164:167], 0\n" \
    "ds_read_b128 v[172:175], v193 offset:3072\n" \
    "v_exp_f32 v118, v118\n" \
    "v_exp_f32 v119, v119\n" \
    "v_exp_f32 v120, v120\n" \
    "v_exp_f32 v121, v121\n" \
    "v_smfmac_f32_32x32x32_bf16 v[144:159], v[184:187], v[112:119], v192\n" \
    "v_exp_f32 v122, v122\n" \
    "v_exp_f32 v123, v123\n" \
    "v_exp_f32 v124, v124\n" \
    "v_exp_f32 v125, v125\n" \
    "v_exp_f32 v126, v126\n" \
    "v_exp_f32 v127, v127\n" \
    "v_exp_f32 v64, v64\n" \
    "v_exp_f32 v65, v65\n" \
    "v_smfmac_f32_32x32x32_bf16 v[144:159], v[188:191], v[120:127], v192\n" \
    "ds_read_b128 v[184:187], v194 offset:3072\n" \
    "v_exp_f32 v66, v66\n" \
    "v_exp_f32 v67, v67\n" \
    "v_exp_f32 v68, v68\n" \
    "v_exp_f32 v69, v69\n" \
    "s_waitcnt lgkmcnt(1)\n" \
    "v_mfma_f32_32x32x16_bf16 v[80:95], v[172:175], v[160:163], 0\n" \
    "ds_read_b128 v[188:191], v194 offset:3584\n" \
    "v_exp_f32 v70, v70\n" \
    "v_exp_f32 v71, v71\n" \
    "v_exp_f32 v72, v72\n" \
    "v_exp_f32 v73, v73\n" \
    "v_smfmac_f32_32x32x32_bf16 v[128:143], v[176:179], v[64:71], v192\n" \
    "v_exp_f32 v74, v74\n" \
    "v_exp_f32 v75, v75\n" \
    "v_exp_f32 v76, v76\n" \
    "global_load_lds_dwordx4 v238, %[wob] offset:2048\n" \
    "v_exp_f32 v77, v77\n" \
    "v_exp_f32 v78, v78\n" \
    "v_exp_f32 v79, v79\n" \
    "v_exp_f32 v96, v96\n" \
    "v_exp_f32 v97, v97\n" \
    "v_smfmac_f32_32x32x32_bf16 v[128:143], v[180:183], v[72:79], v192\n" \
    "v_exp_f32 v98, v98\n" \
    "v_exp_f32 v99, v99\n" \
    "v_exp_f32 v100, v100\n" \
    "v_exp_f32 v101, v101\n" \
    "v_mfma_f32_32x32x16_bf16 v[112:127], v[172:175], v[164:167], 0\n" \
    "ds_read_b128 v[168:171], v193 offset:4096\n" \
    "v_exp_f32 v102, v102\n" \
    "v_exp_f32 v103, v103\n" \
    "v_exp_f32 v104, v104\n" \
    "v_exp_f32 v105, v105\n" \
    "v_smfmac_f32_32x32x32_bf16 v[144:159], v[176:179], v[96:103], v192\n" \
    "v_exp_f32 v106, v106\n" \
    "v_exp_f32 v107, v107\n" \
    "v_exp_f32 v108, v108\n" \
    "v_exp_f32 v109, v109\n" \
    "v_exp_f32 v110, v110\n" \
    "v_exp_f32 v111, v111\n" \
    "v_exp_f32 v80, v80\n" \
    "v_exp_f32 v81, v81\n" \
    "v_smfmac_f32_32x32x32_bf16 v[144:159], v[180:183], v[104:111], v192\n" \
    "ds_read_b128 v[176:179], v194 offset:4096\n" \
    "v_exp_f32 v82, v82\n" \
    "v_exp_f32 v83, v83\n" \
    "v_exp_f32 v84, v84\n" \
    "v_exp_f32 v85, v85\n" \
    "s_waitcnt lgkmcnt(1)\n" \
    "v_mfma_f32_32x32x16_bf16 v[64:79], v[168:171], v[160:163], 0\n" \
    "ds_read_b128 v[180:183], v194 offset:4608\n" \
    "v_exp_f32 v86, v86\n" \
    "v_exp_f32 v87, v87\n" \
    "v_exp_f32 v88, v88\n" \
    "v_exp_f32 v89, v89\n" \
    "v_smfmac_f32_32x32x32_bf16 v[128:143], v[184:187], v[80:87], v192\n" \
    "v_exp_f32 v90, v90\n" \
    "v_exp_f32 v91, v91\n" \
    "v_exp_f32 v92, v92\n" \
    "global_load_lds_dwordx4 v239, %[wob] offset:3072\n" \
    "v_exp_f32 v93, v93\n" \
    "v_exp_f32 v94, v94\n" \
    "v_exp_f32 v95, v95\n" \
    "v_exp_f32 v112, v112\n" \
    "v_exp_f32 v113, v113\n" \
    "v_smfmac_f32_32x32x32_bf16 v[128:143], v[188:191], v[88:95], v192\n" \
    "v_exp_f32 v114, v114\n" \
    "v_exp_f32 v115, v115\n" \
    "v_exp_f32 v116, v116\n" \
    "v_exp_f32 v117, v117\n" \
    "v_mfma_f32_32x32x16_bf16 v[96:111], v[168:171], v[164:167], 0\n" \
    "ds_read_b128 v[172:175], v193 offset:5120\n" \
    "v_exp_f32 v118, v118\n" \
    "v_exp_f32 v119, v119\n" \
    "v_exp_f32 v120, v120\n" \
    "v_exp_f32 v121, v121\n" \
    "v_smfmac_f32_32x32x32_bf16 v[144:159], v[184:187], v[112:119], v192\n" \
    "v_exp_f32 v122, v122\n" \
    "v_exp_f32 v123, v123\n" \
    "v_exp_f32 v124, v124\n" \
    "v_exp_f32 v125, v125\n" \
    "v_exp_f32 v126, v126\n" \
    "v_exp_f32 v127, v127\n" \
    "v_add_u32 v236, 0x8000, v236\n" \
    "v_add_u32 v237, 0x8000, v237\n" \
    "v_add_u32 v238, 0x8000, v238\n" \
    "v_add_u32 v239, 0x8000, v239\n" \
    "s_add_u32 %[swo], %[swo], 0x1000\n" \
    "v_add_u32 v193, 4096, v193\n" \
    "v_add_u32 v194, v195, v194\n" \
    "s_sub_u32 %[cnt], %[cnt], 1\n" \
    "s_cmp_lg_u32 %[cnt], 0\n" \
    "s_cbranch_scc1 1b\n" \
    "s_mov_b32 %[cnt], 2\n" \
    "2:\n" \
    "v_exp_f32 v64, v64\n" \
    "v_exp_f32 v65, v65\n" \
    "v_smfmac_f32_32x32x32_bf16 v[144:159], v[188:191], v[120:127], v192\n" \
    "ds_read_b128 v[184:187], v194 offset:1024\n" \
    "v_exp_f32 v66, v66\n" \
    "v_exp_f32 v67, v67\n" \
    "v_exp_f32 v68, v68\n" \
    "v_exp_f32 v69, v69\n" \
    "s_waitcnt lgkmcnt(1)\n" \
    "v_mfma_f32_32x32x16_bf16 v[80:95], v[172:175], v[160:163], 0\n" \
    "ds_read_b128 v[188:191], v194 offset:1536\n" \
    "v_exp_f32 v70, v70\n" \
    "v_exp_f32 v71, v71\n" \
    "v_exp_f32 v72, v72\n" \
    "v_exp_f32 v73, v73\n" \
    "v_smfmac_f32_32x32x32_bf16 v[128:143], v[176:179], v[64:71], v192\n" \
    "v_exp_f32 v74, v74\n" \
    "v_exp_f32 v75, v75\n" \
    "v_exp_f32 v76, v76\n" \
    "v_exp_f32 v77, v77\n" \
    "v_exp_f32 v78, v78\n" \
    "v_exp_f32 v79, v79\n" \
    "v_exp_f32 v96, v96\n" \
    "v_exp_f32 v97, v97\n" \
    "v_smfmac_f32_32x32x32_bf16 v[128:143], v[180:183], v[72:79], v192\n" \
    "v_exp_f32 v98, v98\n" \
    "v_exp_f32 v99, v99\n" \
    "v_exp_f32 v100, v100\n" \
    "v_exp_f32 v101, v101\n" \
    "v_mfma_f32_32x32x16_bf16 v[112:127], v[172:175], v[164:167], 0\n" \
    "ds_read_b128 v[168:171], v193 offset:2048\n" \
    "v_exp_f32 v102, v102\n" \
    "v_exp_f32 v103, v103\n" \
    "v_exp_f32 v104, v104\n" \
    "v_exp_f32 v105, v105\n" \
    "v_smfmac_f32_32x32x32_bf16 v[144:159], v[176:179], v[96:103], v192\n" \
    "v_exp_f32 v106, v106\n" \
    "v_exp_f32 v107, v107\n" \
    "v_exp_f32 v108, v108\n" \
    "v_exp_f32 v109, v109\n" \
    "v_exp_f32 v110, v110\n" \
    "v_exp_f32 v111, v111\n" \
    "v_exp_f32 v80, v80\n" \
    "v_exp_f32 v81, v81\n" \
    "v_smfmac_f32_32x32x32_bf16 v[144:159], v[180:183], v[104:111], v192\n" \
    "ds_read_b128 v[176:179], v194 offset:2048\n" \
    "v_exp_f32 v82, v82\n" \
    "v_exp_f32 v83, v83\n" \
    "v_exp_f32 v84, v84\n" \
    "v_exp_f32 v85, v85\n" \
    "s_waitcnt lgkmcnt(1)\n" \
    "v_mfma_f32_32x32x16_bf16 v[64:79], v[168:171], v[160:163], 0\n" \
    "ds_read_b128 v[180:183], v194 offset:2560\n" \
    "v_exp_f32 v86, v86\n" \
    "v_exp_f32 v87, v87\n" \
    "v_exp_f32 v88, v88\n" \
    "v_exp_f32 v89, v89\n" \
    "v_smfmac_f32_32x32x32_bf16 v[128:143], v[184:187], v[80:87], v192\n" \
    "v_exp_f32 v90, v90\n" \
    "v_exp_f32 v91, v91\n" \
    "v_exp_f32 v92, v92\n" \
    "v_exp_f32 v93, v93\n" \
    "v_exp_f32 v94, v94\n" \
    "v_exp_f32 v95, v95\n" \
    "v_exp_f32 v112, v112\n" \
    "v_exp_f32 v113, v113\n" \
    "v_smfmac_f32_32x32x32_bf16 v[128:143], v[188:191], v[88:95], v192\n" \
    "v_exp_f32 v114, v114\n" \
    "v_exp_f32 v115, v115\n" \
    "v_exp_f32 v116, v116\n" \
    "v_exp_f32 v117, v117\n" \
    "v_mfma_f32_32x32x16_bf16 v[96:111], v[168:171], v[164:167], 0\n" \
    "ds_read_b128 v[172:175], v193 offset:3072\n" \
    "v_exp_f32 v118, v118\n" \
    "v_exp_f32 v119, v119\n" \
    "v_exp_f32 v120, v120\n" \
    "v_exp_f32 v121, v121\n" \
    "v_smfmac_f32_32x32x32_bf16 v[144:159], v[184:187], v[112:119], v192\n" \
    "v_exp_f32 v122, v122\n" \
    "v_exp_f32 v123, v123\n" \
    "v_exp_f32 v124, v124\n" \
    "v_exp_f32 v125, v125\n" \
    "v_exp_f32 v126, v126\n" \
    "v_exp_f32 v127, v127\n" \
    "v_exp_f32 v64, v64\n" \
    "v_exp_f32 v65, v65\n" \
    "v_smfmac_f32_32x32x32_bf16 v[144:159], v[188:191], v[120:127], v192\n" \
    "ds_read_b128 v[184:187], v194 offset:3072\n" \
    "v_exp_f32 v66, v66\n" \
    "v_exp_f32 v67, v67\n" \
    "v_exp_f32 v68, v68\n" \
    "v_exp_f32 v69, v69\n" \
    "s_waitcnt lgkmcnt(1)\n" \
    "v_mfma_f32_32x32x16_bf16 v[80:95], v[172:175], v[160:163], 0\n" \
    "ds_read_b128 v[188:191], v194 offset:3584\n" \
    "v_exp_f32 v70, v70\n" \
    "v_exp_f32 v71, v71\n" \
    "v_exp_f32 v72, v72\n" \
    "v_exp_f32 v73, v73\n" \
    "v_smfmac_f32_32x32x32_bf16 v[128:143], v[176:179], v[64:71], v192\n" \
    "v_exp_f32 v74, v74\n" \
    "v_exp_f32 v75, v75\n" \
    "v_exp_f32 v76, v76\n" \
    "v_exp_f32 v77, v77\n" \
    "v_exp_f32 v78, v78\n" \
    "v_exp_f32 v79, v79\n" \
    "v_exp_f32 v96, v96\n" \
    "v_exp_f32 v97, v97\n" \
    "v_smfmac_f32_32x32x32_bf16 v[128:143], v[180:183], v[72:79], v192\n" \
    "v_exp_f32 v98, v98\n" \
    "v_exp_f32 v99, v99\n" \
    "v_exp_f32 v100, v100\n" \
    "v_exp_f32 v101, v101\n" \
    "v_mfma_f32_32x32x16_bf16 v[112:127], v[172:175], v[164:167], 0\n" \
    "ds_read_b128 v[168:171], v193 offset:4096\n" \
    "v_exp_f32 v102, v102\n" \
    "v_exp_f32 v103, v103\n" \
    "v_exp_f32 v104, v104\n" \
    "v_exp_f32 v105, v105\n" \
    "v_smfmac_f32_32x32x32_bf16 v[144:159], v[176:179], v[96:103], v192\n" \
    "v_exp_f32 v106, v106\n" \
    "v_exp_f32 v107, v107\n" \
    "v_exp_f32 v108, v108\n" \
    "v_exp_f32 v109, v109\n" \
    "v_exp_f32 v110, v110\n" \
    "v_exp_f32 v111, v111\n" \
    "v_exp_f32 v80, v80\n" \
    "v_exp_f32 v81, v81\n" \
    "v_smfmac_f32_32x32x32_bf16 v[144:159], v[180:183], v[104:111], v192\n" \
    "ds_read_b128 v[176:179], v194 offset:4096\n" \
    "v_exp_f32 v82, v82\n" \
    "v_exp_f32 v83, v83\n" \
    "v_exp_f32 v84, v84\n" \
    "v_exp_f32 v85, v85\n" \
    "s_waitcnt lgkmcnt(1)\n" \
    "v_mfma_f32_32x32x16_bf16 v[64:79], v[168:171], v[160:163], 0\n" \
    "ds_read_b128 v[180:183], v194 offset:4608\n" \
    "v_exp_f32 v86, v86\n" \
    "v_exp_f32 v87, v87\n" \
    "v_exp_f32 v88, v88\n" \
    "v_exp_f32 v89, v89\n" \
    "v_smfmac_f32_32x32x32_bf16 v[128:143], v[184:187], v[80:87], v192\n" \
    "v_exp_f32 v90, v90\n" \
    "v_exp_f32 v91, v91\n" \
    "v_exp_f32 v92, v92\n" \
    "v_exp_f32 v93, v93\n" \
    "v_exp_f32 v94, v94\n" \
    "v_exp_f32 v95, v95\n" \
    "v_exp_f32 v112, v112\n" \
    "v_exp_f32 v113, v113\n" \
    "v_smfmac_f32_32x32x32_bf16 v[128:143], v[188:191], v[88:95], v192\n" \
    "v_exp_f32 v114, v114\n" \
    "v_exp_f32 v115, v115\n" \
    "v_exp_f32 v116, v116\n" \
    "v_exp_f32 v117, v117\n" \
    "v_mfma_f32_32x32x16_bf16 v[96:111], v[168:171], v[164:167], 0\n" \
    "ds_read_b128 v[172:175], v193 offset:5120\n" \
    "v_exp_f32 v118, v118\n" \
    "v_exp_f32 v119, v119\n" \
    "v_exp_f32 v120, v120\n" \
    "v_exp_f32 v121, v121\n" \
    "v_smfmac_f32_32x32x32_bf16 v[144:159], v[184:187], v[112:119], v192\n" \
    "v_exp_f32 v122, v122\n" \
    "v_exp_f32 v123, v123\n" \
    "v_exp_f32 v124, v124\n" \
    "v_exp_f32 v125, v125\n" \
    "v_exp_f32 v126, v126\n" \
    "v_exp_f32 v127, v127\n" \
    "s_cmp_eq_u32 %[cnt], 2\n" \
    "s_cbranch_scc0 8f\n" \
    "s_setprio 0\n" \
    "8:\n" \
    "v_add_u32 v193, 4096, v193\n" \
    "v_add_u32 v194, v195, v194\n" \
    "s_sub_u32 %[cnt], %[cnt], 1\n" \
    "s_cmp_lg_u32 %[cnt], 0\n" \
    "s_cbranch_scc1 2b\n" \
    "s_nop 1\n" \
    "v_smfmac_f32_32x32x32_bf16 v[144:159], v[188:191], v[120:127], v192\n" \
    "s_setprio 0\n" \
    "s_waitcnt vmcnt(0)\n" \
    "s_nop 7\n" \
    "s_nop 7\n" \
    "s_nop 3\n" \
    "s_waitcnt lgkmcnt(0)\n"

__device__ __forceinline__ void slow_block(const char* ws, u32x4 q, int lane, float* o, float& lsum) {
    const u32 koff = WS_K + lane * 16;
    const u32 r32 = lane & 31;
    float m = -3.0e38f;
    for (int t = 0; t < 64; ++t) {
        u32x4 kf;
        GLD128C(kf, ws, koff + t * 1024, 0);
        asm volatile("s_waitcnt vmcnt(0)" : "+v"(kf) :: "memory");
        f32x16 s = {0};
        s = __builtin_amdgcn_mfma_f32_32x32x16_bf16(asbf(kf), asbf(q), s, 0, 0, 0);
#pragma unroll
        for (int i = 0; i < 16; ++i) m = fmaxf(m, s[i]);
    }
    {
        auto sw = __builtin_amdgcn_permlane32_swap(__float_as_uint(m), __float_as_uint(m), false, false);
        m = fmaxf(__uint_as_float(sw[0]), __uint_as_float(sw[1]));
    }
    f32x16 acc = {0};
    const u32 voff = WS_V + (lane >> 5) * 256 + (lane & 15) * 16;
    for (int t = 0; t < 64; ++t) {
        u32x4 kf, v0, v1;
        GLD128C(kf, ws, koff + t * 1024, 0);
        GLD128C(v0, ws, voff + t * 1024, 0);
        GLD128C(v1, ws, voff + t * 1024, 512);
        asm volatile("s_waitcnt vmcnt(0)" : "+v"(kf), "+v"(v0), "+v"(v1) :: "memory");
        if (r32 >= 16) { v0 = (u32x4){0x3f803f80u, 0x3f803f80u, 0x3f803f80u, 0x3f803f80u}; v1 = v0; }
        f32x16 s = {0};
        s = __builtin_amdgcn_mfma_f32_32x32x16_bf16(asbf(kf), asbf(q), s, 0, 0, 0);
        u32 p[8];
#pragma unroll
        for (int i = 0; i < 8; ++i)
            p[i] = pk(__builtin_amdgcn_exp2f(s[2 * i] - m), __builtin_amdgcn_exp2f(s[2 * i + 1] - m));
#pragma unroll
        for (int st = 0; st < 2; ++st) {
            u32x4 vv = st ? v1 : v0;
            auto a = __builtin_amdgcn_permlane32_swap(vv[0], vv[2], false, false);
            auto c = __builtin_amdgcn_permlane32_swap(vv[1], vv[3], false, false);
            u32x4 af = {a[0], c[0], a[1], c[1]};
            u32x4 bf = {p[4 * st], p[4 * st + 1], p[4 * st + 2], p[4 * st + 3]};
            acc = __builtin_amdgcn_mfma_f32_32x32x16_bf16(asbf(af), asbf(bf), acc, 0, 0, 0);
        }
    }
#pragma unroll
    for (int i = 0; i < 8; ++i) o[i] = acc[i];
    lsum = acc[8];
}

#define PRIO_MODE 1
#define PRIO_START() do { if (w >= 4) __builtin_amdgcn_s_setprio(1); } while (0)
#define PRIO_END() __builtin_amdgcn_s_setprio(0)
__global__ void __launch_bounds__(512, 2)
fused_kernel(const u16* x_, const u16* wq_, const u16* wk_, const u16* wv_, const u16* wo_, const u16* wr_, const u16* wg_,
             const u16* wu_, const u16* wd_, const u16* gw_, u16* out_, char* ws_)
{
    u32x16 ka0; u32x8 ka1;
    {
        const void* kp = (const void*)__builtin_amdgcn_kernarg_segment_ptr();
        asm volatile("s_load_dwordx16 %0, %2, 0\n\ts_load_dwordx8 %1, %2, 64\n\ts_waitcnt lgkmcnt(0)"
                     : "=&s"(ka0), "=&s"(ka1) : "s"(kp) : "memory");
    }
#define KP(v, i) ((u64)v[2 * (i)] | ((u64)v[2 * (i) + 1] << 32))
    const u16* x  = (const u16*)KP(ka0, 0); const u16* wq = (const u16*)KP(ka0, 1);
    const u16* wk = (const u16*)KP(ka0, 2); const u16* wv = (const u16*)KP(ka0, 3);
    const u16* wo = (const u16*)KP(ka0, 4); const u16* wr = (const u16*)KP(ka0, 5);
    const u16* wg = (const u16*)KP(ka0, 6); const u16* wu = (const u16*)KP(ka0, 7);
    const u16* wd = (const u16*)KP(ka1, 0); const u16* gw = (const u16*)KP(ka1, 1);
    u16* out = (u16*)KP(ka1, 2); char* ws = (char*)KP(ka1, 3);

    __shared__ __attribute__((aligned(1024))) char smem[L_END];
    LDSP char* lds = (LDSP char*)smem;
    const u32 lb = (u32)(size_t)lds;
    const int tid = threadIdx.x;
    const int lane = tid & 63;
    const int w = __builtin_amdgcn_readfirstlane(tid >> 6);
    const int b = blockIdx.x;
    const int xcd = b & 7, jj = b >> 3;
    const int tb = xcd * 4 + (jj >> 3), hg = jj & 7;
    const int g = lane >> 4, l15 = lane & 15, h = lane >> 5, l31 = lane & 31;
    const bool isK = hg < 4;
    const bool ffn = jj < 28;
    const int fj = xcd * 28 + min(jj, 27);
    STAMP(1); STAMPC(18);

    const int th = w & 1, hp = (w >> 1) & 1, blk = w >> 2;
    const int T0 = tb * 64 + (hg & 3) * 16;
    u32x4 xf0, xf1, vf0, vf1;
    {
        const u16* wsrc = isK ? wk : wv;
#pragma unroll
        for (int i = 0; i < 2; ++i) {
            u32 slot = 64 * i + lane, pos = slot >> 3, B = pos ^ ((pos >> 2) & 1);
            u32 row = 4 * B + ((slot >> 1) & 3);
            dma16(wsrc, (64 * w + row) * 32 + (slot & 1) * 16, lb + L_W + w * 2048 + i * 1024);
        }
        u32 xo = (T0 + l15) * 1024 + (64 * w + 16 * g) * 2;
        GLD128(xf0, x, xo, 0);
        GLD128(xf1, x, xo, 16);
        const u16* vec = isK ? wr : gw;
        u32 vo = (64 * w + 16 * g) * 2;
        GLD128(vf0, vec, vo, 0);
        GLD128(vf1, vec, vo, 16);
    }
    u32 epo;
    { u32 z = 0; GLD32C(epo, ws, z, 0); }
    asm volatile("s_barrier" ::: "memory");
    if (w < 7) {
#pragma unroll
        for (int i = 0; i < 19; ++i) {
            const int p = i < 18 ? w + 7 * i : __builtin_amdgcn_readfirstlane(min(w + 7 * i, 127));
            if (i < 9 || (i == 9 && w == 0)) {
                dma16(x, (tb * 64 + p) * 1024 + ((lane ^ (p & 15)) << 4), lb + L_K + p * 1024);
            } else {
                const int G = p - 64;
                dma16(wq, (8 * G + ((lane >> 2) & 7)) * 1024 + hg * 128 + (lane >> 5) * 64 + (lane & 3) * 16, lb + L_V + G * 1024);
            }
        }
    } else {
        const u32x4 pz = {POISON, POISON, POISON, POISON};
#pragma unroll
        for (int i = 0; i < 8; ++i) {
            u32 po = WS_O + ((tb * 4 + (i >> 1)) * 16 + hg * 2 + (i & 1)) * 1024 + lane * 16;
            GST128C(ws, po, pz);
        }
        u32 yo = (ffn ? WS_Y + fj * 2048 : WS_X + b * 2048) + lane * 16;
        GST128C(ws, yo, pz);
        yo += 1024;
        GST128C(ws, yo, pz);
    }
    if (tid < 256) { u32 o1 = (tid & 15) == 0 ? 0x3f803f80u : 0u; ST128(L_ONE + tid * 16, ((u32x4){o1, o1, o1, o1})); }
    asm volatile("s_cmp_eq_u32 %[w], 7\n\ts_cbranch_scc1 1f\n\ts_waitcnt vmcnt(20)\n\ts_branch 2f\n1:\n\ts_waitcnt vmcnt(11)\n2:"
                 : "+v"(xf0), "+v"(xf1), "+v"(vf0), "+v"(vf1) : [w] "s"(w) : "memory", "scc");
    FSTAMP(2);
    {
        const u32 ta = L_W + w * 2048 + l15 * 8;
        const u32 gx = g & 1;
        u32x4 wf0 = cat(trread(lds, ta + ((4 * g + 0) ^ gx) * 128), trread(lds, ta + ((4 * g + 1) ^ gx) * 128));
        u32x4 wf1 = cat(trread(lds, ta + ((4 * g + 2) ^ gx) * 128), trread(lds, ta + ((4 * g + 3) ^ gx) * 128));
        f32x4 acc = {0, 0, 0, 0}, dacc = {0, 0, 0, 0};
        if (isK) {
            acc = __builtin_amdgcn_mfma_f32_16x16x32_bf16(asbf(wf0), asbf(xf0), acc, 0, 0, 0);
            acc = __builtin_amdgcn_mfma_f32_16x16x32_bf16(asbf(wf1), asbf(xf1), acc, 0, 0, 0);
        } else {
            acc = __builtin_amdgcn_mfma_f32_16x16x32_bf16(asbf(xf0), asbf(wf0), acc, 0, 0, 0);
            acc = __builtin_amdgcn_mfma_f32_16x16x32_bf16(asbf(xf1), asbf(wf1), acc, 0, 0, 0);
        }
        dacc = __builtin_amdgcn_mfma_f32_16x16x32_bf16(asbf(vf0), asbf(xf0), dacc, 0, 0, 0);
        dacc = __builtin_amdgcn_mfma_f32_16x16x32_bf16(asbf(vf1), asbf(xf1), dacc, 0, 0, 0);
        ST128(L_P1 + w * 1024 + lane * 16, __builtin_bit_cast(u32x4, acc));
        if (g == 0) STF(L_PD + w * 64 + l15 * 4, dacc[0]);
    }
    BAR();
    FSTAMP(3);
    asm volatile("s_cmp_eq_u32 %[w], 7\n\ts_cbranch_scc1 1f\n\ts_waitcnt vmcnt(19)\n\ts_branch 2f\n1:\n\ts_waitcnt vmcnt(10)\n2:"
                 : "+v"(epo) : [w] "s"(w) : "memory", "scc");
    const u32 epoch = (u32)__builtin_amdgcn_readfirstlane((int)epo) + 1u;
    if (w == 7) {
        f32x4 ps[8]; float pd[8];
#pragma unroll
        for (int i = 0; i < 8; ++i) {
            ps[i] = __builtin_bit_cast(f32x4, LD128(L_P1 + i * 1024 + lane * 16));
            pd[i] = LDF(L_PD + i * 64 + l15 * 4);
        }
        __builtin_amdgcn_sched_barrier(0);
        const f32x4 s = ((ps[0] + ps[1]) + (ps[2] + ps[3])) + ((ps[4] + ps[5]) + (ps[6] + ps[7]));
        const float d = ((pd[0] + pd[1]) + (pd[2] + pd[3])) + ((pd[4] + pd[5]) + (pd[6] + pd[7]));
        u32x2 pv = {pk(s[0], s[1]), pk(s[2], s[3])};
        const int tile = T0 >> 5, sub = (T0 >> 4) & 1;
        u32 key = 0;
        if (isK) {
            u32 off = WS_K + tile * 1024 + ((g & 1) * 32 + sub * 16 + l15) * 16 + (g >> 1) * 8;
            GST64C(ws, off, pv);
            u32 rb = pk(d, d) & 0xffffu;
            u32 ord = (rb & 0x8000u) ? (~rb & 0xffffu) : (rb | 0x8000u);
            key = wave_max_u32((ord << 16) | (0xffffu - (u32)(T0 + l15)));
        } else {
            u32 off = WS_V + tile * 1024 + sub * 512 + (g >> 1) * 256 + l15 * 16 + (g & 1) * 8;
            GST64C(ws, off, pv);
            if (g == 0) { u32 go = WS_G + (T0 + l15) * 4; u32 dv = __float_as_uint(d); GST32C(ws, go, dv); }
        }
        VMWAIT(0);
        if (lane < 8) { u32x2 fl = {key, epoch}; u32 fo = WS_F1 + lane * 2048 + (tile * 4 + (isK ? 0 : 2) + sub) * 8; GST64C(ws, fo, fl); }
        FSTAMP(4);
    } else {
        VMWAIT(0);
    }
    BAR();
    FSTAMP(5);

    f32x16 qacc = {0};
    if (w < 4) {
        const int qh = w >> 1;
        const int tok = th * 32 + l31;
        u32 abase = L_V + h * 1024 + qh * 512 + ((l15 >> 2) * 64) + ((lane >> 4) & 1) * 32 + (l15 & 3) * 8;
        asm volatile("" : "+v"(abase));
        u32 bb[8];
#pragma unroll
        for (int j = 0; j < 8; ++j) { bb[j] = L_K + tok * 1024 + (((2 * j + h) ^ (tok & 15)) << 4); asm volatile("" : "+v"(bb[j])); }
#pragma unroll
        for (int s = 0; s < 32; ++s) {
            u32x4 af = cat(trread(lds, abase + s * 2048), trread(lds, abase + s * 2048 + 256));
            u32x4 bf = LD128(bb[s & 7] + (s >> 3) * 256);
            qacc = __builtin_amdgcn_mfma_f32_32x32x16_bf16(asbf(af), asbf(bf), qacc, 0, 0, 0);
        }
        FSTAMP(6);
    } else {
        const int j = w - 4;
        const u32 fo = WS_F1 + xcd * 2048 + (j * 64 + l31 * 2) * 8;
        u32x4 f;
        u32 spins = 0;
        while (true) {
            asm volatile("global_load_dwordx4 %0, %1, %2 sc1\n\ts_waitcnt vmcnt(0)" : "=&v"(f) : "v"(fo), "s"(ws) : "memory");
            if (__all((f[1] == epoch) & (f[3] == epoch)) || ++spins > SPIN_MAX) break;
        }
        const u32 key = wave_max_u32(max(f[0], f[2]));
        if (lane == 0) ST32(L_CT + j * 4, key);
        FSTAMP(7);
    }
    BAR();
    FSTAMP(9);
    u32 kvo = (w & 1) * 65536 + ((w >> 1) & 1) * 32768;
    if (w >= 4) {
        dma16c4(ws + WS_K, kvo + lane * 16, lb + L_K + kvo);
        dma16c4(ws + WS_K, kvo + 4096 + lane * 16, lb + L_K + kvo + 4096);
        dma16c2(ws + WS_K, kvo + 8192 + lane * 16, lb + L_K + kvo + 8192);
    } else {
        const float qs = 0.25f * 1.4426950408889634f;
        u32x4 qx, qy;
#pragma unroll
        for (int i = 0; i < 4; ++i) {
            u32 a = pk(qacc[2 * i], qacc[2 * i + 1]), c = pk(qacc[8 + 2 * i], qacc[8 + 2 * i + 1]);
            qx[i] = pk(lo16(a) * qs, hi16(a) * qs);
            qy[i] = pk(lo16(c) * qs, hi16(c) * qs);
        }
        ST128(L_P1 + w * 2048 + lane * 16, qx);
        ST128(L_P1 + w * 2048 + 1024 + lane * 16, qy);
    }
    const u32 key = max(max(LD32(L_CT), LD32(L_CT + 4)), max(LD32(L_CT + 8), LD32(L_CT + 12)));
    const int sel = __builtin_amdgcn_readfirstlane(0xffff - (key & 0xffff)) & 2047;
    u32x8 EXA; u32x4 EXB, WOA;
    u32 skv = lb + L_K + kvo + (10 + 4 * (w >> 2)) * 1024, sg = lb + L_W + w * 2048;
    const u32 skv3 = lb + L_K + kvo + (26 + 2 * (w >> 2)) * 1024;
    u32 swo = lb + ((w & 4) ? L_V : L_K) + ((w >> 1) & 1) * 32768 + (w & 1) * 8192;
    {
        EXA[0] = kvo + (10 + 4 * (w >> 2)) * 1024 + lane * 16;
        EXA[1] = kvo + (26 + 2 * (w >> 2)) * 1024 + lane * 16;
        const u32 pos = lane >> 3, B = pos ^ ((pos >> 2) & 1);
        const u32 row = 64 * w + 4 * B + ((lane >> 1) & 3);
        const u64 g0 = (u64)((lane & 1) ? wu : wg) + (u64)(row * 3584 + fj * 16), g1 = g0 + 32 * 3584;
        EXA[2] = (u32)g0; EXA[3] = (u32)(g0 >> 32); EXA[4] = (u32)g1; EXA[5] = (u32)(g1 >> 32);
        EXA[6] = sel * 1024 + (64 * w + 16 * g) * 2;
        EXA[7] = fj * 8192 + (128 * (w & 3) + 2 * lane) * 2;
        const u32 ta = lb + L_W + w * 2048 + l15 * 8, gx = g & 1;
        EXB[0] = ta + (4 * g + gx) * 128;
        EXB[1] = ta + (4 * g + (gx ^ 1)) * 128;
        EXB[2] = lb + (l15 == 0 ? L_FP + w * 64 + g * 16 : L_P1 + w * 1024 + lane * 16);
        EXB[3] = 0;
#pragma unroll
        for (int i = 0; i < 4; ++i) {
            const u32 r = 64 * w + 8 * i + (lane >> 3), slot = lane & 7;
            const u32 f = ((r >> 1) & 1) | ((i & 1) << 1);
            const u32 c = (((slot >> 1) ^ f) << 1) | (slot & 1);
            WOA[i] = r * 1024 + hg * 128 + c * 16 - i * 1024;
        }
    }
    const int hf = w >> 2;
    u32 ka = lb + L_K + hf * 32768 + lane * 16;
    const bool vlane = l31 < 16;
    u32 va = vlane ? (lb + L_V + hf * 32768 + h * 256 + l15 * 16) : (lb + L_ONE - 1024 + h * 256 + l15 * 16);
    u32 vinc = vlane ? 4096u : 0u;
    u32 va0 = vlane ? va : va + 1024;
    u32 qa = lb + L_P1 + (w & 3) * 2048 + lane * 16;
    asm volatile("" : "+{v[196:203]}"(EXA), "+{v[228:231]}"(EXB), "+{v[236:239]}"(WOA), "+{v193}"(ka), "+{v194}"(va), "+{v195}"(vinc), "+v"(va0), "+v"(qa),
                 "+s"(skv), "+s"(sg), "+s"(swo));
    if (w >= 4) VMWAIT(0);
    BAR();
    STAMP(10); STAMPC(19);
    if (b == 0 && tid == 256) { u32 z = 0; GST32C(ws, z, epoch); }

    u32x4 qx, qy;
    f32x16 SX0, SX1, SY0, SY1, OX, OY;
    u32x4 K0, K1, V00, V01, V10, V11;
    u32 idx = 0xDDDDu;
    {
        asm volatile(
            "ds_read_b128 v[160:163], %[qa]\n"
            "ds_read_b128 v[164:167], %[qa] offset:1024\n"
            "ds_read_b128 v[168:171], v193\n"
            "ds_read_b128 v[172:175], v193 offset:1024\n"
            "ds_read_b128 v[176:179], %[va0]\n"
            "ds_read_b128 v[180:183], %[va0] offset:512\n"
            "v_mov_b32 v128, 0\n v_mov_b32 v129, 0\n v_mov_b32 v130, 0\n v_mov_b32 v131, 0\n"
            "v_mov_b32 v132, 0\n v_mov_b32 v133, 0\n v_mov_b32 v134, 0\n v_mov_b32 v135, 0\n"
            "v_mov_b32 v136, 0\n v_mov_b32 v137, 0\n v_mov_b32 v138, 0\n v_mov_b32 v139, 0\n"
            "v_mov_b32 v140, 0\n v_mov_b32 v141, 0\n v_mov_b32 v142, 0\n v_mov_b32 v143, 0\n"
            "v_mov_b32 v144, 0\n v_mov_b32 v145, 0\n v_mov_b32 v146, 0\n v_mov_b32 v147, 0\n"
            "v_mov_b32 v148, 0\n v_mov_b32 v149, 0\n v_mov_b32 v150, 0\n v_mov_b32 v151, 0\n"
            "v_mov_b32 v152, 0\n v_mov_b32 v153, 0\n v_mov_b32 v154, 0\n v_mov_b32 v155, 0\n"
            "v_mov_b32 v156, 0\n v_mov_b32 v157, 0\n v_mov_b32 v158, 0\n v_mov_b32 v159, 0\n"
            "v_mov_b32 v120, 0\n v_mov_b32 v121, 0\n v_mov_b32 v122, 0\n v_mov_b32 v123, 0\n"
            "v_mov_b32 v124, 0\n v_mov_b32 v125, 0\n v_mov_b32 v126, 0\n v_mov_b32 v127, 0\n"
            "v_mov_b32 v188, 0\n v_mov_b32 v189, 0\n v_mov_b32 v190, 0\n v_mov_b32 v191, 0\n"
            "s_waitcnt lgkmcnt(0)\n"
            "v_mfma_f32_32x32x16_bf16 v[64:79], v[168:171], v[160:163], 0\n"
            "v_mfma_f32_32x32x16_bf16 v[96:111], v[168:171], v[164:167], 0\n"
            "s_nop 7\n s_nop 7\n"
            : "={v[64:79]}"(SX0), "={v[80:95]}"(SX1), "={v[96:111]}"(SY0), "={v[112:127]}"(SY1),
              "={v[128:143]}"(OX), "={v[144:159]}"(OY),
              "={v[168:171]}"(K0), "={v[172:175]}"(K1), "={v[176:179]}"(V00), "={v[180:183]}"(V01),
              "={v[184:187]}"(V10), "={v[188:191]}"(V11), "=&{v[160:163]}"(qx), "=&{v[164:167]}"(qy)
            : "{v193}"(ka), [va0] "v"(va0), [qa] "v"(qa)
            : "memory");
    }
#define ATT_OPS \
            : "+{v[64:79]}"(SX0), "+{v[80:95]}"(SX1), "+{v[96:111]}"(SY0), "+{v[112:127]}"(SY1), \
              "+{v[128:143]}"(OX), "+{v[144:159]}"(OY), \
              "+{v[168:171]}"(K0), "+{v[172:175]}"(K1), "+{v[176:179]}"(V00), "+{v[180:183]}"(V01), \
              "+{v[184:187]}"(V10), "+{v[188:191]}"(V11), "+{v193}"(ka), "+{v194}"(va), [cnt] "=&s"(cnt_)
#define ATT_INS : "{v[160:163]}"(qx), "{v[164:167]}"(qy), "{v192}"(idx), "{v195}"(vinc)
    u32 cnt_;
    const char* kvb = ws + WS_K;
    PRIO_START();
    asm volatile(ATT_A ATT_OPS, "+{v[196:203]}"(EXA), "+{v[228:231]}"(EXB), "+{v[236:239]}"(WOA), [skv] "+s"(skv), [sg] "+s"(sg)
                 ATT_INS, [kvb] "s"(kvb) : "memory", "scc");
    LSTAMP(4);
    BAR();
    LSTAMP(8);
    PRIO_START();
    u32x16 EXD;
    asm volatile(ATT_B ATT_OPS, "+{v[196:203]}"(EXA), "=&{v[204:219]}"(EXD), "+{v[236:239]}"(WOA)
                 ATT_INS, "{v[228:231]}"(EXB), [kvb] "s"(kvb), [skv3] "s"(skv3), [xb] "s"(x), [wdb] "s"(wd)
                 : "memory", "scc", "v220", "v221", "v222", "v223", "v224", "v225", "v226", "v227", "v232", "v233", "v234", "v235");
    LSTAMP(9);
    BAR();
    LSTAMP(11);
    if (ffn && w < 4) {
        float sum = LDF(L_FP + l15 * 4);
#pragma unroll
        for (int i = 1; i < 8; ++i) sum += LDF(L_FP + i * 64 + l15 * 4);
        sum = bfr(sum);
        float up = __shfl(sum, (lane & 7) + 8);
        float gt = __shfl(sum, lane & 7);
        float hv = bfr(bfr(gt * sigm(gt)) * up);
        u32 hb = __float_as_uint(hv);
        float y0 = 0.f, y1 = 0.f;
#pragma unroll
        for (int i = 0; i < 8; ++i) {
            const float hs = __uint_as_float((u32)__builtin_amdgcn_readlane(hb, i));
            y0 += hs * lo16(EXD[8 + i]);
            y1 += hs * hi16(EXD[8 + i]);
        }
        u32x2 gr = {__float_as_uint(y0), __float_as_uint(y1)};
        gr[0] = gr[0] == POISON ? 0x7fc00000u : gr[0];
        gr[1] = gr[1] == POISON ? 0x7fc00000u : gr[1];
        u32 yo = WS_Y + fj * 2048 + (128 * w + 2 * lane) * 4;
        GST64C(ws, yo, gr);
    }
    LSTAMP(22);
    PRIO_START();
    asm volatile(ATT_C ATT_OPS, "+{v[236:239]}"(WOA), [swo] "+s"(swo) ATT_INS, [wob] "s"(wo) : "memory", "scc");
    const int tq = w & 3, kh = w >> 2;
    const bool owner = (sel >> 6) == tb;
    int lane_b = lane;
    asm volatile("" : "+v"(lane_b));
    {
    const int lane = lane_b;
    const int g = lane >> 4, l15 = lane & 15, h = lane >> 5, l31 = lane & 31;
    const int tokg = tb * 64 + tq * 16 + l15;
    const int ncol = hg * 64 + kh * 32 + (g & 1) * 16 + (g >> 1) * 8;
    u32 oslot;
    {
        const int head = hg * 4 + hp * 2 + blk, tl = th * 32 + l31;
        oslot = WS_O + ((tb * 4 + (tl >> 4)) * 16 + (head >> 1)) * 1024 + (((head & 1) * 2 + h) * 16 + (tl & 15)) * 16;
    }
    STAMP(12); STAMPC(20);
    u32x4 xe, Y0, Y1, Y2, Y3, Y4, Y5, Y6; u32 gl;
    const u32 yo0 = WS_Y + (28 * w + g) * 2048 + hg * 256 + l15 * 16;
    {
        u32 xo = tokg * 1024 + ncol * 2;
        GLD128(xe, x, xo, 0);
        u32 go = WS_G + tokg * 4;
        GLD32C(gl, ws, go, 0);
        u32 yo = yo0;
        asm volatile("s_cmp_lg_u32 %[stb], %[tb]\n\ts_cbranch_scc1 3f\n"
                     "global_load_dwordx4 %0, %[yo], %[ws] sc1\n v_add_u32 %[yo], 0x2000, %[yo]\n"
                     "global_load_dwordx4 %1, %[yo], %[ws] sc1\n v_add_u32 %[yo], 0x2000, %[yo]\n"
                     "global_load_dwordx4 %2, %[yo], %[ws] sc1\n v_add_u32 %[yo], 0x2000, %[yo]\n"
                     "global_load_dwordx4 %3, %[yo], %[ws] sc1\n v_add_u32 %[yo], 0x2000, %[yo]\n"
                     "global_load_dwordx4 %4, %[yo], %[ws] sc1\n v_add_u32 %[yo], 0x2000, %[yo]\n"
                     "global_load_dwordx4 %5, %[yo], %[ws] sc1\n v_add_u32 %[yo], 0x2000, %[yo]\n"
                     "global_load_dwordx4 %6, %[yo], %[ws] sc1\n3:"
                     : "=&v"(Y0), "=&v"(Y1), "=&v"(Y2), "=&v"(Y3), "=&v"(Y4), "=&v"(Y5), "=&v"(Y6), [yo] "+v"(yo)
                     : [stb] "s"(sel >> 6), [tb] "s"(tb), [ws] "s"(ws) : "memory", "scc");
    }

    {
        f32x16& mine = blk ? OY : OX;
        f32x16& give = blk ? OX : OY;
        const u32 cw = L_W + w * 2304, cr = L_W + (w ^ 4) * 2304;
        ST128(cw + lane * 16, ((u32x4){__float_as_uint(give[0]), __float_as_uint(give[1]), __float_as_uint(give[2]), __float_as_uint(give[3])}));
        ST128(cw + 1024 + lane * 16, ((u32x4){__float_as_uint(give[4]), __float_as_uint(give[5]), __float_as_uint(give[6]), __float_as_uint(give[7])}));
        STF(cw + 2048 + lane * 4, give[8]);
        BAR();
        f32x4 p0 = __builtin_bit_cast(f32x4, LD128(cr + lane * 16));
        f32x4 p1 = __builtin_bit_cast(f32x4, LD128(cr + 1024 + lane * 16));
        float l = LDF(cr + 2048 + lane * 4) + mine[8];
        float o[8];
#pragma unroll
        for (int i = 0; i < 4; ++i) { o[i] = mine[i] + p0[i]; o[4 + i] = mine[4 + i] + p1[i]; }
        {
            auto sw = __builtin_amdgcn_permlane32_swap(__float_as_uint(l), __float_as_uint(l), false, false);
            l = __uint_as_float(sw[0]);
        }
        float amax = fmaxf(fmaxf(fmaxf(fabsf(o[0]), fabsf(o[1])), fmaxf(fabsf(o[2]), fabsf(o[3]))),
                           fmaxf(fmaxf(fabsf(o[4]), fabsf(o[5])), fmaxf(fabsf(o[6]), fabsf(o[7]))));
        bool safe = (l >= 8.6736174e-19f) && (l <= 1.2676506e30f) && (amax <= 3.0e38f);
        if (!__all(safe)) {
            slow_block(ws, blk ? qy : qx, lane, o, l);
            auto sw = __builtin_amdgcn_permlane32_swap(__float_as_uint(l), __float_as_uint(l), false, false);
            l = __uint_as_float(sw[0]);
        }
        const float ri = __builtin_amdgcn_rcpf(l);
        u32 A0 = pk(o[0] * ri, o[1] * ri), A1 = pk(o[2] * ri, o[3] * ri);
        u32 B0 = pk(o[4] * ri, o[5] * ri), B1 = pk(o[6] * ri, o[7] * ri);
        auto s0 = __builtin_amdgcn_permlane32_swap(A0, B0, false, false);
        auto s1 = __builtin_amdgcn_permlane32_swap(A1, B1, false, false);
        u32x4 ov = {s0[0], s1[0], s0[1], s1[1]};
#pragma unroll
        for (int i = 0; i < 4; ++i) ov[i] = ov[i] == POISON ? 0x7fc07fc0u : ov[i];
        GST128C(ws, oslot, ov);
    }
    STAMP(13);

    u32x4 af[4][8];
    {
        const u32 f = ((l15 >> 3) & 1) | ((g & 1) << 1);
        const u32 rb = (kh ? L_V : L_K) + (8 * g + (l15 >> 2)) * 128 + (l15 & 3) * 8;
#pragma unroll
        for (int nb = 0; nb < 4; ++nb) {
            u32 a = rb + ((nb ^ f) * 32);
            asm volatile("" : "+v"(a));
#pragma unroll
            for (int st = 0; st < 8; ++st) {
                const u32 o_ = (st >> 2) * 32768 + (st & 3) * 4096;
                af[nb][st] = cat(trread(lds, a + o_), trread(lds, a + o_ + 512));
            }
        }
    }
    STAMP(14);
    asm volatile("s_waitcnt vmcnt(1)" : "+v"(xe), "+v"(gl), "+v"(Y0), "+v"(Y1), "+v"(Y2), "+v"(Y3), "+v"(Y4), "+v"(Y5), "+v"(Y6) :: "memory");
    if (owner) {
        u32 spins = 0;
#define YMX(v) max(max(v[0], v[1]), max(v[2], v[3]))
        while (!__all(max(max(max(YMX(Y0), YMX(Y1)), max(YMX(Y2), YMX(Y3))), max(max(YMX(Y4), YMX(Y5)), YMX(Y6))) != POISON) && ++spins <= SPIN_MAX) {
            u32 yo = yo0;
            asm volatile("global_load_dwordx4 %0, %[yo], %[ws] sc1\n v_add_u32 %[yo], 0x2000, %[yo]\n"
                         "global_load_dwordx4 %1, %[yo], %[ws] sc1\n v_add_u32 %[yo], 0x2000, %[yo]\n"
                         "global_load_dwordx4 %2, %[yo], %[ws] sc1\n v_add_u32 %[yo], 0x2000, %[yo]\n"
                         "global_load_dwordx4 %3, %[yo], %[ws] sc1\n v_add_u32 %[yo], 0x2000, %[yo]\n"
                         "global_load_dwordx4 %4, %[yo], %[ws] sc1\n v_add_u32 %[yo], 0x2000, %[yo]\n"
                         "global_load_dwordx4 %5, %[yo], %[ws] sc1\n v_add_u32 %[yo], 0x2000, %[yo]\n"
                         "global_load_dwordx4 %6, %[yo], %[ws] sc1\n s_waitcnt vmcnt(0)"
                         : "=&v"(Y0), "=&v"(Y1), "=&v"(Y2), "=&v"(Y3), "=&v"(Y4), "=&v"(Y5), "=&v"(Y6), [yo] "+v"(yo)
                         : [ws] "s"(ws) : "memory");
        }
        f32x4 ys;
#pragma unroll
        for (int i = 0; i < 4; ++i) {
            float v = ((__uint_as_float(Y0[i]) + __uint_as_float(Y1[i])) + (__uint_as_float(Y2[i]) + __uint_as_float(Y3[i])))
                    + ((__uint_as_float(Y4[i]) + __uint_as_float(Y5[i])) + __uint_as_float(Y6[i]));
            auto a = __builtin_amdgcn_permlane32_swap(__float_as_uint(v), __float_as_uint(v), false, false);
            v = __uint_as_float(a[0]) + __uint_as_float(a[1]);
            auto c = __builtin_amdgcn_permlane16_swap(__float_as_uint(v), __float_as_uint(v), false, false);
            ys[i] = __uint_as_float(c[0]) + __uint_as_float(c[1]);
        }
        if (g == 0) ST128(L_YP + w * 256 + l15 * 16, __builtin_bit_cast(u32x4, ys));
    }
    u32x4 of0, of1, of2, of3, of4, of5, of6, of7;
    {
        const u32 oo = WS_O + ((tb * 4 + tq) * 16 + 8 * kh) * 1024 + lane * 16, o2 = oo + 4096;
        u32 spins = 0;
        while (true) {
            asm volatile("global_load_dwordx4 %0, %8, %10 sc1\n\tglobal_load_dwordx4 %1, %8, %10 offset:1024 sc1\n\t"
                         "global_load_dwordx4 %2, %8, %10 offset:2048 sc1\n\tglobal_load_dwordx4 %3, %8, %10 offset:3072 sc1\n\t"
                         "global_load_dwordx4 %4, %9, %10 sc1\n\tglobal_load_dwordx4 %5, %9, %10 offset:1024 sc1\n\t"
                         "global_load_dwordx4 %6, %9, %10 offset:2048 sc1\n\tglobal_load_dwordx4 %7, %9, %10 offset:3072 sc1\n\t"
                         "s_waitcnt vmcnt(0)"
                         : "=&v"(of0), "=&v"(of1), "=&v"(of2), "=&v"(of3), "=&v"(of4), "=&v"(of5), "=&v"(of6), "=&v"(of7)
                         : "v"(oo), "v"(o2), "s"(ws) : "memory");
            u32 mx = 0;
#define OMX(v) mx = max(max(mx, max(v[0], v[1])), max(v[2], v[3]));
            OMX(of0) OMX(of1) OMX(of2) OMX(of3) OMX(of4) OMX(of5) OMX(of6) OMX(of7)
            if (__all(mx != POISON) || ++spins > SPIN_MAX) break;
        }
    }
    STAMP(15);
    f32x4 acc[4] = {{0, 0, 0, 0}, {0, 0, 0, 0}, {0, 0, 0, 0}, {0, 0, 0, 0}};
#define OSTEP(OF, ST) \
    _Pragma("unroll") for (int nb = 0; nb < 4; ++nb) acc[nb] = __builtin_amdgcn_mfma_f32_16x16x32_bf16(asbf(af[nb][ST]), asbf(OF), acc[nb], 0, 0, 0);
    OSTEP(of0, 0) OSTEP(of1, 1) OSTEP(of2, 2) OSTEP(of3, 3) OSTEP(of4, 4) OSTEP(of5, 5) OSTEP(of6, 6) OSTEP(of7, 7)
    STAMP(16);
    {
        f32x4 k0 = kh ? acc[2] : acc[0], k1 = kh ? acc[3] : acc[1];
        f32x4 g0 = kh ? acc[0] : acc[2], g1 = kh ? acc[1] : acc[3];
        ST128(L_K + 16384 + w * 2048 + lane * 16, __builtin_bit_cast(u32x4, g0));
        ST128(L_K + 16384 + w * 2048 + 1024 + lane * 16, __builtin_bit_cast(u32x4, g1));
        BAR();
        k0 += __builtin_bit_cast(f32x4, LD128(L_K + 16384 + (w ^ 4) * 2048 + lane * 16));
        k1 += __builtin_bit_cast(f32x4, LD128(L_K + 16384 + (w ^ 4) * 2048 + 1024 + lane * 16));
        float a[8];
#pragma unroll
        for (int i = 0; i < 4; ++i) {
            auto sw = __builtin_amdgcn_permlane16_swap(__float_as_uint(k0[i]), __float_as_uint(k1[i]), false, false);
            a[i] = __uint_as_float(sw[0]); a[4 + i] = __uint_as_float(sw[1]);
        }
        const float gt = bfr(sigm(bfr(__uint_as_float(gl))));
        const float og = bfr(1.0f - gt);
        float bb[8];
#pragma unroll
        for (int i = 0; i < 4; ++i) { bb[2 * i] = lo16(xe[i]); bb[2 * i + 1] = hi16(xe[i]); }
        if (owner && tokg == sel) {
            const u32 rord = key >> 16;
            const float rsel = __uint_as_float(((rord & 0x8000u) ? (rord & 0x7fffu) : (~rord & 0xffffu)) << 16);
            const float sg = bfr(sigm(rsel));
            const int c0 = kh * 32 + (g & 1) * 16 + (g >> 1) * 8;
#pragma unroll
            for (int i = 0; i < 8; ++i) {
                float y = 0.f;
#pragma unroll
                for (int p = 0; p < 8; ++p) y += LDF(L_YP + p * 256 + (c0 + i) * 4);
                bb[i] = bfr(bb[i] + bfr(bfr(y) * sg));
            }
        }
        u32x4 ov;
#pragma unroll
        for (int i = 0; i < 4; ++i)
            ov[i] = pk(bfr(gt * bfr(a[2 * i])) + bfr(og * bb[2 * i]), bfr(gt * bfr(a[2 * i + 1])) + bfr(og * bb[2 * i + 1]));
        u32 oo_ = tokg * 1024 + ncol * 2;
        GST128(out, oo_, ov);
    }
    STAMP(17); STAMPC(21);
    BAR();
    if (tid == 0 && (b & 31) == 5) {
        u32 t1 = LD32(L_CT + 64 + 1*4), t10 = LD32(L_CT + 64 + 10*4), t12 = LD32(L_CT + 64 + 12*4), t17 = LD32(L_CT + 64 + 17*4);
        u32 c18 = LD32(L_CT + 64 + 18*4), c19 = LD32(L_CT + 64 + 19*4), c20 = LD32(L_CT + 64 + 20*4), c21 = LD32(L_CT + 64 + 21*4);
        u32 t2 = LD32(L_CT + 64 + 2*4), t5 = LD32(L_CT + 64 + 5*4), t9 = LD32(L_CT + 64 + 9*4), t15 = LD32(L_CT + 64 + 15*4);
        printf("STMP b %d t0 %u prio %u bar2 %u b2b %u att0 %u att %u oseen %u end %u cyc %u %u %u\n", b, t1, t2 - t1, t5 - t1, t9 - t1, t10 - t1, t12 - t1, t15 - t1, t17 - t1, c19 - c18, c20 - c19, c21 - c20);
    }
    }
}

extern "C" void kernel_launch(void* const* d_in, const int* in_sizes, int n_in, void* d_out, int out_size,
                              void* d_ws, size_t ws_size, hipStream_t stream) {
    (void)in_sizes; (void)n_in; (void)out_size; (void)ws_size;
    fused_kernel<<<256, 512, 0, stream>>>((const u16*)d_in[0], (const u16*)d_in[1], (const u16*)d_in[2], (const u16*)d_in[3],
                                          (const u16*)d_in[4], (const u16*)d_in[6], (const u16*)d_in[7], (const u16*)d_in[8],
                                          (const u16*)d_in[9], (const u16*)d_in[10], (u16*)d_out, (char*)d_ws);
}
```

```cpp
#include <hip/hip_runtime.h>
#include <stdint.h>

typedef unsigned int u32;
typedef unsigned long long u64;
typedef unsigned short u16;
typedef u32 u32x2 __attribute__((ext_vector_type(2)));
typedef u32 u32x4 __attribute__((ext_vector_type(4)));
typedef u32 u32x8 __attribute__((ext_vector_type(8)));
typedef u32 u32x16 __attribute__((ext_vector_type(16)));
typedef float f32x2 __attribute__((ext_vector_type(2)));
typedef float f32x4 __attribute__((ext_vector_type(4)));
typedef float f32x16 __attribute__((ext_vector_type(16)));
typedef __bf16 bf16x2 __attribute__((ext_vector_type(2)));
typedef __bf16 bf16x8 __attribute__((ext_vector_type(8)));
#define LDSP __attribute__((address_space(3)))

constexpr u32 WS_E   = 0;
constexpr u32 WS_F2  = 4096;
constexpr u32 WS_G   = 24576;
constexpr u32 WS_K   = 65536;
constexpr u32 WS_V   = 131072;
constexpr u32 WS_F1  = 262144;
constexpr u32 WS_O   = 524288;
constexpr u32 WS_Y   = 2621440;
constexpr u32 WS_DBG = 4194304;

constexpr u32 L_K   = 0;
constexpr u32 L_V   = 65536;
constexpr u32 L_ONE = 131072;
constexpr u32 L_W   = 135168;
constexpr u32 L_M   = 151552;
constexpr u32 L_P1  = L_M;
constexpr u32 L_PD  = L_M + 8192;
constexpr u32 L_FP  = L_M + 8704;
constexpr u32 L_YP  = L_M + 9216;
constexpr u32 L_CT  = L_M + 11264;
constexpr u32 L_END = 163840;

constexpr u32 SPIN_MAX = 1u << 22;
constexpr u32 POISON = 0xffffffffu;

#define LD128(a) (*(const LDSP u32x4*)(lds + (a)))
#define ST128(a, v) (*(LDSP u32x4*)(lds + (a)) = (v))
#define LD32(a) (*(const LDSP u32*)(lds + (a)))
#define ST32(a, v) (*(LDSP u32*)(lds + (a)) = (v))
#define LDF(a) (*(const LDSP float*)(lds + (a)))
#define STF(a, v) (*(LDSP float*)(lds + (a)) = (v))

__device__ __forceinline__ u32x2 trread(LDSP char* lds, u32 a) {
    typedef short v4s __attribute__((ext_vector_type(4)));
    v4s r = __builtin_amdgcn_ds_read_tr16_b64_v4i16((LDSP v4s*)(lds + a));
    return __builtin_bit_cast(u32x2, r);
}
__device__ __forceinline__ bf16x8 asbf(u32x4 v) { return __builtin_bit_cast(bf16x8, v); }
__device__ __forceinline__ u32x4 cat(u32x2 a, u32x2 b) { u32x4 r = {a[0], a[1], b[0], b[1]}; return r; }
__device__ __forceinline__ u32 pk(float a, float b) {
    f32x2 f = {a, b};
    return __builtin_bit_cast(u32, __builtin_convertvector(f, bf16x2));
}
__device__ __forceinline__ float bfr(float a) {
    return __uint_as_float(pk(a, a) & 0xffff0000u);
}
__device__ __forceinline__ float lo16(u32 v) { return __uint_as_float(v << 16); }
__device__ __forceinline__ float hi16(u32 v) { return __uint_as_float(v & 0xffff0000u); }
__device__ __forceinline__ float sigm(float z) { return __builtin_amdgcn_rcpf(1.0f + __builtin_amdgcn_exp2f(-1.4426950408889634f * z)); }

__device__ __forceinline__ void dma16(const void* base, u32 voff, u32 ldsaddr) {
    asm volatile("s_mov_b32 m0, %0\n\ts_nop 0\n\tglobal_load_lds_dwordx4 %1, %2" :: "s"(ldsaddr), "v"(voff), "s"(base) : "memory");
}
__device__ __forceinline__ void dma16c4(const void* base, u32 voff, u32 ldsaddr) {
    asm volatile("s_mov_b32 m0, %0\n\ts_nop 0\n\tglobal_load_lds_dwordx4 %1, %2 sc1\n\tglobal_load_lds_dwordx4 %1, %2 offset:1024 sc1\n\t"
                 "global_load_lds_dwordx4 %1, %2 offset:2048 sc1\n\tglobal_load_lds_dwordx4 %1, %2 offset:3072 sc1"
                 :: "s"(ldsaddr), "v"(voff), "s"(base) : "memory");
}
__device__ __forceinline__ void dma16c3(const void* base, u32 voff, u32 ldsaddr) {
    asm volatile("s_mov_b32 m0, %0\n\ts_nop 0\n\tglobal_load_lds_dwordx4 %1, %2 sc1\n\tglobal_load_lds_dwordx4 %1, %2 offset:1024 sc1\n\t"
                 "global_load_lds_dwordx4 %1, %2 offset:2048 sc1"
                 :: "s"(ldsaddr), "v"(voff), "s"(base) : "memory");
}
__device__ __forceinline__ void dma16c2(const void* base, u32 voff, u32 ldsaddr) {
    asm volatile("s_mov_b32 m0, %0\n\ts_nop 0\n\tglobal_load_lds_dwordx4 %1, %2 sc1\n\tglobal_load_lds_dwordx4 %1, %2 offset:1024 sc1"
                 :: "s"(ldsaddr), "v"(voff), "s"(base) : "memory");
}
__device__ __forceinline__ void dma16c(const void* base, u32 voff, u32 ldsaddr) {
    asm volatile("s_mov_b32 m0, %0\n\ts_nop 0\n\tglobal_load_lds_dwordx4 %1, %2 sc1" :: "s"(ldsaddr), "v"(voff), "s"(base) : "memory");
}
#define GLD128(dst, base, voff, OFF)  asm volatile("global_load_dwordx4 %0, %1, %2 offset:" #OFF : "=v"(dst) : "v"(voff), "s"(base) : "memory")
#define GLD128C(dst, base, voff, OFF) asm volatile("global_load_dwordx4 %0, %1, %2 offset:" #OFF " sc1" : "=v"(dst) : "v"(voff), "s"(base) : "memory")
#define GLD64C(dst, base, voff, OFF)  asm volatile("global_load_dwordx2 %0, %1, %2 offset:" #OFF " sc1" : "=v"(dst) : "v"(voff), "s"(base) : "memory")
#define GLD32C(dst, base, voff, OFF)  asm volatile("global_load_dword %0, %1, %2 offset:" #OFF " sc1" : "=v"(dst) : "v"(voff), "s"(base) : "memory")
#define GLD32(dst, base, voff, OFF)   asm volatile("global_load_dword %0, %1, %2 offset:" #OFF : "=v"(dst) : "v"(voff), "s"(base) : "memory")
#define GLD16(dst, base, voff, OFF)   asm volatile("global_load_ushort %0, %1, %2 offset:" #OFF : "=v"(dst) : "v"(voff), "s"(base) : "memory")
#define GST128(base, voff, val)  asm volatile("global_store_dwordx4 %0, %1, %2\n\ts_nop 1" :: "v"(voff), "v"(val), "s"(base) : "memory")
#define GST128C(base, voff, val) asm volatile("global_store_dwordx4 %0, %1, %2 sc1\n\ts_nop 1" :: "v"(voff), "v"(val), "s"(base) : "memory")
#define GST64C(base, voff, val)  asm volatile("global_store_dwordx2 %0, %1, %2 sc1\n\ts_nop 0" :: "v"(voff), "v"(val), "s"(base) : "memory")
#define GST32C(base, voff, val)  asm volatile("global_store_dword %0, %1, %2 sc1" :: "v"(voff), "v"(val), "s"(base) : "memory")
#define VMWAIT(N) asm volatile("s_waitcnt vmcnt(" #N ")" ::: "memory")
#define BAR() asm volatile("s_waitcnt lgkmcnt(0)\n\ts_barrier" ::: "memory")

#define STAMP(i)
#define STAMPC(i)
#define STAMPDUMP()
#define FSTAMP(i) STAMP(i)
#define LSTAMP(i)

__device__ __forceinline__ u32 wave_max_u32(u32 v) {
    v = max(v, (u32)__builtin_amdgcn_update_dpp(0, (int)v, 0x111, 0xf, 0xf, false));
    v = max(v, (u32)__builtin_amdgcn_update_dpp(0, (int)v, 0x112, 0xf, 0xf, false));
    v = max(v, (u32)__builtin_amdgcn_update_dpp(0, (int)v, 0x114, 0xf, 0xf, false));
    v = max(v, (u32)__builtin_amdgcn_update_dpp(0, (int)v, 0x118, 0xf, 0xf, false));
    v = max(v, (u32)__builtin_amdgcn_update_dpp(0, (int)v, 0x142, 0xa, 0xf, false));
    v = max(v, (u32)__builtin_amdgcn_update_dpp(0, (int)v, 0x143, 0xc, 0xf, false));
    return (u32)__builtin_amdgcn_readlane((int)v, 63);
}
#define ATT_A \
    "s_mov_b32 %[cnt], 2\n" \
    "v_mov_b32 v232, -1\n" \
    "v_mov_b32 v233, -1\n" \
    "v_mov_b32 v234, -1\n" \
    "v_mov_b32 v235, -1\n" \
    "1:\n" \
    "v_exp_f32 v64, v64\n" \
    "v_exp_f32 v65, v65\n" \
    "v_smfmac_f32_32x32x32_bf16 v[144:159], v[188:191], v[120:127], v192\n" \
    "global_store_dwordx4 v220, v[232:235], %[wsb] sc1\n" \
    "ds_read_b128 v[184:187], v194 offset:1024\n" \
    "v_exp_f32 v66, v66\n" \
    "v_exp_f32 v67, v67\n" \
    "v_exp_f32 v68, v68\n" \
    "v_exp_f32 v69, v69\n" \
    "s_waitcnt lgkmcnt(1)\n" \
    "v_mfma_f32_32x32x16_bf16 v[80:95], v[172:175], v[160:163], 0\n" \
    "ds_read_b128 v[188:191], v194 offset:1536\n" \
    "v_exp_f32 v70, v70\n" \
    "v_exp_f32 v71, v71\n" \
    "v_exp_f32 v72, v72\n" \
    "v_exp_f32 v73, v73\n" \
    "v_smfmac_f32_32x32x32_bf16 v[128:143], v[176:179], v[64:71], v192\n" \
    "v_exp_f32 v74, v74\n" \
    "s_mov_b32 m0, %[skv]\n" \
    "v_exp_f32 v75, v75\n" \
    "v_exp_f32 v76, v76\n" \
    "global_load_lds_dwordx4 v196, %[kvb] offset:0 sc1\n" \
    "v_exp_f32 v77, v77\n" \
    "v_exp_f32 v78, v78\n" \
    "v_exp_f32 v79, v79\n" \
    "v_exp_f32 v96, v96\n" \
    "v_exp_f32 v97, v97\n" \
    "v_smfmac_f32_32x32x32_bf16 v[128:143], v[180:183], v[72:79], v192\n" \
    "v_exp_f32 v98, v98\n" \
    "v_exp_f32 v99, v99\n" \
    "v_exp_f32 v100, v100\n" \
    "v_exp_f32 v101, v101\n" \
    "v_mfma_f32_32x32x16_bf16 v[112:127], v[172:175], v[164:167], 0\n" \
    "ds_read_b128 v[168:171], v193 offset:2048\n" \
    "v_exp_f32 v102, v102\n" \
    "v_exp_f32 v103, v103\n" \
    "v_exp_f32 v104, v104\n" \
    "v_exp_f32 v105, v105\n" \
    "v_smfmac_f32_32x32x32_bf16 v[144:159], v[176:179], v[96:103], v192\n" \
    "v_exp_f32 v106, v106\n" \
    "v_exp_f32 v107, v107\n" \
    "v_exp_f32 v108, v108\n" \
    "v_exp_f32 v109, v109\n" \
    "v_exp_f32 v110, v110\n" \
    "v_exp_f32 v111, v111\n" \
    "v_exp_f32 v80, v80\n" \
    "v_exp_f32 v81, v81\n" \
    "v_smfmac_f32_32x32x32_bf16 v[144:159], v[180:183], v[104:111], v192\n" \
    "ds_read_b128 v[176:179], v194 offset:2048\n" \
    "v_exp_f32 v82, v82\n" \
    "v_exp_f32 v83, v83\n" \
    "v_exp_f32 v84, v84\n" \
    "v_exp_f32 v85, v85\n" \
    "s_waitcnt lgkmcnt(1)\n" \
    "v_mfma_f32_32x32x16_bf16 v[64:79], v[168:171], v[160:163], 0\n" \
    "ds_read_b128 v[180:183], v194 offset:2560\n" \
    "v_exp_f32 v86, v86\n" \
    "v_exp_f32 v87, v87\n" \
    "v_exp_f32 v88, v88\n" \
    "v_exp_f32 v89, v89\n" \
    "v_smfmac_f32_32x32x32_bf16 v[128:143], v[184:187], v[80:87], v192\n" \
    "v_exp_f32 v90, v90\n" \
    "s_mov_b32 m0, %[skv]\n" \
    "v_exp_f32 v91, v91\n" \
    "v_exp_f32 v92, v92\n" \
    "global_load_lds_dwordx4 v196, %[kvb] offset:1024 sc1\n" \
    "v_exp_f32 v93, v93\n" \
    "v_exp_f32 v94, v94\n" \
    "v_exp_f32 v95, v95\n" \
    "v_exp_f32 v112, v112\n" \
    "v_exp_f32 v113, v113\n" \
    "v_smfmac_f32_32x32x32_bf16 v[128:143], v[188:191], v[88:95], v192\n" \
    "v_exp_f32 v114, v114\n" \
    "v_exp_f32 v115, v115\n" \
    "v_exp_f32 v116, v116\n" \
    "v_exp_f32 v117, v117\n" \
    "v_mfma_f32_32x32x16_bf16 v[96:111], v[168:171], v[164:167], 0\n" \
    "ds_read_b128 v[172:175], v193 offset:3072\n" \
    "v_exp_f32 v118, v118\n" \
    "v_exp_f32 v119, v119\n" \
    "v_exp_f32 v120, v120\n" \
    "v_exp_f32 v121, v121\n" \
    "v_smfmac_f32_32x32x32_bf16 v[144:159], v[184:187], v[112:119], v192\n" \
    "v_exp_f32 v122, v122\n" \
    "s_mov_b32 m0, %[sg]\n" \
    "v_exp_f32 v123, v123\n" \
    "v_exp_f32 v124, v124\n" \
    "global_load_lds_dwordx4 v[198:199], off\n" \
    "v_exp_f32 v125, v125\n" \
    "v_exp_f32 v126, v126\n" \
    "v_exp_f32 v127, v127\n" \
    "s_cmp_eq_u32 %[cnt], 1\n" \
    "s_cbranch_scc0 8f\n" \
    "s_setprio 0\n" \
    "8:\n" \
    "v_exp_f32 v64, v64\n" \
    "v_exp_f32 v65, v65\n" \
    "v_smfmac_f32_32x32x32_bf16 v[144:159], v[188:191], v[120:127], v192\n" \
    "ds_read_b128 v[184:187], v194 offset:3072\n" \
    "v_exp_f32 v66, v66\n" \
    "v_exp_f32 v67, v67\n" \
    "v_exp_f32 v68, v68\n" \
    "v_exp_f32 v69, v69\n" \
    "s_waitcnt lgkmcnt(1)\n" \
    "v_mfma_f32_32x32x16_bf16 v[80:95], v[172:175], v[160:163], 0\n" \
    "ds_read_b128 v[188:191], v194 offset:3584\n" \
    "v_exp_f32 v70, v70\n" \
    "v_exp_f32 v71, v71\n" \
    "v_exp_f32 v72, v72\n" \
    "v_exp_f32 v73, v73\n" \
    "v_smfmac_f32_32x32x32_bf16 v[128:143], v[176:179], v[64:71], v192\n" \
    "v_exp_f32 v74, v74\n" \
    "s_mov_b32 m0, %[skv]\n" \
    "v_exp_f32 v75, v75\n" \
    "v_exp_f32 v76, v76\n" \
    "global_load_lds_dwordx4 v196, %[kvb] offset:2048 sc1\n" \
    "v_exp_f32 v77, v77\n" \
    "v_exp_f32 v78, v78\n" \
    "v_exp_f32 v79, v79\n" \
    "v_exp_f32 v96, v96\n" \
    "v_exp_f32 v97, v97\n" \
    "v_smfmac_f32_32x32x32_bf16 v[128:143], v[180:183], v[72:79], v192\n" \
    "v_exp_f32 v98, v98\n" \
    "v_exp_f32 v99, v99\n" \
    "v_exp_f32 v100, v100\n" \
    "v_exp_f32 v101, v101\n" \
    "v_mfma_f32_32x32x16_bf16 v[112:127], v[172:175], v[164:167], 0\n" \
    "ds_read_b128 v[168:171], v193 offset:4096\n" \
    "v_exp_f32 v102, v102\n" \
    "v_exp_f32 v103, v103\n" \
    "v_exp_f32 v104, v104\n" \
    "v_exp_f32 v105, v105\n" \
    "v_smfmac_f32_32x32x32_bf16 v[144:159], v[176:179], v[96:103], v192\n" \
    "v_exp_f32 v106, v106\n" \
    "v_exp_f32 v107, v107\n" \
    "v_exp_f32 v108, v108\n" \
    "v_exp_f32 v109, v109\n" \
    "v_exp_f32 v110, v110\n" \
    "v_exp_f32 v111, v111\n" \
    "v_exp_f32 v80, v80\n" \
    "v_exp_f32 v81, v81\n" \
    "v_smfmac_f32_32x32x32_bf16 v[144:159], v[180:183], v[104:111], v192\n" \
    "ds_read_b128 v[176:179], v194 offset:4096\n" \
    "v_exp_f32 v82, v82\n" \
    "v_exp_f32 v83, v83\n" \
    "v_exp_f32 v84, v84\n" \
    "v_exp_f32 v85, v85\n" \
    "s_waitcnt lgkmcnt(1)\n" \
    "v_mfma_f32_32x32x16_bf16 v[64:79], v[168:171], v[160:163], 0\n" \
    "ds_read_b128 v[180:183], v194 offset:4608\n" \
    "v_exp_f32 v86, v86\n" \
    "v_exp_f32 v87, v87\n" \
    "v_exp_f32 v88, v88\n" \
    "v_exp_f32 v89, v89\n" \
    "v_smfmac_f32_32x32x32_bf16 v[128:143], v[184:187], v[80:87], v192\n" \
    "v_exp_f32 v90, v90\n" \
    "s_mov_b32 m0, %[skv]\n" \
    "v_exp_f32 v91, v91\n" \
    "v_exp_f32 v92, v92\n" \
    "global_load_lds_dwordx4 v196, %[kvb] offset:3072 sc1\n" \
    "v_exp_f32 v93, v93\n" \
    "v_exp_f32 v94, v94\n" \
    "v_exp_f32 v95, v95\n" \
    "v_exp_f32 v112, v112\n" \
    "v_exp_f32 v113, v113\n" \
    "v_smfmac_f32_32x32x32_bf16 v[128:143], v[188:191], v[88:95], v192\n" \
    "v_exp_f32 v114, v114\n" \
    "v_exp_f32 v115, v115\n" \
    "v_exp_f32 v116, v116\n" \
    "v_exp_f32 v117, v117\n" \
    "v_mfma_f32_32x32x16_bf16 v[96:111], v[168:171], v[164:167], 0\n" \
    "ds_read_b128 v[172:175], v193 offset:5120\n" \
    "v_exp_f32 v118, v118\n" \
    "v_exp_f32 v119, v119\n" \
    "v_exp_f32 v120, v120\n" \
    "v_exp_f32 v121, v121\n" \
    "v_smfmac_f32_32x32x32_bf16 v[144:159], v[184:187], v[112:119], v192\n" \
    "v_exp_f32 v122, v122\n" \
    "v_exp_f32 v123, v123\n" \
    "v_exp_f32 v124, v124\n" \
    "v_exp_f32 v125, v125\n" \
    "v_exp_f32 v126, v126\n" \
    "v_exp_f32 v127, v127\n" \
    "v_add_u32 v196, 0x2000, v196\n" \
    "v_mov_b32 v198, v200\n" \
    "v_mov_b32 v199, v201\n" \
    "s_add_u32 %[skv], %[skv], 0x2000\n" \
    "s_add_u32 %[sg], %[sg], 0x400\n" \
    "v_mov_b32 v220, v221\n" \
    "v_add_u32 v193, 4096, v193\n" \
    "v_add_u32 v194, v195, v194\n" \
    "s_sub_u32 %[cnt], %[cnt], 1\n" \
    "s_cmp_lg_u32 %[cnt], 0\n" \
    "s_cbranch_scc1 1b\n" \
    "s_waitcnt vmcnt(5)\n"

#define ATT_B \
    "v_exp_f32 v64, v64\n" \
    "v_exp_f32 v65, v65\n" \
    "v_smfmac_f32_32x32x32_bf16 v[144:159], v[188:191], v[120:127], v192\n" \
    "ds_read_b128 v[184:187], v194 offset:1024\n" \
    "v_exp_f32 v66, v66\n" \
    "v_exp_f32 v67, v67\n" \
    "v_exp_f32 v68, v68\n" \
    "v_exp_f32 v69, v69\n" \
    "s_waitcnt lgkmcnt(1)\n" \
    "v_mfma_f32_32x32x16_bf16 v[80:95], v[172:175], v[160:163], 0\n" \
    "ds_read_b128 v[188:191], v194 offset:1536\n" \
    "v_exp_f32 v70, v70\n" \
    "v_exp_f32 v71, v71\n" \
    "v_exp_f32 v72, v72\n" \
    "v_exp_f32 v73, v73\n" \
    "v_smfmac_f32_32x32x32_bf16 v[128:143], v[176:179], v[64:71], v192\n" \
    "v_exp_f32 v74, v74\n" \
    "s_mov_b32 m0, %[skv3]\n" \
    "v_exp_f32 v75, v75\n" \
    "v_exp_f32 v76, v76\n" \
    "global_load_dwordx4 v[204:207], v202, %[xb]\n" \
    "v_exp_f32 v77, v77\n" \
    "v_exp_f32 v78, v78\n" \
    "v_exp_f32 v79, v79\n" \
    "v_exp_f32 v96, v96\n" \
    "v_exp_f32 v97, v97\n" \
    "v_smfmac_f32_32x32x32_bf16 v[128:143], v[180:183], v[72:79], v192\n" \
    "v_exp_f32 v98, v98\n" \
    "v_exp_f32 v99, v99\n" \
    "v_exp_f32 v100, v100\n" \
    "v_exp_f32 v101, v101\n" \
    "v_mfma_f32_32x32x16_bf16 v[112:127], v[172:175], v[164:167], 0\n" \
    "ds_read_b128 v[168:171], v193 offset:2048\n" \
    "v_exp_f32 v102, v102\n" \
    "v_exp_f32 v103, v103\n" \
    "v_exp_f32 v104, v104\n" \
    "v_exp_f32 v105, v105\n" \
    "v_smfmac_f32_32x32x32_bf16 v[144:159], v[176:179], v[96:103], v192\n" \
    "v_exp_f32 v106, v106\n" \
    "v_exp_f32 v107, v107\n" \
    "v_exp_f32 v108, v108\n" \
    "global_load_dwordx4 v[208:211], v202, %[xb] offset:16\n" \
    "v_exp_f32 v109, v109\n" \
    "v_exp_f32 v110, v110\n" \
    "v_exp_f32 v111, v111\n" \
    "v_exp_f32 v80, v80\n" \
    "v_exp_f32 v81, v81\n" \
    "v_smfmac_f32_32x32x32_bf16 v[144:159], v[180:183], v[104:111], v192\n" \
    "ds_read_b128 v[176:179], v194 offset:2048\n" \
    "v_exp_f32 v82, v82\n" \
    "v_exp_f32 v83, v83\n" \
    "v_exp_f32 v84, v84\n" \
    "v_exp_f32 v85, v85\n" \
    "s_waitcnt lgkmcnt(1)\n" \
    "v_mfma_f32_32x32x16_bf16 v[64:79], v[168:171], v[160:163], 0\n" \
    "ds_read_b128 v[180:183], v194 offset:2560\n" \
    "v_exp_f32 v86, v86\n" \
    "v_exp_f32 v87, v87\n" \
    "v_exp_f32 v88, v88\n" \
    "v_exp_f32 v89, v89\n" \
    "v_smfmac_f32_32x32x32_bf16 v[128:143], v[184:187], v[80:87], v192\n" \
    "v_exp_f32 v90, v90\n" \
    "v_exp_f32 v91, v91\n" \
    "v_exp_f32 v92, v92\n" \
    "global_load_lds_dwordx4 v197, %[kvb] offset:0 sc1\n" \
    "v_exp_f32 v93, v93\n" \
    "v_exp_f32 v94, v94\n" \
    "v_exp_f32 v95, v95\n" \
    "v_exp_f32 v112, v112\n" \
    "v_exp_f32 v113, v113\n" \
    "v_smfmac_f32_32x32x32_bf16 v[128:143], v[188:191], v[88:95], v192\n" \
    "v_exp_f32 v114, v114\n" \
    "v_exp_f32 v115, v115\n" \
    "v_exp_f32 v116, v116\n" \
    "v_exp_f32 v117, v117\n" \
    "v_mfma_f32_32x32x16_bf16 v[96:111], v[168:171], v[164:167], 0\n" \
    "ds_read_b128 v[172:175], v193 offset:3072\n" \
    "v_exp_f32 v118, v118\n" \
    "v_exp_f32 v119, v119\n" \
    "v_exp_f32 v120, v120\n" \
    "v_exp_f32 v121, v121\n" \
    "v_smfmac_f32_32x32x32_bf16 v[144:159], v[184:187], v[112:119], v192\n" \
    "v_exp_f32 v122, v122\n" \
    "v_exp_f32 v123, v123\n" \
    "v_exp_f32 v124, v124\n" \
    "global_load_lds_dwordx4 v197, %[kvb] offset:1024 sc1\n" \
    "v_exp_f32 v125, v125\n" \
    "v_exp_f32 v126, v126\n" \
    "v_exp_f32 v127, v127\n" \
    "v_exp_f32 v64, v64\n" \
    "v_exp_f32 v65, v65\n" \
    "v_smfmac_f32_32x32x32_bf16 v[144:159], v[188:191], v[120:127], v192\n" \
    "ds_read_b128 v[184:187], v194 offset:3072\n" \
    "v_exp_f32 v66, v66\n" \
    "v_exp_f32 v67, v67\n" \
    "v_exp_f32 v68, v68\n" \
    "v_exp_f32 v69, v69\n" \
    "s_waitcnt lgkmcnt(1)\n" \
    "v_mfma_f32_32x32x16_bf16 v[80:95], v[172:175], v[160:163], 0\n" \
    "ds_read_b128 v[188:191], v194 offset:3584\n" \
    "v_exp_f32 v70, v70\n" \
    "v_exp_f32 v71, v71\n" \
    "v_exp_f32 v72, v72\n" \
    "v_exp_f32 v73, v73\n" \
    "v_smfmac_f32_32x32x32_bf16 v[128:143], v[176:179], v[64:71], v192\n" \
    "v_exp_f32 v74, v74\n" \
    "v_exp_f32 v75, v75\n" \
    "v_exp_f32 v76, v76\n" \
    "global_load_lds_dwordx4 v197, %[kvb] offset:2048 sc1\n" \
    "v_exp_f32 v77, v77\n" \
    "v_exp_f32 v78, v78\n" \
    "v_exp_f32 v79, v79\n" \
    "v_exp_f32 v96, v96\n" \
    "v_exp_f32 v97, v97\n" \
    "v_smfmac_f32_32x32x32_bf16 v[128:143], v[180:183], v[72:79], v192\n" \
    "v_exp_f32 v98, v98\n" \
    "v_exp_f32 v99, v99\n" \
    "v_exp_f32 v100, v100\n" \
    "v_exp_f32 v101, v101\n" \
    "v_mfma_f32_32x32x16_bf16 v[112:127], v[172:175], v[164:167], 0\n" \
    "ds_read_b128 v[168:171], v193 offset:4096\n" \
    "v_exp_f32 v102, v102\n" \
    "v_exp_f32 v103, v103\n" \
    "v_exp_f32 v104, v104\n" \
    "v_exp_f32 v105, v105\n" \
    "v_smfmac_f32_32x32x32_bf16 v[144:159], v[176:179], v[96:103], v192\n" \
    "v_exp_f32 v106, v106\n" \
    "v_exp_f32 v107, v107\n" \
    "v_exp_f32 v108, v108\n" \
    "global_load_lds_dwordx4 v197, %[kvb] offset:3072 sc1\n" \
    "v_exp_f32 v109, v109\n" \
    "v_exp_f32 v110, v110\n" \
    "v_exp_f32 v111, v111\n" \
    "v_exp_f32 v80, v80\n" \
    "v_exp_f32 v81, v81\n" \
    "v_smfmac_f32_32x32x32_bf16 v[144:159], v[180:183], v[104:111], v192\n" \
    "ds_read_b128 v[176:179], v194 offset:4096\n" \
    "v_exp_f32 v82, v82\n" \
    "v_exp_f32 v83, v83\n" \
    "v_exp_f32 v84, v84\n" \
    "v_exp_f32 v85, v85\n" \
    "s_waitcnt lgkmcnt(1)\n" \
    "v_mfma_f32_32x32x16_bf16 v[64:79], v[168:171], v[160:163], 0\n" \
    "ds_read_b128 v[180:183], v194 offset:4608\n" \
    "v_exp_f32 v86, v86\n" \
    "v_exp_f32 v87, v87\n" \
    "v_exp_f32 v88, v88\n" \
    "v_exp_f32 v89, v89\n" \
    "v_smfmac_f32_32x32x32_bf16 v[128:143], v[184:187], v[80:87], v192\n" \
    "v_exp_f32 v90, v90\n" \
    "v_exp_f32 v91, v91\n" \
    "v_exp_f32 v92, v92\n" \
    "global_load_dword v212, v203, %[wdb] offset:0\n" \
    "global_load_dword v213, v203, %[wdb] offset:1024\n" \
    "v_exp_f32 v93, v93\n" \
    "v_exp_f32 v94, v94\n" \
    "v_exp_f32 v95, v95\n" \
    "v_exp_f32 v112, v112\n" \
    "v_exp_f32 v113, v113\n" \
    "v_smfmac_f32_32x32x32_bf16 v[128:143], v[188:191], v[88:95], v192\n" \
    "v_exp_f32 v114, v114\n" \
    "v_exp_f32 v115, v115\n" \
    "v_exp_f32 v116, v116\n" \
    "v_exp_f32 v117, v117\n" \
    "v_mfma_f32_32x32x16_bf16 v[96:111], v[168:171], v[164:167], 0\n" \
    "ds_read_b128 v[172:175], v193 offset:5120\n" \
    "v_exp_f32 v118, v118\n" \
    "v_exp_f32 v119, v119\n" \
    "v_exp_f32 v120, v120\n" \
    "v_exp_f32 v121, v121\n" \
    "v_smfmac_f32_32x32x32_bf16 v[144:159], v[184:187], v[112:119], v192\n" \
    "v_exp_f32 v122, v122\n" \
    "v_exp_f32 v123, v123\n" \
    "v_exp_f32 v124, v124\n" \
    "global_load_dword v214, v203, %[wdb] offset:2048\n" \
    "global_load_dword v215, v203, %[wdb] offset:3072\n" \
    "v_exp_f32 v125, v125\n" \
    "v_exp_f32 v126, v126\n" \
    "v_exp_f32 v127, v127\n" \
    "v_add_u32 v203, 0x1000, v203\n" \
    "v_add_u32 v193, 4096, v193\n" \
    "v_add_u32 v194, v195, v194\n" \
    "v_exp_f32 v64, v64\n" \
    "v_exp_f32 v65, v65\n" \
    "v_smfmac_f32_32x32x32_bf16 v[144:159], v[188:191], v[120:127], v192\n" \
    "ds_read_b128 v[184:187], v194 offset:1024\n" \
    "v_exp_f32 v66, v66\n" \
    "v_exp_f32 v67, v67\n" \
    "v_exp_f32 v68, v68\n" \
    "v_exp_f32 v69, v69\n" \
    "s_waitcnt lgkmcnt(1)\n" \
    "v_mfma_f32_32x32x16_bf16 v[80:95], v[172:175], v[160:163], 0\n" \
    "ds_read_b128 v[188:191], v194 offset:1536\n" \
    "v_exp_f32 v70, v70\n" \
    "v_exp_f32 v71, v71\n" \
    "v_exp_f32 v72, v72\n" \
    "v_exp_f32 v73, v73\n" \
    "v_smfmac_f32_32x32x32_bf16 v[128:143], v[176:179], v[64:71], v192\n" \
    "v_exp_f32 v74, v74\n" \
    "v_exp_f32 v75, v75\n" \
    "v_exp_f32 v76, v76\n" \
    "global_load_dword v216, v203, %[wdb] offset:0\n" \
    "global_load_dword v217, v203, %[wdb] offset:1024\n" \
    "v_exp_f32 v77, v77\n" \
    "v_exp_f32 v78, v78\n" \
    "v_exp_f32 v79, v79\n" \
    "v_exp_f32 v96, v96\n" \
    "v_exp_f32 v97, v97\n" \
    "v_smfmac_f32_32x32x32_bf16 v[128:143], v[180:183], v[72:79], v192\n" \
    "v_exp_f32 v98, v98\n" \
    "v_exp_f32 v99, v99\n" \
    "v_exp_f32 v100, v100\n" \
    "v_exp_f32 v101, v101\n" \
    "v_mfma_f32_32x32x16_bf16 v[112:127], v[172:175], v[164:167], 0\n" \
    "ds_read_b128 v[168:171], v193 offset:2048\n" \
    "v_exp_f32 v102, v102\n" \
    "v_exp_f32 v103, v103\n" \
    "v_exp_f32 v104, v104\n" \
    "v_exp_f32 v105, v105\n" \
    "v_smfmac_f32_32x32x32_bf16 v[144:159], v[176:179], v[96:103], v192\n" \
    "v_exp_f32 v106, v106\n" \
    "v_exp_f32 v107, v107\n" \
    "v_exp_f32 v108, v108\n" \
    "global_load_dword v218, v203, %[wdb] offset:2048\n" \
    "global_load_dword v219, v203, %[wdb] offset:3072\n" \
    "v_exp_f32 v109, v109\n" \
    "v_exp_f32 v110, v110\n" \
    "v_exp_f32 v111, v111\n" \
    "v_exp_f32 v80, v80\n" \
    "v_exp_f32 v81, v81\n" \
    "v_smfmac_f32_32x32x32_bf16 v[144:159], v[180:183], v[104:111], v192\n" \
    "s_waitcnt vmcnt(12)\n" \
    "ds_read_b64_tr_b16 v[220:221], v228\n" \
    "ds_read_b64_tr_b16 v[222:223], v229\n" \
    "ds_read_b64_tr_b16 v[224:225], v228 offset:256\n" \
    "ds_read_b64_tr_b16 v[226:227], v229 offset:256\n" \
    "ds_read_b128 v[176:179], v194 offset:2048\n" \
    "v_exp_f32 v82, v82\n" \
    "v_exp_f32 v83, v83\n" \
    "v_exp_f32 v84, v84\n" \
    "v_exp_f32 v85, v85\n" \
    "s_waitcnt lgkmcnt(1)\n" \
    "v_mfma_f32_32x32x16_bf16 v[64:79], v[168:171], v[160:163], 0\n" \
    "ds_read_b128 v[180:183], v194 offset:2560\n" \
    "v_exp_f32 v86, v86\n" \
    "v_exp_f32 v87, v87\n" \
    "v_exp_f32 v88, v88\n" \
    "v_exp_f32 v89, v89\n" \
    "v_smfmac_f32_32x32x32_bf16 v[128:143], v[184:187], v[80:87], v192\n" \
    "v_exp_f32 v90, v90\n" \
    "v_exp_f32 v91, v91\n" \
    "v_exp_f32 v92, v92\n" \
    "v_mfma_f32_16x16x32_bf16 v[232:235], v[220:223], v[204:207], 0\n" \
    "v_exp_f32 v93, v93\n" \
    "v_exp_f32 v94, v94\n" \
    "v_exp_f32 v95, v95\n" \
    "v_exp_f32 v112, v112\n" \
    "v_exp_f32 v113, v113\n" \
    "v_smfmac_f32_32x32x32_bf16 v[128:143], v[188:191], v[88:95], v192\n" \
    "v_exp_f32 v114, v114\n" \
    "v_exp_f32 v115, v115\n" \
    "v_exp_f32 v116, v116\n" \
    "v_exp_f32 v117, v117\n" \
    "v_mfma_f32_32x32x16_bf16 v[96:111], v[168:171], v[164:167], 0\n" \
    "ds_read_b128 v[172:175], v193 offset:3072\n" \
    "v_exp_f32 v118, v118\n" \
    "v_exp_f32 v119, v119\n" \
    "v_exp_f32 v120, v120\n" \
    "v_exp_f32 v121, v121\n" \
    "v_smfmac_f32_32x32x32_bf16 v[144:159], v[184:187], v[112:119], v192\n" \
    "v_exp_f32 v122, v122\n" \
    "v_exp_f32 v123, v123\n" \
    "v_exp_f32 v124, v124\n" \
    "v_exp_f32 v125, v125\n" \
    "v_exp_f32 v126, v126\n" \
    "v_exp_f32 v127, v127\n" \
    "s_setprio 0\n" \
    "v_exp_f32 v64, v64\n" \
    "v_exp_f32 v65, v65\n" \
    "v_smfmac_f32_32x32x32_bf16 v[144:159], v[188:191], v[120:127], v192\n" \
    "ds_read_b128 v[184:187], v194 offset:3072\n" \
    "v_exp_f32 v66, v66\n" \
    "v_exp_f32 v67, v67\n" \
    "v_exp_f32 v68, v68\n" \
    "v_exp_f32 v69, v69\n" \
    "s_waitcnt lgkmcnt(1)\n" \
    "v_mfma_f32_32x32x16_bf16 v[80:95], v[172:175], v[160:163], 0\n" \
    "ds_read_b128 v[188:191], v194 offset:3584\n" \
    "v_exp_f32 v70, v70\n" \
    "v_exp_f32 v71, v71\n" \
    "v_exp_f32 v72, v72\n" \
    "v_exp_f32 v73, v73\n" \
    "v_smfmac_f32_32x32x32_bf16 v[128:143], v[176:179], v[64:71], v192\n" \
    "v_exp_f32 v74, v74\n" \
    "v_exp_f32 v75, v75\n" \
    "v_exp_f32 v76, v76\n" \
    "v_mfma_f32_16x16x32_bf16 v[232:235], v[224:227], v[208:211], v[232:235]\n" \
    "v_exp_f32 v77, v77\n" \
    "v_exp_f32 v78, v78\n" \
    "v_exp_f32 v79, v79\n" \
    "v_exp_f32 v96, v96\n" \
    "v_exp_f32 v97, v97\n" \
    "v_smfmac_f32_32x32x32_bf16 v[128:143], v[180:183], v[72:79], v192\n" \
    "v_exp_f32 v98, v98\n" \
    "v_exp_f32 v99, v99\n" \
    "v_exp_f32 v100, v100\n" \
    "v_exp_f32 v101, v101\n" \
    "v_mfma_f32_32x32x16_bf16 v[112:127], v[172:175], v[164:167], 0\n" \
    "ds_read_b128 v[168:171], v193 offset:4096\n" \
    "v_exp_f32 v102, v102\n" \
    "v_exp_f32 v103, v103\n" \
    "v_exp_f32 v104, v104\n" \
    "v_exp_f32 v105, v105\n" \
    "v_smfmac_f32_32x32x32_bf16 v[144:159], v[176:179], v[96:103], v192\n" \
    "v_exp_f32 v106, v106\n" \
    "v_exp_f32 v107, v107\n" \
    "v_exp_f32 v108, v108\n" \
    "v_exp_f32 v109, v109\n" \
    "v_exp_f32 v110, v110\n" \
    "v_exp_f32 v111, v111\n" \
    "v_exp_f32 v80, v80\n" \
    "v_exp_f32 v81, v81\n" \
    "v_smfmac_f32_32x32x32_bf16 v[144:159], v[180:183], v[104:111], v192\n" \
    "ds_write_b128 v230, v[232:235]\n" \
    "ds_read_b128 v[176:179], v194 offset:4096\n" \
    "v_exp_f32 v82, v82\n" \
    "v_exp_f32 v83, v83\n" \
    "v_exp_f32 v84, v84\n" \
    "v_exp_f32 v85, v85\n" \
    "s_waitcnt lgkmcnt(1)\n" \
    "v_mfma_f32_32x32x16_bf16 v[64:79], v[168:171], v[160:163], 0\n" \
    "ds_read_b128 v[180:183], v194 offset:4608\n" \
    "v_exp_f32 v86, v86\n" \
    "v_exp_f32 v87, v87\n" \
    "v_exp_f32 v88, v88\n" \
    "v_exp_f32 v89, v89\n" \
    "v_smfmac_f32_32x32x32_bf16 v[128:143], v[184:187], v[80:87], v192\n" \
    "v_exp_f32 v90, v90\n" \
    "v_exp_f32 v91, v91\n" \
    "v_exp_f32 v92, v92\n" \
    "v_exp_f32 v93, v93\n" \
    "v_exp_f32 v94, v94\n" \
    "v_exp_f32 v95, v95\n" \
    "v_exp_f32 v112, v112\n" \
    "v_exp_f32 v113, v113\n" \
    "v_smfmac_f32_32x32x32_bf16 v[128:143], v[188:191], v[88:95], v192\n" \
    "v_exp_f32 v114, v114\n" \
    "v_exp_f32 v115, v115\n" \
    "v_exp_f32 v116, v116\n" \
    "v_exp_f32 v117, v117\n" \
    "v_mfma_f32_32x32x16_bf16 v[96:111], v[168:171], v[164:167], 0\n" \
    "ds_read_b128 v[172:175], v193 offset:5120\n" \
    "v_exp_f32 v118, v118\n" \
    "v_exp_f32 v119, v119\n" \
    "v_exp_f32 v120, v120\n" \
    "v_exp_f32 v121, v121\n" \
    "v_smfmac_f32_32x32x32_bf16 v[144:159], v[184:187], v[112:119], v192\n" \
    "v_exp_f32 v122, v122\n" \
    "v_exp_f32 v123, v123\n" \
    "v_exp_f32 v124, v124\n" \
    "v_exp_f32 v125, v125\n" \
    "v_exp_f32 v126, v126\n" \
    "v_exp_f32 v127, v127\n" \
    "v_add_u32 v193, 4096, v193\n" \
    "v_add_u32 v194, v195, v194\n" \
    "s_waitcnt vmcnt(0)\n"

#define ATT_C \
    "s_mov_b32 %[cnt], 2\n" \
    "1:\n" \
    "v_exp_f32 v64, v64\n" \
    "v_exp_f32 v65, v65\n" \
    "v_smfmac_f32_32x32x32_bf16 v[144:159], v[188:191], v[120:127], v192\n" \
    "ds_read_b128 v[184:187], v194 offset:1024\n" \
    "v_exp_f32 v66, v66\n" \
    "v_exp_f32 v67, v67\n" \
    "v_exp_f32 v68, v68\n" \
    "v_exp_f32 v69, v69\n" \
    "s_waitcnt lgkmcnt(1)\n" \
    "v_mfma_f32_32x32x16_bf16 v[80:95], v[172:175], v[160:163], 0\n" \
    "ds_read_b128 v[188:191], v194 offset:1536\n" \
    "v_exp_f32 v70, v70\n" \
    "v_exp_f32 v71, v71\n" \
    "v_exp_f32 v72, v72\n" \
    "v_exp_f32 v73, v73\n" \
    "v_smfmac_f32_32x32x32_bf16 v[128:143], v[176:179], v[64:71], v192\n" \
    "v_exp_f32 v74, v74\n" \
    "s_mov_b32 m0, %[swo]\n" \
    "v_exp_f32 v75, v75\n" \
    "v_exp_f32 v76, v76\n" \
    "global_load_lds_dwordx4 v236, %[wob] offset:0\n" \
    "v_exp_f32 v77, v77\n" \
    "v_exp_f32 v78, v78\n" \
    "v_exp_f32 v79, v79\n" \
    "v_exp_f32 v96, v96\n" \
    "v_exp_f32 v97, v97\n" \
    "v_smfmac_f32_32x32x32_bf16 v[128:143], v[180:183], v[72:79], v192\n" \
    "v_exp_f32 v98, v98\n" \
    "v_exp_f32 v99, v99\n" \
    "v_exp_f32 v100, v100\n" \
    "v_exp_f32 v101, v101\n" \
    "v_mfma_f32_32x32x16_bf16 v[112:127], v[172:175], v[164:167], 0\n" \
    "ds_read_b128 v[168:171], v193 offset:2048\n" \
    "v_exp_f32 v102, v102\n" \
    "v_exp_f32 v103, v103\n" \
    "v_exp_f32 v104, v104\n" \
    "v_exp_f32 v105, v105\n" \
    "v_smfmac_f32_32x32x32_bf16 v[144:159], v[176:179], v[96:103], v192\n" \
    "v_exp_f32 v106, v106\n" \
    "v_exp_f32 v107, v107\n" \
    "v_exp_f32 v108, v108\n" \
    "v_exp_f32 v109, v109\n" \
    "v_exp_f32 v110, v110\n" \
    "v_exp_f32 v111, v111\n" \
    "v_exp_f32 v80, v80\n" \
    "v_exp_f32 v81, v81\n" \
    "v_smfmac_f32_32x32x32_bf16 v[144:159], v[180:183], v[104:111], v192\n" \
    "ds_read_b128 v[176:179], v194 offset:2048\n" \
    "v_exp_f32 v82, v82\n" \
    "v_exp_f32 v83, v83\n" \
    "v_exp_f32 v84, v84\n" \
    "v_exp_f32 v85, v85\n" \
    "s_waitcnt lgkmcnt(1)\n" \
    "v_mfma_f32_32x32x16_bf16 v[64:79], v[168:171], v[160:163], 0\n" \
    "ds_read_b128 v[180:183], v194 offset:2560\n" \
    "v_exp_f32 v86, v86\n" \
    "v_exp_f32 v87, v87\n" \
    "v_exp_f32 v88, v88\n" \
    "v_exp_f32 v89, v89\n" \
    "v_smfmac_f32_32x32x32_bf16 v[128:143], v[184:187], v[80:87], v192\n" \
    "v_exp_f32 v90, v90\n" \
    "v_exp_f32 v91, v91\n" \
    "v_exp_f32 v92, v92\n" \
    "global_load_lds_dwordx4 v237, %[wob] offset:1024\n" \
    "v_exp_f32 v93, v93\n" \
    "v_exp_f32 v94, v94\n" \
    "v_exp_f32 v95, v95\n" \
    "v_exp_f32 v112, v112\n" \
    "v_exp_f32 v113, v113\n" \
    "v_smfmac_f32_32x32x32_bf16 v[128:143], v[188:191], v[88:95], v192\n" \
    "v_exp_f32 v114, v114\n" \
    "v_exp_f32 v115, v115\n" \
    "v_exp_f32 v116, v116\n" \
    "v_exp_f32 v117, v117\n" \
    "v_mfma_f32_32x32x16_bf16 v[96:111], v[168:171], v[164:167], 0\n" \
    "ds_read_b128 v[172:175], v193 offset:3072\n" \
    "v_exp_f32 v118, v118\n" \
    "v_exp_f32 v119, v119\n" \
    "v_exp_f32 v120, v120\n" \
    "v_exp_f32 v121, v121\n" \
    "v_smfmac_f32_32x32x32_bf16 v[144:159], v[184:187], v[112:119], v192\n" \
    "v_exp_f32 v122, v122\n" \
    "v_exp_f32 v123, v123\n" \
    "v_exp_f32 v124, v124\n" \
    "v_exp_f32 v125, v125\n" \
    "v_exp_f32 v126, v126\n" \
    "v_exp_f32 v127, v127\n" \
    "v_exp_f32 v64, v64\n" \
    "v_exp_f32 v65, v65\n" \
    "v_smfmac_f32_32x32x32_bf16 v[144:159], v[188:191], v[120:127], v192\n" \
    "ds_read_b128 v[184:187], v194 offset:3072\n" \
    "v_exp_f32 v66, v66\n" \
    "v_exp_f32 v67, v67\n" \
    "v_exp_f32 v68, v68\n" \
    "v_exp_f32 v69, v69\n" \
    "s_waitcnt lgkmcnt(1)\n" \
    "v_mfma_f32_32x32x16_bf16 v[80:95], v[172:175], v[160:163], 0\n" \
    "ds_read_b128 v[188:191], v194 offset:3584\n" \
    "v_exp_f32 v70, v70\n" \
    "v_exp_f32 v71, v71\n" \
    "v_exp_f32 v72, v72\n" \
    "v_exp_f32 v73, v73\n" \
    "v_smfmac_f32_32x32x32_bf16 v[128:143], v[176:179], v[64:71], v192\n" \
    "v_exp_f32 v74, v74\n" \
    "v_exp_f32 v75, v75\n" \
    "v_exp_f32 v76, v76\n" \
    "global_load_lds_dwordx4 v238, %[wob] offset:2048\n" \
    "v_exp_f32 v77, v77\n" \
    "v_exp_f32 v78, v78\n" \
    "v_exp_f32 v79, v79\n" \
    "v_exp_f32 v96, v96\n" \
    "v_exp_f32 v97, v97\n" \
    "v_smfmac_f32_32x32x32_bf16 v[128:143], v[180:183], v[72:79], v192\n" \
    "v_exp_f32 v98, v98\n" \
    "v_exp_f32 v99, v99\n" \
    "v_exp_f32 v100, v100\n" \
    "v_exp_f32 v101, v101\n" \
    "v_mfma_f32_32x32x16_bf16 v[112:127], v[172:175], v[164:167], 0\n" \
    "ds_read_b128 v[168:171], v193 offset:4096\n" \
    "v_exp_f32 v102, v102\n" \
    "v_exp_f32 v103, v103\n" \
    "v_exp_f32 v104, v104\n" \
    "v_exp_f32 v105, v105\n" \
    "v_smfmac_f32_32x32x32_bf16 v[144:159], v[176:179], v[96:103], v192\n" \
    "v_exp_f32 v106, v106\n" \
    "v_exp_f32 v107, v107\n" \
    "v_exp_f32 v108, v108\n" \
    "v_exp_f32 v109, v109\n" \
    "v_exp_f32 v110, v110\n" \
    "v_exp_f32 v111, v111\n" \
    "v_exp_f32 v80, v80\n" \
    "v_exp_f32 v81, v81\n" \
    "v_smfmac_f32_32x32x32_bf16 v[144:159], v[180:183], v[104:111], v192\n" \
    "ds_read_b128 v[176:179], v194 offset:4096\n" \
    "v_exp_f32 v82, v82\n" \
    "v_exp_f32 v83, v83\n" \
    "v_exp_f32 v84, v84\n" \
    "v_exp_f32 v85, v85\n" \
    "s_waitcnt lgkmcnt(1)\n" \
    "v_mfma_f32_32x32x16_bf16 v[64:79], v[168:171], v[160:163], 0\n" \
    "ds_read_b128 v[180:183], v194 offset:4608\n" \
    "v_exp_f32 v86, v86\n" \
    "v_exp_f32 v87, v87\n" \
    "v_exp_f32 v88, v88\n" \
    "v_exp_f32 v89, v89\n" \
    "v_smfmac_f32_32x32x32_bf16 v[128:143], v[184:187], v[80:87], v192\n" \
    "v_exp_f32 v90, v90\n" \
    "v_exp_f32 v91, v91\n" \
    "v_exp_f32 v92, v92\n" \
    "global_load_lds_dwordx4 v239, %[wob] offset:3072\n" \
    "v_exp_f32 v93, v93\n" \
    "v_exp_f32 v94, v94\n" \
    "v_exp_f32 v95, v95\n" \
    "v_exp_f32 v112, v112\n" \
    "v_exp_f32 v113, v113\n" \
    "v_smfmac_f32_32x32x32_bf16 v[128:143], v[188:191], v[88:95], v192\n" \
    "v_exp_f32 v114, v114\n" \
    "v_exp_f32 v115, v115\n" \
    "v_exp_f32 v116, v116\n" \
    "v_exp_f32 v117, v117\n" \
    "v_mfma_f32_32x32x16_bf16 v[96:111], v[168:171], v[164:167], 0\n" \
    "ds_read_b128 v[172:175], v193 offset:5120\n" \
    "v_exp_f32 v118, v118\n" \
    "v_exp_f32 v119, v119\n" \
    "v_exp_f32 v120, v120\n" \
    "v_exp_f32 v121, v121\n" \
    "v_smfmac_f32_32x32x32_bf16 v[144:159], v[184:187], v[112:119], v192\n" \
    "v_exp_f32 v122, v122\n" \
    "v_exp_f32 v123, v123\n" \
    "v_exp_f32 v124, v124\n" \
    "v_exp_f32 v125, v125\n" \
    "v_exp_f32 v126, v126\n" \
    "v_exp_f32 v127, v127\n" \
    "v_add_u32 v236, 0x8000, v236\n" \
    "v_add_u32 v237, 0x8000, v237\n" \
    "v_add_u32 v238, 0x8000, v238\n" \
    "v_add_u32 v239, 0x8000, v239\n" \
    "s_add_u32 %[swo], %[swo], 0x1000\n" \
    "v_add_u32 v193, 4096, v193\n" \
    "v_add_u32 v194, v195, v194\n" \
    "s_sub_u32 %[cnt], %[cnt], 1\n" \
    "s_cmp_lg_u32 %[cnt], 0\n" \
    "s_cbranch_scc1 1b\n" \
    "global_load_dword v220, v221, %[wsb] sc1\n" \
    "s_mov_b32 %[cnt], 2\n" \
    "2:\n" \
    "v_exp_f32 v64, v64\n" \
    "v_exp_f32 v65, v65\n" \
    "v_smfmac_f32_32x32x32_bf16 v[144:159], v[188:191], v[120:127], v192\n" \
    "ds_read_b128 v[184:187], v194 offset:1024\n" \
    "v_exp_f32 v66, v66\n" \
    "v_exp_f32 v67, v67\n" \
    "v_exp_f32 v68, v68\n" \
    "v_exp_f32 v69, v69\n" \
    "s_waitcnt lgkmcnt(1)\n" \
    "v_mfma_f32_32x32x16_bf16 v[80:95], v[172:175], v[160:163], 0\n" \
    "ds_read_b128 v[188:191], v194 offset:1536\n" \
    "v_exp_f32 v70, v70\n" \
    "v_exp_f32 v71, v71\n" \
    "v_exp_f32 v72, v72\n" \
    "v_exp_f32 v73, v73\n" \
    "v_smfmac_f32_32x32x32_bf16 v[128:143], v[176:179], v[64:71], v192\n" \
    "v_exp_f32 v74, v74\n" \
    "v_exp_f32 v75, v75\n" \
    "v_exp_f32 v76, v76\n" \
    "v_exp_f32 v77, v77\n" \
    "v_exp_f32 v78, v78\n" \
    "v_exp_f32 v79, v79\n" \
    "v_exp_f32 v96, v96\n" \
    "v_exp_f32 v97, v97\n" \
    "v_smfmac_f32_32x32x32_bf16 v[128:143], v[180:183], v[72:79], v192\n" \
    "v_exp_f32 v98, v98\n" \
    "v_exp_f32 v99, v99\n" \
    "v_exp_f32 v100, v100\n" \
    "v_exp_f32 v101, v101\n" \
    "v_mfma_f32_32x32x16_bf16 v[112:127], v[172:175], v[164:167], 0\n" \
    "ds_read_b128 v[168:171], v193 offset:2048\n" \
    "v_exp_f32 v102, v102\n" \
    "v_exp_f32 v103, v103\n" \
    "v_exp_f32 v104, v104\n" \
    "v_exp_f32 v105, v105\n" \
    "v_smfmac_f32_32x32x32_bf16 v[144:159], v[176:179], v[96:103], v192\n" \
    "v_exp_f32 v106, v106\n" \
    "v_exp_f32 v107, v107\n" \
    "v_exp_f32 v108, v108\n" \
    "v_exp_f32 v109, v109\n" \
    "v_exp_f32 v110, v110\n" \
    "v_exp_f32 v111, v111\n" \
    "v_exp_f32 v80, v80\n" \
    "v_exp_f32 v81, v81\n" \
    "v_smfmac_f32_32x32x32_bf16 v[144:159], v[180:183], v[104:111], v192\n" \
    "ds_read_b128 v[176:179], v194 offset:2048\n" \
    "v_exp_f32 v82, v82\n" \
    "v_exp_f32 v83, v83\n" \
    "v_exp_f32 v84, v84\n" \
    "v_exp_f32 v85, v85\n" \
    "s_waitcnt lgkmcnt(1)\n" \
    "v_mfma_f32_32x32x16_bf16 v[64:79], v[168:171], v[160:163], 0\n" \
    "ds_read_b128 v[180:183], v194 offset:2560\n" \
    "v_exp_f32 v86, v86\n" \
    "v_exp_f32 v87, v87\n" \
    "v_exp_f32 v88, v88\n" \
    "v_exp_f32 v89, v89\n" \
    "v_smfmac_f32_32x32x32_bf16 v[128:143], v[184:187], v[80:87], v192\n" \
    "v_exp_f32 v90, v90\n" \
    "v_exp_f32 v91, v91\n" \
    "v_exp_f32 v92, v92\n" \
    "v_exp_f32 v93, v93\n" \
    "v_exp_f32 v94, v94\n" \
    "v_exp_f32 v95, v95\n" \
    "v_exp_f32 v112, v112\n" \
    "v_exp_f32 v113, v113\n" \
    "v_smfmac_f32_32x32x32_bf16 v[128:143], v[188:191], v[88:95], v192\n" \
    "v_exp_f32 v114, v114\n" \
    "v_exp_f32 v115, v115\n" \
    "v_exp_f32 v116, v116\n" \
    "v_exp_f32 v117, v117\n" \
    "v_mfma_f32_32x32x16_bf16 v[96:111], v[168:171], v[164:167], 0\n" \
    "ds_read_b128 v[172:175], v193 offset:3072\n" \
    "v_exp_f32 v118, v118\n" \
    "v_exp_f32 v119, v119\n" \
    "v_exp_f32 v120, v120\n" \
    "v_exp_f32 v121, v121\n" \
    "v_smfmac_f32_32x32x32_bf16 v[144:159], v[184:187], v[112:119], v192\n" \
    "v_exp_f32 v122, v122\n" \
    "v_exp_f32 v123, v123\n" \
    "v_exp_f32 v124, v124\n" \
    "v_exp_f32 v125, v125\n" \
    "v_exp_f32 v126, v126\n" \
    "v_exp_f32 v127, v127\n" \
    "v_exp_f32 v64, v64\n" \
    "v_exp_f32 v65, v65\n" \
    "v_smfmac_f32_32x32x32_bf16 v[144:159], v[188:191], v[120:127], v192\n" \
    "ds_read_b128 v[184:187], v194 offset:3072\n" \
    "v_exp_f32 v66, v66\n" \
    "v_exp_f32 v67, v67\n" \
    "v_exp_f32 v68, v68\n" \
    "v_exp_f32 v69, v69\n" \
    "s_waitcnt lgkmcnt(1)\n" \
    "v_mfma_f32_32x32x16_bf16 v[80:95], v[172:175], v[160:163], 0\n" \
    "ds_read_b128 v[188:191], v194 offset:3584\n" \
    "v_exp_f32 v70, v70\n" \
    "v_exp_f32 v71, v71\n" \
    "v_exp_f32 v72, v72\n" \
    "v_exp_f32 v73, v73\n" \
    "v_smfmac_f32_32x32x32_bf16 v[128:143], v[176:179], v[64:71], v192\n" \
    "v_exp_f32 v74, v74\n" \
    "v_exp_f32 v75, v75\n" \
    "v_exp_f32 v76, v76\n" \
    "v_exp_f32 v77, v77\n" \
    "v_exp_f32 v78, v78\n" \
    "v_exp_f32 v79, v79\n" \
    "v_exp_f32 v96, v96\n" \
    "v_exp_f32 v97, v97\n" \
    "v_smfmac_f32_32x32x32_bf16 v[128:143], v[180:183], v[72:79], v192\n" \
    "v_exp_f32 v98, v98\n" \
    "v_exp_f32 v99, v99\n" \
    "v_exp_f32 v100, v100\n" \
    "v_exp_f32 v101, v101\n" \
    "v_mfma_f32_32x32x16_bf16 v[112:127], v[172:175], v[164:167], 0\n" \
    "ds_read_b128 v[168:171], v193 offset:4096\n" \
    "v_exp_f32 v102, v102\n" \
    "v_exp_f32 v103, v103\n" \
    "v_exp_f32 v104, v104\n" \
    "v_exp_f32 v105, v105\n" \
    "v_smfmac_f32_32x32x32_bf16 v[144:159], v[176:179], v[96:103], v192\n" \
    "v_exp_f32 v106, v106\n" \
    "v_exp_f32 v107, v107\n" \
    "v_exp_f32 v108, v108\n" \
    "v_exp_f32 v109, v109\n" \
    "v_exp_f32 v110, v110\n" \
    "v_exp_f32 v111, v111\n" \
    "v_exp_f32 v80, v80\n" \
    "v_exp_f32 v81, v81\n" \
    "v_smfmac_f32_32x32x32_bf16 v[144:159], v[180:183], v[104:111], v192\n" \
    "ds_read_b128 v[176:179], v194 offset:4096\n" \
    "v_exp_f32 v82, v82\n" \
    "v_exp_f32 v83, v83\n" \
    "v_exp_f32 v84, v84\n" \
    "v_exp_f32 v85, v85\n" \
    "s_waitcnt lgkmcnt(1)\n" \
    "v_mfma_f32_32x32x16_bf16 v[64:79], v[168:171], v[160:163], 0\n" \
    "ds_read_b128 v[180:183], v194 offset:4608\n" \
    "v_exp_f32 v86, v86\n" \
    "v_exp_f32 v87, v87\n" \
    "v_exp_f32 v88, v88\n" \
    "v_exp_f32 v89, v89\n" \
    "v_smfmac_f32_32x32x32_bf16 v[128:143], v[184:187], v[80:87], v192\n" \
    "v_exp_f32 v90, v90\n" \
    "v_exp_f32 v91, v91\n" \
    "v_exp_f32 v92, v92\n" \
    "v_exp_f32 v93, v93\n" \
    "v_exp_f32 v94, v94\n" \
    "v_exp_f32 v95, v95\n" \
    "v_exp_f32 v112, v112\n" \
    "v_exp_f32 v113, v113\n" \
    "v_smfmac_f32_32x32x32_bf16 v[128:143], v[188:191], v[88:95], v192\n" \
    "v_exp_f32 v114, v114\n" \
    "v_exp_f32 v115, v115\n" \
    "v_exp_f32 v116, v116\n" \
    "v_exp_f32 v117, v117\n" \
    "v_mfma_f32_32x32x16_bf16 v[96:111], v[168:171], v[164:167], 0\n" \
    "ds_read_b128 v[172:175], v193 offset:5120\n" \
    "v_exp_f32 v118, v118\n" \
    "v_exp_f32 v119, v119\n" \
    "v_exp_f32 v120, v120\n" \
    "v_exp_f32 v121, v121\n" \
    "v_smfmac_f32_32x32x32_bf16 v[144:159], v[184:187], v[112:119], v192\n" \
    "v_exp_f32 v122, v122\n" \
    "v_exp_f32 v123, v123\n" \
    "v_exp_f32 v124, v124\n" \
    "v_exp_f32 v125, v125\n" \
    "v_exp_f32 v126, v126\n" \
    "v_exp_f32 v127, v127\n" \
    "s_cmp_eq_u32 %[cnt], 2\n" \
    "s_cbranch_scc0 8f\n" \
    "s_setprio 0\n" \
    "8:\n" \
    "v_add_u32 v193, 4096, v193\n" \
    "v_add_u32 v194, v195, v194\n" \
    "s_sub_u32 %[cnt], %[cnt], 1\n" \
    "s_cmp_lg_u32 %[cnt], 0\n" \
    "s_cbranch_scc1 2b\n" \
    "s_nop 1\n" \
    "v_smfmac_f32_32x32x32_bf16 v[144:159], v[188:191], v[120:127], v192\n" \
    "s_setprio 0\n" \
    "s_waitcnt vmcnt(0)\n" \
    "s_nop 7\n" \
    "s_nop 7\n" \
    "s_nop 3\n" \
    "s_waitcnt lgkmcnt(0)\n"

__device__ __forceinline__ void slow_block(const char* ws, u32x4 q, int lane, float* o, float& lsum) {
    const u32 koff = WS_K + lane * 16;
    const u32 r32 = lane & 31;
    float m = -3.0e38f;
    for (int t = 0; t < 64; ++t) {
        u32x4 kf;
        GLD128C(kf, ws, koff + t * 1024, 0);
        asm volatile("s_waitcnt vmcnt(0)" : "+v"(kf) :: "memory");
        f32x16 s = {0};
        s = __builtin_amdgcn_mfma_f32_32x32x16_bf16(asbf(kf), asbf(q), s, 0, 0, 0);
#pragma unroll
        for (int i = 0; i < 16; ++i) m = fmaxf(m, s[i]);
    }
    {
        auto sw = __builtin_amdgcn_permlane32_swap(__float_as_uint(m), __float_as_uint(m), false, false);
        m = fmaxf(__uint_as_float(sw[0]), __uint_as_float(sw[1]));
    }
    f32x16 acc = {0};
    const u32 voff = WS_V + (lane >> 5) * 256 + (lane & 15) * 16;
    for (int t = 0; t < 64; ++t) {
        u32x4 kf, v0, v1;
        GLD128C(kf, ws, koff + t * 1024, 0);
        GLD128C(v0, ws, voff + t * 1024, 0);
        GLD128C(v1, ws, voff + t * 1024, 512);
        asm volatile("s_waitcnt vmcnt(0)" : "+v"(kf), "+v"(v0), "+v"(v1) :: "memory");
        if (r32 >= 16) { v0 = (u32x4){0x3f803f80u, 0x3f803f80u, 0x3f803f80u, 0x3f803f80u}; v1 = v0; }
        f32x16 s = {0};
        s = __builtin_amdgcn_mfma_f32_32x32x16_bf16(asbf(kf), asbf(q), s, 0, 0, 0);
        u32 p[8];
#pragma unroll
        for (int i = 0; i < 8; ++i)
            p[i] = pk(__builtin_amdgcn_exp2f(s[2 * i] - m), __builtin_amdgcn_exp2f(s[2 * i + 1] - m));
#pragma unroll
        for (int st = 0; st < 2; ++st) {
            u32x4 vv = st ? v1 : v0;
            auto a = __builtin_amdgcn_permlane32_swap(vv[0], vv[2], false, false);
            auto c = __builtin_amdgcn_permlane32_swap(vv[1], vv[3], false, false);
            u32x4 af = {a[0], c[0], a[1], c[1]};
            u32x4 bf = {p[4 * st], p[4 * st + 1], p[4 * st + 2], p[4 * st + 3]};
            acc = __builtin_amdgcn_mfma_f32_32x32x16_bf16(asbf(af), asbf(bf), acc, 0, 0, 0);
        }
    }
#pragma unroll
    for (int i = 0; i < 8; ++i) o[i] = acc[i];
    lsum = acc[8];
}

#define PRIO_MODE 1
#define PRIO_START() do { if (w >= 4) __builtin_amdgcn_s_setprio(1); } while (0)
#define PRIO_END() __builtin_amdgcn_s_setprio(0)
__global__ void __launch_bounds__(512, 2)
fused_kernel(const u16* x_, const u16* wq_, const u16* wk_, const u16* wv_, const u16* wo_, const u16* wr_, const u16* wg_,
             const u16* wu_, const u16* wd_, const u16* gw_, u16* out_, char* ws_)
{
    u32x16 ka0; u32x8 ka1;
    {
        const void* kp = (const void*)__builtin_amdgcn_kernarg_segment_ptr();
        asm volatile("s_load_dwordx16 %0, %2, 0\n\ts_load_dwordx8 %1, %2, 64\n\ts_waitcnt lgkmcnt(0)"
                     : "=&s"(ka0), "=&s"(ka1) : "s"(kp) : "memory");
    }
#define KP(v, i) ((u64)v[2 * (i)] | ((u64)v[2 * (i) + 1] << 32))
    const u16* x  = (const u16*)KP(ka0, 0); const u16* wq = (const u16*)KP(ka0, 1);
    const u16* wk = (const u16*)KP(ka0, 2); const u16* wv = (const u16*)KP(ka0, 3);
    const u16* wo = (const u16*)KP(ka0, 4); const u16* wr = (const u16*)KP(ka0, 5);
    const u16* wg = (const u16*)KP(ka0, 6); const u16* wu = (const u16*)KP(ka0, 7);
    const u16* wd = (const u16*)KP(ka1, 0); const u16* gw = (const u16*)KP(ka1, 1);
    u16* out = (u16*)KP(ka1, 2); char* ws = (char*)KP(ka1, 3);

    __shared__ __attribute__((aligned(1024))) char smem[L_END];
    LDSP char* lds = (LDSP char*)smem;
    const u32 lb = (u32)(size_t)lds;
    const int tid = threadIdx.x;
    const int lane = tid & 63;
    const int w = __builtin_amdgcn_readfirstlane(tid >> 6);
    const int b = blockIdx.x;
    const int xcd = b & 7, jj = b >> 3;
    const int tb = xcd * 4 + (jj >> 3), hg = jj & 7;
    const int g = lane >> 4, l15 = lane & 15, h = lane >> 5, l31 = lane & 31;
    const bool isK = hg < 4;
    const bool ffn = jj < 28;
    const int fj = xcd * 28 + min(jj, 27);
    STAMP(1); STAMPC(18);

    const int th = w & 1, hp = (w >> 1) & 1, blk = w >> 2;
    const int T0 = tb * 64 + (hg & 3) * 16;
    u32x4 xf0, xf1, vf0, vf1;
    {
        const u16* wsrc = isK ? wk : wv;
#pragma unroll
        for (int i = 0; i < 2; ++i) {
            u32 slot = 64 * i + lane, pos = slot >> 3, B = pos ^ ((pos >> 2) & 1);
            u32 row = 4 * B + ((slot >> 1) & 3);
            dma16(wsrc, (64 * w + row) * 32 + (slot & 1) * 16, lb + L_W + w * 2048 + i * 1024);
        }
        u32 xo = (T0 + l15) * 1024 + (64 * w + 16 * g) * 2;
        GLD128(xf0, x, xo, 0);
        GLD128(xf1, x, xo, 16);
        const u16* vec = isK ? wr : gw;
        u32 vo = (64 * w + 16 * g) * 2;
        GLD128(vf0, vec, vo, 0);
        GLD128(vf1, vec, vo, 16);
    }
    u32 epo;
    { u32 z = 0; GLD32C(epo, ws, z, 0); }
    asm volatile("s_barrier" ::: "memory");
    if (w < 7) {
#pragma unroll
        for (int i = 0; i < 19; ++i) {
            const int p = i < 18 ? w + 7 * i : __builtin_amdgcn_readfirstlane(min(w + 7 * i, 127));
            if (i < 9 || (i == 9 && w == 0)) {
                dma16(x, (tb * 64 + p) * 1024 + ((lane ^ (p & 15)) << 4), lb + L_K + p * 1024);
            } else {
                const int G = p - 64;
                dma16(wq, (8 * G + ((lane >> 2) & 7)) * 1024 + hg * 128 + (lane >> 5) * 64 + (lane & 3) * 16, lb + L_V + G * 1024);
            }
        }
    }
    if (tid < 256) { u32 o1 = (tid & 15) == 0 ? 0x3f803f80u : 0u; ST128(L_ONE + tid * 16, ((u32x4){o1, o1, o1, o1})); }
    asm volatile("s_cmp_eq_u32 %[w], 7\n\ts_cbranch_scc1 1f\n\ts_waitcnt vmcnt(20)\n\ts_branch 2f\n1:\n\ts_waitcnt vmcnt(1)\n2:"
                 : "+v"(xf0), "+v"(xf1), "+v"(vf0), "+v"(vf1) : [w] "s"(w) : "memory", "scc");
    FSTAMP(2);
    {
        const u32 ta = L_W + w * 2048 + l15 * 8;
        const u32 gx = g & 1;
        u32x4 wf0 = cat(trread(lds, ta + ((4 * g + 0) ^ gx) * 128), trread(lds, ta + ((4 * g + 1) ^ gx) * 128));
        u32x4 wf1 = cat(trread(lds, ta + ((4 * g + 2) ^ gx) * 128), trread(lds, ta + ((4 * g + 3) ^ gx) * 128));
        f32x4 acc = {0, 0, 0, 0}, dacc = {0, 0, 0, 0};
        if (isK) {
            acc = __builtin_amdgcn_mfma_f32_16x16x32_bf16(asbf(wf0), asbf(xf0), acc, 0, 0, 0);
            acc = __builtin_amdgcn_mfma_f32_16x16x32_bf16(asbf(wf1), asbf(xf1), acc, 0, 0, 0);
        } else {
            acc = __builtin_amdgcn_mfma_f32_16x16x32_bf16(asbf(xf0), asbf(wf0), acc, 0, 0, 0);
            acc = __builtin_amdgcn_mfma_f32_16x16x32_bf16(asbf(xf1), asbf(wf1), acc, 0, 0, 0);
        }
        dacc = __builtin_amdgcn_mfma_f32_16x16x32_bf16(asbf(vf0), asbf(xf0), dacc, 0, 0, 0);
        dacc = __builtin_amdgcn_mfma_f32_16x16x32_bf16(asbf(vf1), asbf(xf1), dacc, 0, 0, 0);
        ST128(L_P1 + w * 1024 + lane * 16, __builtin_bit_cast(u32x4, acc));
        if (g == 0) STF(L_PD + w * 64 + l15 * 4, dacc[0]);
    }
    BAR();
    FSTAMP(3);
    asm volatile("s_cmp_eq_u32 %[w], 7\n\ts_cbranch_scc1 1f\n\ts_waitcnt vmcnt(19)\n\ts_branch 2f\n1:\n\ts_waitcnt vmcnt(0)\n2:"
                 : "+v"(epo) : [w] "s"(w) : "memory", "scc");
    const u32 epoch = (u32)__builtin_amdgcn_readfirstlane((int)epo) + 1u;
    if (w == 7) {
        f32x4 ps[8]; float pd[8];
#pragma unroll
        for (int i = 0; i < 8; ++i) {
            ps[i] = __builtin_bit_cast(f32x4, LD128(L_P1 + i * 1024 + lane * 16));
            pd[i] = LDF(L_PD + i * 64 + l15 * 4);
        }
        __builtin_amdgcn_sched_barrier(0);
        const f32x4 s = ((ps[0] + ps[1]) + (ps[2] + ps[3])) + ((ps[4] + ps[5]) + (ps[6] + ps[7]));
        const float d = ((pd[0] + pd[1]) + (pd[2] + pd[3])) + ((pd[4] + pd[5]) + (pd[6] + pd[7]));
        u32x2 pv = {pk(s[0], s[1]), pk(s[2], s[3])};
        const int tile = T0 >> 5, sub = (T0 >> 4) & 1;
        u32 key = 0;
        if (isK) {
            u32 off = WS_K + tile * 1024 + ((g & 1) * 32 + sub * 16 + l15) * 16 + (g >> 1) * 8;
            GST64C(ws, off, pv);
            u32 rb = pk(d, d) & 0xffffu;
            u32 ord = (rb & 0x8000u) ? (~rb & 0xffffu) : (rb | 0x8000u);
            key = wave_max_u32((ord << 16) | (0xffffu - (u32)(T0 + l15)));
        } else {
            u32 off = WS_V + tile * 1024 + sub * 512 + (g >> 1) * 256 + l15 * 16 + (g & 1) * 8;
            GST64C(ws, off, pv);
            if (g == 0) { u32 go = WS_G + (T0 + l15) * 4; u32 dv = __float_as_uint(d); GST32C(ws, go, dv); }
        }
        VMWAIT(0);
        if (lane < 8) { u32x2 fl = {key, epoch}; u32 fo = WS_F1 + lane * 32768 + (tile * 4 + (isK ? 0 : 2) + sub) * 128; GST64C(ws, fo, fl); }
        FSTAMP(4);
    } else {
        VMWAIT(0);
    }
    BAR();
    FSTAMP(5);

    f32x16 qacc = {0};
    if (w < 4) {
        const int qh = w >> 1;
        const int tok = th * 32 + l31;
        u32 abase = L_V + h * 1024 + qh * 512 + ((l15 >> 2) * 64) + ((lane >> 4) & 1) * 32 + (l15 & 3) * 8;
        asm volatile("" : "+v"(abase));
        u32 bb[8];
#pragma unroll
        for (int j = 0; j < 8; ++j) { bb[j] = L_K + tok * 1024 + (((2 * j + h) ^ (tok & 15)) << 4); asm volatile("" : "+v"(bb[j])); }
#pragma unroll
        for (int s = 0; s < 32; ++s) {
            u32x4 af = cat(trread(lds, abase + s * 2048), trread(lds, abase + s * 2048 + 256));
            u32x4 bf = LD128(bb[s & 7] + (s >> 3) * 256);
            qacc = __builtin_amdgcn_mfma_f32_32x32x16_bf16(asbf(af), asbf(bf), qacc, 0, 0, 0);
        }
        FSTAMP(6);
    } else {
        const int j = w - 4;
        const u32 fo = WS_F1 + xcd * 32768 + (j * 64 + lane) * 128;
        u32x2 f;
        u32 spins = 0;
        do {
            asm volatile("global_load_dwordx2 %0, %1, %2 sc1\n\ts_waitcnt vmcnt(0)" : "=&v"(f) : "v"(fo), "s"(ws) : "memory");
        } while ((f[1] != epoch) && ++spins <= SPIN_MAX);
        const u32 key = wave_max_u32(f[0]);
        if (lane == 0) ST32(L_CT + j * 4, key);
        FSTAMP(7);
    }
    BAR();
    FSTAMP(9);
    u32 kvo = (w & 1) * 65536 + ((w >> 1) & 1) * 32768;
    if (w >= 4) {
        dma16c4(ws + WS_K, kvo + lane * 16, lb + L_K + kvo);
        dma16c4(ws + WS_K, kvo + 4096 + lane * 16, lb + L_K + kvo + 4096);
        dma16c2(ws + WS_K, kvo + 8192 + lane * 16, lb + L_K + kvo + 8192);
    } else {
        const float qs = 0.25f * 1.4426950408889634f;
        u32x4 qx, qy;
#pragma unroll
        for (int i = 0; i < 4; ++i) {
            u32 a = pk(qacc[2 * i], qacc[2 * i + 1]), c = pk(qacc[8 + 2 * i], qacc[8 + 2 * i + 1]);
            qx[i] = pk(lo16(a) * qs, hi16(a) * qs);
            qy[i] = pk(lo16(c) * qs, hi16(c) * qs);
        }
        ST128(L_P1 + w * 2048 + lane * 16, qx);
        ST128(L_P1 + w * 2048 + 1024 + lane * 16, qy);
    }
    const u32 key = max(max(LD32(L_CT), LD32(L_CT + 4)), max(LD32(L_CT + 8), LD32(L_CT + 12)));
    const int sel = __builtin_amdgcn_readfirstlane(0xffff - (key & 0xffff)) & 2047;
    u32x8 EXA; u32x4 EXB, WOA;
    u32 skv = lb + L_K + kvo + (10 + 4 * (w >> 2)) * 1024, sg = lb + L_W + w * 2048;
    const u32 skv3 = lb + L_K + kvo + (26 + 2 * (w >> 2)) * 1024;
    u32 swo = lb + ((w & 4) ? L_V : L_K) + ((w >> 1) & 1) * 32768 + (w & 1) * 8192;
    {
        EXA[0] = kvo + (10 + 4 * (w >> 2)) * 1024 + lane * 16;
        EXA[1] = kvo + (26 + 2 * (w >> 2)) * 1024 + lane * 16;
        const u32 pos = lane >> 3, B = pos ^ ((pos >> 2) & 1);
        const u32 row = 64 * w + 4 * B + ((lane >> 1) & 3);
        const u64 g0 = (u64)((lane & 1) ? wu : wg) + (u64)(row * 3584 + fj * 16), g1 = g0 + 32 * 3584;
        EXA[2] = (u32)g0; EXA[3] = (u32)(g0 >> 32); EXA[4] = (u32)g1; EXA[5] = (u32)(g1 >> 32);
        EXA[6] = sel * 1024 + (64 * w + 16 * g) * 2;
        EXA[7] = fj * 8192 + (128 * (w & 3) + 2 * lane) * 2;
        const u32 ta = lb + L_W + w * 2048 + l15 * 8, gx = g & 1;
        EXB[0] = ta + (4 * g + gx) * 128;
        EXB[1] = ta + (4 * g + (gx ^ 1)) * 128;
        EXB[2] = lb + (l15 == 0 ? L_FP + w * 64 + g * 16 : L_P1 + w * 1024 + lane * 16);
        EXB[3] = 0;
#pragma unroll
        for (int i = 0; i < 4; ++i) {
            const u32 r = 64 * w + 8 * i + (lane >> 3), slot = lane & 7;
            const u32 f = ((r >> 1) & 1) | ((i & 1) << 1);
            const u32 c = (((slot >> 1) ^ f) << 1) | (slot & 1);
            WOA[i] = r * 1024 + hg * 128 + c * 16 - i * 1024;
        }
    }
    u32x2 PZA;
    {
        const int head = hg * 4 + hp * 2 + blk, tl = th * 32 + l31;
        PZA[0] = WS_O + ((tb * 4 + (tl >> 4)) * 16 + (head >> 1)) * 1024 + (((head & 1) * 2 + h) * 16 + (tl & 15)) * 16;
        PZA[1] = (ffn && w < 4) ? WS_Y + fj * 2048 + w * 512 + l31 * 16 : PZA[0];
    }
    const int hf = w >> 2;
    u32 ka = lb + L_K + hf * 32768 + lane * 16;
    const bool vlane = l31 < 16;
    u32 va = vlane ? (lb + L_V + hf * 32768 + h * 256 + l15 * 16) : (lb + L_ONE - 1024 + h * 256 + l15 * 16);
    u32 vinc = vlane ? 4096u : 0u;
    u32 va0 = vlane ? va : va + 1024;
    u32 qa = lb + L_P1 + (w & 3) * 2048 + lane * 16;
    asm volatile("" : "+{v[196:203]}"(EXA), "+{v[228:231]}"(EXB), "+{v[236:239]}"(WOA), "+{v[220:221]}"(PZA), "+{v193}"(ka), "+{v194}"(va), "+{v195}"(vinc), "+v"(va0), "+v"(qa),
                 "+s"(skv), "+s"(sg), "+s"(swo));
    if (w >= 4) VMWAIT(0);
    BAR();
    STAMP(10); STAMPC(19);
    if (b == 0 && tid == 256) { u32 z = 0; GST32C(ws, z, epoch); }

    u32x4 qx, qy;
    f32x16 SX0, SX1, SY0, SY1, OX, OY;
    u32x4 K0, K1, V00, V01, V10, V11;
    u32 idx = 0xDDDDu;
    {
        asm volatile(
            "ds_read_b128 v[160:163], %[qa]\n"
            "ds_read_b128 v[164:167], %[qa] offset:1024\n"
            "ds_read_b128 v[168:171], v193\n"
            "ds_read_b128 v[172:175], v193 offset:1024\n"
            "ds_read_b128 v[176:179], %[va0]\n"
            "ds_read_b128 v[180:183], %[va0] offset:512\n"
            "v_mov_b32 v128, 0\n v_mov_b32 v129, 0\n v_mov_b32 v130, 0\n v_mov_b32 v131, 0\n"
            "v_mov_b32 v132, 0\n v_mov_b32 v133, 0\n v_mov_b32 v134, 0\n v_mov_b32 v135, 0\n"
            "v_mov_b32 v136, 0\n v_mov_b32 v137, 0\n v_mov_b32 v138, 0\n v_mov_b32 v139, 0\n"
            "v_mov_b32 v140, 0\n v_mov_b32 v141, 0\n v_mov_b32 v142, 0\n v_mov_b32 v143, 0\n"
            "v_mov_b32 v144, 0\n v_mov_b32 v145, 0\n v_mov_b32 v146, 0\n v_mov_b32 v147, 0\n"
            "v_mov_b32 v148, 0\n v_mov_b32 v149, 0\n v_mov_b32 v150, 0\n v_mov_b32 v151, 0\n"
            "v_mov_b32 v152, 0\n v_mov_b32 v153, 0\n v_mov_b32 v154, 0\n v_mov_b32 v155, 0\n"
            "v_mov_b32 v156, 0\n v_mov_b32 v157, 0\n v_mov_b32 v158, 0\n v_mov_b32 v159, 0\n"
            "v_mov_b32 v120, 0\n v_mov_b32 v121, 0\n v_mov_b32 v122, 0\n v_mov_b32 v123, 0\n"
            "v_mov_b32 v124, 0\n v_mov_b32 v125, 0\n v_mov_b32 v126, 0\n v_mov_b32 v127, 0\n"
            "v_mov_b32 v188, 0\n v_mov_b32 v189, 0\n v_mov_b32 v190, 0\n v_mov_b32 v191, 0\n"
            "s_waitcnt lgkmcnt(0)\n"
            "v_mfma_f32_32x32x16_bf16 v[64:79], v[168:171], v[160:163], 0\n"
            "v_mfma_f32_32x32x16_bf16 v[96:111], v[168:171], v[164:167], 0\n"
            "s_nop 7\n s_nop 7\n"
            : "={v[64:79]}"(SX0), "={v[80:95]}"(SX1), "={v[96:111]}"(SY0), "={v[112:127]}"(SY1),
              "={v[128:143]}"(OX), "={v[144:159]}"(OY),
              "={v[168:171]}"(K0), "={v[172:175]}"(K1), "={v[176:179]}"(V00), "={v[180:183]}"(V01),
              "={v[184:187]}"(V10), "={v[188:191]}"(V11), "=&{v[160:163]}"(qx), "=&{v[164:167]}"(qy)
            : "{v193}"(ka), [va0] "v"(va0), [qa] "v"(qa)
            : "memory");
    }
#define ATT_OPS \
            : "+{v[64:79]}"(SX0), "+{v[80:95]}"(SX1), "+{v[96:111]}"(SY0), "+{v[112:127]}"(SY1), \
              "+{v[128:143]}"(OX), "+{v[144:159]}"(OY), \
              "+{v[168:171]}"(K0), "+{v[172:175]}"(K1), "+{v[176:179]}"(V00), "+{v[180:183]}"(V01), \
              "+{v[184:187]}"(V10), "+{v[188:191]}"(V11), "+{v193}"(ka), "+{v194}"(va), [cnt] "=&s"(cnt_)
#define ATT_INS : "{v[160:163]}"(qx), "{v[164:167]}"(qy), "{v192}"(idx), "{v195}"(vinc)
    u32 cnt_;
    const char* kvb = ws + WS_K;
    PRIO_START();
    asm volatile(ATT_A ATT_OPS, "+{v[196:203]}"(EXA), "+{v[228:231]}"(EXB), "+{v[236:239]}"(WOA), "+{v[220:221]}"(PZA), [skv] "+s"(skv), [sg] "+s"(sg)
                 ATT_INS, [kvb] "s"(kvb), [wsb] "s"(ws) : "memory", "scc", "v232", "v233", "v234", "v235");
    LSTAMP(4);
    BAR();
    LSTAMP(8);
    if (tid == 448) { u32 fo = WS_F2 + b * 4; GST32C(ws, fo, epoch); }
    PRIO_START();
    u32x16 EXD;
    asm volatile(ATT_B ATT_OPS, "+{v[196:203]}"(EXA), "=&{v[204:219]}"(EXD), "+{v[236:239]}"(WOA)
                 ATT_INS, "{v[228:231]}"(EXB), [kvb] "s"(kvb), [skv3] "s"(skv3), [xb] "s"(x), [wdb] "s"(wd)
                 : "memory", "scc", "v220", "v221", "v222", "v223", "v224", "v225", "v226", "v227", "v232", "v233", "v234", "v235");
    LSTAMP(9);
    BAR();
    LSTAMP(11);
    if (ffn && w < 4) {
        float sum = LDF(L_FP + l15 * 4);
#pragma unroll
        for (int i = 1; i < 8; ++i) sum += LDF(L_FP + i * 64 + l15 * 4);
        sum = bfr(sum);
        float up = __shfl(sum, (lane & 7) + 8);
        float gt = __shfl(sum, lane & 7);
        float hv = bfr(bfr(gt * sigm(gt)) * up);
        u32 hb = __float_as_uint(hv);
        float y0 = 0.f, y1 = 0.f;
#pragma unroll
        for (int i = 0; i < 8; ++i) {
            const float hs = __uint_as_float((u32)__builtin_amdgcn_readlane(hb, i));
            y0 += hs * lo16(EXD[8 + i]);
            y1 += hs * hi16(EXD[8 + i]);
        }
        u32x2 gr = {__float_as_uint(y0), __float_as_uint(y1)};
        gr[0] = gr[0] == POISON ? 0x7fc00000u : gr[0];
        gr[1] = gr[1] == POISON ? 0x7fc00000u : gr[1];
        u32 yo = WS_Y + fj * 2048 + (128 * w + 2 * lane) * 4;
        GST64C(ws, yo, gr);
    }
    LSTAMP(22);
    PRIO_START();
    u32 f2v; const u32 f2a = WS_F2 + (32 * w + l31) * 4;
    asm volatile(ATT_C ATT_OPS, "+{v[236:239]}"(WOA), "=&{v220}"(f2v), [swo] "+s"(swo) ATT_INS, "{v221}"(f2a), [wob] "s"(wo), [wsb] "s"(ws) : "memory", "scc");
    const int tq = w & 3, kh = w >> 2;
    const bool owner = (sel >> 6) == tb;
    int lane_b = lane;
    asm volatile("" : "+v"(lane_b));
    {
    const int lane = lane_b;
    const int g = lane >> 4, l15 = lane & 15, h = lane >> 5, l31 = lane & 31;
    const int tokg = tb * 64 + tq * 16 + l15;
    const int ncol = hg * 64 + kh * 32 + (g & 1) * 16 + (g >> 1) * 8;
    u32 oslot;
    {
        const int head = hg * 4 + hp * 2 + blk, tl = th * 32 + l31;
        oslot = WS_O + ((tb * 4 + (tl >> 4)) * 16 + (head >> 1)) * 1024 + (((head & 1) * 2 + h) * 16 + (tl & 15)) * 16;
    }
    STAMP(12); STAMPC(20);
    {
        u32 spins = 0;
        while (!__all(f2v == epoch) && ++spins <= SPIN_MAX) {
            const u32 a = WS_F2 + (32 * w + l31) * 4;
            asm volatile("global_load_dword %0, %1, %2 sc1\n\ts_waitcnt vmcnt(0)" : "=&v"(f2v) : "v"(a), "s"(ws) : "memory");
        }
    }
    u32x4 xe, Y0, Y1, Y2, Y3, Y4, Y5, Y6; u32 gl;
    const u32 yo0 = WS_Y + (28 * w + g) * 2048 + hg * 256 + l15 * 16;
    {
        u32 xo = tokg * 1024 + ncol * 2;
        GLD128(xe, x, xo, 0);
        u32 go = WS_G + tokg * 4;
        GLD32C(gl, ws, go, 0);
    }

    {
        f32x16& mine = blk ? OY : OX;
        f32x16& give = blk ? OX : OY;
        const u32 cw = L_W + w * 2304, cr = L_W + (w ^ 4) * 2304;
        ST128(cw + lane * 16, ((u32x4){__float_as_uint(give[0]), __float_as_uint(give[1]), __float_as_uint(give[2]), __float_as_uint(give[3])}));
        ST128(cw + 1024 + lane * 16, ((u32x4){__float_as_uint(give[4]), __float_as_uint(give[5]), __float_as_uint(give[6]), __float_as_uint(give[7])}));
        STF(cw + 2048 + lane * 4, give[8]);
        BAR();
        f32x4 p0 = __builtin_bit_cast(f32x4, LD128(cr + lane * 16));
        f32x4 p1 = __builtin_bit_cast(f32x4, LD128(cr + 1024 + lane * 16));
        float l = LDF(cr + 2048 + lane * 4) + mine[8];
        float o[8];
#pragma unroll
        for (int i = 0; i < 4; ++i) { o[i] = mine[i] + p0[i]; o[4 + i] = mine[4 + i] + p1[i]; }
        {
            auto sw = __builtin_amdgcn_permlane32_swap(__float_as_uint(l), __float_as_uint(l), false, false);
            l = __uint_as_float(sw[0]);
        }
        float amax = fmaxf(fmaxf(fmaxf(fabsf(o[0]), fabsf(o[1])), fmaxf(fabsf(o[2]), fabsf(o[3]))),
                           fmaxf(fmaxf(fabsf(o[4]), fabsf(o[5])), fmaxf(fabsf(o[6]), fabsf(o[7]))));
        bool safe = (l >= 8.6736174e-19f) && (l <= 1.2676506e30f) && (amax <= 3.0e38f);
        if (!__all(safe)) {
            slow_block(ws, blk ? qy : qx, lane, o, l);
            auto sw = __builtin_amdgcn_permlane32_swap(__float_as_uint(l), __float_as_uint(l), false, false);
            l = __uint_as_float(sw[0]);
        }
        const float ri = __builtin_amdgcn_rcpf(l);
        u32 A0 = pk(o[0] * ri, o[1] * ri), A1 = pk(o[2] * ri, o[3] * ri);
        u32 B0 = pk(o[4] * ri, o[5] * ri), B1 = pk(o[6] * ri, o[7] * ri);
        auto s0 = __builtin_amdgcn_permlane32_swap(A0, B0, false, false);
        auto s1 = __builtin_amdgcn_permlane32_swap(A1, B1, false, false);
        u32x4 ov = {s0[0], s1[0], s0[1], s1[1]};
#pragma unroll
        for (int i = 0; i < 4; ++i) ov[i] = ov[i] == POISON ? 0x7fc07fc0u : ov[i];
        GST128C(ws, oslot, ov);
    }
    {
        u32 yo = yo0;
        asm volatile("s_cmp_lg_u32 %[stb], %[tb]\n\ts_cbranch_scc1 3f\n"
                     "global_load_dwordx4 %0, %[yo], %[ws] sc1\n v_add_u32 %[yo], 0x2000, %[yo]\n"
                     "global_load_dwordx4 %1, %[yo], %[ws] sc1\n v_add_u32 %[yo], 0x2000, %[yo]\n"
                     "global_load_dwordx4 %2, %[yo], %[ws] sc1\n v_add_u32 %[yo], 0x2000, %[yo]\n"
                     "global_load_dwordx4 %3, %[yo], %[ws] sc1\n v_add_u32 %[yo], 0x2000, %[yo]\n"
                     "global_load_dwordx4 %4, %[yo], %[ws] sc1\n v_add_u32 %[yo], 0x2000, %[yo]\n"
                     "global_load_dwordx4 %5, %[yo], %[ws] sc1\n v_add_u32 %[yo], 0x2000, %[yo]\n"
                     "global_load_dwordx4 %6, %[yo], %[ws] sc1\n3:"
                     : "=&v"(Y0), "=&v"(Y1), "=&v"(Y2), "=&v"(Y3), "=&v"(Y4), "=&v"(Y5), "=&v"(Y6), [yo] "+v"(yo)
                     : [stb] "s"(sel >> 6), [tb] "s"(tb), [ws] "s"(ws) : "memory", "scc");
    }
    STAMP(13);

    u32x4 af[4][8];
    {
        const u32 f = ((l15 >> 3) & 1) | ((g & 1) << 1);
        const u32 rb = (kh ? L_V : L_K) + (8 * g + (l15 >> 2)) * 128 + (l15 & 3) * 8;
#pragma unroll
        for (int nb = 0; nb < 4; ++nb) {
            u32 a = rb + ((nb ^ f) * 32);
            asm volatile("" : "+v"(a));
#pragma unroll
            for (int st = 0; st < 8; ++st) {
                const u32 o_ = (st >> 2) * 32768 + (st & 3) * 4096;
                af[nb][st] = cat(trread(lds, a + o_), trread(lds, a + o_ + 512));
            }
        }
    }
    STAMP(14);
    asm volatile("s_cmp_lg_u32 %[stb], %[tb]\n\ts_cbranch_scc1 3f\n\ts_waitcnt vmcnt(0)\n3:\n\ts_waitcnt vmcnt(1)"
                 : "+v"(xe), "+v"(gl), "+v"(Y0), "+v"(Y1), "+v"(Y2), "+v"(Y3), "+v"(Y4), "+v"(Y5), "+v"(Y6)
                 : [stb] "s"(sel >> 6), [tb] "s"(tb) : "memory", "scc");
    if (owner) {
        u32 spins = 0;
#define YMX(v) max(max(v[0], v[1]), max(v[2], v[3]))
        while (!__all(max(max(max(YMX(Y0), YMX(Y1)), max(YMX(Y2), YMX(Y3))), max(max(YMX(Y4), YMX(Y5)), YMX(Y6))) != POISON) && ++spins <= SPIN_MAX) {
            u32 yo = yo0;
            asm volatile("global_load_dwordx4 %0, %[yo], %[ws] sc1\n v_add_u32 %[yo], 0x2000, %[yo]\n"
                         "global_load_dwordx4 %1, %[yo], %[ws] sc1\n v_add_u32 %[yo], 0x2000, %[yo]\n"
                         "global_load_dwordx4 %2, %[yo], %[ws] sc1\n v_add_u32 %[yo], 0x2000, %[yo]\n"
                         "global_load_dwordx4 %3, %[yo], %[ws] sc1\n v_add_u32 %[yo], 0x2000, %[yo]\n"
                         "global_load_dwordx4 %4, %[yo], %[ws] sc1\n v_add_u32 %[yo], 0x2000, %[yo]\n"
                         "global_load_dwordx4 %5, %[yo], %[ws] sc1\n v_add_u32 %[yo], 0x2000, %[yo]\n"
                         "global_load_dwordx4 %6, %[yo], %[ws] sc1\n s_waitcnt vmcnt(0)"
                         : "=&v"(Y0), "=&v"(Y1), "=&v"(Y2), "=&v"(Y3), "=&v"(Y4), "=&v"(Y5), "=&v"(Y6), [yo] "+v"(yo)
                         : [ws] "s"(ws) : "memory");
        }
        f32x4 ys;
#pragma unroll
        for (int i = 0; i < 4; ++i) {
            float v = ((__uint_as_float(Y0[i]) + __uint_as_float(Y1[i])) + (__uint_as_float(Y2[i]) + __uint_as_float(Y3[i])))
                    + ((__uint_as_float(Y4[i]) + __uint_as_float(Y5[i])) + __uint_as_float(Y6[i]));
            auto a = __builtin_amdgcn_permlane32_swap(__float_as_uint(v), __float_as_uint(v), false, false);
            v = __uint_as_float(a[0]) + __uint_as_float(a[1]);
            auto c = __builtin_amdgcn_permlane16_swap(__float_as_uint(v), __float_as_uint(v), false, false);
            ys[i] = __uint_as_float(c[0]) + __uint_as_float(c[1]);
        }
        if (g == 0) ST128(L_YP + w * 256 + l15 * 16, __builtin_bit_cast(u32x4, ys));
    }
    u32x4 of0, of1, of2, of3, of4, of5, of6, of7;
    {
        const u32 oo = WS_O + ((tb * 4 + tq) * 16 + 8 * kh) * 1024 + lane * 16, o2 = oo + 4096;
        u32 spins = 0;
        while (true) {
            asm volatile("global_load_dwordx4 %0, %8, %10 sc1\n\tglobal_load_dwordx4 %1, %8, %10 offset:1024 sc1\n\t"
                         "global_load_dwordx4 %2, %8, %10 offset:2048 sc1\n\tglobal_load_dwordx4 %3, %8, %10 offset:3072 sc1\n\t"
                         "global_load_dwordx4 %4, %9, %10 sc1\n\tglobal_load_dwordx4 %5, %9, %10 offset:1024 sc1\n\t"
                         "global_load_dwordx4 %6, %9, %10 offset:2048 sc1\n\tglobal_load_dwordx4 %7, %9, %10 offset:3072 sc1\n\t"
                         "s_waitcnt vmcnt(0)"
                         : "=&v"(of0), "=&v"(of1), "=&v"(of2), "=&v"(of3), "=&v"(of4), "=&v"(of5), "=&v"(of6), "=&v"(of7)
                         : "v"(oo), "v"(o2), "s"(ws) : "memory");
            u32 mx = 0;
#define OMX(v) mx = max(max(mx, max(v[0], v[1])), max(v[2], v[3]));
            OMX(of0) OMX(of1) OMX(of2) OMX(of3) OMX(of4) OMX(of5) OMX(of6) OMX(of7)
            if (__all(mx != POISON) || ++spins > SPIN_MAX) break;
        }
    }
    STAMP(15);
    f32x4 acc[4] = {{0, 0, 0, 0}, {0, 0, 0, 0}, {0, 0, 0, 0}, {0, 0, 0, 0}};
#define OSTEP(OF, ST) \
    _Pragma("unroll") for (int nb = 0; nb < 4; ++nb) acc[nb] = __builtin_amdgcn_mfma_f32_16x16x32_bf16(asbf(af[nb][ST]), asbf(OF), acc[nb], 0, 0, 0);
    OSTEP(of0, 0) OSTEP(of1, 1) OSTEP(of2, 2) OSTEP(of3, 3) OSTEP(of4, 4) OSTEP(of5, 5) OSTEP(of6, 6) OSTEP(of7, 7)
    STAMP(16);
    {
        f32x4 k0 = kh ? acc[2] : acc[0], k1 = kh ? acc[3] : acc[1];
        f32x4 g0 = kh ? acc[0] : acc[2], g1 = kh ? acc[1] : acc[3];
        ST128(L_K + 16384 + w * 2048 + lane * 16, __builtin_bit_cast(u32x4, g0));
        ST128(L_K + 16384 + w * 2048 + 1024 + lane * 16, __builtin_bit_cast(u32x4, g1));
        BAR();
        k0 += __builtin_bit_cast(f32x4, LD128(L_K + 16384 + (w ^ 4) * 2048 + lane * 16));
        k1 += __builtin_bit_cast(f32x4, LD128(L_K + 16384 + (w ^ 4) * 2048 + 1024 + lane * 16));
        float a[8];
#pragma unroll
        for (int i = 0; i < 4; ++i) {
            auto sw = __builtin_amdgcn_permlane16_swap(__float_as_uint(k0[i]), __float_as_uint(k1[i]), false, false);
            a[i] = __uint_as_float(sw[0]); a[4 + i] = __uint_as_float(sw[1]);
        }
        const float gt = bfr(sigm(bfr(__uint_as_float(gl))));
        const float og = bfr(1.0f - gt);
        float bb[8];
#pragma unroll
        for (int i = 0; i < 4; ++i) { bb[2 * i] = lo16(xe[i]); bb[2 * i + 1] = hi16(xe[i]); }
        if (owner && tokg == sel) {
            const u32 rord = key >> 16;
            const float rsel = __uint_as_float(((rord & 0x8000u) ? (rord & 0x7fffu) : (~rord & 0xffffu)) << 16);
            const float sg = bfr(sigm(rsel));
            const int c0 = kh * 32 + (g & 1) * 16 + (g >> 1) * 8;
#pragma unroll
            for (int i = 0; i < 8; ++i) {
                float y = 0.f;
#pragma unroll
                for (int p = 0; p < 8; ++p) y += LDF(L_YP + p * 256 + (c0 + i) * 4);
                bb[i] = bfr(bb[i] + bfr(bfr(y) * sg));
            }
        }
        u32x4 ov;
#pragma unroll
        for (int i = 0; i < 4; ++i)
            ov[i] = pk(bfr(gt * bfr(a[2 * i])) + bfr(og * bb[2 * i]), bfr(gt * bfr(a[2 * i + 1])) + bfr(og * bb[2 * i + 1]));
        u32 oo_ = tokg * 1024 + ncol * 2;
        GST128(out, oo_, ov);
    }
    STAMP(17); STAMPC(21); STAMPDUMP();
    }
}

extern "C" void kernel_launch(void* const* d_in, const int* in_sizes, int n_in, void* d_out, int out_size,
                              void* d_ws, size_t ws_size, hipStream_t stream) {
    (void)in_sizes; (void)n_in; (void)out_size; (void)ws_size;
    fused_kernel<<<256, 512, 0, stream>>>((const u16*)d_in[0], (const u16*)d_in[1], (const u16*)d_in[2], (const u16*)d_in[3],
                                          (const u16*)d_in[4], (const u16*)d_in[6], (const u16*)d_in[7], (const u16*)d_in[8],
                                          (const u16*)d_in[9], (const u16*)d_in[10], (u16*)d_out, (char*)d_ws);
}
```

```cpp
#include <hip/hip_runtime.h>
#include <stdint.h>
#include <stdio.h>
#define DBG_STAMP
#define V2N

typedef unsigned int u32;
typedef unsigned long long u64;
typedef unsigned short u16;
typedef u32 u32x2 __attribute__((ext_vector_type(2)));
typedef u32 u32x4 __attribute__((ext_vector_type(4)));
typedef u32 u32x8 __attribute__((ext_vector_type(8)));
typedef u32 u32x16 __attribute__((ext_vector_type(16)));
typedef float f32x2 __attribute__((ext_vector_type(2)));
typedef float f32x4 __attribute__((ext_vector_type(4)));
typedef float f32x16 __attribute__((ext_vector_type(16)));
typedef __bf16 bf16x2 __attribute__((ext_vector_type(2)));
typedef __bf16 bf16x8 __attribute__((ext_vector_type(8)));
#define LDSP __attribute__((address_space(3)))

constexpr u32 WS_E   = 0;
constexpr u32 WS_F2  = 4096;
constexpr u32 WS_G   = 24576;
constexpr u32 WS_K   = 65536;
constexpr u32 WS_V   = 131072;
constexpr u32 WS_F1  = 262144;
constexpr u32 WS_O   = 524288;
constexpr u32 WS_Y   = 2621440;
constexpr u32 WS_DBG = 4194304;

constexpr u32 L_K   = 0;
constexpr u32 L_V   = 65536;
constexpr u32 L_ONE = 131072;
constexpr u32 L_W   = 135168;
constexpr u32 L_M   = 151552;
constexpr u32 L_P1  = L_M;
constexpr u32 L_PD  = L_M + 8192;
constexpr u32 L_FP  = L_M + 8704;
constexpr u32 L_YP  = L_M + 9216;
constexpr u32 L_CT  = L_M + 11264;
constexpr u32 L_END = 163840;

constexpr u32 SPIN_MAX = 1u << 22;
constexpr u32 POISON = 0xffffffffu;

#define LD128(a) (*(const LDSP u32x4*)(lds + (a)))
#define ST128(a, v) (*(LDSP u32x4*)(lds + (a)) = (v))
#define LD32(a) (*(const LDSP u32*)(lds + (a)))
#define ST32(a, v) (*(LDSP u32*)(lds + (a)) = (v))
#define LDF(a) (*(const LDSP float*)(lds + (a)))
#define STF(a, v) (*(LDSP float*)(lds + (a)) = (v))

__device__ __forceinline__ u32x2 trread(LDSP char* lds, u32 a) {
    typedef short v4s __attribute__((ext_vector_type(4)));
    v4s r = __builtin_amdgcn_ds_read_tr16_b64_v4i16((LDSP v4s*)(lds + a));
    return __builtin_bit_cast(u32x2, r);
}
__device__ __forceinline__ bf16x8 asbf(u32x4 v) { return __builtin_bit_cast(bf16x8, v); }
__device__ __forceinline__ u32x4 cat(u32x2 a, u32x2 b) { u32x4 r = {a[0], a[1], b[0], b[1]}; return r; }
__device__ __forceinline__ u32 pk(float a, float b) {
    f32x2 f = {a, b};
    return __builtin_bit_cast(u32, __builtin_convertvector(f, bf16x2));
}
__device__ __forceinline__ float bfr(float a) {
    return __uint_as_float(pk(a, a) & 0xffff0000u);
}
__device__ __forceinline__ float lo16(u32 v) { return __uint_as_float(v << 16); }
__device__ __forceinline__ float hi16(u32 v) { return __uint_as_float(v & 0xffff0000u); }
__device__ __forceinline__ float sigm(float z) { return __builtin_amdgcn_rcpf(1.0f + __builtin_amdgcn_exp2f(-1.4426950408889634f * z)); }

__device__ __forceinline__ void dma16(const void* base, u32 voff, u32 ldsaddr) {
    asm volatile("s_mov_b32 m0, %0\n\ts_nop 0\n\tglobal_load_lds_dwordx4 %1, %2" :: "s"(ldsaddr), "v"(voff), "s"(base) : "memory");
}
__device__ __forceinline__ void dma16c4(const void* base, u32 voff, u32 ldsaddr) {
    asm volatile("s_mov_b32 m0, %0\n\ts_nop 0\n\tglobal_load_lds_dwordx4 %1, %2 sc1\n\tglobal_load_lds_dwordx4 %1, %2 offset:1024 sc1\n\t"
                 "global_load_lds_dwordx4 %1, %2 offset:2048 sc1\n\tglobal_load_lds_dwordx4 %1, %2 offset:3072 sc1"
                 :: "s"(ldsaddr), "v"(voff), "s"(base) : "memory");
}
__device__ __forceinline__ void dma16c3(const void* base, u32 voff, u32 ldsaddr) {
    asm volatile("s_mov_b32 m0, %0\n\ts_nop 0\n\tglobal_load_lds_dwordx4 %1, %2 sc1\n\tglobal_load_lds_dwordx4 %1, %2 offset:1024 sc1\n\t"
                 "global_load_lds_dwordx4 %1, %2 offset:2048 sc1"
                 :: "s"(ldsaddr), "v"(voff), "s"(base) : "memory");
}
__device__ __forceinline__ void dma16c2(const void* base, u32 voff, u32 ldsaddr) {
    asm volatile("s_mov_b32 m0, %0\n\ts_nop 0\n\tglobal_load_lds_dwordx4 %1, %2 sc1\n\tglobal_load_lds_dwordx4 %1, %2 offset:1024 sc1"
                 :: "s"(ldsaddr), "v"(voff), "s"(base) : "memory");
}
__device__ __forceinline__ void dma16c(const void* base, u32 voff, u32 ldsaddr) {
    asm volatile("s_mov_b32 m0, %0\n\ts_nop 0\n\tglobal_load_lds_dwordx4 %1, %2 sc1" :: "s"(ldsaddr), "v"(voff), "s"(base) : "memory");
}
#define GLD128(dst, base, voff, OFF)  asm volatile("global_load_dwordx4 %0, %1, %2 offset:" #OFF : "=v"(dst) : "v"(voff), "s"(base) : "memory")
#define GLD128C(dst, base, voff, OFF) asm volatile("global_load_dwordx4 %0, %1, %2 offset:" #OFF " sc1" : "=v"(dst) : "v"(voff), "s"(base) : "memory")
#define GLD64C(dst, base, voff, OFF)  asm volatile("global_load_dwordx2 %0, %1, %2 offset:" #OFF " sc1" : "=v"(dst) : "v"(voff), "s"(base) : "memory")
#define GLD32C(dst, base, voff, OFF)  asm volatile("global_load_dword %0, %1, %2 offset:" #OFF " sc1" : "=v"(dst) : "v"(voff), "s"(base) : "memory")
#define GLD32(dst, base, voff, OFF)   asm volatile("global_load_dword %0, %1, %2 offset:" #OFF : "=v"(dst) : "v"(voff), "s"(base) : "memory")
#define GLD16(dst, base, voff, OFF)   asm volatile("global_load_ushort %0, %1, %2 offset:" #OFF : "=v"(dst) : "v"(voff), "s"(base) : "memory")
#define GST128(base, voff, val)  asm volatile("global_store_dwordx4 %0, %1, %2\n\ts_nop 1" :: "v"(voff), "v"(val), "s"(base) : "memory")
#define GST128C(base, voff, val) asm volatile("global_store_dwordx4 %0, %1, %2 sc1\n\ts_nop 1" :: "v"(voff), "v"(val), "s"(base) : "memory")
#define GST64C(base, voff, val)  asm volatile("global_store_dwordx2 %0, %1, %2 sc1\n\ts_nop 0" :: "v"(voff), "v"(val), "s"(base) : "memory")
#define GST32C(base, voff, val)  asm volatile("global_store_dword %0, %1, %2 sc1" :: "v"(voff), "v"(val), "s"(base) : "memory")
#define VMWAIT(N) asm volatile("s_waitcnt vmcnt(" #N ")" ::: "memory")
#define BAR() asm volatile("s_waitcnt lgkmcnt(0)\n\ts_barrier" ::: "memory")

#define STAMP(i) do { u32 t_ = (u32)__builtin_amdgcn_s_memrealtime(); if (lane == 0) ST32(L_CT + 64 + (w * 24 + (i)) * 4, t_); } while (0)
#define STAMPC(i) do { u32 t_ = (u32)__builtin_amdgcn_s_memtime(); if (lane == 0) ST32(L_CT + 64 + (w * 24 + (i)) * 4, t_); } while (0)
#define STAMPDUMP() do { if (lane < 24) ((u32*)(ws + WS_DBG))[(blockIdx.x * 8 + w) * 32 + lane] = LD32(L_CT + 64 + (w * 24 + lane) * 4); } while (0)
#define FSTAMP(i) STAMP(i)
#define LSTAMP(i)

__device__ __forceinline__ u32 wave_max_u32(u32 v) {
    v = max(v, (u32)__builtin_amdgcn_update_dpp(0, (int)v, 0x111, 0xf, 0xf, false));
    v = max(v, (u32)__builtin_amdgcn_update_dpp(0, (int)v, 0x112, 0xf, 0xf, false));
    v = max(v, (u32)__builtin_amdgcn_update_dpp(0, (int)v, 0x114, 0xf, 0xf, false));
    v = max(v, (u32)__builtin_amdgcn_update_dpp(0, (int)v, 0x118, 0xf, 0xf, false));
    v = max(v, (u32)__builtin_amdgcn_update_dpp(0, (int)v, 0x142, 0xa, 0xf, false));
    v = max(v, (u32)__builtin_amdgcn_update_dpp(0, (int)v, 0x143, 0xc, 0xf, false));
    return (u32)__builtin_amdgcn_readlane((int)v, 63);
}
#define ATT_A \
    "s_mov_b32 %[cnt], 2\n" \
    "v_mov_b32 v232, -1\n" \
    "v_mov_b32 v233, -1\n" \
    "v_mov_b32 v234, -1\n" \
    "v_mov_b32 v235, -1\n" \
    "1:\n" \
    "v_exp_f32 v64, v64\n" \
    "v_exp_f32 v65, v65\n" \
    "v_smfmac_f32_32x32x32_bf16 v[144:159], v[188:191], v[120:127], v192\n" \
    "global_store_dwordx4 v220, v[232:235], %[wsb] sc1\n" \
    "ds_read_b128 v[184:187], v194 offset:1024\n" \
    "v_exp_f32 v66, v66\n" \
    "v_exp_f32 v67, v67\n" \
    "v_exp_f32 v68, v68\n" \
    "v_exp_f32 v69, v69\n" \
    "s_waitcnt lgkmcnt(1)\n" \
    "v_mfma_f32_32x32x16_bf16 v[80:95], v[172:175], v[160:163], 0\n" \
    "ds_read_b128 v[188:191], v194 offset:1536\n" \
    "v_exp_f32 v70, v70\n" \
    "v_exp_f32 v71, v71\n" \
    "v_exp_f32 v72, v72\n" \
    "v_exp_f32 v73, v73\n" \
    "v_smfmac_f32_32x32x32_bf16 v[128:143], v[176:179], v[64:71], v192\n" \
    "v_exp_f32 v74, v74\n" \
    "s_mov_b32 m0, %[skv]\n" \
    "v_exp_f32 v75, v75\n" \
    "v_exp_f32 v76, v76\n" \
    "global_load_lds_dwordx4 v196, %[kvb] offset:0 sc1\n" \
    "v_exp_f32 v77, v77\n" \
    "v_exp_f32 v78, v78\n" \
    "v_exp_f32 v79, v79\n" \
    "v_exp_f32 v96, v96\n" \
    "v_exp_f32 v97, v97\n" \
    "v_smfmac_f32_32x32x32_bf16 v[128:143], v[180:183], v[72:79], v192\n" \
    "v_exp_f32 v98, v98\n" \
    "v_exp_f32 v99, v99\n" \
    "v_exp_f32 v100, v100\n" \
    "v_exp_f32 v101, v101\n" \
    "v_mfma_f32_32x32x16_bf16 v[112:127], v[172:175], v[164:167], 0\n" \
    "ds_read_b128 v[168:171], v193 offset:2048\n" \
    "v_exp_f32 v102, v102\n" \
    "v_exp_f32 v103, v103\n" \
    "v_exp_f32 v104, v104\n" \
    "v_exp_f32 v105, v105\n" \
    "v_smfmac_f32_32x32x32_bf16 v[144:159], v[176:179], v[96:103], v192\n" \
    "v_exp_f32 v106, v106\n" \
    "v_exp_f32 v107, v107\n" \
    "v_exp_f32 v108, v108\n" \
    "v_exp_f32 v109, v109\n" \
    "v_exp_f32 v110, v110\n" \
    "v_exp_f32 v111, v111\n" \
    "v_exp_f32 v80, v80\n" \
    "v_exp_f32 v81, v81\n" \
    "v_smfmac_f32_32x32x32_bf16 v[144:159], v[180:183], v[104:111], v192\n" \
    "ds_read_b128 v[176:179], v194 offset:2048\n" \
    "v_exp_f32 v82, v82\n" \
    "v_exp_f32 v83, v83\n" \
    "v_exp_f32 v84, v84\n" \
    "v_exp_f32 v85, v85\n" \
    "s_waitcnt lgkmcnt(1)\n" \
    "v_mfma_f32_32x32x16_bf16 v[64:79], v[168:171], v[160:163], 0\n" \
    "ds_read_b128 v[180:183], v194 offset:2560\n" \
    "v_exp_f32 v86, v86\n" \
    "v_exp_f32 v87, v87\n" \
    "v_exp_f32 v88, v88\n" \
    "v_exp_f32 v89, v89\n" \
    "v_smfmac_f32_32x32x32_bf16 v[128:143], v[184:187], v[80:87], v192\n" \
    "v_exp_f32 v90, v90\n" \
    "s_mov_b32 m0, %[skv]\n" \
    "v_exp_f32 v91, v91\n" \
    "v_exp_f32 v92, v92\n" \
    "global_load_lds_dwordx4 v196, %[kvb] offset:1024 sc1\n" \
    "v_exp_f32 v93, v93\n" \
    "v_exp_f32 v94, v94\n" \
    "v_exp_f32 v95, v95\n" \
    "v_exp_f32 v112, v112\n" \
    "v_exp_f32 v113, v113\n" \
    "v_smfmac_f32_32x32x32_bf16 v[128:143], v[188:191], v[88:95], v192\n" \
    "v_exp_f32 v114, v114\n" \
    "v_exp_f32 v115, v115\n" \
    "v_exp_f32 v116, v116\n" \
    "v_exp_f32 v117, v117\n" \
    "v_mfma_f32_32x32x16_bf16 v[96:111], v[168:171], v[164:167], 0\n" \
    "ds_read_b128 v[172:175], v193 offset:3072\n" \
    "v_exp_f32 v118, v118\n" \
    "v_exp_f32 v119, v119\n" \
    "v_exp_f32 v120, v120\n" \
    "v_exp_f32 v121, v121\n" \
    "v_smfmac_f32_32x32x32_bf16 v[144:159], v[184:187], v[112:119], v192\n" \
    "v_exp_f32 v122, v122\n" \
    "s_mov_b32 m0, %[sg]\n" \
    "v_exp_f32 v123, v123\n" \
    "v_exp_f32 v124, v124\n" \
    "global_load_lds_dwordx4 v[198:199], off\n" \
    "v_exp_f32 v125, v125\n" \
    "v_exp_f32 v126, v126\n" \
    "v_exp_f32 v127, v127\n" \
    "s_cmp_eq_u32 %[cnt], 1\n" \
    "s_cbranch_scc0 8f\n" \
    "s_setprio 0\n" \
    "8:\n" \
    "v_exp_f32 v64, v64\n" \
    "v_exp_f32 v65, v65\n" \
    "v_smfmac_f32_32x32x32_bf16 v[144:159], v[188:191], v[120:127], v192\n" \
    "ds_read_b128 v[184:187], v194 offset:3072\n" \
    "v_exp_f32 v66, v66\n" \
    "v_exp_f32 v67, v67\n" \
    "v_exp_f32 v68, v68\n" \
    "v_exp_f32 v69, v69\n" \
    "s_waitcnt lgkmcnt(1)\n" \
    "v_mfma_f32_32x32x16_bf16 v[80:95], v[172:175], v[160:163], 0\n" \
    "ds_read_b128 v[188:191], v194 offset:3584\n" \
    "v_exp_f32 v70, v70\n" \
    "v_exp_f32 v71, v71\n" \
    "v_exp_f32 v72, v72\n" \
    "v_exp_f32 v73, v73\n" \
    "v_smfmac_f32_32x32x32_bf16 v[128:143], v[176:179], v[64:71], v192\n" \
    "v_exp_f32 v74, v74\n" \
    "s_mov_b32 m0, %[skv]\n" \
    "v_exp_f32 v75, v75\n" \
    "v_exp_f32 v76, v76\n" \
    "global_load_lds_dwordx4 v196, %[kvb] offset:2048 sc1\n" \
    "v_exp_f32 v77, v77\n" \
    "v_exp_f32 v78, v78\n" \
    "v_exp_f32 v79, v79\n" \
    "v_exp_f32 v96, v96\n" \
    "v_exp_f32 v97, v97\n" \
    "v_smfmac_f32_32x32x32_bf16 v[128:143], v[180:183], v[72:79], v192\n" \
    "v_exp_f32 v98, v98\n" \
    "v_exp_f32 v99, v99\n" \
    "v_exp_f32 v100, v100\n" \
    "v_exp_f32 v101, v101\n" \
    "v_mfma_f32_32x32x16_bf16 v[112:127], v[172:175], v[164:167], 0\n" \
    "ds_read_b128 v[168:171], v193 offset:4096\n" \
    "v_exp_f32 v102, v102\n" \
    "v_exp_f32 v103, v103\n" \
    "v_exp_f32 v104, v104\n" \
    "v_exp_f32 v105, v105\n" \
    "v_smfmac_f32_32x32x32_bf16 v[144:159], v[176:179], v[96:103], v192\n" \
    "v_exp_f32 v106, v106\n" \
    "v_exp_f32 v107, v107\n" \
    "v_exp_f32 v108, v108\n" \
    "v_exp_f32 v109, v109\n" \
    "v_exp_f32 v110, v110\n" \
    "v_exp_f32 v111, v111\n" \
    "v_exp_f32 v80, v80\n" \
    "v_exp_f32 v81, v81\n" \
    "v_smfmac_f32_32x32x32_bf16 v[144:159], v[180:183], v[104:111], v192\n" \
    "ds_read_b128 v[176:179], v194 offset:4096\n" \
    "v_exp_f32 v82, v82\n" \
    "v_exp_f32 v83, v83\n" \
    "v_exp_f32 v84, v84\n" \
    "v_exp_f32 v85, v85\n" \
    "s_waitcnt lgkmcnt(1)\n" \
    "v_mfma_f32_32x32x16_bf16 v[64:79], v[168:171], v[160:163], 0\n" \
    "ds_read_b128 v[180:183], v194 offset:4608\n" \
    "v_exp_f32 v86, v86\n" \
    "v_exp_f32 v87, v87\n" \
    "v_exp_f32 v88, v88\n" \
    "v_exp_f32 v89, v89\n" \
    "v_smfmac_f32_32x32x32_bf16 v[128:143], v[184:187], v[80:87], v192\n" \
    "v_exp_f32 v90, v90\n" \
    "s_mov_b32 m0, %[skv]\n" \
    "v_exp_f32 v91, v91\n" \
    "v_exp_f32 v92, v92\n" \
    "global_load_lds_dwordx4 v196, %[kvb] offset:3072 sc1\n" \
    "v_exp_f32 v93, v93\n" \
    "v_exp_f32 v94, v94\n" \
    "v_exp_f32 v95, v95\n" \
    "v_exp_f32 v112, v112\n" \
    "v_exp_f32 v113, v113\n" \
    "v_smfmac_f32_32x32x32_bf16 v[128:143], v[188:191], v[88:95], v192\n" \
    "v_exp_f32 v114, v114\n" \
    "v_exp_f32 v115, v115\n" \
    "v_exp_f32 v116, v116\n" \
    "v_exp_f32 v117, v117\n" \
    "v_mfma_f32_32x32x16_bf16 v[96:111], v[168:171], v[164:167], 0\n" \
    "ds_read_b128 v[172:175], v193 offset:5120\n" \
    "v_exp_f32 v118, v118\n" \
    "v_exp_f32 v119, v119\n" \
    "v_exp_f32 v120, v120\n" \
    "v_exp_f32 v121, v121\n" \
    "v_smfmac_f32_32x32x32_bf16 v[144:159], v[184:187], v[112:119], v192\n" \
    "v_exp_f32 v122, v122\n" \
    "v_exp_f32 v123, v123\n" \
    "v_exp_f32 v124, v124\n" \
    "v_exp_f32 v125, v125\n" \
    "v_exp_f32 v126, v126\n" \
    "v_exp_f32 v127, v127\n" \
    "v_add_u32 v196, 0x2000, v196\n" \
    "v_mov_b32 v198, v200\n" \
    "v_mov_b32 v199, v201\n" \
    "s_add_u32 %[skv], %[skv], 0x2000\n" \
    "s_add_u32 %[sg], %[sg], 0x400\n" \
    "v_mov_b32 v220, v221\n" \
    "v_add_u32 v193, 4096, v193\n" \
    "v_add_u32 v194, v195, v194\n" \
    "s_sub_u32 %[cnt], %[cnt], 1\n" \
    "s_cmp_lg_u32 %[cnt], 0\n" \
    "s_cbranch_scc1 1b\n" \
    "s_waitcnt vmcnt(5)\n"

#define ATT_B \
    "v_exp_f32 v64, v64\n" \
    "v_exp_f32 v65, v65\n" \
    "v_smfmac_f32_32x32x32_bf16 v[144:159], v[188:191], v[120:127], v192\n" \
    "ds_read_b128 v[184:187], v194 offset:1024\n" \
    "v_exp_f32 v66, v66\n" \
    "v_exp_f32 v67, v67\n" \
    "v_exp_f32 v68, v68\n" \
    "v_exp_f32 v69, v69\n" \
    "s_waitcnt lgkmcnt(1)\n" \
    "v_mfma_f32_32x32x16_bf16 v[80:95], v[172:175], v[160:163], 0\n" \
    "ds_read_b128 v[188:191], v194 offset:1536\n" \
    "v_exp_f32 v70, v70\n" \
    "v_exp_f32 v71, v71\n" \
    "v_exp_f32 v72, v72\n" \
    "v_exp_f32 v73, v73\n" \
    "v_smfmac_f32_32x32x32_bf16 v[128:143], v[176:179], v[64:71], v192\n" \
    "v_exp_f32 v74, v74\n" \
    "s_mov_b32 m0, %[skv3]\n" \
    "v_exp_f32 v75, v75\n" \
    "v_exp_f32 v76, v76\n" \
    "global_load_dwordx4 v[204:207], v202, %[xb]\n" \
    "v_exp_f32 v77, v77\n" \
    "v_exp_f32 v78, v78\n" \
    "v_exp_f32 v79, v79\n" \
    "v_exp_f32 v96, v96\n" \
    "v_exp_f32 v97, v97\n" \
    "v_smfmac_f32_32x32x32_bf16 v[128:143], v[180:183], v[72:79], v192\n" \
    "v_exp_f32 v98, v98\n" \
    "v_exp_f32 v99, v99\n" \
    "v_exp_f32 v100, v100\n" \
    "v_exp_f32 v101, v101\n" \
    "v_mfma_f32_32x32x16_bf16 v[112:127], v[172:175], v[164:167], 0\n" \
    "ds_read_b128 v[168:171], v193 offset:2048\n" \
    "v_exp_f32 v102, v102\n" \
    "v_exp_f32 v103, v103\n" \
    "v_exp_f32 v104, v104\n" \
    "v_exp_f32 v105, v105\n" \
    "v_smfmac_f32_32x32x32_bf16 v[144:159], v[176:179], v[96:103], v192\n" \
    "v_exp_f32 v106, v106\n" \
    "v_exp_f32 v107, v107\n" \
    "v_exp_f32 v108, v108\n" \
    "global_load_dwordx4 v[208:211], v202, %[xb] offset:16\n" \
    "v_exp_f32 v109, v109\n" \
    "v_exp_f32 v110, v110\n" \
    "v_exp_f32 v111, v111\n" \
    "v_exp_f32 v80, v80\n" \
    "v_exp_f32 v81, v81\n" \
    "v_smfmac_f32_32x32x32_bf16 v[144:159], v[180:183], v[104:111], v192\n" \
    "ds_read_b128 v[176:179], v194 offset:2048\n" \
    "v_exp_f32 v82, v82\n" \
    "v_exp_f32 v83, v83\n" \
    "v_exp_f32 v84, v84\n" \
    "v_exp_f32 v85, v85\n" \
    "s_waitcnt lgkmcnt(1)\n" \
    "v_mfma_f32_32x32x16_bf16 v[64:79], v[168:171], v[160:163], 0\n" \
    "ds_read_b128 v[180:183], v194 offset:2560\n" \
    "v_exp_f32 v86, v86\n" \
    "v_exp_f32 v87, v87\n" \
    "v_exp_f32 v88, v88\n" \
    "v_exp_f32 v89, v89\n" \
    "v_smfmac_f32_32x32x32_bf16 v[128:143], v[184:187], v[80:87], v192\n" \
    "v_exp_f32 v90, v90\n" \
    "v_exp_f32 v91, v91\n" \
    "v_exp_f32 v92, v92\n" \
    "global_load_lds_dwordx4 v197, %[kvb] offset:0 sc1\n" \
    "v_exp_f32 v93, v93\n" \
    "v_exp_f32 v94, v94\n" \
    "v_exp_f32 v95, v95\n" \
    "v_exp_f32 v112, v112\n" \
    "v_exp_f32 v113, v113\n" \
    "v_smfmac_f32_32x32x32_bf16 v[128:143], v[188:191], v[88:95], v192\n" \
    "v_exp_f32 v114, v114\n" \
    "v_exp_f32 v115, v115\n" \
    "v_exp_f32 v116, v116\n" \
    "v_exp_f32 v117, v117\n" \
    "v_mfma_f32_32x32x16_bf16 v[96:111], v[168:171], v[164:167], 0\n" \
    "ds_read_b128 v[172:175], v193 offset:3072\n" \
    "v_exp_f32 v118, v118\n" \
    "v_exp_f32 v119, v119\n" \
    "v_exp_f32 v120, v120\n" \
    "v_exp_f32 v121, v121\n" \
    "v_smfmac_f32_32x32x32_bf16 v[144:159], v[184:187], v[112:119], v192\n" \
    "v_exp_f32 v122, v122\n" \
    "v_exp_f32 v123, v123\n" \
    "v_exp_f32 v124, v124\n" \
    "global_load_lds_dwordx4 v197, %[kvb] offset:1024 sc1\n" \
    "v_exp_f32 v125, v125\n" \
    "v_exp_f32 v126, v126\n" \
    "v_exp_f32 v127, v127\n" \
    "v_exp_f32 v64, v64\n" \
    "v_exp_f32 v65, v65\n" \
    "v_smfmac_f32_32x32x32_bf16 v[144:159], v[188:191], v[120:127], v192\n" \
    "ds_read_b128 v[184:187], v194 offset:3072\n" \
    "v_exp_f32 v66, v66\n" \
    "v_exp_f32 v67, v67\n" \
    "v_exp_f32 v68, v68\n" \
    "v_exp_f32 v69, v69\n" \
    "s_waitcnt lgkmcnt(1)\n" \
    "v_mfma_f32_32x32x16_bf16 v[80:95], v[172:175], v[160:163], 0\n" \
    "ds_read_b128 v[188:191], v194 offset:3584\n" \
    "v_exp_f32 v70, v70\n" \
    "v_exp_f32 v71, v71\n" \
    "v_exp_f32 v72, v72\n" \
    "v_exp_f32 v73, v73\n" \
    "v_smfmac_f32_32x32x32_bf16 v[128:143], v[176:179], v[64:71], v192\n" \
    "v_exp_f32 v74, v74\n" \
    "v_exp_f32 v75, v75\n" \
    "v_exp_f32 v76, v76\n" \
    "global_load_lds_dwordx4 v197, %[kvb] offset:2048 sc1\n" \
    "v_exp_f32 v77, v77\n" \
    "v_exp_f32 v78, v78\n" \
    "v_exp_f32 v79, v79\n" \
    "v_exp_f32 v96, v96\n" \
    "v_exp_f32 v97, v97\n" \
    "v_smfmac_f32_32x32x32_bf16 v[128:143], v[180:183], v[72:79], v192\n" \
    "v_exp_f32 v98, v98\n" \
    "v_exp_f32 v99, v99\n" \
    "v_exp_f32 v100, v100\n" \
    "v_exp_f32 v101, v101\n" \
    "v_mfma_f32_32x32x16_bf16 v[112:127], v[172:175], v[164:167], 0\n" \
    "ds_read_b128 v[168:171], v193 offset:4096\n" \
    "v_exp_f32 v102, v102\n" \
    "v_exp_f32 v103, v103\n" \
    "v_exp_f32 v104, v104\n" \
    "v_exp_f32 v105, v105\n" \
    "v_smfmac_f32_32x32x32_bf16 v[144:159], v[176:179], v[96:103], v192\n" \
    "v_exp_f32 v106, v106\n" \
    "v_exp_f32 v107, v107\n" \
    "v_exp_f32 v108, v108\n" \
    "global_load_lds_dwordx4 v197, %[kvb] offset:3072 sc1\n" \
    "v_exp_f32 v109, v109\n" \
    "v_exp_f32 v110, v110\n" \
    "v_exp_f32 v111, v111\n" \
    "v_exp_f32 v80, v80\n" \
    "v_exp_f32 v81, v81\n" \
    "v_smfmac_f32_32x32x32_bf16 v[144:159], v[180:183], v[104:111], v192\n" \
    "ds_read_b128 v[176:179], v194 offset:4096\n" \
    "v_exp_f32 v82, v82\n" \
    "v_exp_f32 v83, v83\n" \
    "v_exp_f32 v84, v84\n" \
    "v_exp_f32 v85, v85\n" \
    "s_waitcnt lgkmcnt(1)\n" \
    "v_mfma_f32_32x32x16_bf16 v[64:79], v[168:171], v[160:163], 0\n" \
    "ds_read_b128 v[180:183], v194 offset:4608\n" \
    "v_exp_f32 v86, v86\n" \
    "v_exp_f32 v87, v87\n" \
    "v_exp_f32 v88, v88\n" \
    "v_exp_f32 v89, v89\n" \
    "v_smfmac_f32_32x32x32_bf16 v[128:143], v[184:187], v[80:87], v192\n" \
    "v_exp_f32 v90, v90\n" \
    "v_exp_f32 v91, v91\n" \
    "v_exp_f32 v92, v92\n" \
    "global_load_dword v212, v203, %[wdb] offset:0\n" \
    "global_load_dword v213, v203, %[wdb] offset:1024\n" \
    "v_exp_f32 v93, v93\n" \
    "v_exp_f32 v94, v94\n" \
    "v_exp_f32 v95, v95\n" \
    "v_exp_f32 v112, v112\n" \
    "v_exp_f32 v113, v113\n" \
    "v_smfmac_f32_32x32x32_bf16 v[128:143], v[188:191], v[88:95], v192\n" \
    "v_exp_f32 v114, v114\n" \
    "v_exp_f32 v115, v115\n" \
    "v_exp_f32 v116, v116\n" \
    "v_exp_f32 v117, v117\n" \
    "v_mfma_f32_32x32x16_bf16 v[96:111], v[168:171], v[164:167], 0\n" \
    "ds_read_b128 v[172:175], v193 offset:5120\n" \
    "v_exp_f32 v118, v118\n" \
    "v_exp_f32 v119, v119\n" \
    "v_exp_f32 v120, v120\n" \
    "v_exp_f32 v121, v121\n" \
    "v_smfmac_f32_32x32x32_bf16 v[144:159], v[184:187], v[112:119], v192\n" \
    "v_exp_f32 v122, v122\n" \
    "v_exp_f32 v123, v123\n" \
    "v_exp_f32 v124, v124\n" \
    "global_load_dword v214, v203, %[wdb] offset:2048\n" \
    "global_load_dword v215, v203, %[wdb] offset:3072\n" \
    "v_exp_f32 v125, v125\n" \
    "v_exp_f32 v126, v126\n" \
    "v_exp_f32 v127, v127\n" \
    "v_add_u32 v203, 0x1000, v203\n" \
    "v_add_u32 v193, 4096, v193\n" \
    "v_add_u32 v194, v195, v194\n" \
    "v_exp_f32 v64, v64\n" \
    "v_exp_f32 v65, v65\n" \
    "v_smfmac_f32_32x32x32_bf16 v[144:159], v[188:191], v[120:127], v192\n" \
    "ds_read_b128 v[184:187], v194 offset:1024\n" \
    "v_exp_f32 v66, v66\n" \
    "v_exp_f32 v67, v67\n" \
    "v_exp_f32 v68, v68\n" \
    "v_exp_f32 v69, v69\n" \
    "s_waitcnt lgkmcnt(1)\n" \
    "v_mfma_f32_32x32x16_bf16 v[80:95], v[172:175], v[160:163], 0\n" \
    "ds_read_b128 v[188:191], v194 offset:1536\n" \
    "v_exp_f32 v70, v70\n" \
    "v_exp_f32 v71, v71\n" \
    "v_exp_f32 v72, v72\n" \
    "v_exp_f32 v73, v73\n" \
    "v_smfmac_f32_32x32x32_bf16 v[128:143], v[176:179], v[64:71], v192\n" \
    "v_exp_f32 v74, v74\n" \
    "v_exp_f32 v75, v75\n" \
    "v_exp_f32 v76, v76\n" \
    "global_load_dword v216, v203, %[wdb] offset:0\n" \
    "global_load_dword v217, v203, %[wdb] offset:1024\n" \
    "v_exp_f32 v77, v77\n" \
    "v_exp_f32 v78, v78\n" \
    "v_exp_f32 v79, v79\n" \
    "v_exp_f32 v96, v96\n" \
    "v_exp_f32 v97, v97\n" \
    "v_smfmac_f32_32x32x32_bf16 v[128:143], v[180:183], v[72:79], v192\n" \
    "v_exp_f32 v98, v98\n" \
    "v_exp_f32 v99, v99\n" \
    "v_exp_f32 v100, v100\n" \
    "v_exp_f32 v101, v101\n" \
    "v_mfma_f32_32x32x16_bf16 v[112:127], v[172:175], v[164:167], 0\n" \
    "ds_read_b128 v[168:171], v193 offset:2048\n" \
    "v_exp_f32 v102, v102\n" \
    "v_exp_f32 v103, v103\n" \
    "v_exp_f32 v104, v104\n" \
    "v_exp_f32 v105, v105\n" \
    "v_smfmac_f32_32x32x32_bf16 v[144:159], v[176:179], v[96:103], v192\n" \
    "v_exp_f32 v106, v106\n" \
    "v_exp_f32 v107, v107\n" \
    "v_exp_f32 v108, v108\n" \
    "global_load_dword v218, v203, %[wdb] offset:2048\n" \
    "global_load_dword v219, v203, %[wdb] offset:3072\n" \
    "v_exp_f32 v109, v109\n" \
    "v_exp_f32 v110, v110\n" \
    "v_exp_f32 v111, v111\n" \
    "v_exp_f32 v80, v80\n" \
    "v_exp_f32 v81, v81\n" \
    "v_smfmac_f32_32x32x32_bf16 v[144:159], v[180:183], v[104:111], v192\n" \
    "s_waitcnt vmcnt(12)\n" \
    "ds_read_b64_tr_b16 v[220:221], v228\n" \
    "ds_read_b64_tr_b16 v[222:223], v229\n" \
    "ds_read_b64_tr_b16 v[224:225], v228 offset:256\n" \
    "ds_read_b64_tr_b16 v[226:227], v229 offset:256\n" \
    "ds_read_b128 v[176:179], v194 offset:2048\n" \
    "v_exp_f32 v82, v82\n" \
    "v_exp_f32 v83, v83\n" \
    "v_exp_f32 v84, v84\n" \
    "v_exp_f32 v85, v85\n" \
    "s_waitcnt lgkmcnt(1)\n" \
    "v_mfma_f32_32x32x16_bf16 v[64:79], v[168:171], v[160:163], 0\n" \
    "ds_read_b128 v[180:183], v194 offset:2560\n" \
    "v_exp_f32 v86, v86\n" \
    "v_exp_f32 v87, v87\n" \
    "v_exp_f32 v88, v88\n" \
    "v_exp_f32 v89, v89\n" \
    "v_smfmac_f32_32x32x32_bf16 v[128:143], v[184:187], v[80:87], v192\n" \
    "v_exp_f32 v90, v90\n" \
    "v_exp_f32 v91, v91\n" \
    "v_exp_f32 v92, v92\n" \
    "v_mfma_f32_16x16x32_bf16 v[232:235], v[220:223], v[204:207], 0\n" \
    "v_exp_f32 v93, v93\n" \
    "v_exp_f32 v94, v94\n" \
    "v_exp_f32 v95, v95\n" \
    "v_exp_f32 v112, v112\n" \
    "v_exp_f32 v113, v113\n" \
    "v_smfmac_f32_32x32x32_bf16 v[128:143], v[188:191], v[88:95], v192\n" \
    "v_exp_f32 v114, v114\n" \
    "v_exp_f32 v115, v115\n" \
    "v_exp_f32 v116, v116\n" \
    "v_exp_f32 v117, v117\n" \
    "v_mfma_f32_32x32x16_bf16 v[96:111], v[168:171], v[164:167], 0\n" \
    "ds_read_b128 v[172:175], v193 offset:3072\n" \
    "v_exp_f32 v118, v118\n" \
    "v_exp_f32 v119, v119\n" \
    "v_exp_f32 v120, v120\n" \
    "v_exp_f32 v121, v121\n" \
    "v_smfmac_f32_32x32x32_bf16 v[144:159], v[184:187], v[112:119], v192\n" \
    "v_exp_f32 v122, v122\n" \
    "v_exp_f32 v123, v123\n" \
    "v_exp_f32 v124, v124\n" \
    "v_exp_f32 v125, v125\n" \
    "v_exp_f32 v126, v126\n" \
    "v_exp_f32 v127, v127\n" \
    "s_setprio 0\n" \
    "v_exp_f32 v64, v64\n" \
    "v_exp_f32 v65, v65\n" \
    "v_smfmac_f32_32x32x32_bf16 v[144:159], v[188:191], v[120:127], v192\n" \
    "ds_read_b128 v[184:187], v194 offset:3072\n" \
    "v_exp_f32 v66, v66\n" \
    "v_exp_f32 v67, v67\n" \
    "v_exp_f32 v68, v68\n" \
    "v_exp_f32 v69, v69\n" \
    "s_waitcnt lgkmcnt(1)\n" \
    "v_mfma_f32_32x32x16_bf16 v[80:95], v[172:175], v[160:163], 0\n" \
    "ds_read_b128 v[188:191], v194 offset:3584\n" \
    "v_exp_f32 v70, v70\n" \
    "v_exp_f32 v71, v71\n" \
    "v_exp_f32 v72, v72\n" \
    "v_exp_f32 v73, v73\n" \
    "v_smfmac_f32_32x32x32_bf16 v[128:143], v[176:179], v[64:71], v192\n" \
    "v_exp_f32 v74, v74\n" \
    "v_exp_f32 v75, v75\n" \
    "v_exp_f32 v76, v76\n" \
    "v_mfma_f32_16x16x32_bf16 v[232:235], v[224:227], v[208:211], v[232:235]\n" \
    "v_exp_f32 v77, v77\n" \
    "v_exp_f32 v78, v78\n" \
    "v_exp_f32 v79, v79\n" \
    "v_exp_f32 v96, v96\n" \
    "v_exp_f32 v97, v97\n" \
    "v_smfmac_f32_32x32x32_bf16 v[128:143], v[180:183], v[72:79], v192\n" \
    "v_exp_f32 v98, v98\n" \
    "v_exp_f32 v99, v99\n" \
    "v_exp_f32 v100, v100\n" \
    "v_exp_f32 v101, v101\n" \
    "v_mfma_f32_32x32x16_bf16 v[112:127], v[172:175], v[164:167], 0\n" \
    "ds_read_b128 v[168:171], v193 offset:4096\n" \
    "v_exp_f32 v102, v102\n" \
    "v_exp_f32 v103, v103\n" \
    "v_exp_f32 v104, v104\n" \
    "v_exp_f32 v105, v105\n" \
    "v_smfmac_f32_32x32x32_bf16 v[144:159], v[176:179], v[96:103], v192\n" \
    "v_exp_f32 v106, v106\n" \
    "v_exp_f32 v107, v107\n" \
    "v_exp_f32 v108, v108\n" \
    "v_exp_f32 v109, v109\n" \
    "v_exp_f32 v110, v110\n" \
    "v_exp_f32 v111, v111\n" \
    "v_exp_f32 v80, v80\n" \
    "v_exp_f32 v81, v81\n" \
    "v_smfmac_f32_32x32x32_bf16 v[144:159], v[180:183], v[104:111], v192\n" \
    "ds_write_b128 v230, v[232:235]\n" \
    "ds_read_b128 v[176:179], v194 offset:4096\n" \
    "v_exp_f32 v82, v82\n" \
    "v_exp_f32 v83, v83\n" \
    "v_exp_f32 v84, v84\n" \
    "v_exp_f32 v85, v85\n" \
    "s_waitcnt lgkmcnt(1)\n" \
    "v_mfma_f32_32x32x16_bf16 v[64:79], v[168:171], v[160:163], 0\n" \
    "ds_read_b128 v[180:183], v194 offset:4608\n" \
    "v_exp_f32 v86, v86\n" \
    "v_exp_f32 v87, v87\n" \
    "v_exp_f32 v88, v88\n" \
    "v_exp_f32 v89, v89\n" \
    "v_smfmac_f32_32x32x32_bf16 v[128:143], v[184:187], v[80:87], v192\n" \
    "v_exp_f32 v90, v90\n" \
    "v_exp_f32 v91, v91\n" \
    "v_exp_f32 v92, v92\n" \
    "v_exp_f32 v93, v93\n" \
    "v_exp_f32 v94, v94\n" \
    "v_exp_f32 v95, v95\n" \
    "v_exp_f32 v112, v112\n" \
    "v_exp_f32 v113, v113\n" \
    "v_smfmac_f32_32x32x32_bf16 v[128:143], v[188:191], v[88:95], v192\n" \
    "v_exp_f32 v114, v114\n" \
    "v_exp_f32 v115, v115\n" \
    "v_exp_f32 v116, v116\n" \
    "v_exp_f32 v117, v117\n" \
    "v_mfma_f32_32x32x16_bf16 v[96:111], v[168:171], v[164:167], 0\n" \
    "ds_read_b128 v[172:175], v193 offset:5120\n" \
    "v_exp_f32 v118, v118\n" \
    "v_exp_f32 v119, v119\n" \
    "v_exp_f32 v120, v120\n" \
    "v_exp_f32 v121, v121\n" \
    "v_smfmac_f32_32x32x32_bf16 v[144:159], v[184:187], v[112:119], v192\n" \
    "v_exp_f32 v122, v122\n" \
    "v_exp_f32 v123, v123\n" \
    "v_exp_f32 v124, v124\n" \
    "v_exp_f32 v125, v125\n" \
    "v_exp_f32 v126, v126\n" \
    "v_exp_f32 v127, v127\n" \
    "v_add_u32 v193, 4096, v193\n" \
    "v_add_u32 v194, v195, v194\n" \
    "s_waitcnt vmcnt(0)\n"

#define ATT_C \
    "s_mov_b32 %[cnt], 2\n" \
    "1:\n" \
    "v_exp_f32 v64, v64\n" \
    "v_exp_f32 v65, v65\n" \
    "v_smfmac_f32_32x32x32_bf16 v[144:159], v[188:191], v[120:127], v192\n" \
    "ds_read_b128 v[184:187], v194 offset:1024\n" \
    "v_exp_f32 v66, v66\n" \
    "v_exp_f32 v67, v67\n" \
    "v_exp_f32 v68, v68\n" \
    "v_exp_f32 v69, v69\n" \
    "s_waitcnt lgkmcnt(1)\n" \
    "v_mfma_f32_32x32x16_bf16 v[80:95], v[172:175], v[160:163], 0\n" \
    "ds_read_b128 v[188:191], v194 offset:1536\n" \
    "v_exp_f32 v70, v70\n" \
    "v_exp_f32 v71, v71\n" \
    "v_exp_f32 v72, v72\n" \
    "v_exp_f32 v73, v73\n" \
    "v_smfmac_f32_32x32x32_bf16 v[128:143], v[176:179], v[64:71], v192\n" \
    "v_exp_f32 v74, v74\n" \
    "s_mov_b32 m0, %[swo]\n" \
    "v_exp_f32 v75, v75\n" \
    "v_exp_f32 v76, v76\n" \
    "global_load_lds_dwordx4 v236, %[wob] offset:0\n" \
    "v_exp_f32 v77, v77\n" \
    "v_exp_f32 v78, v78\n" \
    "v_exp_f32 v79, v79\n" \
    "v_exp_f32 v96, v96\n" \
    "v_exp_f32 v97, v97\n" \
    "v_smfmac_f32_32x32x32_bf16 v[128:143], v[180:183], v[72:79], v192\n" \
    "v_exp_f32 v98, v98\n" \
    "v_exp_f32 v99, v99\n" \
    "v_exp_f32 v100, v100\n" \
    "v_exp_f32 v101, v101\n" \
    "v_mfma_f32_32x32x16_bf16 v[112:127], v[172:175], v[164:167], 0\n" \
    "ds_read_b128 v[168:171], v193 offset:2048\n" \
    "v_exp_f32 v102, v102\n" \
    "v_exp_f32 v103, v103\n" \
    "v_exp_f32 v104, v104\n" \
    "v_exp_f32 v105, v105\n" \
    "v_smfmac_f32_32x32x32_bf16 v[144:159], v[176:179], v[96:103], v192\n" \
    "v_exp_f32 v106, v106\n" \
    "v_exp_f32 v107, v107\n" \
    "v_exp_f32 v108, v108\n" \
    "v_exp_f32 v109, v109\n" \
    "v_exp_f32 v110, v110\n" \
    "v_exp_f32 v111, v111\n" \
    "v_exp_f32 v80, v80\n" \
    "v_exp_f32 v81, v81\n" \
    "v_smfmac_f32_32x32x32_bf16 v[144:159], v[180:183], v[104:111], v192\n" \
    "ds_read_b128 v[176:179], v194 offset:2048\n" \
    "v_exp_f32 v82, v82\n" \
    "v_exp_f32 v83, v83\n" \
    "v_exp_f32 v84, v84\n" \
    "v_exp_f32 v85, v85\n" \
    "s_waitcnt lgkmcnt(1)\n" \
    "v_mfma_f32_32x32x16_bf16 v[64:79], v[168:171], v[160:163], 0\n" \
    "ds_read_b128 v[180:183], v194 offset:2560\n" \
    "v_exp_f32 v86, v86\n" \
    "v_exp_f32 v87, v87\n" \
    "v_exp_f32 v88, v88\n" \
    "v_exp_f32 v89, v89\n" \
    "v_smfmac_f32_32x32x32_bf16 v[128:143], v[184:187], v[80:87], v192\n" \
    "v_exp_f32 v90, v90\n" \
    "v_exp_f32 v91, v91\n" \
    "v_exp_f32 v92, v92\n" \
    "global_load_lds_dwordx4 v237, %[wob] offset:1024\n" \
    "v_exp_f32 v93, v93\n" \
    "v_exp_f32 v94, v94\n" \
    "v_exp_f32 v95, v95\n" \
    "v_exp_f32 v112, v112\n" \
    "v_exp_f32 v113, v113\n" \
    "v_smfmac_f32_32x32x32_bf16 v[128:143], v[188:191], v[88:95], v192\n" \
    "v_exp_f32 v114, v114\n" \
    "v_exp_f32 v115, v115\n" \
    "v_exp_f32 v116, v116\n" \
    "v_exp_f32 v117, v117\n" \
    "v_mfma_f32_32x32x16_bf16 v[96:111], v[168:171], v[164:167], 0\n" \
    "ds_read_b128 v[172:175], v193 offset:3072\n" \
    "v_exp_f32 v118, v118\n" \
    "v_exp_f32 v119, v119\n" \
    "v_exp_f32 v120, v120\n" \
    "v_exp_f32 v121, v121\n" \
    "v_smfmac_f32_32x32x32_bf16 v[144:159], v[184:187], v[112:119], v192\n" \
    "v_exp_f32 v122, v122\n" \
    "v_exp_f32 v123, v123\n" \
    "v_exp_f32 v124, v124\n" \
    "v_exp_f32 v125, v125\n" \
    "v_exp_f32 v126, v126\n" \
    "v_exp_f32 v127, v127\n" \
    "v_exp_f32 v64, v64\n" \
    "v_exp_f32 v65, v65\n" \
    "v_smfmac_f32_32x32x32_bf16 v[144:159], v[188:191], v[120:127], v192\n" \
    "ds_read_b128 v[184:187], v194 offset:3072\n" \
    "v_exp_f32 v66, v66\n" \
    "v_exp_f32 v67, v67\n" \
    "v_exp_f32 v68, v68\n" \
    "v_exp_f32 v69, v69\n" \
    "s_waitcnt lgkmcnt(1)\n" \
    "v_mfma_f32_32x32x16_bf16 v[80:95], v[172:175], v[160:163], 0\n" \
    "ds_read_b128 v[188:191], v194 offset:3584\n" \
    "v_exp_f32 v70, v70\n" \
    "v_exp_f32 v71, v71\n" \
    "v_exp_f32 v72, v72\n" \
    "v_exp_f32 v73, v73\n" \
    "v_smfmac_f32_32x32x32_bf16 v[128:143], v[176:179], v[64:71], v192\n" \
    "v_exp_f32 v74, v74\n" \
    "v_exp_f32 v75, v75\n" \
    "v_exp_f32 v76, v76\n" \
    "global_load_lds_dwordx4 v238, %[wob] offset:2048\n" \
    "v_exp_f32 v77, v77\n" \
    "v_exp_f32 v78, v78\n" \
    "v_exp_f32 v79, v79\n" \
    "v_exp_f32 v96, v96\n" \
    "v_exp_f32 v97, v97\n" \
    "v_smfmac_f32_32x32x32_bf16 v[128:143], v[180:183], v[72:79], v192\n" \
    "v_exp_f32 v98, v98\n" \
    "v_exp_f32 v99, v99\n" \
    "v_exp_f32 v100, v100\n" \
    "v_exp_f32 v101, v101\n" \
    "v_mfma_f32_32x32x16_bf16 v[112:127], v[172:175], v[164:167], 0\n" \
    "ds_read_b128 v[168:171], v193 offset:4096\n" \
    "v_exp_f32 v102, v102\n" \
    "v_exp_f32 v103, v103\n" \
    "v_exp_f32 v104, v104\n" \
    "v_exp_f32 v105, v105\n" \
    "v_smfmac_f32_32x32x32_bf16 v[144:159], v[176:179], v[96:103], v192\n" \
    "v_exp_f32 v106, v106\n" \
    "v_exp_f32 v107, v107\n" \
    "v_exp_f32 v108, v108\n" \
    "v_exp_f32 v109, v109\n" \
    "v_exp_f32 v110, v110\n" \
    "v_exp_f32 v111, v111\n" \
    "v_exp_f32 v80, v80\n" \
    "v_exp_f32 v81, v81\n" \
    "v_smfmac_f32_32x32x32_bf16 v[144:159], v[180:183], v[104:111], v192\n" \
    "ds_read_b128 v[176:179], v194 offset:4096\n" \
    "v_exp_f32 v82, v82\n" \
    "v_exp_f32 v83, v83\n" \
    "v_exp_f32 v84, v84\n" \
    "v_exp_f32 v85, v85\n" \
    "s_waitcnt lgkmcnt(1)\n" \
    "v_mfma_f32_32x32x16_bf16 v[64:79], v[168:171], v[160:163], 0\n" \
    "ds_read_b128 v[180:183], v194 offset:4608\n" \
    "v_exp_f32 v86, v86\n" \
    "v_exp_f32 v87, v87\n" \
    "v_exp_f32 v88, v88\n" \
    "v_exp_f32 v89, v89\n" \
    "v_smfmac_f32_32x32x32_bf16 v[128:143], v[184:187], v[80:87], v192\n" \
    "v_exp_f32 v90, v90\n" \
    "v_exp_f32 v91, v91\n" \
    "v_exp_f32 v92, v92\n" \
    "global_load_lds_dwordx4 v239, %[wob] offset:3072\n" \
    "v_exp_f32 v93, v93\n" \
    "v_exp_f32 v94, v94\n" \
    "v_exp_f32 v95, v95\n" \
    "v_exp_f32 v112, v112\n" \
    "v_exp_f32 v113, v113\n" \
    "v_smfmac_f32_32x32x32_bf16 v[128:143], v[188:191], v[88:95], v192\n" \
    "v_exp_f32 v114, v114\n" \
    "v_exp_f32 v115, v115\n" \
    "v_exp_f32 v116, v116\n" \
    "v_exp_f32 v117, v117\n" \
    "v_mfma_f32_32x32x16_bf16 v[96:111], v[168:171], v[164:167], 0\n" \
    "ds_read_b128 v[172:175], v193 offset:5120\n" \
    "v_exp_f32 v118, v118\n" \
    "v_exp_f32 v119, v119\n" \
    "v_exp_f32 v120, v120\n" \
    "v_exp_f32 v121, v121\n" \
    "v_smfmac_f32_32x32x32_bf16 v[144:159], v[184:187], v[112:119], v192\n" \
    "v_exp_f32 v122, v122\n" \
    "v_exp_f32 v123, v123\n" \
    "v_exp_f32 v124, v124\n" \
    "v_exp_f32 v125, v125\n" \
    "v_exp_f32 v126, v126\n" \
    "v_exp_f32 v127, v127\n" \
    "v_add_u32 v236, 0x8000, v236\n" \
    "v_add_u32 v237, 0x8000, v237\n" \
    "v_add_u32 v238, 0x8000, v238\n" \
    "v_add_u32 v239, 0x8000, v239\n" \
    "s_add_u32 %[swo], %[swo], 0x1000\n" \
    "v_add_u32 v193, 4096, v193\n" \
    "v_add_u32 v194, v195, v194\n" \
    "s_sub_u32 %[cnt], %[cnt], 1\n" \
    "s_cmp_lg_u32 %[cnt], 0\n" \
    "s_cbranch_scc1 1b\n" \
    "global_load_dword v220, v221, %[wsb] sc1\n" \
    "s_mov_b32 %[cnt], 2\n" \
    "2:\n" \
    "v_exp_f32 v64, v64\n" \
    "v_exp_f32 v65, v65\n" \
    "v_smfmac_f32_32x32x32_bf16 v[144:159], v[188:191], v[120:127], v192\n" \
    "ds_read_b128 v[184:187], v194 offset:1024\n" \
    "v_exp_f32 v66, v66\n" \
    "v_exp_f32 v67, v67\n" \
    "v_exp_f32 v68, v68\n" \
    "v_exp_f32 v69, v69\n" \
    "s_waitcnt lgkmcnt(1)\n" \
    "v_mfma_f32_32x32x16_bf16 v[80:95], v[172:175], v[160:163], 0\n" \
    "ds_read_b128 v[188:191], v194 offset:1536\n" \
    "v_exp_f32 v70, v70\n" \
    "v_exp_f32 v71, v71\n" \
    "v_exp_f32 v72, v72\n" \
    "v_exp_f32 v73, v73\n" \
    "v_smfmac_f32_32x32x32_bf16 v[128:143], v[176:179], v[64:71], v192\n" \
    "v_exp_f32 v74, v74\n" \
    "v_exp_f32 v75, v75\n" \
    "v_exp_f32 v76, v76\n" \
    "v_exp_f32 v77, v77\n" \
    "v_exp_f32 v78, v78\n" \
    "v_exp_f32 v79, v79\n" \
    "v_exp_f32 v96, v96\n" \
    "v_exp_f32 v97, v97\n" \
    "v_smfmac_f32_32x32x32_bf16 v[128:143], v[180:183], v[72:79], v192\n" \
    "v_exp_f32 v98, v98\n" \
    "v_exp_f32 v99, v99\n" \
    "v_exp_f32 v100, v100\n" \
    "v_exp_f32 v101, v101\n" \
    "v_mfma_f32_32x32x16_bf16 v[112:127], v[172:175], v[164:167], 0\n" \
    "ds_read_b128 v[168:171], v193 offset:2048\n" \
    "v_exp_f32 v102, v102\n" \
    "v_exp_f32 v103, v103\n" \
    "v_exp_f32 v104, v104\n" \
    "v_exp_f32 v105, v105\n" \
    "v_smfmac_f32_32x32x32_bf16 v[144:159], v[176:179], v[96:103], v192\n" \
    "v_exp_f32 v106, v106\n" \
    "v_exp_f32 v107, v107\n" \
    "v_exp_f32 v108, v108\n" \
    "v_exp_f32 v109, v109\n" \
    "v_exp_f32 v110, v110\n" \
    "v_exp_f32 v111, v111\n" \
    "v_exp_f32 v80, v80\n" \
    "v_exp_f32 v81, v81\n" \
    "v_smfmac_f32_32x32x32_bf16 v[144:159], v[180:183], v[104:111], v192\n" \
    "ds_read_b128 v[176:179], v194 offset:2048\n" \
    "v_exp_f32 v82, v82\n" \
    "v_exp_f32 v83, v83\n" \
    "v_exp_f32 v84, v84\n" \
    "v_exp_f32 v85, v85\n" \
    "s_waitcnt lgkmcnt(1)\n" \
    "v_mfma_f32_32x32x16_bf16 v[64:79], v[168:171], v[160:163], 0\n" \
    "ds_read_b128 v[180:183], v194 offset:2560\n" \
    "v_exp_f32 v86, v86\n" \
    "v_exp_f32 v87, v87\n" \
    "v_exp_f32 v88, v88\n" \
    "v_exp_f32 v89, v89\n" \
    "v_smfmac_f32_32x32x32_bf16 v[128:143], v[184:187], v[80:87], v192\n" \
    "v_exp_f32 v90, v90\n" \
    "v_exp_f32 v91, v91\n" \
    "v_exp_f32 v92, v92\n" \
    "v_exp_f32 v93, v93\n" \
    "v_exp_f32 v94, v94\n" \
    "v_exp_f32 v95, v95\n" \
    "v_exp_f32 v112, v112\n" \
    "v_exp_f32 v113, v113\n" \
    "v_smfmac_f32_32x32x32_bf16 v[128:143], v[188:191], v[88:95], v192\n" \
    "v_exp_f32 v114, v114\n" \
    "v_exp_f32 v115, v115\n" \
    "v_exp_f32 v116, v116\n" \
    "v_exp_f32 v117, v117\n" \
    "v_mfma_f32_32x32x16_bf16 v[96:111], v[168:171], v[164:167], 0\n" \
    "ds_read_b128 v[172:175], v193 offset:3072\n" \
    "v_exp_f32 v118, v118\n" \
    "v_exp_f32 v119, v119\n" \
    "v_exp_f32 v120, v120\n" \
    "v_exp_f32 v121, v121\n" \
    "v_smfmac_f32_32x32x32_bf16 v[144:159], v[184:187], v[112:119], v192\n" \
    "v_exp_f32 v122, v122\n" \
    "v_exp_f32 v123, v123\n" \
    "v_exp_f32 v124, v124\n" \
    "v_exp_f32 v125, v125\n" \
    "v_exp_f32 v126, v126\n" \
    "v_exp_f32 v127, v127\n" \
    "v_exp_f32 v64, v64\n" \
    "v_exp_f32 v65, v65\n" \
    "v_smfmac_f32_32x32x32_bf16 v[144:159], v[188:191], v[120:127], v192\n" \
    "ds_read_b128 v[184:187], v194 offset:3072\n" \
    "v_exp_f32 v66, v66\n" \
    "v_exp_f32 v67, v67\n" \
    "v_exp_f32 v68, v68\n" \
    "v_exp_f32 v69, v69\n" \
    "s_waitcnt lgkmcnt(1)\n" \
    "v_mfma_f32_32x32x16_bf16 v[80:95], v[172:175], v[160:163], 0\n" \
    "ds_read_b128 v[188:191], v194 offset:3584\n" \
    "v_exp_f32 v70, v70\n" \
    "v_exp_f32 v71, v71\n" \
    "v_exp_f32 v72, v72\n" \
    "v_exp_f32 v73, v73\n" \
    "v_smfmac_f32_32x32x32_bf16 v[128:143], v[176:179], v[64:71], v192\n" \
    "v_exp_f32 v74, v74\n" \
    "v_exp_f32 v75, v75\n" \
    "v_exp_f32 v76, v76\n" \
    "v_exp_f32 v77, v77\n" \
    "v_exp_f32 v78, v78\n" \
    "v_exp_f32 v79, v79\n" \
    "v_exp_f32 v96, v96\n" \
    "v_exp_f32 v97, v97\n" \
    "v_smfmac_f32_32x32x32_bf16 v[128:143], v[180:183], v[72:79], v192\n" \
    "v_exp_f32 v98, v98\n" \
    "v_exp_f32 v99, v99\n" \
    "v_exp_f32 v100, v100\n" \
    "v_exp_f32 v101, v101\n" \
    "v_mfma_f32_32x32x16_bf16 v[112:127], v[172:175], v[164:167], 0\n" \
    "ds_read_b128 v[168:171], v193 offset:4096\n" \
    "v_exp_f32 v102, v102\n" \
    "v_exp_f32 v103, v103\n" \
    "v_exp_f32 v104, v104\n" \
    "v_exp_f32 v105, v105\n" \
    "v_smfmac_f32_32x32x32_bf16 v[144:159], v[176:179], v[96:103], v192\n" \
    "v_exp_f32 v106, v106\n" \
    "v_exp_f32 v107, v107\n" \
    "v_exp_f32 v108, v108\n" \
    "v_exp_f32 v109, v109\n" \
    "v_exp_f32 v110, v110\n" \
    "v_exp_f32 v111, v111\n" \
    "v_exp_f32 v80, v80\n" \
    "v_exp_f32 v81, v81\n" \
    "v_smfmac_f32_32x32x32_bf16 v[144:159], v[180:183], v[104:111], v192\n" \
    "ds_read_b128 v[176:179], v194 offset:4096\n" \
    "v_exp_f32 v82, v82\n" \
    "v_exp_f32 v83, v83\n" \
    "v_exp_f32 v84, v84\n" \
    "v_exp_f32 v85, v85\n" \
    "s_waitcnt lgkmcnt(1)\n" \
    "v_mfma_f32_32x32x16_bf16 v[64:79], v[168:171], v[160:163], 0\n" \
    "ds_read_b128 v[180:183], v194 offset:4608\n" \
    "v_exp_f32 v86, v86\n" \
    "v_exp_f32 v87, v87\n" \
    "v_exp_f32 v88, v88\n" \
    "v_exp_f32 v89, v89\n" \
    "v_smfmac_f32_32x32x32_bf16 v[128:143], v[184:187], v[80:87], v192\n" \
    "v_exp_f32 v90, v90\n" \
    "v_exp_f32 v91, v91\n" \
    "v_exp_f32 v92, v92\n" \
    "v_exp_f32 v93, v93\n" \
    "v_exp_f32 v94, v94\n" \
    "v_exp_f32 v95, v95\n" \
    "v_exp_f32 v112, v112\n" \
    "v_exp_f32 v113, v113\n" \
    "v_smfmac_f32_32x32x32_bf16 v[128:143], v[188:191], v[88:95], v192\n" \
    "v_exp_f32 v114, v114\n" \
    "v_exp_f32 v115, v115\n" \
    "v_exp_f32 v116, v116\n" \
    "v_exp_f32 v117, v117\n" \
    "v_mfma_f32_32x32x16_bf16 v[96:111], v[168:171], v[164:167], 0\n" \
    "ds_read_b128 v[172:175], v193 offset:5120\n" \
    "v_exp_f32 v118, v118\n" \
    "v_exp_f32 v119, v119\n" \
    "v_exp_f32 v120, v120\n" \
    "v_exp_f32 v121, v121\n" \
    "v_smfmac_f32_32x32x32_bf16 v[144:159], v[184:187], v[112:119], v192\n" \
    "v_exp_f32 v122, v122\n" \
    "v_exp_f32 v123, v123\n" \
    "v_exp_f32 v124, v124\n" \
    "v_exp_f32 v125, v125\n" \
    "v_exp_f32 v126, v126\n" \
    "v_exp_f32 v127, v127\n" \
    "s_cmp_eq_u32 %[cnt], 2\n" \
    "s_cbranch_scc0 8f\n" \
    "s_setprio 0\n" \
    "8:\n" \
    "v_add_u32 v193, 4096, v193\n" \
    "v_add_u32 v194, v195, v194\n" \
    "s_sub_u32 %[cnt], %[cnt], 1\n" \
    "s_cmp_lg_u32 %[cnt], 0\n" \
    "s_cbranch_scc1 2b\n" \
    "s_nop 1\n" \
    "v_smfmac_f32_32x32x32_bf16 v[144:159], v[188:191], v[120:127], v192\n" \
    "s_setprio 0\n" \
    "s_waitcnt vmcnt(0)\n" \
    "s_nop 7\n" \
    "s_nop 7\n" \
    "s_nop 3\n" \
    "s_waitcnt lgkmcnt(0)\n"

__device__ __forceinline__ void slow_block(const char* ws, u32x4 q, int lane, float* o, float& lsum) {
    const u32 koff = WS_K + lane * 16;
    const u32 r32 = lane & 31;
    float m = -3.0e38f;
    for (int t = 0; t < 64; ++t) {
        u32x4 kf;
        GLD128C(kf, ws, koff + t * 1024, 0);
        asm volatile("s_waitcnt vmcnt(0)" : "+v"(kf) :: "memory");
        f32x16 s = {0};
        s = __builtin_amdgcn_mfma_f32_32x32x16_bf16(asbf(kf), asbf(q), s, 0, 0, 0);
#pragma unroll
        for (int i = 0; i < 16; ++i) m = fmaxf(m, s[i]);
    }
    {
        auto sw = __builtin_amdgcn_permlane32_swap(__float_as_uint(m), __float_as_uint(m), false, false);
        m = fmaxf(__uint_as_float(sw[0]), __uint_as_float(sw[1]));
    }
    f32x16 acc = {0};
    const u32 voff = WS_V + (lane >> 5) * 256 + (lane & 15) * 16;
    for (int t = 0; t < 64; ++t) {
        u32x4 kf, v0, v1;
        GLD128C(kf, ws, koff + t * 1024, 0);
        GLD128C(v0, ws, voff + t * 1024, 0);
        GLD128C(v1, ws, voff + t * 1024, 512);
        asm volatile("s_waitcnt vmcnt(0)" : "+v"(kf), "+v"(v0), "+v"(v1) :: "memory");
        if (r32 >= 16) { v0 = (u32x4){0x3f803f80u, 0x3f803f80u, 0x3f803f80u, 0x3f803f80u}; v1 = v0; }
        f32x16 s = {0};
        s = __builtin_amdgcn_mfma_f32_32x32x16_bf16(asbf(kf), asbf(q), s, 0, 0, 0);
        u32 p[8];
#pragma unroll
        for (int i = 0; i < 8; ++i)
            p[i] = pk(__builtin_amdgcn_exp2f(s[2 * i] - m), __builtin_amdgcn_exp2f(s[2 * i + 1] - m));
#pragma unroll
        for (int st = 0; st < 2; ++st) {
            u32x4 vv = st ? v1 : v0;
            auto a = __builtin_amdgcn_permlane32_swap(vv[0], vv[2], false, false);
            auto c = __builtin_amdgcn_permlane32_swap(vv[1], vv[3], false, false);
            u32x4 af = {a[0], c[0], a[1], c[1]};
            u32x4 bf = {p[4 * st], p[4 * st + 1], p[4 * st + 2], p[4 * st + 3]};
            acc = __builtin_amdgcn_mfma_f32_32x32x16_bf16(asbf(af), asbf(bf), acc, 0, 0, 0);
        }
    }
#pragma unroll
    for (int i = 0; i < 8; ++i) o[i] = acc[i];
    lsum = acc[8];
}

#define PRIO_MODE 1
#define PRIO_START() do { if (w >= 4) __builtin_amdgcn_s_setprio(1); } while (0)
#define PRIO_END() __builtin_amdgcn_s_setprio(0)
__global__ void __launch_bounds__(512, 2)
fused_kernel(const u16* x_, const u16* wq_, const u16* wk_, const u16* wv_, const u16* wo_, const u16* wr_, const u16* wg_,
             const u16* wu_, const u16* wd_, const u16* gw_, u16* out_, char* ws_)
{
    u32x16 ka0; u32x8 ka1;
    {
        const void* kp = (const void*)__builtin_amdgcn_kernarg_segment_ptr();
        asm volatile("s_load_dwordx16 %0, %2, 0\n\ts_load_dwordx8 %1, %2, 64\n\ts_waitcnt lgkmcnt(0)"
                     : "=&s"(ka0), "=&s"(ka1) : "s"(kp) : "memory");
    }
#define KP(v, i) ((u64)v[2 * (i)] | ((u64)v[2 * (i) + 1] << 32))
    const u16* x  = (const u16*)KP(ka0, 0); const u16* wq = (const u16*)KP(ka0, 1);
    const u16* wk = (const u16*)KP(ka0, 2); const u16* wv = (const u16*)KP(ka0, 3);
    const u16* wo = (const u16*)KP(ka0, 4); const u16* wr = (const u16*)KP(ka0, 5);
    const u16* wg = (const u16*)KP(ka0, 6); const u16* wu = (const u16*)KP(ka0, 7);
    const u16* wd = (const u16*)KP(ka1, 0); const u16* gw = (const u16*)KP(ka1, 1);
    u16* out = (u16*)KP(ka1, 2); char* ws = (char*)KP(ka1, 3);

    __shared__ __attribute__((aligned(1024))) char smem[L_END];
    LDSP char* lds = (LDSP char*)smem;
    const u32 lb = (u32)(size_t)lds;
    const int tid = threadIdx.x;
    const int lane = tid & 63;
    const int w = __builtin_amdgcn_readfirstlane(tid >> 6);
    const int b = blockIdx.x;
    const int xcd = b & 7, jj = b >> 3;
    const int tb = xcd * 4 + (jj >> 3), hg = jj & 7;
    const int g = lane >> 4, l15 = lane & 15, h = lane >> 5, l31 = lane & 31;
    const bool isK = hg < 4;
    const bool ffn = jj < 28;
    const int fj = xcd * 28 + min(jj, 27);
    STAMP(1); STAMPC(18);

    const int th = w & 1, hp = (w >> 1) & 1, blk = w >> 2;
    const int T0 = tb * 64 + (hg & 3) * 16;
    u32x4 xf0, xf1, vf0, vf1;
    {
        const u16* wsrc = isK ? wk : wv;
#pragma unroll
        for (int i = 0; i < 2; ++i) {
            u32 slot = 64 * i + lane, pos = slot >> 3, B = pos ^ ((pos >> 2) & 1);
            u32 row = 4 * B + ((slot >> 1) & 3);
            dma16(wsrc, (64 * w + row) * 32 + (slot & 1) * 16, lb + L_W + w * 2048 + i * 1024);
        }
        u32 xo = (T0 + l15) * 1024 + (64 * w + 16 * g) * 2;
        GLD128(xf0, x, xo, 0);
        GLD128(xf1, x, xo, 16);
        const u16* vec = isK ? wr : gw;
        u32 vo = (64 * w + 16 * g) * 2;
        GLD128(vf0, vec, vo, 0);
        GLD128(vf1, vec, vo, 16);
    }
    u32 epo;
    { u32 z = 0; GLD32C(epo, ws, z, 0); }
    asm volatile("s_barrier" ::: "memory");
    if (w < 7) {
#pragma unroll
        for (int i = 0; i < 19; ++i) {
            const int p = i < 18 ? w + 7 * i : __builtin_amdgcn_readfirstlane(min(w + 7 * i, 127));
            if (i < 9 || (i == 9 && w == 0)) {
                dma16(x, (tb * 64 + p) * 1024 + ((lane ^ (p & 15)) << 4), lb + L_K + p * 1024);
            } else {
                const int G = p - 64;
                dma16(wq, (8 * G + ((lane >> 2) & 7)) * 1024 + hg * 128 + (lane >> 5) * 64 + (lane & 3) * 16, lb + L_V + G * 1024);
            }
        }
    }
    if (tid < 256) { u32 o1 = (tid & 15) == 0 ? 0x3f803f80u : 0u; ST128(L_ONE + tid * 16, ((u32x4){o1, o1, o1, o1})); }
    asm volatile("s_cmp_eq_u32 %[w], 7\n\ts_cbranch_scc1 1f\n\ts_waitcnt vmcnt(20)\n\ts_branch 2f\n1:\n\ts_waitcnt vmcnt(1)\n2:"
                 : "+v"(xf0), "+v"(xf1), "+v"(vf0), "+v"(vf1) : [w] "s"(w) : "memory", "scc");
    FSTAMP(2);
    {
        const u32 ta = L_W + w * 2048 + l15 * 8;
        const u32 gx = g & 1;
        u32x4 wf0 = cat(trread(lds, ta + ((4 * g + 0) ^ gx) * 128), trread(lds, ta + ((4 * g + 1) ^ gx) * 128));
        u32x4 wf1 = cat(trread(lds, ta + ((4 * g + 2) ^ gx) * 128), trread(lds, ta + ((4 * g + 3) ^ gx) * 128));
        f32x4 acc = {0, 0, 0, 0}, dacc = {0, 0, 0, 0};
        if (isK) {
            acc = __builtin_amdgcn_mfma_f32_16x16x32_bf16(asbf(wf0), asbf(xf0), acc, 0, 0, 0);
            acc = __builtin_amdgcn_mfma_f32_16x16x32_bf16(asbf(wf1), asbf(xf1), acc, 0, 0, 0);
        } else {
            acc = __builtin_amdgcn_mfma_f32_16x16x32_bf16(asbf(xf0), asbf(wf0), acc, 0, 0, 0);
            acc = __builtin_amdgcn_mfma_f32_16x16x32_bf16(asbf(xf1), asbf(wf1), acc, 0, 0, 0);
        }
        dacc = __builtin_amdgcn_mfma_f32_16x16x32_bf16(asbf(vf0), asbf(xf0), dacc, 0, 0, 0);
        dacc = __builtin_amdgcn_mfma_f32_16x16x32_bf16(asbf(vf1), asbf(xf1), dacc, 0, 0, 0);
        ST128(L_P1 + w * 1024 + lane * 16, __builtin_bit_cast(u32x4, acc));
        if (g == 0) STF(L_PD + w * 64 + l15 * 4, dacc[0]);
    }
    BAR();
    FSTAMP(3);
    asm volatile("s_cmp_eq_u32 %[w], 7\n\ts_cbranch_scc1 1f\n\ts_waitcnt vmcnt(19)\n\ts_branch 2f\n1:\n\ts_waitcnt vmcnt(0)\n2:"
                 : "+v"(epo) : [w] "s"(w) : "memory", "scc");
    const u32 epoch = (u32)__builtin_amdgcn_readfirstlane((int)epo) + 1u;
    if (w == 7) {
        f32x4 ps[8]; float pd[8];
#pragma unroll
        for (int i = 0; i < 8; ++i) {
            ps[i] = __builtin_bit_cast(f32x4, LD128(L_P1 + i * 1024 + lane * 16));
            pd[i] = LDF(L_PD + i * 64 + l15 * 4);
        }
        __builtin_amdgcn_sched_barrier(0);
        const f32x4 s = ((ps[0] + ps[1]) + (ps[2] + ps[3])) + ((ps[4] + ps[5]) + (ps[6] + ps[7]));
        const float d = ((pd[0] + pd[1]) + (pd[2] + pd[3])) + ((pd[4] + pd[5]) + (pd[6] + pd[7]));
        u32x2 pv = {pk(s[0], s[1]), pk(s[2], s[3])};
        const int tile = T0 >> 5, sub = (T0 >> 4) & 1;
        u32 key = 0;
        if (isK) {
            u32 off = WS_K + tile * 1024 + ((g & 1) * 32 + sub * 16 + l15) * 16 + (g >> 1) * 8;
            GST64C(ws, off, pv);
            u32 rb = pk(d, d) & 0xffffu;
            u32 ord = (rb & 0x8000u) ? (~rb & 0xffffu) : (rb | 0x8000u);
            key = wave_max_u32((ord << 16) | (0xffffu - (u32)(T0 + l15)));
        } else {
            u32 off = WS_V + tile * 1024 + sub * 512 + (g >> 1) * 256 + l15 * 16 + (g & 1) * 8;
            GST64C(ws, off, pv);
            if (g == 0) { u32 go = WS_G + (T0 + l15) * 4; u32 dv = __float_as_uint(d); GST32C(ws, go, dv); }
        }
        VMWAIT(0);
        if (lane < 8) { u32x2 fl = {key, epoch}; u32 fo = WS_F1 + lane * 32768 + (tile * 4 + (isK ? 0 : 2) + sub) * 128; GST64C(ws, fo, fl); }
        FSTAMP(4);
    } else {
        VMWAIT(0);
    }
    BAR();
    FSTAMP(5);

    f32x16 qacc = {0};
    if (w < 4) {
        const int qh = w >> 1;
        const int tok = th * 32 + l31;
        u32 abase = L_V + h * 1024 + qh * 512 + ((l15 >> 2) * 64) + ((lane >> 4) & 1) * 32 + (l15 & 3) * 8;
        asm volatile("" : "+v"(abase));
        u32 bb[8];
#pragma unroll
        for (int j = 0; j < 8; ++j) { bb[j] = L_K + tok * 1024 + (((2 * j + h) ^ (tok & 15)) << 4); asm volatile("" : "+v"(bb[j])); }
#pragma unroll
        for (int s = 0; s < 32; ++s) {
            u32x4 af = cat(trread(lds, abase + s * 2048), trread(lds, abase + s * 2048 + 256));
            u32x4 bf = LD128(bb[s & 7] + (s >> 3) * 256);
            qacc = __builtin_amdgcn_mfma_f32_32x32x16_bf16(asbf(af), asbf(bf), qacc, 0, 0, 0);
        }
        FSTAMP(6);
    } else {
        const int j = w - 4;
        const u32 fo = WS_F1 + xcd * 32768 + (j * 64 + lane) * 128;
        u32x2 f;
        u32 spins = 0;
        do {
            asm volatile("global_load_dwordx2 %0, %1, %2 sc1\n\ts_waitcnt vmcnt(0)" : "=&v"(f) : "v"(fo), "s"(ws) : "memory");
        } while ((f[1] != epoch) && ++spins <= SPIN_MAX);
        const u32 key = wave_max_u32(f[0]);
        if (lane == 0) ST32(L_CT + j * 4, key);
        FSTAMP(7);
    }
    BAR();
    FSTAMP(9);
    u32 kvo = (w & 1) * 65536 + ((w >> 1) & 1) * 32768;
    if (w >= 4) {
        dma16c4(ws + WS_K, kvo + lane * 16, lb + L_K + kvo);
        dma16c4(ws + WS_K, kvo + 4096 + lane * 16, lb + L_K + kvo + 4096);
        dma16c2(ws + WS_K, kvo + 8192 + lane * 16, lb + L_K + kvo + 8192);
    } else {
        const float qs = 0.25f * 1.4426950408889634f;
        u32x4 qx, qy;
#pragma unroll
        for (int i = 0; i < 4; ++i) {
            u32 a = pk(qacc[2 * i], qacc[2 * i + 1]), c = pk(qacc[8 + 2 * i], qacc[8 + 2 * i + 1]);
            qx[i] = pk(lo16(a) * qs, hi16(a) * qs);
            qy[i] = pk(lo16(c) * qs, hi16(c) * qs);
        }
        ST128(L_P1 + w * 2048 + lane * 16, qx);
        ST128(L_P1 + w * 2048 + 1024 + lane * 16, qy);
    }
    const u32 key = max(max(LD32(L_CT), LD32(L_CT + 4)), max(LD32(L_CT + 8), LD32(L_CT + 12)));
    const int sel = __builtin_amdgcn_readfirstlane(0xffff - (key & 0xffff)) & 2047;
    u32x8 EXA; u32x4 EXB, WOA;
    u32 skv = lb + L_K + kvo + (10 + 4 * (w >> 2)) * 1024, sg = lb + L_W + w * 2048;
    const u32 skv3 = lb + L_K + kvo + (26 + 2 * (w >> 2)) * 1024;
    u32 swo = lb + ((w & 4) ? L_V : L_K) + ((w >> 1) & 1) * 32768 + (w & 1) * 8192;
    {
        EXA[0] = kvo + (10 + 4 * (w >> 2)) * 1024 + lane * 16;
        EXA[1] = kvo + (26 + 2 * (w >> 2)) * 1024 + lane * 16;
        const u32 pos = lane >> 3, B = pos ^ ((pos >> 2) & 1);
        const u32 row = 64 * w + 4 * B + ((lane >> 1) & 3);
        const u64 g0 = (u64)((lane & 1) ? wu : wg) + (u64)(row * 3584 + fj * 16), g1 = g0 + 32 * 3584;
        EXA[2] = (u32)g0; EXA[3] = (u32)(g0 >> 32); EXA[4] = (u32)g1; EXA[5] = (u32)(g1 >> 32);
        EXA[6] = sel * 1024 + (64 * w + 16 * g) * 2;
        EXA[7] = fj * 8192 + (128 * (w & 3) + 2 * lane) * 2;
        const u32 ta = lb + L_W + w * 2048 + l15 * 8, gx = g & 1;
        EXB[0] = ta + (4 * g + gx) * 128;
        EXB[1] = ta + (4 * g + (gx ^ 1)) * 128;
        EXB[2] = lb + (l15 == 0 ? L_FP + w * 64 + g * 16 : L_P1 + w * 1024 + lane * 16);
        EXB[3] = 0;
#pragma unroll
        for (int i = 0; i < 4; ++i) {
            const u32 r = 64 * w + 8 * i + (lane >> 3), slot = lane & 7;
            const u32 f = ((r >> 1) & 1) | ((i & 1) << 1);
            const u32 c = (((slot >> 1) ^ f) << 1) | (slot & 1);
            WOA[i] = r * 1024 + hg * 128 + c * 16 - i * 1024;
        }
    }
    u32x2 PZA;
    {
        const int head = hg * 4 + hp * 2 + blk, tl = th * 32 + l31;
        PZA[0] = WS_O + ((tb * 4 + (tl >> 4)) * 16 + (head >> 1)) * 1024 + (((head & 1) * 2 + h) * 16 + (tl & 15)) * 16;
        PZA[1] = (ffn && w < 4) ? WS_Y + fj * 2048 + w * 512 + l31 * 16 : PZA[0];
    }
    const int hf = w >> 2;
    u32 ka = lb + L_K + hf * 32768 + lane * 16;
    const bool vlane = l31 < 16;
    u32 va = vlane ? (lb + L_V + hf * 32768 + h * 256 + l15 * 16) : (lb + L_ONE - 1024 + h * 256 + l15 * 16);
    u32 vinc = vlane ? 4096u : 0u;
    u32 va0 = vlane ? va : va + 1024;
    u32 qa = lb + L_P1 + (w & 3) * 2048 + lane * 16;
    asm volatile("" : "+{v[196:203]}"(EXA), "+{v[228:231]}"(EXB), "+{v[236:239]}"(WOA), "+{v[220:221]}"(PZA), "+{v193}"(ka), "+{v194}"(va), "+{v195}"(vinc), "+v"(va0), "+v"(qa),
                 "+s"(skv), "+s"(sg), "+s"(swo));
    if (w >= 4) VMWAIT(0);
    BAR();
    STAMP(10); STAMPC(19);
    if (b == 0 && tid == 256) { u32 z = 0; GST32C(ws, z, epoch); }

    u32x4 qx, qy;
    f32x16 SX0, SX1, SY0, SY1, OX, OY;
    u32x4 K0, K1, V00, V01, V10, V11;
    u32 idx = 0xDDDDu;
    {
        asm volatile(
            "ds_read_b128 v[160:163], %[qa]\n"
            "ds_read_b128 v[164:167], %[qa] offset:1024\n"
            "ds_read_b128 v[168:171], v193\n"
            "ds_read_b128 v[172:175], v193 offset:1024\n"
            "ds_read_b128 v[176:179], %[va0]\n"
            "ds_read_b128 v[180:183], %[va0] offset:512\n"
            "v_mov_b32 v128, 0\n v_mov_b32 v129, 0\n v_mov_b32 v130, 0\n v_mov_b32 v131, 0\n"
            "v_mov_b32 v132, 0\n v_mov_b32 v133, 0\n v_mov_b32 v134, 0\n v_mov_b32 v135, 0\n"
            "v_mov_b32 v136, 0\n v_mov_b32 v137, 0\n v_mov_b32 v138, 0\n v_mov_b32 v139, 0\n"
            "v_mov_b32 v140, 0\n v_mov_b32 v141, 0\n v_mov_b32 v142, 0\n v_mov_b32 v143, 0\n"
            "v_mov_b32 v144, 0\n v_mov_b32 v145, 0\n v_mov_b32 v146, 0\n v_mov_b32 v147, 0\n"
            "v_mov_b32 v148, 0\n v_mov_b32 v149, 0\n v_mov_b32 v150, 0\n v_mov_b32 v151, 0\n"
            "v_mov_b32 v152, 0\n v_mov_b32 v153, 0\n v_mov_b32 v154, 0\n v_mov_b32 v155, 0\n"
            "v_mov_b32 v156, 0\n v_mov_b32 v157, 0\n v_mov_b32 v158, 0\n v_mov_b32 v159, 0\n"
            "v_mov_b32 v120, 0\n v_mov_b32 v121, 0\n v_mov_b32 v122, 0\n v_mov_b32 v123, 0\n"
            "v_mov_b32 v124, 0\n v_mov_b32 v125, 0\n v_mov_b32 v126, 0\n v_mov_b32 v127, 0\n"
            "v_mov_b32 v188, 0\n v_mov_b32 v189, 0\n v_mov_b32 v190, 0\n v_mov_b32 v191, 0\n"
            "s_waitcnt lgkmcnt(0)\n"
            "v_mfma_f32_32x32x16_bf16 v[64:79], v[168:171], v[160:163], 0\n"
            "v_mfma_f32_32x32x16_bf16 v[96:111], v[168:171], v[164:167], 0\n"
            "s_nop 7\n s_nop 7\n"
            : "={v[64:79]}"(SX0), "={v[80:95]}"(SX1), "={v[96:111]}"(SY0), "={v[112:127]}"(SY1),
              "={v[128:143]}"(OX), "={v[144:159]}"(OY),
              "={v[168:171]}"(K0), "={v[172:175]}"(K1), "={v[176:179]}"(V00), "={v[180:183]}"(V01),
              "={v[184:187]}"(V10), "={v[188:191]}"(V11), "=&{v[160:163]}"(qx), "=&{v[164:167]}"(qy)
            : "{v193}"(ka), [va0] "v"(va0), [qa] "v"(qa)
            : "memory");
    }
#define ATT_OPS \
            : "+{v[64:79]}"(SX0), "+{v[80:95]}"(SX1), "+{v[96:111]}"(SY0), "+{v[112:127]}"(SY1), \
              "+{v[128:143]}"(OX), "+{v[144:159]}"(OY), \
              "+{v[168:171]}"(K0), "+{v[172:175]}"(K1), "+{v[176:179]}"(V00), "+{v[180:183]}"(V01), \
              "+{v[184:187]}"(V10), "+{v[188:191]}"(V11), "+{v193}"(ka), "+{v194}"(va), [cnt] "=&s"(cnt_)
#define ATT_INS : "{v[160:163]}"(qx), "{v[164:167]}"(qy), "{v192}"(idx), "{v195}"(vinc)
    u32 cnt_;
    const char* kvb = ws + WS_K;
    PRIO_START();
    asm volatile(ATT_A ATT_OPS, "+{v[196:203]}"(EXA), "+{v[228:231]}"(EXB), "+{v[236:239]}"(WOA), "+{v[220:221]}"(PZA), [skv] "+s"(skv), [sg] "+s"(sg)
                 ATT_INS, [kvb] "s"(kvb), [wsb] "s"(ws) : "memory", "scc", "v232", "v233", "v234", "v235");
    LSTAMP(4);
    BAR();
    LSTAMP(8);
    if (tid == 448) { u32 fo = WS_F2 + b * 4; GST32C(ws, fo, epoch); }
    PRIO_START();
    u32x16 EXD;
    asm volatile(ATT_B ATT_OPS, "+{v[196:203]}"(EXA), "=&{v[204:219]}"(EXD), "+{v[236:239]}"(WOA)
                 ATT_INS, "{v[228:231]}"(EXB), [kvb] "s"(kvb), [skv3] "s"(skv3), [xb] "s"(x), [wdb] "s"(wd)
                 : "memory", "scc", "v220", "v221", "v222", "v223", "v224", "v225", "v226", "v227", "v232", "v233", "v234", "v235");
    LSTAMP(9);
    BAR();
    LSTAMP(11);
    if (ffn && w < 4) {
        float sum = LDF(L_FP + l15 * 4);
#pragma unroll
        for (int i = 1; i < 8; ++i) sum += LDF(L_FP + i * 64 + l15 * 4);
        sum = bfr(sum);
        float up = __shfl(sum, (lane & 7) + 8);
        float gt = __shfl(sum, lane & 7);
        float hv = bfr(bfr(gt * sigm(gt)) * up);
        u32 hb = __float_as_uint(hv);
        float y0 = 0.f, y1 = 0.f;
#pragma unroll
        for (int i = 0; i < 8; ++i) {
            const float hs = __uint_as_float((u32)__builtin_amdgcn_readlane(hb, i));
            y0 += hs * lo16(EXD[8 + i]);
            y1 += hs * hi16(EXD[8 + i]);
        }
        u32x2 gr = {__float_as_uint(y0), __float_as_uint(y1)};
        gr[0] = gr[0] == POISON ? 0x7fc00000u : gr[0];
        gr[1] = gr[1] == POISON ? 0x7fc00000u : gr[1];
        u32 yo = WS_Y + fj * 2048 + (128 * w + 2 * lane) * 4;
        GST64C(ws, yo, gr);
    }
    LSTAMP(22);
    PRIO_START();
    u32 f2v; const u32 f2a = WS_F2 + (32 * w + l31) * 4;
    asm volatile(ATT_C ATT_OPS, "+{v[236:239]}"(WOA), "=&{v220}"(f2v), [swo] "+s"(swo) ATT_INS, "{v221}"(f2a), [wob] "s"(wo), [wsb] "s"(ws) : "memory", "scc");
    const int tq = w & 3, kh = w >> 2;
    const bool owner = (sel >> 6) == tb;
    int lane_b = lane;
    asm volatile("" : "+v"(lane_b));
    {
    const int lane = lane_b;
    const int g = lane >> 4, l15 = lane & 15, h = lane >> 5, l31 = lane & 31;
    const int tokg = tb * 64 + tq * 16 + l15;
    const int ncol = hg * 64 + kh * 32 + (g & 1) * 16 + (g >> 1) * 8;
    u32 oslot;
    {
        const int head = hg * 4 + hp * 2 + blk, tl = th * 32 + l31;
        oslot = WS_O + ((tb * 4 + (tl >> 4)) * 16 + (head >> 1)) * 1024 + (((head & 1) * 2 + h) * 16 + (tl & 15)) * 16;
    }
    STAMP(12); STAMPC(20);
    {
        u32 spins = 0;
        while (!__all(f2v == epoch) && ++spins <= SPIN_MAX) {
            const u32 a = WS_F2 + (32 * w + l31) * 4;
            asm volatile("global_load_dword %0, %1, %2 sc1\n\ts_waitcnt vmcnt(0)" : "=&v"(f2v) : "v"(a), "s"(ws) : "memory");
        }
    }
    u32x4 xe, Y0, Y1, Y2, Y3, Y4, Y5, Y6; u32 gl;
    const u32 yo0 = WS_Y + (28 * w + g) * 2048 + hg * 256 + l15 * 16;
    {
        u32 xo = tokg * 1024 + ncol * 2;
        GLD128(xe, x, xo, 0);
        u32 go = WS_G + tokg * 4;
        GLD32C(gl, ws, go, 0);
    }

    {
        f32x16& mine = blk ? OY : OX;
        f32x16& give = blk ? OX : OY;
        const u32 cw = L_W + w * 2304, cr = L_W + (w ^ 4) * 2304;
        ST128(cw + lane * 16, ((u32x4){__float_as_uint(give[0]), __float_as_uint(give[1]), __float_as_uint(give[2]), __float_as_uint(give[3])}));
        ST128(cw + 1024 + lane * 16, ((u32x4){__float_as_uint(give[4]), __float_as_uint(give[5]), __float_as_uint(give[6]), __float_as_uint(give[7])}));
        STF(cw + 2048 + lane * 4, give[8]);
        BAR();
        f32x4 p0 = __builtin_bit_cast(f32x4, LD128(cr + lane * 16));
        f32x4 p1 = __builtin_bit_cast(f32x4, LD128(cr + 1024 + lane * 16));
        float l = LDF(cr + 2048 + lane * 4) + mine[8];
        float o[8];
#pragma unroll
        for (int i = 0; i < 4; ++i) { o[i] = mine[i] + p0[i]; o[4 + i] = mine[4 + i] + p1[i]; }
        {
            auto sw = __builtin_amdgcn_permlane32_swap(__float_as_uint(l), __float_as_uint(l), false, false);
            l = __uint_as_float(sw[0]);
        }
        float amax = fmaxf(fmaxf(fmaxf(fabsf(o[0]), fabsf(o[1])), fmaxf(fabsf(o[2]), fabsf(o[3]))),
                           fmaxf(fmaxf(fabsf(o[4]), fabsf(o[5])), fmaxf(fabsf(o[6]), fabsf(o[7]))));
        bool safe = (l >= 8.6736174e-19f) && (l <= 1.2676506e30f) && (amax <= 3.0e38f);
        if (!__all(safe)) {
            slow_block(ws, blk ? qy : qx, lane, o, l);
            auto sw = __builtin_amdgcn_permlane32_swap(__float_as_uint(l), __float_as_uint(l), false, false);
            l = __uint_as_float(sw[0]);
        }
        const float ri = __builtin_amdgcn_rcpf(l);
        u32 A0 = pk(o[0] * ri, o[1] * ri), A1 = pk(o[2] * ri, o[3] * ri);
        u32 B0 = pk(o[4] * ri, o[5] * ri), B1 = pk(o[6] * ri, o[7] * ri);
        auto s0 = __builtin_amdgcn_permlane32_swap(A0, B0, false, false);
        auto s1 = __builtin_amdgcn_permlane32_swap(A1, B1, false, false);
        u32x4 ov = {s0[0], s1[0], s0[1], s1[1]};
#pragma unroll
        for (int i = 0; i < 4; ++i) ov[i] = ov[i] == POISON ? 0x7fc07fc0u : ov[i];
        GST128C(ws, oslot, ov);
    }
    {
        u32 yo = yo0;
        asm volatile("s_cmp_lg_u32 %[stb], %[tb]\n\ts_cbranch_scc1 3f\n"
                     "global_load_dwordx4 %0, %[yo], %[ws] sc1\n v_add_u32 %[yo], 0x2000, %[yo]\n"
                     "global_load_dwordx4 %1, %[yo], %[ws] sc1\n v_add_u32 %[yo], 0x2000, %[yo]\n"
                     "global_load_dwordx4 %2, %[yo], %[ws] sc1\n v_add_u32 %[yo], 0x2000, %[yo]\n"
                     "global_load_dwordx4 %3, %[yo], %[ws] sc1\n v_add_u32 %[yo], 0x2000, %[yo]\n"
                     "global_load_dwordx4 %4, %[yo], %[ws] sc1\n v_add_u32 %[yo], 0x2000, %[yo]\n"
                     "global_load_dwordx4 %5, %[yo], %[ws] sc1\n v_add_u32 %[yo], 0x2000, %[yo]\n"
                     "global_load_dwordx4 %6, %[yo], %[ws] sc1\n3:"
                     : "=&v"(Y0), "=&v"(Y1), "=&v"(Y2), "=&v"(Y3), "=&v"(Y4), "=&v"(Y5), "=&v"(Y6), [yo] "+v"(yo)
                     : [stb] "s"(sel >> 6), [tb] "s"(tb), [ws] "s"(ws) : "memory", "scc");
    }
    STAMP(13);

    u32x4 af[4][8];
    {
        const u32 f = ((l15 >> 3) & 1) | ((g & 1) << 1);
        const u32 rb = (kh ? L_V : L_K) + (8 * g + (l15 >> 2)) * 128 + (l15 & 3) * 8;
#pragma unroll
        for (int nb = 0; nb < 4; ++nb) {
            u32 a = rb + ((nb ^ f) * 32);
            asm volatile("" : "+v"(a));
#pragma unroll
            for (int st = 0; st < 8; ++st) {
                const u32 o_ = (st >> 2) * 32768 + (st & 3) * 4096;
                af[nb][st] = cat(trread(lds, a + o_), trread(lds, a + o_ + 512));
            }
        }
    }
    STAMP(14);
    asm volatile("s_cmp_lg_u32 %[stb], %[tb]\n\ts_cbranch_scc1 3f\n\ts_waitcnt vmcnt(0)\n3:\n\ts_waitcnt vmcnt(1)"
                 : "+v"(xe), "+v"(gl), "+v"(Y0), "+v"(Y1), "+v"(Y2), "+v"(Y3), "+v"(Y4), "+v"(Y5), "+v"(Y6)
                 : [stb] "s"(sel >> 6), [tb] "s"(tb) : "memory", "scc");
    if (owner) {
        u32 spins = 0;
#define YMX(v) max(max(v[0], v[1]), max(v[2], v[3]))
        while (!__all(max(max(max(YMX(Y0), YMX(Y1)), max(YMX(Y2), YMX(Y3))), max(max(YMX(Y4), YMX(Y5)), YMX(Y6))) != POISON) && ++spins <= SPIN_MAX) {
            u32 yo = yo0;
            asm volatile("global_load_dwordx4 %0, %[yo], %[ws] sc1\n v_add_u32 %[yo], 0x2000, %[yo]\n"
                         "global_load_dwordx4 %1, %[yo], %[ws] sc1\n v_add_u32 %[yo], 0x2000, %[yo]\n"
                         "global_load_dwordx4 %2, %[yo], %[ws] sc1\n v_add_u32 %[yo], 0x2000, %[yo]\n"
                         "global_load_dwordx4 %3, %[yo], %[ws] sc1\n v_add_u32 %[yo], 0x2000, %[yo]\n"
                         "global_load_dwordx4 %4, %[yo], %[ws] sc1\n v_add_u32 %[yo], 0x2000, %[yo]\n"
                         "global_load_dwordx4 %5, %[yo], %[ws] sc1\n v_add_u32 %[yo], 0x2000, %[yo]\n"
                         "global_load_dwordx4 %6, %[yo], %[ws] sc1\n s_waitcnt vmcnt(0)"
                         : "=&v"(Y0), "=&v"(Y1), "=&v"(Y2), "=&v"(Y3), "=&v"(Y4), "=&v"(Y5), "=&v"(Y6), [yo] "+v"(yo)
                         : [ws] "s"(ws) : "memory");
        }
        f32x4 ys;
#pragma unroll
        for (int i = 0; i < 4; ++i) {
            float v = ((__uint_as_float(Y0[i]) + __uint_as_float(Y1[i])) + (__uint_as_float(Y2[i]) + __uint_as_float(Y3[i])))
                    + ((__uint_as_float(Y4[i]) + __uint_as_float(Y5[i])) + __uint_as_float(Y6[i]));
            auto a = __builtin_amdgcn_permlane32_swap(__float_as_uint(v), __float_as_uint(v), false, false);
            v = __uint_as_float(a[0]) + __uint_as_float(a[1]);
            auto c = __builtin_amdgcn_permlane16_swap(__float_as_uint(v), __float_as_uint(v), false, false);
            ys[i] = __uint_as_float(c[0]) + __uint_as_float(c[1]);
        }
        if (g == 0) ST128(L_YP + w * 256 + l15 * 16, __builtin_bit_cast(u32x4, ys));
    }
    u32x4 of0, of1, of2, of3, of4, of5, of6, of7;
    {
        const u32 oo = WS_O + ((tb * 4 + tq) * 16 + 8 * kh) * 1024 + lane * 16, o2 = oo + 4096;
        u32 spins = 0;
        while (true) {
            asm volatile("global_load_dwordx4 %0, %8, %10 sc1\n\tglobal_load_dwordx4 %1, %8, %10 offset:1024 sc1\n\t"
                         "global_load_dwordx4 %2, %8, %10 offset:2048 sc1\n\tglobal_load_dwordx4 %3, %8, %10 offset:3072 sc1\n\t"
                         "global_load_dwordx4 %4, %9, %10 sc1\n\tglobal_load_dwordx4 %5, %9, %10 offset:1024 sc1\n\t"
                         "global_load_dwordx4 %6, %9, %10 offset:2048 sc1\n\tglobal_load_dwordx4 %7, %9, %10 offset:3072 sc1\n\t"
                         "s_waitcnt vmcnt(0)"
                         : "=&v"(of0), "=&v"(of1), "=&v"(of2), "=&v"(of3), "=&v"(of4), "=&v"(of5), "=&v"(of6), "=&v"(of7)
                         : "v"(oo), "v"(o2), "s"(ws) : "memory");
            u32 mx = 0;
#define OMX(v) mx = max(max(mx, max(v[0], v[1])), max(v[2], v[3]));
            OMX(of0) OMX(of1) OMX(of2) OMX(of3) OMX(of4) OMX(of5) OMX(of6) OMX(of7)
            if (__all(mx != POISON) || ++spins > SPIN_MAX) break;
        }
    }
    STAMP(15);
    f32x4 acc[4] = {{0, 0, 0, 0}, {0, 0, 0, 0}, {0, 0, 0, 0}, {0, 0, 0, 0}};
#define OSTEP(OF, ST) \
    _Pragma("unroll") for (int nb = 0; nb < 4; ++nb) acc[nb] = __builtin_amdgcn_mfma_f32_16x16x32_bf16(asbf(af[nb][ST]), asbf(OF), acc[nb], 0, 0, 0);
    OSTEP(of0, 0) OSTEP(of1, 1) OSTEP(of2, 2) OSTEP(of3, 3) OSTEP(of4, 4) OSTEP(of5, 5) OSTEP(of6, 6) OSTEP(of7, 7)
    STAMP(16);
    {
        f32x4 k0 = kh ? acc[2] : acc[0], k1 = kh ? acc[3] : acc[1];
        f32x4 g0 = kh ? acc[0] : acc[2], g1 = kh ? acc[1] : acc[3];
        ST128(L_K + 16384 + w * 2048 + lane * 16, __builtin_bit_cast(u32x4, g0));
        ST128(L_K + 16384 + w * 2048 + 1024 + lane * 16, __builtin_bit_cast(u32x4, g1));
        BAR();
        k0 += __builtin_bit_cast(f32x4, LD128(L_K + 16384 + (w ^ 4) * 2048 + lane * 16));
        k1 += __builtin_bit_cast(f32x4, LD128(L_K + 16384 + (w ^ 4) * 2048 + 1024 + lane * 16));
        float a[8];
#pragma unroll
        for (int i = 0; i < 4; ++i) {
            auto sw = __builtin_amdgcn_permlane16_swap(__float_as_uint(k0[i]), __float_as_uint(k1[i]), false, false);
            a[i] = __uint_as_float(sw[0]); a[4 + i] = __uint_as_float(sw[1]);
        }
        const float gt = bfr(sigm(bfr(__uint_as_float(gl))));
        const float og = bfr(1.0f - gt);
        float bb[8];
#pragma unroll
        for (int i = 0; i < 4; ++i) { bb[2 * i] = lo16(xe[i]); bb[2 * i + 1] = hi16(xe[i]); }
        if (owner && tokg == sel) {
            const u32 rord = key >> 16;
            const float rsel = __uint_as_float(((rord & 0x8000u) ? (rord & 0x7fffu) : (~rord & 0xffffu)) << 16);
            const float sg = bfr(sigm(rsel));
            const int c0 = kh * 32 + (g & 1) * 16 + (g >> 1) * 8;
#pragma unroll
            for (int i = 0; i < 8; ++i) {
                float y = 0.f;
#pragma unroll
                for (int p = 0; p < 8; ++p) y += LDF(L_YP + p * 256 + (c0 + i) * 4);
                bb[i] = bfr(bb[i] + bfr(bfr(y) * sg));
            }
        }
        u32x4 ov;
#pragma unroll
        for (int i = 0; i < 4; ++i)
            ov[i] = pk(bfr(gt * bfr(a[2 * i])) + bfr(og * bb[2 * i]), bfr(gt * bfr(a[2 * i + 1])) + bfr(og * bb[2 * i + 1]));
        u32 oo_ = tokg * 1024 + ncol * 2;
        GST128(out, oo_, ov);
    }
    STAMP(17); STAMPC(21);
    BAR();
    if (tid == 0 && (b >> 3) == 5) {
#define RD(wv, i) LD32(L_CT + 64 + ((wv) * 24 + (i)) * 4)
        u32 t1 = RD(0, 1);
        printf("STMP b %d t0 %u prio %u/%u bar1 %u pub %u bar2 %u qp %u poll %u b2b %u att0 %u att %u/%u ost %u oseen %u/%u end %u/%u cyc %u %u %u\n", b, t1 % 100000, RD(0,2) - t1, RD(7,2) - t1, RD(0,3) - t1, RD(7,4) - t1, RD(0,5) - t1, RD(0,6) - t1, RD(4,7) - t1,
               RD(0,9) - t1, RD(0,10) - t1, RD(0,12) - t1, RD(4,12) - t1, RD(0,13) - t1, RD(0,15) - t1, RD(4,15) - t1, RD(0,17) - t1, RD(4,17) - t1, RD(0,19) - RD(0,18), RD(0,20) - RD(0,19), RD(0,21) - RD(0,20));
    }
    }
}

extern "C" void kernel_launch(void* const* d_in, const int* in_sizes, int n_in, void* d_out, int out_size,
                              void* d_ws, size_t ws_size, hipStream_t stream) {
    (void)in_sizes; (void)n_in; (void)out_size; (void)ws_size;
    fused_kernel<<<256, 512, 0, stream>>>((const u16*)d_in[0], (const u16*)d_in[1], (const u16*)d_in[2], (const u16*)d_in[3],
                                          (const u16*)d_in[4], (const u16*)d_in[6], (const u16*)d_in[7], (const u16*)d_in[8],
                                          (const u16*)d_in[9], (const u16*)d_in[10], (u16*)d_out, (char*)d_ws);
}
```

```cpp
#include <hip/hip_runtime.h>
#include <stdint.h>
#include <stdio.h>
#define DBG_STAMP
#define LOOPC

typedef unsigned int u32;
typedef unsigned long long u64;
typedef unsigned short u16;
typedef u32 u32x2 __attribute__((ext_vector_type(2)));
typedef u32 u32x4 __attribute__((ext_vector_type(4)));
typedef u32 u32x8 __attribute__((ext_vector_type(8)));
typedef u32 u32x16 __attribute__((ext_vector_type(16)));
typedef float f32x2 __attribute__((ext_vector_type(2)));
typedef float f32x4 __attribute__((ext_vector_type(4)));
typedef float f32x16 __attribute__((ext_vector_type(16)));
typedef __bf16 bf16x2 __attribute__((ext_vector_type(2)));
typedef __bf16 bf16x8 __attribute__((ext_vector_type(8)));
#define LDSP __attribute__((address_space(3)))

constexpr u32 WS_E   = 0;
constexpr u32 WS_F1  = 4096;
constexpr u32 WS_G   = 24576;
constexpr u32 WS_K   = 65536;
constexpr u32 WS_V   = 131072;
constexpr u32 WS_O   = 262144;
constexpr u32 WS_Y   = 2621440;
constexpr u32 WS_X   = 3145728;
constexpr u32 WS_DBG = 4194304;

constexpr u32 L_K   = 0;
constexpr u32 L_V   = 65536;
constexpr u32 L_ONE = 131072;
constexpr u32 L_W   = 135168;
constexpr u32 L_M   = 151552;
constexpr u32 L_P1  = L_M;
constexpr u32 L_PD  = L_M + 8192;
constexpr u32 L_FP  = L_M + 8704;
constexpr u32 L_YP  = L_M + 9216;
constexpr u32 L_CT  = L_M + 11264;
constexpr u32 L_END = 163840;

constexpr u32 SPIN_MAX = 1u << 22;
constexpr u32 POISON = 0xffffffffu;

#define LD128(a) (*(const LDSP u32x4*)(lds + (a)))
#define ST128(a, v) (*(LDSP u32x4*)(lds + (a)) = (v))
#define LD32(a) (*(const LDSP u32*)(lds + (a)))
#define ST32(a, v) (*(LDSP u32*)(lds + (a)) = (v))
#define LDF(a) (*(const LDSP float*)(lds + (a)))
#define STF(a, v) (*(LDSP float*)(lds + (a)) = (v))

__device__ __forceinline__ u32x2 trread(LDSP char* lds, u32 a) {
    typedef short v4s __attribute__((ext_vector_type(4)));
    v4s r = __builtin_amdgcn_ds_read_tr16_b64_v4i16((LDSP v4s*)(lds + a));
    return __builtin_bit_cast(u32x2, r);
}
__device__ __forceinline__ bf16x8 asbf(u32x4 v) { return __builtin_bit_cast(bf16x8, v); }
__device__ __forceinline__ u32x4 cat(u32x2 a, u32x2 b) { u32x4 r = {a[0], a[1], b[0], b[1]}; return r; }
__device__ __forceinline__ u32 pk(float a, float b) {
    f32x2 f = {a, b};
    return __builtin_bit_cast(u32, __builtin_convertvector(f, bf16x2));
}
__device__ __forceinline__ float bfr(float a) {
    return __uint_as_float(pk(a, a) & 0xffff0000u);
}
__device__ __forceinline__ float lo16(u32 v) { return __uint_as_float(v << 16); }
__device__ __forceinline__ float hi16(u32 v) { return __uint_as_float(v & 0xffff0000u); }
__device__ __forceinline__ float sigm(float z) { return __builtin_amdgcn_rcpf(1.0f + __builtin_amdgcn_exp2f(-1.4426950408889634f * z)); }

__device__ __forceinline__ void dma16(const void* base, u32 voff, u32 ldsaddr) {
    asm volatile("s_mov_b32 m0, %0\n\ts_nop 0\n\tglobal_load_lds_dwordx4 %1, %2" :: "s"(ldsaddr), "v"(voff), "s"(base) : "memory");
}
__device__ __forceinline__ void dma16c4(const void* base, u32 voff, u32 ldsaddr) {
    asm volatile("s_mov_b32 m0, %0\n\ts_nop 0\n\tglobal_load_lds_dwordx4 %1, %2 sc1\n\tglobal_load_lds_dwordx4 %1, %2 offset:1024 sc1\n\t"
                 "global_load_lds_dwordx4 %1, %2 offset:2048 sc1\n\tglobal_load_lds_dwordx4 %1, %2 offset:3072 sc1"
                 :: "s"(ldsaddr), "v"(voff), "s"(base) : "memory");
}
__device__ __forceinline__ void dma16c3(const void* base, u32 voff, u32 ldsaddr) {
    asm volatile("s_mov_b32 m0, %0\n\ts_nop 0\n\tglobal_load_lds_dwordx4 %1, %2 sc1\n\tglobal_load_lds_dwordx4 %1, %2 offset:1024 sc1\n\t"
                 "global_load_lds_dwordx4 %1, %2 offset:2048 sc1"
                 :: "s"(ldsaddr), "v"(voff), "s"(base) : "memory");
}
__device__ __forceinline__ void dma16c2(const void* base, u32 voff, u32 ldsaddr) {
    asm volatile("s_mov_b32 m0, %0\n\ts_nop 0\n\tglobal_load_lds_dwordx4 %1, %2 sc1\n\tglobal_load_lds_dwordx4 %1, %2 offset:1024 sc1"
                 :: "s"(ldsaddr), "v"(voff), "s"(base) : "memory");
}
__device__ __forceinline__ void dma16c(const void* base, u32 voff, u32 ldsaddr) {
    asm volatile("s_mov_b32 m0, %0\n\ts_nop 0\n\tglobal_load_lds_dwordx4 %1, %2 sc1" :: "s"(ldsaddr), "v"(voff), "s"(base) : "memory");
}
#define GLD128(dst, base, voff, OFF)  asm volatile("global_load_dwordx4 %0, %1, %2 offset:" #OFF : "=v"(dst) : "v"(voff), "s"(base) : "memory")
#define GLD128C(dst, base, voff, OFF) asm volatile("global_load_dwordx4 %0, %1, %2 offset:" #OFF " sc1" : "=v"(dst) : "v"(voff), "s"(base) : "memory")
#define GLD64C(dst, base, voff, OFF)  asm volatile("global_load_dwordx2 %0, %1, %2 offset:" #OFF " sc1" : "=v"(dst) : "v"(voff), "s"(base) : "memory")
#define GLD32C(dst, base, voff, OFF)  asm volatile("global_load_dword %0, %1, %2 offset:" #OFF " sc1" : "=v"(dst) : "v"(voff), "s"(base) : "memory")
#define GLD32(dst, base, voff, OFF)   asm volatile("global_load_dword %0, %1, %2 offset:" #OFF : "=v"(dst) : "v"(voff), "s"(base) : "memory")
#define GLD16(dst, base, voff, OFF)   asm volatile("global_load_ushort %0, %1, %2 offset:" #OFF : "=v"(dst) : "v"(voff), "s"(base) : "memory")
#define GST128(base, voff, val)  asm volatile("global_store_dwordx4 %0, %1, %2\n\ts_nop 1" :: "v"(voff), "v"(val), "s"(base) : "memory")
#define GST128C(base, voff, val) asm volatile("global_store_dwordx4 %0, %1, %2 sc1\n\ts_nop 1" :: "v"(voff), "v"(val), "s"(base) : "memory")
#define GST64C(base, voff, val)  asm volatile("global_store_dwordx2 %0, %1, %2 sc1\n\ts_nop 0" :: "v"(voff), "v"(val), "s"(base) : "memory")
#define GST32C(base, voff, val)  asm volatile("global_store_dword %0, %1, %2 sc1" :: "v"(voff), "v"(val), "s"(base) : "memory")
#define VMWAIT(N) asm volatile("s_waitcnt vmcnt(" #N ")" ::: "memory")
#define BAR() asm volatile("s_waitcnt lgkmcnt(0)\n\ts_barrier" ::: "memory")

#define STAMP(i) do { u32 t_ = (u32)__builtin_amdgcn_s_memrealtime(); if (lane == 0) ST32(L_CT + 64 + (w * 24 + (i)) * 4, t_); } while (0)
#define STAMPC(i) do { u32 t_ = (u32)__builtin_amdgcn_s_memtime(); if (lane == 0) ST32(L_CT + 64 + (w * 24 + (i)) * 4, t_); } while (0)
#define STAMPDUMP() do { if (lane < 24) ((u32*)(ws + WS_DBG))[(blockIdx.x * 8 + w) * 32 + lane] = LD32(L_CT + 64 + (w * 24 + lane) * 4); } while (0)
#define FSTAMP(i)
#define LSTAMP(i) STAMPC(i)

__device__ __forceinline__ u32 wave_max_u32(u32 v) {
    v = max(v, (u32)__builtin_amdgcn_update_dpp(0, (int)v, 0x111, 0xf, 0xf, false));
    v = max(v, (u32)__builtin_amdgcn_update_dpp(0, (int)v, 0x112, 0xf, 0xf, false));
    v = max(v, (u32)__builtin_amdgcn_update_dpp(0, (int)v, 0x114, 0xf, 0xf, false));
    v = max(v, (u32)__builtin_amdgcn_update_dpp(0, (int)v, 0x118, 0xf, 0xf, false));
    v = max(v, (u32)__builtin_amdgcn_update_dpp(0, (int)v, 0x142, 0xa, 0xf, false));
    v = max(v, (u32)__builtin_amdgcn_update_dpp(0, (int)v, 0x143, 0xc, 0xf, false));
    return (u32)__builtin_amdgcn_readlane((int)v, 63);
}
#define ATT_A \
    "s_mov_b32 %[cnt], 2\n" \
    "1:\n" \
    "v_exp_f32 v64, v64\n" \
    "v_exp_f32 v65, v65\n" \
    "v_smfmac_f32_32x32x32_bf16 v[144:159], v[188:191], v[120:127], v192\n" \
    "ds_read_b128 v[184:187], v194 offset:1024\n" \
    "v_exp_f32 v66, v66\n" \
    "v_exp_f32 v67, v67\n" \
    "v_exp_f32 v68, v68\n" \
    "v_exp_f32 v69, v69\n" \
    "s_waitcnt lgkmcnt(1)\n" \
    "v_mfma_f32_32x32x16_bf16 v[80:95], v[172:175], v[160:163], 0\n" \
    "ds_read_b128 v[188:191], v194 offset:1536\n" \
    "v_exp_f32 v70, v70\n" \
    "v_exp_f32 v71, v71\n" \
    "v_exp_f32 v72, v72\n" \
    "v_exp_f32 v73, v73\n" \
    "v_smfmac_f32_32x32x32_bf16 v[128:143], v[176:179], v[64:71], v192\n" \
    "v_exp_f32 v74, v74\n" \
    "s_mov_b32 m0, %[skv]\n" \
    "v_exp_f32 v75, v75\n" \
    "v_exp_f32 v76, v76\n" \
    "global_load_lds_dwordx4 v196, %[kvb] offset:0 sc1\n" \
    "v_exp_f32 v77, v77\n" \
    "v_exp_f32 v78, v78\n" \
    "v_exp_f32 v79, v79\n" \
    "v_exp_f32 v96, v96\n" \
    "v_exp_f32 v97, v97\n" \
    "v_smfmac_f32_32x32x32_bf16 v[128:143], v[180:183], v[72:79], v192\n" \
    "v_exp_f32 v98, v98\n" \
    "v_exp_f32 v99, v99\n" \
    "v_exp_f32 v100, v100\n" \
    "v_exp_f32 v101, v101\n" \
    "v_mfma_f32_32x32x16_bf16 v[112:127], v[172:175], v[164:167], 0\n" \
    "ds_read_b128 v[168:171], v193 offset:2048\n" \
    "v_exp_f32 v102, v102\n" \
    "v_exp_f32 v103, v103\n" \
    "v_exp_f32 v104, v104\n" \
    "v_exp_f32 v105, v105\n" \
    "v_smfmac_f32_32x32x32_bf16 v[144:159], v[176:179], v[96:103], v192\n" \
    "v_exp_f32 v106, v106\n" \
    "v_exp_f32 v107, v107\n" \
    "v_exp_f32 v108, v108\n" \
    "v_exp_f32 v109, v109\n" \
    "v_exp_f32 v110, v110\n" \
    "v_exp_f32 v111, v111\n" \
    "v_exp_f32 v80, v80\n" \
    "v_exp_f32 v81, v81\n" \
    "v_smfmac_f32_32x32x32_bf16 v[144:159], v[180:183], v[104:111], v192\n" \
    "ds_read_b128 v[176:179], v194 offset:2048\n" \
    "v_exp_f32 v82, v82\n" \
    "v_exp_f32 v83, v83\n" \
    "v_exp_f32 v84, v84\n" \
    "v_exp_f32 v85, v85\n" \
    "s_waitcnt lgkmcnt(1)\n" \
    "v_mfma_f32_32x32x16_bf16 v[64:79], v[168:171], v[160:163], 0\n" \
    "ds_read_b128 v[180:183], v194 offset:2560\n" \
    "v_exp_f32 v86, v86\n" \
    "v_exp_f32 v87, v87\n" \
    "v_exp_f32 v88, v88\n" \
    "v_exp_f32 v89, v89\n" \
    "v_smfmac_f32_32x32x32_bf16 v[128:143], v[184:187], v[80:87], v192\n" \
    "v_exp_f32 v90, v90\n" \
    "s_mov_b32 m0, %[skv]\n" \
    "v_exp_f32 v91, v91\n" \
    "v_exp_f32 v92, v92\n" \
    "global_load_lds_dwordx4 v196, %[kvb] offset:1024 sc1\n" \
    "v_exp_f32 v93, v93\n" \
    "v_exp_f32 v94, v94\n" \
    "v_exp_f32 v95, v95\n" \
    "v_exp_f32 v112, v112\n" \
    "v_exp_f32 v113, v113\n" \
    "v_smfmac_f32_32x32x32_bf16 v[128:143], v[188:191], v[88:95], v192\n" \
    "v_exp_f32 v114, v114\n" \
    "v_exp_f32 v115, v115\n" \
    "v_exp_f32 v116, v116\n" \
    "v_exp_f32 v117, v117\n" \
    "v_mfma_f32_32x32x16_bf16 v[96:111], v[168:171], v[164:167], 0\n" \
    "ds_read_b128 v[172:175], v193 offset:3072\n" \
    "v_exp_f32 v118, v118\n" \
    "v_exp_f32 v119, v119\n" \
    "v_exp_f32 v120, v120\n" \
    "v_exp_f32 v121, v121\n" \
    "v_smfmac_f32_32x32x32_bf16 v[144:159], v[184:187], v[112:119], v192\n" \
    "v_exp_f32 v122, v122\n" \
    "s_mov_b32 m0, %[sg]\n" \
    "v_exp_f32 v123, v123\n" \
    "v_exp_f32 v124, v124\n" \
    "global_load_lds_dwordx4 v[198:199], off\n" \
    "v_exp_f32 v125, v125\n" \
    "v_exp_f32 v126, v126\n" \
    "v_exp_f32 v127, v127\n" \
    "s_cmp_eq_u32 %[cnt], 1\n" \
    "s_cbranch_scc0 8f\n" \
    "s_setprio 0\n" \
    "8:\n" \
    "v_exp_f32 v64, v64\n" \
    "v_exp_f32 v65, v65\n" \
    "v_smfmac_f32_32x32x32_bf16 v[144:159], v[188:191], v[120:127], v192\n" \
    "ds_read_b128 v[184:187], v194 offset:3072\n" \
    "v_exp_f32 v66, v66\n" \
    "v_exp_f32 v67, v67\n" \
    "v_exp_f32 v68, v68\n" \
    "v_exp_f32 v69, v69\n" \
    "s_waitcnt lgkmcnt(1)\n" \
    "v_mfma_f32_32x32x16_bf16 v[80:95], v[172:175], v[160:163], 0\n" \
    "ds_read_b128 v[188:191], v194 offset:3584\n" \
    "v_exp_f32 v70, v70\n" \
    "v_exp_f32 v71, v71\n" \
    "v_exp_f32 v72, v72\n" \
    "v_exp_f32 v73, v73\n" \
    "v_smfmac_f32_32x32x32_bf16 v[128:143], v[176:179], v[64:71], v192\n" \
    "v_exp_f32 v74, v74\n" \
    "s_mov_b32 m0, %[skv]\n" \
    "v_exp_f32 v75, v75\n" \
    "v_exp_f32 v76, v76\n" \
    "global_load_lds_dwordx4 v196, %[kvb] offset:2048 sc1\n" \
    "v_exp_f32 v77, v77\n" \
    "v_exp_f32 v78, v78\n" \
    "v_exp_f32 v79, v79\n" \
    "v_exp_f32 v96, v96\n" \
    "v_exp_f32 v97, v97\n" \
    "v_smfmac_f32_32x32x32_bf16 v[128:143], v[180:183], v[72:79], v192\n" \
    "v_exp_f32 v98, v98\n" \
    "v_exp_f32 v99, v99\n" \
    "v_exp_f32 v100, v100\n" \
    "v_exp_f32 v101, v101\n" \
    "v_mfma_f32_32x32x16_bf16 v[112:127], v[172:175], v[164:167], 0\n" \
    "ds_read_b128 v[168:171], v193 offset:4096\n" \
    "v_exp_f32 v102, v102\n" \
    "v_exp_f32 v103, v103\n" \
    "v_exp_f32 v104, v104\n" \
    "v_exp_f32 v105, v105\n" \
    "v_smfmac_f32_32x32x32_bf16 v[144:159], v[176:179], v[96:103], v192\n" \
    "v_exp_f32 v106, v106\n" \
    "v_exp_f32 v107, v107\n" \
    "v_exp_f32 v108, v108\n" \
    "v_exp_f32 v109, v109\n" \
    "v_exp_f32 v110, v110\n" \
    "v_exp_f32 v111, v111\n" \
    "v_exp_f32 v80, v80\n" \
    "v_exp_f32 v81, v81\n" \
    "v_smfmac_f32_32x32x32_bf16 v[144:159], v[180:183], v[104:111], v192\n" \
    "ds_read_b128 v[176:179], v194 offset:4096\n" \
    "v_exp_f32 v82, v82\n" \
    "v_exp_f32 v83, v83\n" \
    "v_exp_f32 v84, v84\n" \
    "v_exp_f32 v85, v85\n" \
    "s_waitcnt lgkmcnt(1)\n" \
    "v_mfma_f32_32x32x16_bf16 v[64:79], v[168:171], v[160:163], 0\n" \
    "ds_read_b128 v[180:183], v194 offset:4608\n" \
    "v_exp_f32 v86, v86\n" \
    "v_exp_f32 v87, v87\n" \
    "v_exp_f32 v88, v88\n" \
    "v_exp_f32 v89, v89\n" \
    "v_smfmac_f32_32x32x32_bf16 v[128:143], v[184:187], v[80:87], v192\n" \
    "v_exp_f32 v90, v90\n" \
    "s_mov_b32 m0, %[skv]\n" \
    "v_exp_f32 v91, v91\n" \
    "v_exp_f32 v92, v92\n" \
    "global_load_lds_dwordx4 v196, %[kvb] offset:3072 sc1\n" \
    "v_exp_f32 v93, v93\n" \
    "v_exp_f32 v94, v94\n" \
    "v_exp_f32 v95, v95\n" \
    "v_exp_f32 v112, v112\n" \
    "v_exp_f32 v113, v113\n" \
    "v_smfmac_f32_32x32x32_bf16 v[128:143], v[188:191], v[88:95], v192\n" \
    "v_exp_f32 v114, v114\n" \
    "v_exp_f32 v115, v115\n" \
    "v_exp_f32 v116, v116\n" \
    "v_exp_f32 v117, v117\n" \
    "v_mfma_f32_32x32x16_bf16 v[96:111], v[168:171], v[164:167], 0\n" \
    "ds_read_b128 v[172:175], v193 offset:5120\n" \
    "v_exp_f32 v118, v118\n" \
    "v_exp_f32 v119, v119\n" \
    "v_exp_f32 v120, v120\n" \
    "v_exp_f32 v121, v121\n" \
    "v_smfmac_f32_32x32x32_bf16 v[144:159], v[184:187], v[112:119], v192\n" \
    "v_exp_f32 v122, v122\n" \
    "v_exp_f32 v123, v123\n" \
    "v_exp_f32 v124, v124\n" \
    "v_exp_f32 v125, v125\n" \
    "v_exp_f32 v126, v126\n" \
    "v_exp_f32 v127, v127\n" \
    "v_add_u32 v196, 0x2000, v196\n" \
    "v_mov_b32 v198, v200\n" \
    "v_mov_b32 v199, v201\n" \
    "s_add_u32 %[skv], %[skv], 0x2000\n" \
    "s_add_u32 %[sg], %[sg], 0x400\n" \
    "v_add_u32 v193, 4096, v193\n" \
    "v_add_u32 v194, v195, v194\n" \
    "s_sub_u32 %[cnt], %[cnt], 1\n" \
    "s_cmp_lg_u32 %[cnt], 0\n" \
    "s_cbranch_scc1 1b\n" \
    "s_waitcnt vmcnt(5)\n"

#define ATT_B \
    "v_exp_f32 v64, v64\n" \
    "v_exp_f32 v65, v65\n" \
    "v_smfmac_f32_32x32x32_bf16 v[144:159], v[188:191], v[120:127], v192\n" \
    "ds_read_b128 v[184:187], v194 offset:1024\n" \
    "v_exp_f32 v66, v66\n" \
    "v_exp_f32 v67, v67\n" \
    "v_exp_f32 v68, v68\n" \
    "v_exp_f32 v69, v69\n" \
    "s_waitcnt lgkmcnt(1)\n" \
    "v_mfma_f32_32x32x16_bf16 v[80:95], v[172:175], v[160:163], 0\n" \
    "ds_read_b128 v[188:191], v194 offset:1536\n" \
    "v_exp_f32 v70, v70\n" \
    "v_exp_f32 v71, v71\n" \
    "v_exp_f32 v72, v72\n" \
    "v_exp_f32 v73, v73\n" \
    "v_smfmac_f32_32x32x32_bf16 v[128:143], v[176:179], v[64:71], v192\n" \
    "v_exp_f32 v74, v74\n" \
    "s_mov_b32 m0, %[skv3]\n" \
    "v_exp_f32 v75, v75\n" \
    "v_exp_f32 v76, v76\n" \
    "global_load_dwordx4 v[204:207], v202, %[xb]\n" \
    "v_exp_f32 v77, v77\n" \
    "v_exp_f32 v78, v78\n" \
    "v_exp_f32 v79, v79\n" \
    "v_exp_f32 v96, v96\n" \
    "v_exp_f32 v97, v97\n" \
    "v_smfmac_f32_32x32x32_bf16 v[128:143], v[180:183], v[72:79], v192\n" \
    "v_exp_f32 v98, v98\n" \
    "v_exp_f32 v99, v99\n" \
    "v_exp_f32 v100, v100\n" \
    "v_exp_f32 v101, v101\n" \
    "v_mfma_f32_32x32x16_bf16 v[112:127], v[172:175], v[164:167], 0\n" \
    "ds_read_b128 v[168:171], v193 offset:2048\n" \
    "v_exp_f32 v102, v102\n" \
    "v_exp_f32 v103, v103\n" \
    "v_exp_f32 v104, v104\n" \
    "v_exp_f32 v105, v105\n" \
    "v_smfmac_f32_32x32x32_bf16 v[144:159], v[176:179], v[96:103], v192\n" \
    "v_exp_f32 v106, v106\n" \
    "v_exp_f32 v107, v107\n" \
    "v_exp_f32 v108, v108\n" \
    "global_load_dwordx4 v[208:211], v202, %[xb] offset:16\n" \
    "v_exp_f32 v109, v109\n" \
    "v_exp_f32 v110, v110\n" \
    "v_exp_f32 v111, v111\n" \
    "v_exp_f32 v80, v80\n" \
    "v_exp_f32 v81, v81\n" \
    "v_smfmac_f32_32x32x32_bf16 v[144:159], v[180:183], v[104:111], v192\n" \
    "ds_read_b128 v[176:179], v194 offset:2048\n" \
    "v_exp_f32 v82, v82\n" \
    "v_exp_f32 v83, v83\n" \
    "v_exp_f32 v84, v84\n" \
    "v_exp_f32 v85, v85\n" \
    "s_waitcnt lgkmcnt(1)\n" \
    "v_mfma_f32_32x32x16_bf16 v[64:79], v[168:171], v[160:163], 0\n" \
    "ds_read_b128 v[180:183], v194 offset:2560\n" \
    "v_exp_f32 v86, v86\n" \
    "v_exp_f32 v87, v87\n" \
    "v_exp_f32 v88, v88\n" \
    "v_exp_f32 v89, v89\n" \
    "v_smfmac_f32_32x32x32_bf16 v[128:143], v[184:187], v[80:87], v192\n" \
    "v_exp_f32 v90, v90\n" \
    "v_exp_f32 v91, v91\n" \
    "v_exp_f32 v92, v92\n" \
    "global_load_lds_dwordx4 v197, %[kvb] offset:0 sc1\n" \
    "v_exp_f32 v93, v93\n" \
    "v_exp_f32 v94, v94\n" \
    "v_exp_f32 v95, v95\n" \
    "v_exp_f32 v112, v112\n" \
    "v_exp_f32 v113, v113\n" \
    "v_smfmac_f32_32x32x32_bf16 v[128:143], v[188:191], v[88:95], v192\n" \
    "v_exp_f32 v114, v114\n" \
    "v_exp_f32 v115, v115\n" \
    "v_exp_f32 v116, v116\n" \
    "v_exp_f32 v117, v117\n" \
    "v_mfma_f32_32x32x16_bf16 v[96:111], v[168:171], v[164:167], 0\n" \
    "ds_read_b128 v[172:175], v193 offset:3072\n" \
    "v_exp_f32 v118, v118\n" \
    "v_exp_f32 v119, v119\n" \
    "v_exp_f32 v120, v120\n" \
    "v_exp_f32 v121, v121\n" \
    "v_smfmac_f32_32x32x32_bf16 v[144:159], v[184:187], v[112:119], v192\n" \
    "v_exp_f32 v122, v122\n" \
    "v_exp_f32 v123, v123\n" \
    "v_exp_f32 v124, v124\n" \
    "global_load_lds_dwordx4 v197, %[kvb] offset:1024 sc1\n" \
    "v_exp_f32 v125, v125\n" \
    "v_exp_f32 v126, v126\n" \
    "v_exp_f32 v127, v127\n" \
    "v_exp_f32 v64, v64\n" \
    "v_exp_f32 v65, v65\n" \
    "v_smfmac_f32_32x32x32_bf16 v[144:159], v[188:191], v[120:127], v192\n" \
    "ds_read_b128 v[184:187], v194 offset:3072\n" \
    "v_exp_f32 v66, v66\n" \
    "v_exp_f32 v67, v67\n" \
    "v_exp_f32 v68, v68\n" \
    "v_exp_f32 v69, v69\n" \
    "s_waitcnt lgkmcnt(1)\n" \
    "v_mfma_f32_32x32x16_bf16 v[80:95], v[172:175], v[160:163], 0\n" \
    "ds_read_b128 v[188:191], v194 offset:3584\n" \
    "v_exp_f32 v70, v70\n" \
    "v_exp_f32 v71, v71\n" \
    "v_exp_f32 v72, v72\n" \
    "v_exp_f32 v73, v73\n" \
    "v_smfmac_f32_32x32x32_bf16 v[128:143], v[176:179], v[64:71], v192\n" \
    "v_exp_f32 v74, v74\n" \
    "v_exp_f32 v75, v75\n" \
    "v_exp_f32 v76, v76\n" \
    "global_load_lds_dwordx4 v197, %[kvb] offset:2048 sc1\n" \
    "v_exp_f32 v77, v77\n" \
    "v_exp_f32 v78, v78\n" \
    "v_exp_f32 v79, v79\n" \
    "v_exp_f32 v96, v96\n" \
    "v_exp_f32 v97, v97\n" \
    "v_smfmac_f32_32x32x32_bf16 v[128:143], v[180:183], v[72:79], v192\n" \
    "v_exp_f32 v98, v98\n" \
    "v_exp_f32 v99, v99\n" \
    "v_exp_f32 v100, v100\n" \
    "v_exp_f32 v101, v101\n" \
    "v_mfma_f32_32x32x16_bf16 v[112:127], v[172:175], v[164:167], 0\n" \
    "ds_read_b128 v[168:171], v193 offset:4096\n" \
    "v_exp_f32 v102, v102\n" \
    "v_exp_f32 v103, v103\n" \
    "v_exp_f32 v104, v104\n" \
    "v_exp_f32 v105, v105\n" \
    "v_smfmac_f32_32x32x32_bf16 v[144:159], v[176:179], v[96:103], v192\n" \
    "v_exp_f32 v106, v106\n" \
    "v_exp_f32 v107, v107\n" \
    "v_exp_f32 v108, v108\n" \
    "global_load_lds_dwordx4 v197, %[kvb] offset:3072 sc1\n" \
    "v_exp_f32 v109, v109\n" \
    "v_exp_f32 v110, v110\n" \
    "v_exp_f32 v111, v111\n" \
    "v_exp_f32 v80, v80\n" \
    "v_exp_f32 v81, v81\n" \
    "v_smfmac_f32_32x32x32_bf16 v[144:159], v[180:183], v[104:111], v192\n" \
    "ds_read_b128 v[176:179], v194 offset:4096\n" \
    "v_exp_f32 v82, v82\n" \
    "v_exp_f32 v83, v83\n" \
    "v_exp_f32 v84, v84\n" \
    "v_exp_f32 v85, v85\n" \
    "s_waitcnt lgkmcnt(1)\n" \
    "v_mfma_f32_32x32x16_bf16 v[64:79], v[168:171], v[160:163], 0\n" \
    "ds_read_b128 v[180:183], v194 offset:4608\n" \
    "v_exp_f32 v86, v86\n" \
    "v_exp_f32 v87, v87\n" \
    "v_exp_f32 v88, v88\n" \
    "v_exp_f32 v89, v89\n" \
    "v_smfmac_f32_32x32x32_bf16 v[128:143], v[184:187], v[80:87], v192\n" \
    "v_exp_f32 v90, v90\n" \
    "v_exp_f32 v91, v91\n" \
    "v_exp_f32 v92, v92\n" \
    "global_load_dword v212, v203, %[wdb] offset:0\n" \
    "global_load_dword v213, v203, %[wdb] offset:1024\n" \
    "v_exp_f32 v93, v93\n" \
    "v_exp_f32 v94, v94\n" \
    "v_exp_f32 v95, v95\n" \
    "v_exp_f32 v112, v112\n" \
    "v_exp_f32 v113, v113\n" \
    "v_smfmac_f32_32x32x32_bf16 v[128:143], v[188:191], v[88:95], v192\n" \
    "v_exp_f32 v114, v114\n" \
    "v_exp_f32 v115, v115\n" \
    "v_exp_f32 v116, v116\n" \
    "v_exp_f32 v117, v117\n" \
    "v_mfma_f32_32x32x16_bf16 v[96:111], v[168:171], v[164:167], 0\n" \
    "ds_read_b128 v[172:175], v193 offset:5120\n" \
    "v_exp_f32 v118, v118\n" \
    "v_exp_f32 v119, v119\n" \
    "v_exp_f32 v120, v120\n" \
    "v_exp_f32 v121, v121\n" \
    "v_smfmac_f32_32x32x32_bf16 v[144:159], v[184:187], v[112:119], v192\n" \
    "v_exp_f32 v122, v122\n" \
    "v_exp_f32 v123, v123\n" \
    "v_exp_f32 v124, v124\n" \
    "global_load_dword v214, v203, %[wdb] offset:2048\n" \
    "global_load_dword v215, v203, %[wdb] offset:3072\n" \
    "v_exp_f32 v125, v125\n" \
    "v_exp_f32 v126, v126\n" \
    "v_exp_f32 v127, v127\n" \
    "v_add_u32 v203, 0x1000, v203\n" \
    "v_add_u32 v193, 4096, v193\n" \
    "v_add_u32 v194, v195, v194\n" \
    "v_exp_f32 v64, v64\n" \
    "v_exp_f32 v65, v65\n" \
    "v_smfmac_f32_32x32x32_bf16 v[144:159], v[188:191], v[120:127], v192\n" \
    "ds_read_b128 v[184:187], v194 offset:1024\n" \
    "v_exp_f32 v66, v66\n" \
    "v_exp_f32 v67, v67\n" \
    "v_exp_f32 v68, v68\n" \
    "v_exp_f32 v69, v69\n" \
    "s_waitcnt lgkmcnt(1)\n" \
    "v_mfma_f32_32x32x16_bf16 v[80:95], v[172:175], v[160:163], 0\n" \
    "ds_read_b128 v[188:191], v194 offset:1536\n" \
    "v_exp_f32 v70, v70\n" \
    "v_exp_f32 v71, v71\n" \
    "v_exp_f32 v72, v72\n" \
    "v_exp_f32 v73, v73\n" \
    "v_smfmac_f32_32x32x32_bf16 v[128:143], v[176:179], v[64:71], v192\n" \
    "v_exp_f32 v74, v74\n" \
    "v_exp_f32 v75, v75\n" \
    "v_exp_f32 v76, v76\n" \
    "global_load_dword v216, v203, %[wdb] offset:0\n" \
    "global_load_dword v217, v203, %[wdb] offset:1024\n" \
    "v_exp_f32 v77, v77\n" \
    "v_exp_f32 v78, v78\n" \
    "v_exp_f32 v79, v79\n" \
    "v_exp_f32 v96, v96\n" \
    "v_exp_f32 v97, v97\n" \
    "v_smfmac_f32_32x32x32_bf16 v[128:143], v[180:183], v[72:79], v192\n" \
    "v_exp_f32 v98, v98\n" \
    "v_exp_f32 v99, v99\n" \
    "v_exp_f32 v100, v100\n" \
    "v_exp_f32 v101, v101\n" \
    "v_mfma_f32_32x32x16_bf16 v[112:127], v[172:175], v[164:167], 0\n" \
    "ds_read_b128 v[168:171], v193 offset:2048\n" \
    "v_exp_f32 v102, v102\n" \
    "v_exp_f32 v103, v103\n" \
    "v_exp_f32 v104, v104\n" \
    "v_exp_f32 v105, v105\n" \
    "v_smfmac_f32_32x32x32_bf16 v[144:159], v[176:179], v[96:103], v192\n" \
    "v_exp_f32 v106, v106\n" \
    "v_exp_f32 v107, v107\n" \
    "v_exp_f32 v108, v108\n" \
    "global_load_dword v218, v203, %[wdb] offset:2048\n" \
    "global_load_dword v219, v203, %[wdb] offset:3072\n" \
    "v_exp_f32 v109, v109\n" \
    "v_exp_f32 v110, v110\n" \
    "v_exp_f32 v111, v111\n" \
    "v_exp_f32 v80, v80\n" \
    "v_exp_f32 v81, v81\n" \
    "v_smfmac_f32_32x32x32_bf16 v[144:159], v[180:183], v[104:111], v192\n" \
    "s_waitcnt vmcnt(12)\n" \
    "ds_read_b64_tr_b16 v[220:221], v228\n" \
    "ds_read_b64_tr_b16 v[222:223], v229\n" \
    "ds_read_b64_tr_b16 v[224:225], v228 offset:256\n" \
    "ds_read_b64_tr_b16 v[226:227], v229 offset:256\n" \
    "ds_read_b128 v[176:179], v194 offset:2048\n" \
    "v_exp_f32 v82, v82\n" \
    "v_exp_f32 v83, v83\n" \
    "v_exp_f32 v84, v84\n" \
    "v_exp_f32 v85, v85\n" \
    "s_waitcnt lgkmcnt(1)\n" \
    "v_mfma_f32_32x32x16_bf16 v[64:79], v[168:171], v[160:163], 0\n" \
    "ds_read_b128 v[180:183], v194 offset:2560\n" \
    "v_exp_f32 v86, v86\n" \
    "v_exp_f32 v87, v87\n" \
    "v_exp_f32 v88, v88\n" \
    "v_exp_f32 v89, v89\n" \
    "v_smfmac_f32_32x32x32_bf16 v[128:143], v[184:187], v[80:87], v192\n" \
    "v_exp_f32 v90, v90\n" \
    "v_exp_f32 v91, v91\n" \
    "v_exp_f32 v92, v92\n" \
    "v_mfma_f32_16x16x32_bf16 v[232:235], v[220:223], v[204:207], 0\n" \
    "v_exp_f32 v93, v93\n" \
    "v_exp_f32 v94, v94\n" \
    "v_exp_f32 v95, v95\n" \
    "v_exp_f32 v112, v112\n" \
    "v_exp_f32 v113, v113\n" \
    "v_smfmac_f32_32x32x32_bf16 v[128:143], v[188:191], v[88:95], v192\n" \
    "v_exp_f32 v114, v114\n" \
    "v_exp_f32 v115, v115\n" \
    "v_exp_f32 v116, v116\n" \
    "v_exp_f32 v117, v117\n" \
    "v_mfma_f32_32x32x16_bf16 v[96:111], v[168:171], v[164:167], 0\n" \
    "ds_read_b128 v[172:175], v193 offset:3072\n" \
    "v_exp_f32 v118, v118\n" \
    "v_exp_f32 v119, v119\n" \
    "v_exp_f32 v120, v120\n" \
    "v_exp_f32 v121, v121\n" \
    "v_smfmac_f32_32x32x32_bf16 v[144:159], v[184:187], v[112:119], v192\n" \
    "v_exp_f32 v122, v122\n" \
    "v_exp_f32 v123, v123\n" \
    "v_exp_f32 v124, v124\n" \
    "v_exp_f32 v125, v125\n" \
    "v_exp_f32 v126, v126\n" \
    "v_exp_f32 v127, v127\n" \
    "s_setprio 0\n" \
    "v_exp_f32 v64, v64\n" \
    "v_exp_f32 v65, v65\n" \
    "v_smfmac_f32_32x32x32_bf16 v[144:159], v[188:191], v[120:127], v192\n" \
    "ds_read_b128 v[184:187], v194 offset:3072\n" \
    "v_exp_f32 v66, v66\n" \
    "v_exp_f32 v67, v67\n" \
    "v_exp_f32 v68, v68\n" \
    "v_exp_f32 v69, v69\n" \
    "s_waitcnt lgkmcnt(1)\n" \
    "v_mfma_f32_32x32x16_bf16 v[80:95], v[172:175], v[160:163], 0\n" \
    "ds_read_b128 v[188:191], v194 offset:3584\n" \
    "v_exp_f32 v70, v70\n" \
    "v_exp_f32 v71, v71\n" \
    "v_exp_f32 v72, v72\n" \
    "v_exp_f32 v73, v73\n" \
    "v_smfmac_f32_32x32x32_bf16 v[128:143], v[176:179], v[64:71], v192\n" \
    "v_exp_f32 v74, v74\n" \
    "v_exp_f32 v75, v75\n" \
    "v_exp_f32 v76, v76\n" \
    "v_mfma_f32_16x16x32_bf16 v[232:235], v[224:227], v[208:211], v[232:235]\n" \
    "v_exp_f32 v77, v77\n" \
    "v_exp_f32 v78, v78\n" \
    "v_exp_f32 v79, v79\n" \
    "v_exp_f32 v96, v96\n" \
    "v_exp_f32 v97, v97\n" \
    "v_smfmac_f32_32x32x32_bf16 v[128:143], v[180:183], v[72:79], v192\n" \
    "v_exp_f32 v98, v98\n" \
    "v_exp_f32 v99, v99\n" \
    "v_exp_f32 v100, v100\n" \
    "v_exp_f32 v101, v101\n" \
    "v_mfma_f32_32x32x16_bf16 v[112:127], v[172:175], v[164:167], 0\n" \
    "ds_read_b128 v[168:171], v193 offset:4096\n" \
    "v_exp_f32 v102, v102\n" \
    "v_exp_f32 v103, v103\n" \
    "v_exp_f32 v104, v104\n" \
    "v_exp_f32 v105, v105\n" \
    "v_smfmac_f32_32x32x32_bf16 v[144:159], v[176:179], v[96:103], v192\n" \
    "v_exp_f32 v106, v106\n" \
    "v_exp_f32 v107, v107\n" \
    "v_exp_f32 v108, v108\n" \
    "v_exp_f32 v109, v109\n" \
    "v_exp_f32 v110, v110\n" \
    "v_exp_f32 v111, v111\n" \
    "v_exp_f32 v80, v80\n" \
    "v_exp_f32 v81, v81\n" \
    "v_smfmac_f32_32x32x32_bf16 v[144:159], v[180:183], v[104:111], v192\n" \
    "ds_write_b128 v230, v[232:235]\n" \
    "ds_read_b128 v[176:179], v194 offset:4096\n" \
    "v_exp_f32 v82, v82\n" \
    "v_exp_f32 v83, v83\n" \
    "v_exp_f32 v84, v84\n" \
    "v_exp_f32 v85, v85\n" \
    "s_waitcnt lgkmcnt(1)\n" \
    "v_mfma_f32_32x32x16_bf16 v[64:79], v[168:171], v[160:163], 0\n" \
    "ds_read_b128 v[180:183], v194 offset:4608\n" \
    "v_exp_f32 v86, v86\n" \
    "v_exp_f32 v87, v87\n" \
    "v_exp_f32 v88, v88\n" \
    "v_exp_f32 v89, v89\n" \
    "v_smfmac_f32_32x32x32_bf16 v[128:143], v[184:187], v[80:87], v192\n" \
    "v_exp_f32 v90, v90\n" \
    "v_exp_f32 v91, v91\n" \
    "v_exp_f32 v92, v92\n" \
    "v_exp_f32 v93, v93\n" \
    "v_exp_f32 v94, v94\n" \
    "v_exp_f32 v95, v95\n" \
    "v_exp_f32 v112, v112\n" \
    "v_exp_f32 v113, v113\n" \
    "v_smfmac_f32_32x32x32_bf16 v[128:143], v[188:191], v[88:95], v192\n" \
    "v_exp_f32 v114, v114\n" \
    "v_exp_f32 v115, v115\n" \
    "v_exp_f32 v116, v116\n" \
    "v_exp_f32 v117, v117\n" \
    "v_mfma_f32_32x32x16_bf16 v[96:111], v[168:171], v[164:167], 0\n" \
    "ds_read_b128 v[172:175], v193 offset:5120\n" \
    "v_exp_f32 v118, v118\n" \
    "v_exp_f32 v119, v119\n" \
    "v_exp_f32 v120, v120\n" \
    "v_exp_f32 v121, v121\n" \
    "v_smfmac_f32_32x32x32_bf16 v[144:159], v[184:187], v[112:119], v192\n" \
    "v_exp_f32 v122, v122\n" \
    "v_exp_f32 v123, v123\n" \
    "v_exp_f32 v124, v124\n" \
    "v_exp_f32 v125, v125\n" \
    "v_exp_f32 v126, v126\n" \
    "v_exp_f32 v127, v127\n" \
    "v_add_u32 v193, 4096, v193\n" \
    "v_add_u32 v194, v195, v194\n" \
    "s_waitcnt vmcnt(0)\n"

#define ATT_C \
    "s_mov_b32 %[cnt], 2\n" \
    "1:\n" \
    "v_exp_f32 v64, v64\n" \
    "v_exp_f32 v65, v65\n" \
    "v_smfmac_f32_32x32x32_bf16 v[144:159], v[188:191], v[120:127], v192\n" \
    "ds_read_b128 v[184:187], v194 offset:1024\n" \
    "v_exp_f32 v66, v66\n" \
    "v_exp_f32 v67, v67\n" \
    "v_exp_f32 v68, v68\n" \
    "v_exp_f32 v69, v69\n" \
    "s_waitcnt lgkmcnt(1)\n" \
    "v_mfma_f32_32x32x16_bf16 v[80:95], v[172:175], v[160:163], 0\n" \
    "ds_read_b128 v[188:191], v194 offset:1536\n" \
    "v_exp_f32 v70, v70\n" \
    "v_exp_f32 v71, v71\n" \
    "v_exp_f32 v72, v72\n" \
    "v_exp_f32 v73, v73\n" \
    "v_smfmac_f32_32x32x32_bf16 v[128:143], v[176:179], v[64:71], v192\n" \
    "v_exp_f32 v74, v74\n" \
    "s_mov_b32 m0, %[swo]\n" \
    "v_exp_f32 v75, v75\n" \
    "v_exp_f32 v76, v76\n" \
    "global_load_lds_dwordx4 v236, %[wob] offset:0\n" \
    "v_exp_f32 v77, v77\n" \
    "v_exp_f32 v78, v78\n" \
    "v_exp_f32 v79, v79\n" \
    "v_exp_f32 v96, v96\n" \
    "v_exp_f32 v97, v97\n" \
    "v_smfmac_f32_32x32x32_bf16 v[128:143], v[180:183], v[72:79], v192\n" \
    "v_exp_f32 v98, v98\n" \
    "v_exp_f32 v99, v99\n" \
    "v_exp_f32 v100, v100\n" \
    "v_exp_f32 v101, v101\n" \
    "v_mfma_f32_32x32x16_bf16 v[112:127], v[172:175], v[164:167], 0\n" \
    "ds_read_b128 v[168:171], v193 offset:2048\n" \
    "v_exp_f32 v102, v102\n" \
    "v_exp_f32 v103, v103\n" \
    "v_exp_f32 v104, v104\n" \
    "v_exp_f32 v105, v105\n" \
    "v_smfmac_f32_32x32x32_bf16 v[144:159], v[176:179], v[96:103], v192\n" \
    "v_exp_f32 v106, v106\n" \
    "v_exp_f32 v107, v107\n" \
    "v_exp_f32 v108, v108\n" \
    "v_exp_f32 v109, v109\n" \
    "v_exp_f32 v110, v110\n" \
    "v_exp_f32 v111, v111\n" \
    "v_exp_f32 v80, v80\n" \
    "v_exp_f32 v81, v81\n" \
    "v_smfmac_f32_32x32x32_bf16 v[144:159], v[180:183], v[104:111], v192\n" \
    "ds_read_b128 v[176:179], v194 offset:2048\n" \
    "v_exp_f32 v82, v82\n" \
    "v_exp_f32 v83, v83\n" \
    "v_exp_f32 v84, v84\n" \
    "v_exp_f32 v85, v85\n" \
    "s_waitcnt lgkmcnt(1)\n" \
    "v_mfma_f32_32x32x16_bf16 v[64:79], v[168:171], v[160:163], 0\n" \
    "ds_read_b128 v[180:183], v194 offset:2560\n" \
    "v_exp_f32 v86, v86\n" \
    "v_exp_f32 v87, v87\n" \
    "v_exp_f32 v88, v88\n" \
    "v_exp_f32 v89, v89\n" \
    "v_smfmac_f32_32x32x32_bf16 v[128:143], v[184:187], v[80:87], v192\n" \
    "v_exp_f32 v90, v90\n" \
    "v_exp_f32 v91, v91\n" \
    "v_exp_f32 v92, v92\n" \
    "global_load_lds_dwordx4 v237, %[wob] offset:1024\n" \
    "v_exp_f32 v93, v93\n" \
    "v_exp_f32 v94, v94\n" \
    "v_exp_f32 v95, v95\n" \
    "v_exp_f32 v112, v112\n" \
    "v_exp_f32 v113, v113\n" \
    "v_smfmac_f32_32x32x32_bf16 v[128:143], v[188:191], v[88:95], v192\n" \
    "v_exp_f32 v114, v114\n" \
    "v_exp_f32 v115, v115\n" \
    "v_exp_f32 v116, v116\n" \
    "v_exp_f32 v117, v117\n" \
    "v_mfma_f32_32x32x16_bf16 v[96:111], v[168:171], v[164:167], 0\n" \
    "ds_read_b128 v[172:175], v193 offset:3072\n" \
    "v_exp_f32 v118, v118\n" \
    "v_exp_f32 v119, v119\n" \
    "v_exp_f32 v120, v120\n" \
    "v_exp_f32 v121, v121\n" \
    "v_smfmac_f32_32x32x32_bf16 v[144:159], v[184:187], v[112:119], v192\n" \
    "v_exp_f32 v122, v122\n" \
    "v_exp_f32 v123, v123\n" \
    "v_exp_f32 v124, v124\n" \
    "v_exp_f32 v125, v125\n" \
    "v_exp_f32 v126, v126\n" \
    "v_exp_f32 v127, v127\n" \
    "v_exp_f32 v64, v64\n" \
    "v_exp_f32 v65, v65\n" \
    "v_smfmac_f32_32x32x32_bf16 v[144:159], v[188:191], v[120:127], v192\n" \
    "ds_read_b128 v[184:187], v194 offset:3072\n" \
    "v_exp_f32 v66, v66\n" \
    "v_exp_f32 v67, v67\n" \
    "v_exp_f32 v68, v68\n" \
    "v_exp_f32 v69, v69\n" \
    "s_waitcnt lgkmcnt(1)\n" \
    "v_mfma_f32_32x32x16_bf16 v[80:95], v[172:175], v[160:163], 0\n" \
    "ds_read_b128 v[188:191], v194 offset:3584\n" \
    "v_exp_f32 v70, v70\n" \
    "v_exp_f32 v71, v71\n" \
    "v_exp_f32 v72, v72\n" \
    "v_exp_f32 v73, v73\n" \
    "v_smfmac_f32_32x32x32_bf16 v[128:143], v[176:179], v[64:71], v192\n" \
    "v_exp_f32 v74, v74\n" \
    "v_exp_f32 v75, v75\n" \
    "v_exp_f32 v76, v76\n" \
    "global_load_lds_dwordx4 v238, %[wob] offset:2048\n" \
    "v_exp_f32 v77, v77\n" \
    "v_exp_f32 v78, v78\n" \
    "v_exp_f32 v79, v79\n" \
    "v_exp_f32 v96, v96\n" \
    "v_exp_f32 v97, v97\n" \
    "v_smfmac_f32_32x32x32_bf16 v[128:143], v[180:183], v[72:79], v192\n" \
    "v_exp_f32 v98, v98\n" \
    "v_exp_f32 v99, v99\n" \
    "v_exp_f32 v100, v100\n" \
    "v_exp_f32 v101, v101\n" \
    "v_mfma_f32_32x32x16_bf16 v[112:127], v[172:175], v[164:167], 0\n" \
    "ds_read_b128 v[168:171], v193 offset:4096\n" \
    "v_exp_f32 v102, v102\n" \
    "v_exp_f32 v103, v103\n" \
    "v_exp_f32 v104, v104\n" \
    "v_exp_f32 v105, v105\n" \
    "v_smfmac_f32_32x32x32_bf16 v[144:159], v[176:179], v[96:103], v192\n" \
    "v_exp_f32 v106, v106\n" \
    "v_exp_f32 v107, v107\n" \
    "v_exp_f32 v108, v108\n" \
    "v_exp_f32 v109, v109\n" \
    "v_exp_f32 v110, v110\n" \
    "v_exp_f32 v111, v111\n" \
    "v_exp_f32 v80, v80\n" \
    "v_exp_f32 v81, v81\n" \
    "v_smfmac_f32_32x32x32_bf16 v[144:159], v[180:183], v[104:111], v192\n" \
    "ds_read_b128 v[176:179], v194 offset:4096\n" \
    "v_exp_f32 v82, v82\n" \
    "v_exp_f32 v83, v83\n" \
    "v_exp_f32 v84, v84\n" \
    "v_exp_f32 v85, v85\n" \
    "s_waitcnt lgkmcnt(1)\n" \
    "v_mfma_f32_32x32x16_bf16 v[64:79], v[168:171], v[160:163], 0\n" \
    "ds_read_b128 v[180:183], v194 offset:4608\n" \
    "v_exp_f32 v86, v86\n" \
    "v_exp_f32 v87, v87\n" \
    "v_exp_f32 v88, v88\n" \
    "v_exp_f32 v89, v89\n" \
    "v_smfmac_f32_32x32x32_bf16 v[128:143], v[184:187], v[80:87], v192\n" \
    "v_exp_f32 v90, v90\n" \
    "v_exp_f32 v91, v91\n" \
    "v_exp_f32 v92, v92\n" \
    "global_load_lds_dwordx4 v239, %[wob] offset:3072\n" \
    "v_exp_f32 v93, v93\n" \
    "v_exp_f32 v94, v94\n" \
    "v_exp_f32 v95, v95\n" \
    "v_exp_f32 v112, v112\n" \
    "v_exp_f32 v113, v113\n" \
    "v_smfmac_f32_32x32x32_bf16 v[128:143], v[188:191], v[88:95], v192\n" \
    "v_exp_f32 v114, v114\n" \
    "v_exp_f32 v115, v115\n" \
    "v_exp_f32 v116, v116\n" \
    "v_exp_f32 v117, v117\n" \
    "v_mfma_f32_32x32x16_bf16 v[96:111], v[168:171], v[164:167], 0\n" \
    "ds_read_b128 v[172:175], v193 offset:5120\n" \
    "v_exp_f32 v118, v118\n" \
    "v_exp_f32 v119, v119\n" \
    "v_exp_f32 v120, v120\n" \
    "v_exp_f32 v121, v121\n" \
    "v_smfmac_f32_32x32x32_bf16 v[144:159], v[184:187], v[112:119], v192\n" \
    "v_exp_f32 v122, v122\n" \
    "v_exp_f32 v123, v123\n" \
    "v_exp_f32 v124, v124\n" \
    "v_exp_f32 v125, v125\n" \
    "v_exp_f32 v126, v126\n" \
    "v_exp_f32 v127, v127\n" \
    "v_add_u32 v236, 0x8000, v236\n" \
    "v_add_u32 v237, 0x8000, v237\n" \
    "v_add_u32 v238, 0x8000, v238\n" \
    "v_add_u32 v239, 0x8000, v239\n" \
    "s_add_u32 %[swo], %[swo], 0x1000\n" \
    "v_add_u32 v193, 4096, v193\n" \
    "v_add_u32 v194, v195, v194\n" \
    "s_sub_u32 %[cnt], %[cnt], 1\n" \
    "s_cmp_lg_u32 %[cnt], 0\n" \
    "s_cbranch_scc1 1b\n" \
    "s_mov_b32 %[cnt], 2\n" \
    "2:\n" \
    "v_exp_f32 v64, v64\n" \
    "v_exp_f32 v65, v65\n" \
    "v_smfmac_f32_32x32x32_bf16 v[144:159], v[188:191], v[120:127], v192\n" \
    "ds_read_b128 v[184:187], v194 offset:1024\n" \
    "v_exp_f32 v66, v66\n" \
    "v_exp_f32 v67, v67\n" \
    "v_exp_f32 v68, v68\n" \
    "v_exp_f32 v69, v69\n" \
    "s_waitcnt lgkmcnt(1)\n" \
    "v_mfma_f32_32x32x16_bf16 v[80:95], v[172:175], v[160:163], 0\n" \
    "ds_read_b128 v[188:191], v194 offset:1536\n" \
    "v_exp_f32 v70, v70\n" \
    "v_exp_f32 v71, v71\n" \
    "v_exp_f32 v72, v72\n" \
    "v_exp_f32 v73, v73\n" \
    "v_smfmac_f32_32x32x32_bf16 v[128:143], v[176:179], v[64:71], v192\n" \
    "v_exp_f32 v74, v74\n" \
    "v_exp_f32 v75, v75\n" \
    "v_exp_f32 v76, v76\n" \
    "v_exp_f32 v77, v77\n" \
    "v_exp_f32 v78, v78\n" \
    "v_exp_f32 v79, v79\n" \
    "v_exp_f32 v96, v96\n" \
    "v_exp_f32 v97, v97\n" \
    "v_smfmac_f32_32x32x32_bf16 v[128:143], v[180:183], v[72:79], v192\n" \
    "v_exp_f32 v98, v98\n" \
    "v_exp_f32 v99, v99\n" \
    "v_exp_f32 v100, v100\n" \
    "v_exp_f32 v101, v101\n" \
    "v_mfma_f32_32x32x16_bf16 v[112:127], v[172:175], v[164:167], 0\n" \
    "ds_read_b128 v[168:171], v193 offset:2048\n" \
    "v_exp_f32 v102, v102\n" \
    "v_exp_f32 v103, v103\n" \
    "v_exp_f32 v104, v104\n" \
    "v_exp_f32 v105, v105\n" \
    "v_smfmac_f32_32x32x32_bf16 v[144:159], v[176:179], v[96:103], v192\n" \
    "v_exp_f32 v106, v106\n" \
    "v_exp_f32 v107, v107\n" \
    "v_exp_f32 v108, v108\n" \
    "v_exp_f32 v109, v109\n" \
    "v_exp_f32 v110, v110\n" \
    "v_exp_f32 v111, v111\n" \
    "v_exp_f32 v80, v80\n" \
    "v_exp_f32 v81, v81\n" \
    "v_smfmac_f32_32x32x32_bf16 v[144:159], v[180:183], v[104:111], v192\n" \
    "ds_read_b128 v[176:179], v194 offset:2048\n" \
    "v_exp_f32 v82, v82\n" \
    "v_exp_f32 v83, v83\n" \
    "v_exp_f32 v84, v84\n" \
    "v_exp_f32 v85, v85\n" \
    "s_waitcnt lgkmcnt(1)\n" \
    "v_mfma_f32_32x32x16_bf16 v[64:79], v[168:171], v[160:163], 0\n" \
    "ds_read_b128 v[180:183], v194 offset:2560\n" \
    "v_exp_f32 v86, v86\n" \
    "v_exp_f32 v87, v87\n" \
    "v_exp_f32 v88, v88\n" \
    "v_exp_f32 v89, v89\n" \
    "v_smfmac_f32_32x32x32_bf16 v[128:143], v[184:187], v[80:87], v192\n" \
    "v_exp_f32 v90, v90\n" \
    "v_exp_f32 v91, v91\n" \
    "v_exp_f32 v92, v92\n" \
    "v_exp_f32 v93, v93\n" \
    "v_exp_f32 v94, v94\n" \
    "v_exp_f32 v95, v95\n" \
    "v_exp_f32 v112, v112\n" \
    "v_exp_f32 v113, v113\n" \
    "v_smfmac_f32_32x32x32_bf16 v[128:143], v[188:191], v[88:95], v192\n" \
    "v_exp_f32 v114, v114\n" \
    "v_exp_f32 v115, v115\n" \
    "v_exp_f32 v116, v116\n" \
    "v_exp_f32 v117, v117\n" \
    "v_mfma_f32_32x32x16_bf16 v[96:111], v[168:171], v[164:167], 0\n" \
    "ds_read_b128 v[172:175], v193 offset:3072\n" \
    "v_exp_f32 v118, v118\n" \
    "v_exp_f32 v119, v119\n" \
    "v_exp_f32 v120, v120\n" \
    "v_exp_f32 v121, v121\n" \
    "v_smfmac_f32_32x32x32_bf16 v[144:159], v[184:187], v[112:119], v192\n" \
    "v_exp_f32 v122, v122\n" \
    "v_exp_f32 v123, v123\n" \
    "v_exp_f32 v124, v124\n" \
    "v_exp_f32 v125, v125\n" \
    "v_exp_f32 v126, v126\n" \
    "v_exp_f32 v127, v127\n" \
    "v_exp_f32 v64, v64\n" \
    "v_exp_f32 v65, v65\n" \
    "v_smfmac_f32_32x32x32_bf16 v[144:159], v[188:191], v[120:127], v192\n" \
    "ds_read_b128 v[184:187], v194 offset:3072\n" \
    "v_exp_f32 v66, v66\n" \
    "v_exp_f32 v67, v67\n" \
    "v_exp_f32 v68, v68\n" \
    "v_exp_f32 v69, v69\n" \
    "s_waitcnt lgkmcnt(1)\n" \
    "v_mfma_f32_32x32x16_bf16 v[80:95], v[172:175], v[160:163], 0\n" \
    "ds_read_b128 v[188:191], v194 offset:3584\n" \
    "v_exp_f32 v70, v70\n" \
    "v_exp_f32 v71, v71\n" \
    "v_exp_f32 v72, v72\n" \
    "v_exp_f32 v73, v73\n" \
    "v_smfmac_f32_32x32x32_bf16 v[128:143], v[176:179], v[64:71], v192\n" \
    "v_exp_f32 v74, v74\n" \
    "v_exp_f32 v75, v75\n" \
    "v_exp_f32 v76, v76\n" \
    "v_exp_f32 v77, v77\n" \
    "v_exp_f32 v78, v78\n" \
    "v_exp_f32 v79, v79\n" \
    "v_exp_f32 v96, v96\n" \
    "v_exp_f32 v97, v97\n" \
    "v_smfmac_f32_32x32x32_bf16 v[128:143], v[180:183], v[72:79], v192\n" \
    "v_exp_f32 v98, v98\n" \
    "v_exp_f32 v99, v99\n" \
    "v_exp_f32 v100, v100\n" \
    "v_exp_f32 v101, v101\n" \
    "v_mfma_f32_32x32x16_bf16 v[112:127], v[172:175], v[164:167], 0\n" \
    "ds_read_b128 v[168:171], v193 offset:4096\n" \
    "v_exp_f32 v102, v102\n" \
    "v_exp_f32 v103, v103\n" \
    "v_exp_f32 v104, v104\n" \
    "v_exp_f32 v105, v105\n" \
    "v_smfmac_f32_32x32x32_bf16 v[144:159], v[176:179], v[96:103], v192\n" \
    "v_exp_f32 v106, v106\n" \
    "v_exp_f32 v107, v107\n" \
    "v_exp_f32 v108, v108\n" \
    "v_exp_f32 v109, v109\n" \
    "v_exp_f32 v110, v110\n" \
    "v_exp_f32 v111, v111\n" \
    "v_exp_f32 v80, v80\n" \
    "v_exp_f32 v81, v81\n" \
    "v_smfmac_f32_32x32x32_bf16 v[144:159], v[180:183], v[104:111], v192\n" \
    "ds_read_b128 v[176:179], v194 offset:4096\n" \
    "v_exp_f32 v82, v82\n" \
    "v_exp_f32 v83, v83\n" \
    "v_exp_f32 v84, v84\n" \
    "v_exp_f32 v85, v85\n" \
    "s_waitcnt lgkmcnt(1)\n" \
    "v_mfma_f32_32x32x16_bf16 v[64:79], v[168:171], v[160:163], 0\n" \
    "ds_read_b128 v[180:183], v194 offset:4608\n" \
    "v_exp_f32 v86, v86\n" \
    "v_exp_f32 v87, v87\n" \
    "v_exp_f32 v88, v88\n" \
    "v_exp_f32 v89, v89\n" \
    "v_smfmac_f32_32x32x32_bf16 v[128:143], v[184:187], v[80:87], v192\n" \
    "v_exp_f32 v90, v90\n" \
    "v_exp_f32 v91, v91\n" \
    "v_exp_f32 v92, v92\n" \
    "v_exp_f32 v93, v93\n" \
    "v_exp_f32 v94, v94\n" \
    "v_exp_f32 v95, v95\n" \
    "v_exp_f32 v112, v112\n" \
    "v_exp_f32 v113, v113\n" \
    "v_smfmac_f32_32x32x32_bf16 v[128:143], v[188:191], v[88:95], v192\n" \
    "v_exp_f32 v114, v114\n" \
    "v_exp_f32 v115, v115\n" \
    "v_exp_f32 v116, v116\n" \
    "v_exp_f32 v117, v117\n" \
    "v_mfma_f32_32x32x16_bf16 v[96:111], v[168:171], v[164:167], 0\n" \
    "ds_read_b128 v[172:175], v193 offset:5120\n" \
    "v_exp_f32 v118, v118\n" \
    "v_exp_f32 v119, v119\n" \
    "v_exp_f32 v120, v120\n" \
    "v_exp_f32 v121, v121\n" \
    "v_smfmac_f32_32x32x32_bf16 v[144:159], v[184:187], v[112:119], v192\n" \
    "v_exp_f32 v122, v122\n" \
    "v_exp_f32 v123, v123\n" \
    "v_exp_f32 v124, v124\n" \
    "v_exp_f32 v125, v125\n" \
    "v_exp_f32 v126, v126\n" \
    "v_exp_f32 v127, v127\n" \
    "s_cmp_eq_u32 %[cnt], 2\n" \
    "s_cbranch_scc0 8f\n" \
    "s_setprio 0\n" \
    "8:\n" \
    "v_add_u32 v193, 4096, v193\n" \
    "v_add_u32 v194, v195, v194\n" \
    "s_sub_u32 %[cnt], %[cnt], 1\n" \
    "s_cmp_lg_u32 %[cnt], 0\n" \
    "s_cbranch_scc1 2b\n" \
    "s_nop 1\n" \
    "v_smfmac_f32_32x32x32_bf16 v[144:159], v[188:191], v[120:127], v192\n" \
    "s_setprio 0\n" \
    "s_waitcnt vmcnt(0)\n" \
    "s_nop 7\n" \
    "s_nop 7\n" \
    "s_nop 3\n" \
    "s_waitcnt lgkmcnt(0)\n"

__device__ __forceinline__ void slow_block(const char* ws, u32x4 q, int lane, float* o, float& lsum) {
    const u32 koff = WS_K + lane * 16;
    const u32 r32 = lane & 31;
    float m = -3.0e38f;
    for (int t = 0; t < 64; ++t) {
        u32x4 kf;
        GLD128C(kf, ws, koff + t * 1024, 0);
        asm volatile("s_waitcnt vmcnt(0)" : "+v"(kf) :: "memory");
        f32x16 s = {0};
        s = __builtin_amdgcn_mfma_f32_32x32x16_bf16(asbf(kf), asbf(q), s, 0, 0, 0);
#pragma unroll
        for (int i = 0; i < 16; ++i) m = fmaxf(m, s[i]);
    }
    {
        auto sw = __builtin_amdgcn_permlane32_swap(__float_as_uint(m), __float_as_uint(m), false, false);
        m = fmaxf(__uint_as_float(sw[0]), __uint_as_float(sw[1]));
    }
    f32x16 acc = {0};
    const u32 voff = WS_V + (lane >> 5) * 256 + (lane & 15) * 16;
    for (int t = 0; t < 64; ++t) {
        u32x4 kf, v0, v1;
        GLD128C(kf, ws, koff + t * 1024, 0);
        GLD128C(v0, ws, voff + t * 1024, 0);
        GLD128C(v1, ws, voff + t * 1024, 512);
        asm volatile("s_waitcnt vmcnt(0)" : "+v"(kf), "+v"(v0), "+v"(v1) :: "memory");
        if (r32 >= 16) { v0 = (u32x4){0x3f803f80u, 0x3f803f80u, 0x3f803f80u, 0x3f803f80u}; v1 = v0; }
        f32x16 s = {0};
        s = __builtin_amdgcn_mfma_f32_32x32x16_bf16(asbf(kf), asbf(q), s, 0, 0, 0);
        u32 p[8];
#pragma unroll
        for (int i = 0; i < 8; ++i)
            p[i] = pk(__builtin_amdgcn_exp2f(s[2 * i] - m), __builtin_amdgcn_exp2f(s[2 * i + 1] - m));
#pragma unroll
        for (int st = 0; st < 2; ++st) {
            u32x4 vv = st ? v1 : v0;
            auto a = __builtin_amdgcn_permlane32_swap(vv[0], vv[2], false, false);
            auto c = __builtin_amdgcn_permlane32_swap(vv[1], vv[3], false, false);
            u32x4 af = {a[0], c[0], a[1], c[1]};
            u32x4 bf = {p[4 * st], p[4 * st + 1], p[4 * st + 2], p[4 * st + 3]};
            acc = __builtin_amdgcn_mfma_f32_32x32x16_bf16(asbf(af), asbf(bf), acc, 0, 0, 0);
        }
    }
#pragma unroll
    for (int i = 0; i < 8; ++i) o[i] = acc[i];
    lsum = acc[8];
}

#define PRIO_MODE 1
#define PRIO_START() do { if (w >= 4) __builtin_amdgcn_s_setprio(1); } while (0)
#define PRIO_END() __builtin_amdgcn_s_setprio(0)
__global__ void __launch_bounds__(512, 2)
fused_kernel(const u16* x_, const u16* wq_, const u16* wk_, const u16* wv_, const u16* wo_, const u16* wr_, const u16* wg_,
             const u16* wu_, const u16* wd_, const u16* gw_, u16* out_, char* ws_)
{
    u32x16 ka0; u32x8 ka1;
    {
        const void* kp = (const void*)__builtin_amdgcn_kernarg_segment_ptr();
        asm volatile("s_load_dwordx16 %0, %2, 0\n\ts_load_dwordx8 %1, %2, 64\n\ts_waitcnt lgkmcnt(0)"
                     : "=&s"(ka0), "=&s"(ka1) : "s"(kp) : "memory");
    }
#define KP(v, i) ((u64)v[2 * (i)] | ((u64)v[2 * (i) + 1] << 32))
    const u16* x  = (const u16*)KP(ka0, 0); const u16* wq = (const u16*)KP(ka0, 1);
    const u16* wk = (const u16*)KP(ka0, 2); const u16* wv = (const u16*)KP(ka0, 3);
    const u16* wo = (const u16*)KP(ka0, 4); const u16* wr = (const u16*)KP(ka0, 5);
    const u16* wg = (const u16*)KP(ka0, 6); const u16* wu = (const u16*)KP(ka0, 7);
    const u16* wd = (const u16*)KP(ka1, 0); const u16* gw = (const u16*)KP(ka1, 1);
    u16* out = (u16*)KP(ka1, 2); char* ws = (char*)KP(ka1, 3);

    __shared__ __attribute__((aligned(1024))) char smem[L_END];
    LDSP char* lds = (LDSP char*)smem;
    const u32 lb = (u32)(size_t)lds;
    const int tid = threadIdx.x;
    const int lane = tid & 63;
    const int w = __builtin_amdgcn_readfirstlane(tid >> 6);
    const int b = blockIdx.x;
    const int xcd = b & 7, jj = b >> 3;
    const int tb = xcd * 4 + (jj >> 3), hg = jj & 7;
    const int g = lane >> 4, l15 = lane & 15, h = lane >> 5, l31 = lane & 31;
    const bool isK = hg < 4;
    const bool ffn = jj < 28;
    const int fj = xcd * 28 + min(jj, 27);
    STAMP(1); STAMPC(18);

    const int th = w & 1, hp = (w >> 1) & 1, blk = w >> 2;
    const int T0 = tb * 64 + (hg & 3) * 16;
    u32x4 xf0, xf1, vf0, vf1;
    {
        const u16* wsrc = isK ? wk : wv;
#pragma unroll
        for (int i = 0; i < 2; ++i) {
            u32 slot = 64 * i + lane, pos = slot >> 3, B = pos ^ ((pos >> 2) & 1);
            u32 row = 4 * B + ((slot >> 1) & 3);
            dma16(wsrc, (64 * w + row) * 32 + (slot & 1) * 16, lb + L_W + w * 2048 + i * 1024);
        }
        u32 xo = (T0 + l15) * 1024 + (64 * w + 16 * g) * 2;
        GLD128(xf0, x, xo, 0);
        GLD128(xf1, x, xo, 16);
        const u16* vec = isK ? wr : gw;
        u32 vo = (64 * w + 16 * g) * 2;
        GLD128(vf0, vec, vo, 0);
        GLD128(vf1, vec, vo, 16);
    }
    u32 epo;
    { u32 z = 0; GLD32C(epo, ws, z, 0); }
    asm volatile("s_barrier" ::: "memory");
    if (w < 7) {
#pragma unroll
        for (int i = 0; i < 19; ++i) {
            const int p = i < 18 ? w + 7 * i : __builtin_amdgcn_readfirstlane(min(w + 7 * i, 127));
            if (i < 9 || (i == 9 && w == 0)) {
                dma16(x, (tb * 64 + p) * 1024 + ((lane ^ (p & 15)) << 4), lb + L_K + p * 1024);
            } else {
                const int G = p - 64;
                dma16(wq, (8 * G + ((lane >> 2) & 7)) * 1024 + hg * 128 + (lane >> 5) * 64 + (lane & 3) * 16, lb + L_V + G * 1024);
            }
        }
    } else {
        const u32x4 pz = {POISON, POISON, POISON, POISON};
#pragma unroll
        for (int i = 0; i < 8; ++i) {
            u32 po = WS_O + ((tb * 4 + (i >> 1)) * 16 + hg * 2 + (i & 1)) * 1024 + lane * 16;
            GST128C(ws, po, pz);
        }
        u32 yo = (ffn ? WS_Y + fj * 2048 : WS_X + b * 2048) + lane * 16;
        GST128C(ws, yo, pz);
        yo += 1024;
        GST128C(ws, yo, pz);
    }
    if (tid < 256) { u32 o1 = (tid & 15) == 0 ? 0x3f803f80u : 0u; ST128(L_ONE + tid * 16, ((u32x4){o1, o1, o1, o1})); }
    asm volatile("s_cmp_eq_u32 %[w], 7\n\ts_cbranch_scc1 1f\n\ts_waitcnt vmcnt(20)\n\ts_branch 2f\n1:\n\ts_waitcnt vmcnt(11)\n2:"
                 : "+v"(xf0), "+v"(xf1), "+v"(vf0), "+v"(vf1) : [w] "s"(w) : "memory", "scc");
    FSTAMP(2);
    {
        const u32 ta = L_W + w * 2048 + l15 * 8;
        const u32 gx = g & 1;
        u32x4 wf0 = cat(trread(lds, ta + ((4 * g + 0) ^ gx) * 128), trread(lds, ta + ((4 * g + 1) ^ gx) * 128));
        u32x4 wf1 = cat(trread(lds, ta + ((4 * g + 2) ^ gx) * 128), trread(lds, ta + ((4 * g + 3) ^ gx) * 128));
        f32x4 acc = {0, 0, 0, 0}, dacc = {0, 0, 0, 0};
        if (isK) {
            acc = __builtin_amdgcn_mfma_f32_16x16x32_bf16(asbf(wf0), asbf(xf0), acc, 0, 0, 0);
            acc = __builtin_amdgcn_mfma_f32_16x16x32_bf16(asbf(wf1), asbf(xf1), acc, 0, 0, 0);
        } else {
            acc = __builtin_amdgcn_mfma_f32_16x16x32_bf16(asbf(xf0), asbf(wf0), acc, 0, 0, 0);
            acc = __builtin_amdgcn_mfma_f32_16x16x32_bf16(asbf(xf1), asbf(wf1), acc, 0, 0, 0);
        }
        dacc = __builtin_amdgcn_mfma_f32_16x16x32_bf16(asbf(vf0), asbf(xf0), dacc, 0, 0, 0);
        dacc = __builtin_amdgcn_mfma_f32_16x16x32_bf16(asbf(vf1), asbf(xf1), dacc, 0, 0, 0);
        ST128(L_P1 + w * 1024 + lane * 16, __builtin_bit_cast(u32x4, acc));
        if (g == 0) STF(L_PD + w * 64 + l15 * 4, dacc[0]);
    }
    BAR();
    FSTAMP(3);
    asm volatile("s_cmp_eq_u32 %[w], 7\n\ts_cbranch_scc1 1f\n\ts_waitcnt vmcnt(19)\n\ts_branch 2f\n1:\n\ts_waitcnt vmcnt(10)\n2:"
                 : "+v"(epo) : [w] "s"(w) : "memory", "scc");
    const u32 epoch = (u32)__builtin_amdgcn_readfirstlane((int)epo) + 1u;
    if (w == 7) {
        f32x4 ps[8]; float pd[8];
#pragma unroll
        for (int i = 0; i < 8; ++i) {
            ps[i] = __builtin_bit_cast(f32x4, LD128(L_P1 + i * 1024 + lane * 16));
            pd[i] = LDF(L_PD + i * 64 + l15 * 4);
        }
        __builtin_amdgcn_sched_barrier(0);
        const f32x4 s = ((ps[0] + ps[1]) + (ps[2] + ps[3])) + ((ps[4] + ps[5]) + (ps[6] + ps[7]));
        const float d = ((pd[0] + pd[1]) + (pd[2] + pd[3])) + ((pd[4] + pd[5]) + (pd[6] + pd[7]));
        u32x2 pv = {pk(s[0], s[1]), pk(s[2], s[3])};
        const int tile = T0 >> 5, sub = (T0 >> 4) & 1;
        u32 key = 0;
        if (isK) {
            u32 off = WS_K + tile * 1024 + ((g & 1) * 32 + sub * 16 + l15) * 16 + (g >> 1) * 8;
            GST64C(ws, off, pv);
            u32 rb = pk(d, d) & 0xffffu;
            u32 ord = (rb & 0x8000u) ? (~rb & 0xffffu) : (rb | 0x8000u);
            key = wave_max_u32((ord << 16) | (0xffffu - (u32)(T0 + l15)));
        } else {
            u32 off = WS_V + tile * 1024 + sub * 512 + (g >> 1) * 256 + l15 * 16 + (g & 1) * 8;
            GST64C(ws, off, pv);
            if (g == 0) { u32 go = WS_G + (T0 + l15) * 4; u32 dv = __float_as_uint(d); GST32C(ws, go, dv); }
        }
        VMWAIT(0);
        if (lane < 8) { u32x2 fl = {key, epoch}; u32 fo = WS_F1 + lane * 2048 + (tile * 4 + (isK ? 0 : 2) + sub) * 8; GST64C(ws, fo, fl); }
        FSTAMP(4);
    } else {
        VMWAIT(0);
    }
    BAR();
    FSTAMP(5);

    f32x16 qacc = {0};
    if (w < 4) {
        const int qh = w >> 1;
        const int tok = th * 32 + l31;
        u32 abase = L_V + h * 1024 + qh * 512 + ((l15 >> 2) * 64) + ((lane >> 4) & 1) * 32 + (l15 & 3) * 8;
        asm volatile("" : "+v"(abase));
        u32 bb[8];
#pragma unroll
        for (int j = 0; j < 8; ++j) { bb[j] = L_K + tok * 1024 + (((2 * j + h) ^ (tok & 15)) << 4); asm volatile("" : "+v"(bb[j])); }
#pragma unroll
        for (int s = 0; s < 32; ++s) {
            u32x4 af = cat(trread(lds, abase + s * 2048), trread(lds, abase + s * 2048 + 256));
            u32x4 bf = LD128(bb[s & 7] + (s >> 3) * 256);
            qacc = __builtin_amdgcn_mfma_f32_32x32x16_bf16(asbf(af), asbf(bf), qacc, 0, 0, 0);
        }
        FSTAMP(6);
    } else {
        const int j = w - 4;
        const u32 fo = WS_F1 + xcd * 2048 + (j * 64 + l31 * 2) * 8;
        u32x4 f;
        u32 spins = 0;
        while (true) {
            asm volatile("global_load_dwordx4 %0, %1, %2 sc1\n\ts_waitcnt vmcnt(0)" : "=&v"(f) : "v"(fo), "s"(ws) : "memory");
            if (__all((f[1] == epoch) & (f[3] == epoch)) || ++spins > SPIN_MAX) break;
        }
        const u32 key = wave_max_u32(max(f[0], f[2]));
        if (lane == 0) ST32(L_CT + j * 4, key);
        FSTAMP(7);
    }
    BAR();
    FSTAMP(9);
    u32 kvo = (w & 1) * 65536 + ((w >> 1) & 1) * 32768;
    if (w >= 4) {
        dma16c4(ws + WS_K, kvo + lane * 16, lb + L_K + kvo);
        dma16c4(ws + WS_K, kvo + 4096 + lane * 16, lb + L_K + kvo + 4096);
        dma16c2(ws + WS_K, kvo + 8192 + lane * 16, lb + L_K + kvo + 8192);
    } else {
        const float qs = 0.25f * 1.4426950408889634f;
        u32x4 qx, qy;
#pragma unroll
        for (int i = 0; i < 4; ++i) {
            u32 a = pk(qacc[2 * i], qacc[2 * i + 1]), c = pk(qacc[8 + 2 * i], qacc[8 + 2 * i + 1]);
            qx[i] = pk(lo16(a) * qs, hi16(a) * qs);
            qy[i] = pk(lo16(c) * qs, hi16(c) * qs);
        }
        ST128(L_P1 + w * 2048 + lane * 16, qx);
        ST128(L_P1 + w * 2048 + 1024 + lane * 16, qy);
    }
    const u32 key = max(max(LD32(L_CT), LD32(L_CT + 4)), max(LD32(L_CT + 8), LD32(L_CT + 12)));
    const int sel = __builtin_amdgcn_readfirstlane(0xffff - (key & 0xffff)) & 2047;
    u32x8 EXA; u32x4 EXB, WOA;
    u32 skv = lb + L_K + kvo + (10 + 4 * (w >> 2)) * 1024, sg = lb + L_W + w * 2048;
    const u32 skv3 = lb + L_K + kvo + (26 + 2 * (w >> 2)) * 1024;
    u32 swo = lb + ((w & 4) ? L_V : L_K) + ((w >> 1) & 1) * 32768 + (w & 1) * 8192;
    {
        EXA[0] = kvo + (10 + 4 * (w >> 2)) * 1024 + lane * 16;
        EXA[1] = kvo + (26 + 2 * (w >> 2)) * 1024 + lane * 16;
        const u32 pos = lane >> 3, B = pos ^ ((pos >> 2) & 1);
        const u32 row = 64 * w + 4 * B + ((lane >> 1) & 3);
        const u64 g0 = (u64)((lane & 1) ? wu : wg) + (u64)(row * 3584 + fj * 16), g1 = g0 + 32 * 3584;
        EXA[2] = (u32)g0; EXA[3] = (u32)(g0 >> 32); EXA[4] = (u32)g1; EXA[5] = (u32)(g1 >> 32);
        EXA[6] = sel * 1024 + (64 * w + 16 * g) * 2;
        EXA[7] = fj * 8192 + (128 * (w & 3) + 2 * lane) * 2;
        const u32 ta = lb + L_W + w * 2048 + l15 * 8, gx = g & 1;
        EXB[0] = ta + (4 * g + gx) * 128;
        EXB[1] = ta + (4 * g + (gx ^ 1)) * 128;
        EXB[2] = lb + (l15 == 0 ? L_FP + w * 64 + g * 16 : L_P1 + w * 1024 + lane * 16);
        EXB[3] = 0;
#pragma unroll
        for (int i = 0; i < 4; ++i) {
            const u32 r = 64 * w + 8 * i + (lane >> 3), slot = lane & 7;
            const u32 f = ((r >> 1) & 1) | ((i & 1) << 1);
            const u32 c = (((slot >> 1) ^ f) << 1) | (slot & 1);
            WOA[i] = r * 1024 + hg * 128 + c * 16 - i * 1024;
        }
    }
    const int hf = w >> 2;
    u32 ka = lb + L_K + hf * 32768 + lane * 16;
    const bool vlane = l31 < 16;
    u32 va = vlane ? (lb + L_V + hf * 32768 + h * 256 + l15 * 16) : (lb + L_ONE - 1024 + h * 256 + l15 * 16);
    u32 vinc = vlane ? 4096u : 0u;
    u32 va0 = vlane ? va : va + 1024;
    u32 qa = lb + L_P1 + (w & 3) * 2048 + lane * 16;
    asm volatile("" : "+{v[196:203]}"(EXA), "+{v[228:231]}"(EXB), "+{v[236:239]}"(WOA), "+{v193}"(ka), "+{v194}"(va), "+{v195}"(vinc), "+v"(va0), "+v"(qa),
                 "+s"(skv), "+s"(sg), "+s"(swo));
    if (w >= 4) VMWAIT(0);
    BAR();
    STAMP(10); STAMPC(19);
    if (b == 0 && tid == 256) { u32 z = 0; GST32C(ws, z, epoch); }

    u32x4 qx, qy;
    f32x16 SX0, SX1, SY0, SY1, OX, OY;
    u32x4 K0, K1, V00, V01, V10, V11;
    u32 idx = 0xDDDDu;
    {
        asm volatile(
            "ds_read_b128 v[160:163], %[qa]\n"
            "ds_read_b128 v[164:167], %[qa] offset:1024\n"
            "ds_read_b128 v[168:171], v193\n"
            "ds_read_b128 v[172:175], v193 offset:1024\n"
            "ds_read_b128 v[176:179], %[va0]\n"
            "ds_read_b128 v[180:183], %[va0] offset:512\n"
            "v_mov_b32 v128, 0\n v_mov_b32 v129, 0\n v_mov_b32 v130, 0\n v_mov_b32 v131, 0\n"
            "v_mov_b32 v132, 0\n v_mov_b32 v133, 0\n v_mov_b32 v134, 0\n v_mov_b32 v135, 0\n"
            "v_mov_b32 v136, 0\n v_mov_b32 v137, 0\n v_mov_b32 v138, 0\n v_mov_b32 v139, 0\n"
            "v_mov_b32 v140, 0\n v_mov_b32 v141, 0\n v_mov_b32 v142, 0\n v_mov_b32 v143, 0\n"
            "v_mov_b32 v144, 0\n v_mov_b32 v145, 0\n v_mov_b32 v146, 0\n v_mov_b32 v147, 0\n"
            "v_mov_b32 v148, 0\n v_mov_b32 v149, 0\n v_mov_b32 v150, 0\n v_mov_b32 v151, 0\n"
            "v_mov_b32 v152, 0\n v_mov_b32 v153, 0\n v_mov_b32 v154, 0\n v_mov_b32 v155, 0\n"
            "v_mov_b32 v156, 0\n v_mov_b32 v157, 0\n v_mov_b32 v158, 0\n v_mov_b32 v159, 0\n"
            "v_mov_b32 v120, 0\n v_mov_b32 v121, 0\n v_mov_b32 v122, 0\n v_mov_b32 v123, 0\n"
            "v_mov_b32 v124, 0\n v_mov_b32 v125, 0\n v_mov_b32 v126, 0\n v_mov_b32 v127, 0\n"
            "v_mov_b32 v188, 0\n v_mov_b32 v189, 0\n v_mov_b32 v190, 0\n v_mov_b32 v191, 0\n"
            "s_waitcnt lgkmcnt(0)\n"
            "v_mfma_f32_32x32x16_bf16 v[64:79], v[168:171], v[160:163], 0\n"
            "v_mfma_f32_32x32x16_bf16 v[96:111], v[168:171], v[164:167], 0\n"
            "s_nop 7\n s_nop 7\n"
            : "={v[64:79]}"(SX0), "={v[80:95]}"(SX1), "={v[96:111]}"(SY0), "={v[112:127]}"(SY1),
              "={v[128:143]}"(OX), "={v[144:159]}"(OY),
              "={v[168:171]}"(K0), "={v[172:175]}"(K1), "={v[176:179]}"(V00), "={v[180:183]}"(V01),
              "={v[184:187]}"(V10), "={v[188:191]}"(V11), "=&{v[160:163]}"(qx), "=&{v[164:167]}"(qy)
            : "{v193}"(ka), [va0] "v"(va0), [qa] "v"(qa)
            : "memory");
    }
#define ATT_OPS \
            : "+{v[64:79]}"(SX0), "+{v[80:95]}"(SX1), "+{v[96:111]}"(SY0), "+{v[112:127]}"(SY1), \
              "+{v[128:143]}"(OX), "+{v[144:159]}"(OY), \
              "+{v[168:171]}"(K0), "+{v[172:175]}"(K1), "+{v[176:179]}"(V00), "+{v[180:183]}"(V01), \
              "+{v[184:187]}"(V10), "+{v[188:191]}"(V11), "+{v193}"(ka), "+{v194}"(va), [cnt] "=&s"(cnt_)
#define ATT_INS : "{v[160:163]}"(qx), "{v[164:167]}"(qy), "{v192}"(idx), "{v195}"(vinc)
    u32 cnt_;
    const char* kvb = ws + WS_K;
    PRIO_START();
    asm volatile(ATT_A ATT_OPS, "+{v[196:203]}"(EXA), "+{v[228:231]}"(EXB), "+{v[236:239]}"(WOA), [skv] "+s"(skv), [sg] "+s"(sg)
                 ATT_INS, [kvb] "s"(kvb) : "memory", "scc");
    LSTAMP(4);
    BAR();
    LSTAMP(8);
    PRIO_START();
    u32x16 EXD;
    asm volatile(ATT_B ATT_OPS, "+{v[196:203]}"(EXA), "=&{v[204:219]}"(EXD), "+{v[236:239]}"(WOA)
                 ATT_INS, "{v[228:231]}"(EXB), [kvb] "s"(kvb), [skv3] "s"(skv3), [xb] "s"(x), [wdb] "s"(wd)
                 : "memory", "scc", "v220", "v221", "v222", "v223", "v224", "v225", "v226", "v227", "v232", "v233", "v234", "v235");
    LSTAMP(9);
    BAR();
    LSTAMP(11);
    if (ffn && w < 4) {
        float sum = LDF(L_FP + l15 * 4);
#pragma unroll
        for (int i = 1; i < 8; ++i) sum += LDF(L_FP + i * 64 + l15 * 4);
        sum = bfr(sum);
        float up = __shfl(sum, (lane & 7) + 8);
        float gt = __shfl(sum, lane & 7);
        float hv = bfr(bfr(gt * sigm(gt)) * up);
        u32 hb = __float_as_uint(hv);
        float y0 = 0.f, y1 = 0.f;
#pragma unroll
        for (int i = 0; i < 8; ++i) {
            const float hs = __uint_as_float((u32)__builtin_amdgcn_readlane(hb, i));
            y0 += hs * lo16(EXD[8 + i]);
            y1 += hs * hi16(EXD[8 + i]);
        }
        u32x2 gr = {__float_as_uint(y0), __float_as_uint(y1)};
        gr[0] = gr[0] == POISON ? 0x7fc00000u : gr[0];
        gr[1] = gr[1] == POISON ? 0x7fc00000u : gr[1];
        u32 yo = WS_Y + fj * 2048 + (128 * w + 2 * lane) * 4;
        GST64C(ws, yo, gr);
    }
    LSTAMP(22);
    PRIO_START();
    asm volatile(ATT_C ATT_OPS, "+{v[236:239]}"(WOA), [swo] "+s"(swo) ATT_INS, [wob] "s"(wo) : "memory", "scc");
    const int tq = w & 3, kh = w >> 2;
    const bool owner = (sel >> 6) == tb;
    int lane_b = lane;
    asm volatile("" : "+v"(lane_b));
    {
    const int lane = lane_b;
    const int g = lane >> 4, l15 = lane & 15, h = lane >> 5, l31 = lane & 31;
    const int tokg = tb * 64 + tq * 16 + l15;
    const int ncol = hg * 64 + kh * 32 + (g & 1) * 16 + (g >> 1) * 8;
    u32 oslot;
    {
        const int head = hg * 4 + hp * 2 + blk, tl = th * 32 + l31;
        oslot = WS_O + ((tb * 4 + (tl >> 4)) * 16 + (head >> 1)) * 1024 + (((head & 1) * 2 + h) * 16 + (tl & 15)) * 16;
    }
    STAMP(12); STAMPC(20);
    u32x4 xe, Y0, Y1, Y2, Y3, Y4, Y5, Y6; u32 gl;
    const u32 yo0 = WS_Y + (28 * w + g) * 2048 + hg * 256 + l15 * 16;
    {
        u32 xo = tokg * 1024 + ncol * 2;
        GLD128(xe, x, xo, 0);
        u32 go = WS_G + tokg * 4;
        GLD32C(gl, ws, go, 0);
        u32 yo = yo0;
        asm volatile("s_cmp_lg_u32 %[stb], %[tb]\n\ts_cbranch_scc1 3f\n"
                     "global_load_dwordx4 %0, %[yo], %[ws] sc1\n v_add_u32 %[yo], 0x2000, %[yo]\n"
                     "global_load_dwordx4 %1, %[yo], %[ws] sc1\n v_add_u32 %[yo], 0x2000, %[yo]\n"
                     "global_load_dwordx4 %2, %[yo], %[ws] sc1\n v_add_u32 %[yo], 0x2000, %[yo]\n"
                     "global_load_dwordx4 %3, %[yo], %[ws] sc1\n v_add_u32 %[yo], 0x2000, %[yo]\n"
                     "global_load_dwordx4 %4, %[yo], %[ws] sc1\n v_add_u32 %[yo], 0x2000, %[yo]\n"
                     "global_load_dwordx4 %5, %[yo], %[ws] sc1\n v_add_u32 %[yo], 0x2000, %[yo]\n"
                     "global_load_dwordx4 %6, %[yo], %[ws] sc1\n3:"
                     : "=&v"(Y0), "=&v"(Y1), "=&v"(Y2), "=&v"(Y3), "=&v"(Y4), "=&v"(Y5), "=&v"(Y6), [yo] "+v"(yo)
                     : [stb] "s"(sel >> 6), [tb] "s"(tb), [ws] "s"(ws) : "memory", "scc");
    }

    {
        f32x16& mine = blk ? OY : OX;
        f32x16& give = blk ? OX : OY;
        const u32 cw = L_W + w * 2304, cr = L_W + (w ^ 4) * 2304;
        ST128(cw + lane * 16, ((u32x4){__float_as_uint(give[0]), __float_as_uint(give[1]), __float_as_uint(give[2]), __float_as_uint(give[3])}));
        ST128(cw + 1024 + lane * 16, ((u32x4){__float_as_uint(give[4]), __float_as_uint(give[5]), __float_as_uint(give[6]), __float_as_uint(give[7])}));
        STF(cw + 2048 + lane * 4, give[8]);
        BAR();
        f32x4 p0 = __builtin_bit_cast(f32x4, LD128(cr + lane * 16));
        f32x4 p1 = __builtin_bit_cast(f32x4, LD128(cr + 1024 + lane * 16));
        float l = LDF(cr + 2048 + lane * 4) + mine[8];
        float o[8];
#pragma unroll
        for (int i = 0; i < 4; ++i) { o[i] = mine[i] + p0[i]; o[4 + i] = mine[4 + i] + p1[i]; }
        {
            auto sw = __builtin_amdgcn_permlane32_swap(__float_as_uint(l), __float_as_uint(l), false, false);
            l = __uint_as_float(sw[0]);
        }
        float amax = fmaxf(fmaxf(fmaxf(fabsf(o[0]), fabsf(o[1])), fmaxf(fabsf(o[2]), fabsf(o[3]))),
                           fmaxf(fmaxf(fabsf(o[4]), fabsf(o[5])), fmaxf(fabsf(o[6]), fabsf(o[7]))));
        bool safe = (l >= 8.6736174e-19f) && (l <= 1.2676506e30f) && (amax <= 3.0e38f);
        if (!__all(safe)) {
            slow_block(ws, blk ? qy : qx, lane, o, l);
            auto sw = __builtin_amdgcn_permlane32_swap(__float_as_uint(l), __float_as_uint(l), false, false);
            l = __uint_as_float(sw[0]);
        }
        const float ri = __builtin_amdgcn_rcpf(l);
        u32 A0 = pk(o[0] * ri, o[1] * ri), A1 = pk(o[2] * ri, o[3] * ri);
        u32 B0 = pk(o[4] * ri, o[5] * ri), B1 = pk(o[6] * ri, o[7] * ri);
        auto s0 = __builtin_amdgcn_permlane32_swap(A0, B0, false, false);
        auto s1 = __builtin_amdgcn_permlane32_swap(A1, B1, false, false);
        u32x4 ov = {s0[0], s1[0], s0[1], s1[1]};
#pragma unroll
        for (int i = 0; i < 4; ++i) ov[i] = ov[i] == POISON ? 0x7fc07fc0u : ov[i];
        GST128C(ws, oslot, ov);
    }
    STAMP(13);

    u32x4 af[4][8];
    {
        const u32 f = ((l15 >> 3) & 1) | ((g & 1) << 1);
        const u32 rb = (kh ? L_V : L_K) + (8 * g + (l15 >> 2)) * 128 + (l15 & 3) * 8;
#pragma unroll
        for (int nb = 0; nb < 4; ++nb) {
            u32 a = rb + ((nb ^ f) * 32);
            asm volatile("" : "+v"(a));
#pragma unroll
            for (int st = 0; st < 8; ++st) {
                const u32 o_ = (st >> 2) * 32768 + (st & 3) * 4096;
                af[nb][st] = cat(trread(lds, a + o_), trread(lds, a + o_ + 512));
            }
        }
    }
    STAMP(14);
    asm volatile("s_waitcnt vmcnt(1)" : "+v"(xe), "+v"(gl), "+v"(Y0), "+v"(Y1), "+v"(Y2), "+v"(Y3), "+v"(Y4), "+v"(Y5), "+v"(Y6) :: "memory");
    if (owner) {
        u32 spins = 0;
#define YMX(v) max(max(v[0], v[1]), max(v[2], v[3]))
        while (!__all(max(max(max(YMX(Y0), YMX(Y1)), max(YMX(Y2), YMX(Y3))), max(max(YMX(Y4), YMX(Y5)), YMX(Y6))) != POISON) && ++spins <= SPIN_MAX) {
            u32 yo = yo0;
            asm volatile("global_load_dwordx4 %0, %[yo], %[ws] sc1\n v_add_u32 %[yo], 0x2000, %[yo]\n"
                         "global_load_dwordx4 %1, %[yo], %[ws] sc1\n v_add_u32 %[yo], 0x2000, %[yo]\n"
                         "global_load_dwordx4 %2, %[yo], %[ws] sc1\n v_add_u32 %[yo], 0x2000, %[yo]\n"
                         "global_load_dwordx4 %3, %[yo], %[ws] sc1\n v_add_u32 %[yo], 0x2000, %[yo]\n"
                         "global_load_dwordx4 %4, %[yo], %[ws] sc1\n v_add_u32 %[yo], 0x2000, %[yo]\n"
                         "global_load_dwordx4 %5, %[yo], %[ws] sc1\n v_add_u32 %[yo], 0x2000, %[yo]\n"
                         "global_load_dwordx4 %6, %[yo], %[ws] sc1\n s_waitcnt vmcnt(0)"
                         : "=&v"(Y0), "=&v"(Y1), "=&v"(Y2), "=&v"(Y3), "=&v"(Y4), "=&v"(Y5), "=&v"(Y6), [yo] "+v"(yo)
                         : [ws] "s"(ws) : "memory");
        }
        f32x4 ys;
#pragma unroll
        for (int i = 0; i < 4; ++i) {
            float v = ((__uint_as_float(Y0[i]) + __uint_as_float(Y1[i])) + (__uint_as_float(Y2[i]) + __uint_as_float(Y3[i])))
                    + ((__uint_as_float(Y4[i]) + __uint_as_float(Y5[i])) + __uint_as_float(Y6[i]));
            auto a = __builtin_amdgcn_permlane32_swap(__float_as_uint(v), __float_as_uint(v), false, false);
            v = __uint_as_float(a[0]) + __uint_as_float(a[1]);
            auto c = __builtin_amdgcn_permlane16_swap(__float_as_uint(v), __float_as_uint(v), false, false);
            ys[i] = __uint_as_float(c[0]) + __uint_as_float(c[1]);
        }
        if (g == 0) ST128(L_YP + w * 256 + l15 * 16, __builtin_bit_cast(u32x4, ys));
    }
    u32x4 of0, of1, of2, of3, of4, of5, of6, of7;
    {
        const u32 oo = WS_O + ((tb * 4 + tq) * 16 + 8 * kh) * 1024 + lane * 16, o2 = oo + 4096;
        u32 spins = 0;
        while (true) {
            asm volatile("global_load_dwordx4 %0, %8, %10 sc1\n\tglobal_load_dwordx4 %1, %8, %10 offset:1024 sc1\n\t"
                         "global_load_dwordx4 %2, %8, %10 offset:2048 sc1\n\tglobal_load_dwordx4 %3, %8, %10 offset:3072 sc1\n\t"
                         "global_load_dwordx4 %4, %9, %10 sc1\n\tglobal_load_dwordx4 %5, %9, %10 offset:1024 sc1\n\t"
                         "global_load_dwordx4 %6, %9, %10 offset:2048 sc1\n\tglobal_load_dwordx4 %7, %9, %10 offset:3072 sc1\n\t"
                         "s_waitcnt vmcnt(0)"
                         : "=&v"(of0), "=&v"(of1), "=&v"(of2), "=&v"(of3), "=&v"(of4), "=&v"(of5), "=&v"(of6), "=&v"(of7)
                         : "v"(oo), "v"(o2), "s"(ws) : "memory");
            u32 mx = 0;
#define OMX(v) mx = max(max(mx, max(v[0], v[1])), max(v[2], v[3]));
            OMX(of0) OMX(of1) OMX(of2) OMX(of3) OMX(of4) OMX(of5) OMX(of6) OMX(of7)
            if (__all(mx != POISON) || ++spins > SPIN_MAX) break;
        }
    }
    STAMP(15);
    f32x4 acc[4] = {{0, 0, 0, 0}, {0, 0, 0, 0}, {0, 0, 0, 0}, {0, 0, 0, 0}};
#define OSTEP(OF, ST) \
    _Pragma("unroll") for (int nb = 0; nb < 4; ++nb) acc[nb] = __builtin_amdgcn_mfma_f32_16x16x32_bf16(asbf(af[nb][ST]), asbf(OF), acc[nb], 0, 0, 0);
    OSTEP(of0, 0) OSTEP(of1, 1) OSTEP(of2, 2) OSTEP(of3, 3) OSTEP(of4, 4) OSTEP(of5, 5) OSTEP(of6, 6) OSTEP(of7, 7)
    STAMP(16);
    {
        f32x4 k0 = kh ? acc[2] : acc[0], k1 = kh ? acc[3] : acc[1];
        f32x4 g0 = kh ? acc[0] : acc[2], g1 = kh ? acc[1] : acc[3];
        ST128(L_K + 16384 + w * 2048 + lane * 16, __builtin_bit_cast(u32x4, g0));
        ST128(L_K + 16384 + w * 2048 + 1024 + lane * 16, __builtin_bit_cast(u32x4, g1));
        BAR();
        k0 += __builtin_bit_cast(f32x4, LD128(L_K + 16384 + (w ^ 4) * 2048 + lane * 16));
        k1 += __builtin_bit_cast(f32x4, LD128(L_K + 16384 + (w ^ 4) * 2048 + 1024 + lane * 16));
        float a[8];
#pragma unroll
        for (int i = 0; i < 4; ++i) {
            auto sw = __builtin_amdgcn_permlane16_swap(__float_as_uint(k0[i]), __float_as_uint(k1[i]), false, false);
            a[i] = __uint_as_float(sw[0]); a[4 + i] = __uint_as_float(sw[1]);
        }
        const float gt = bfr(sigm(bfr(__uint_as_float(gl))));
        const float og = bfr(1.0f - gt);
        float bb[8];
#pragma unroll
        for (int i = 0; i < 4; ++i) { bb[2 * i] = lo16(xe[i]); bb[2 * i + 1] = hi16(xe[i]); }
        if (owner && tokg == sel) {
            const u32 rord = key >> 16;
            const float rsel = __uint_as_float(((rord & 0x8000u) ? (rord & 0x7fffu) : (~rord & 0xffffu)) << 16);
            const float sg = bfr(sigm(rsel));
            const int c0 = kh * 32 + (g & 1) * 16 + (g >> 1) * 8;
#pragma unroll
            for (int i = 0; i < 8; ++i) {
                float y = 0.f;
#pragma unroll
                for (int p = 0; p < 8; ++p) y += LDF(L_YP + p * 256 + (c0 + i) * 4);
                bb[i] = bfr(bb[i] + bfr(bfr(y) * sg));
            }
        }
        u32x4 ov;
#pragma unroll
        for (int i = 0; i < 4; ++i)
            ov[i] = pk(bfr(gt * bfr(a[2 * i])) + bfr(og * bb[2 * i]), bfr(gt * bfr(a[2 * i + 1])) + bfr(og * bb[2 * i + 1]));
        u32 oo_ = tokg * 1024 + ncol * 2;
        GST128(out, oo_, ov);
    }
    STAMP(17); STAMPC(21);
    BAR();
    if (tid == 0 && (b >> 3) == 5) {
#define RD(wv, i) LD32(L_CT + 64 + ((wv) * 24 + (i)) * 4)
        u32 t1 = RD(0, 1), c0 = RD(0, 19), c4 = RD(4, 19);
        printf("STMP b %d t0 %u att0 %u att %u/%u end %u | g1e %u/%u B1 %u/%u g3e %u/%u B2 %u/%u 1b %u/%u att %u/%u\n", b, t1 % 100000, RD(0,10) - t1, RD(0,12) - t1, RD(4,12) - t1, RD(4,17) - t1,
               RD(0,4) - c0, RD(4,4) - c4, RD(0,8) - c0, RD(4,8) - c4, RD(0,9) - c0, RD(4,9) - c4, RD(0,11) - c0, RD(4,11) - c4, RD(0,22) - c0, RD(4,22) - c4, RD(0,20) - c0, RD(4,20) - c4);
    }
    }
}

extern "C" void kernel_launch(void* const* d_in, const int* in_sizes, int n_in, void* d_out, int out_size,
                              void* d_ws, size_t ws_size, hipStream_t stream) {
    (void)in_sizes; (void)n_in; (void)out_size; (void)ws_size;
    fused_kernel<<<256, 512, 0, stream>>>((const u16*)d_in[0], (const u16*)d_in[1], (const u16*)d_in[2], (const u16*)d_in[3],
                                          (const u16*)d_in[4], (const u16*)d_in[6], (const u16*)d_in[7], (const u16*)d_in[8],
                                          (const u16*)d_in[9], (const u16*)d_in[10], (u16*)d_out, (char*)d_ws);
}
```

```cpp
#include <hip/hip_runtime.h>
#include <stdint.h>
#include <stdio.h>
#define DBG_STAMP
#define LOOPC
#define SPLITA

typedef unsigned int u32;
typedef unsigned long long u64;
typedef unsigned short u16;
typedef u32 u32x2 __attribute__((ext_vector_type(2)));
typedef u32 u32x4 __attribute__((ext_vector_type(4)));
typedef u32 u32x8 __attribute__((ext_vector_type(8)));
typedef u32 u32x16 __attribute__((ext_vector_type(16)));
typedef float f32x2 __attribute__((ext_vector_type(2)));
typedef float f32x4 __attribute__((ext_vector_type(4)));
typedef float f32x16 __attribute__((ext_vector_type(16)));
typedef __bf16 bf16x2 __attribute__((ext_vector_type(2)));
typedef __bf16 bf16x8 __attribute__((ext_vector_type(8)));
#define LDSP __attribute__((address_space(3)))

constexpr u32 WS_E   = 0;
constexpr u32 WS_F2  = 4096;
constexpr u32 WS_G   = 24576;
constexpr u32 WS_K   = 65536;
constexpr u32 WS_V   = 131072;
constexpr u32 WS_F1  = 262144;
constexpr u32 WS_O   = 524288;
constexpr u32 WS_Y   = 2621440;
constexpr u32 WS_DBG = 4194304;

constexpr u32 L_K   = 0;
constexpr u32 L_V   = 65536;
constexpr u32 L_ONE = 131072;
constexpr u32 L_W   = 135168;
constexpr u32 L_M   = 151552;
constexpr u32 L_P1  = L_M;
constexpr u32 L_PD  = L_M + 8192;
constexpr u32 L_FP  = L_M + 8704;
constexpr u32 L_YP  = L_M + 9216;
constexpr u32 L_CT  = L_M + 11264;
constexpr u32 L_END = 163840;

constexpr u32 SPIN_MAX = 1u << 22;
constexpr u32 POISON = 0xffffffffu;

#define LD128(a) (*(const LDSP u32x4*)(lds + (a)))
#define ST128(a, v) (*(LDSP u32x4*)(lds + (a)) = (v))
#define LD32(a) (*(const LDSP u32*)(lds + (a)))
#define ST32(a, v) (*(LDSP u32*)(lds + (a)) = (v))
#define LDF(a) (*(const LDSP float*)(lds + (a)))
#define STF(a, v) (*(LDSP float*)(lds + (a)) = (v))

__device__ __forceinline__ u32x2 trread(LDSP char* lds, u32 a) {
    typedef short v4s __attribute__((ext_vector_type(4)));
    v4s r = __builtin_amdgcn_ds_read_tr16_b64_v4i16((LDSP v4s*)(lds + a));
    return __builtin_bit_cast(u32x2, r);
}
__device__ __forceinline__ bf16x8 asbf(u32x4 v) { return __builtin_bit_cast(bf16x8, v); }
__device__ __forceinline__ u32x4 cat(u32x2 a, u32x2 b) { u32x4 r = {a[0], a[1], b[0], b[1]}; return r; }
__device__ __forceinline__ u32 pk(float a, float b) {
    f32x2 f = {a, b};
    return __builtin_bit_cast(u32, __builtin_convertvector(f, bf16x2));
}
__device__ __forceinline__ float bfr(float a) {
    return __uint_as_float(pk(a, a) & 0xffff0000u);
}
__device__ __forceinline__ float lo16(u32 v) { return __uint_as_float(v << 16); }
__device__ __forceinline__ float hi16(u32 v) { return __uint_as_float(v & 0xffff0000u); }
__device__ __forceinline__ float sigm(float z) { return __builtin_amdgcn_rcpf(1.0f + __builtin_amdgcn_exp2f(-1.4426950408889634f * z)); }

__device__ __forceinline__ void dma16(const void* base, u32 voff, u32 ldsaddr) {
    asm volatile("s_mov_b32 m0, %0\n\ts_nop 0\n\tglobal_load_lds_dwordx4 %1, %2" :: "s"(ldsaddr), "v"(voff), "s"(base) : "memory");
}
__device__ __forceinline__ void dma16c4(const void* base, u32 voff, u32 ldsaddr) {
    asm volatile("s_mov_b32 m0, %0\n\ts_nop 0\n\tglobal_load_lds_dwordx4 %1, %2 sc1\n\tglobal_load_lds_dwordx4 %1, %2 offset:1024 sc1\n\t"
                 "global_load_lds_dwordx4 %1, %2 offset:2048 sc1\n\tglobal_load_lds_dwordx4 %1, %2 offset:3072 sc1"
                 :: "s"(ldsaddr), "v"(voff), "s"(base) : "memory");
}
__device__ __forceinline__ void dma16c3(const void* base, u32 voff, u32 ldsaddr) {
    asm volatile("s_mov_b32 m0, %0\n\ts_nop 0\n\tglobal_load_lds_dwordx4 %1, %2 sc1\n\tglobal_load_lds_dwordx4 %1, %2 offset:1024 sc1\n\t"
                 "global_load_lds_dwordx4 %1, %2 offset:2048 sc1"
                 :: "s"(ldsaddr), "v"(voff), "s"(base) : "memory");
}
__device__ __forceinline__ void dma16c2(const void* base, u32 voff, u32 ldsaddr) {
    asm volatile("s_mov_b32 m0, %0\n\ts_nop 0\n\tglobal_load_lds_dwordx4 %1, %2 sc1\n\tglobal_load_lds_dwordx4 %1, %2 offset:1024 sc1"
                 :: "s"(ldsaddr), "v"(voff), "s"(base) : "memory");
}
__device__ __forceinline__ void dma16c(const void* base, u32 voff, u32 ldsaddr) {
    asm volatile("s_mov_b32 m0, %0\n\ts_nop 0\n\tglobal_load_lds_dwordx4 %1, %2 sc1" :: "s"(ldsaddr), "v"(voff), "s"(base) : "memory");
}
#define GLD128(dst, base, voff, OFF)  asm volatile("global_load_dwordx4 %0, %1, %2 offset:" #OFF : "=v"(dst) : "v"(voff), "s"(base) : "memory")
#define GLD128C(dst, base, voff, OFF) asm volatile("global_load_dwordx4 %0, %1, %2 offset:" #OFF " sc1" : "=v"(dst) : "v"(voff), "s"(base) : "memory")
#define GLD64C(dst, base, voff, OFF)  asm volatile("global_load_dwordx2 %0, %1, %2 offset:" #OFF " sc1" : "=v"(dst) : "v"(voff), "s"(base) : "memory")
#define GLD32C(dst, base, voff, OFF)  asm volatile("global_load_dword %0, %1, %2 offset:" #OFF " sc1" : "=v"(dst) : "v"(voff), "s"(base) : "memory")
#define GLD32(dst, base, voff, OFF)   asm volatile("global_load_dword %0, %1, %2 offset:" #OFF : "=v"(dst) : "v"(voff), "s"(base) : "memory")
#define GLD16(dst, base, voff, OFF)   asm volatile("global_load_ushort %0, %1, %2 offset:" #OFF : "=v"(dst) : "v"(voff), "s"(base) : "memory")
#define GST128(base, voff, val)  asm volatile("global_store_dwordx4 %0, %1, %2\n\ts_nop 1" :: "v"(voff), "v"(val), "s"(base) : "memory")
#define GST128C(base, voff, val) asm volatile("global_store_dwordx4 %0, %1, %2 sc1\n\ts_nop 1" :: "v"(voff), "v"(val), "s"(base) : "memory")
#define GST64C(base, voff, val)  asm volatile("global_store_dwordx2 %0, %1, %2 sc1\n\ts_nop 0" :: "v"(voff), "v"(val), "s"(base) : "memory")
#define GST32C(base, voff, val)  asm volatile("global_store_dword %0, %1, %2 sc1" :: "v"(voff), "v"(val), "s"(base) : "memory")
#define VMWAIT(N) asm volatile("s_waitcnt vmcnt(" #N ")" ::: "memory")
#define BAR() asm volatile("s_waitcnt lgkmcnt(0)\n\ts_barrier" ::: "memory")

#define STAMP(i) do { u32 t_ = (u32)__builtin_amdgcn_s_memrealtime(); if (lane == 0) ST32(L_CT + 64 + (w * 24 + (i)) * 4, t_); } while (0)
#define STAMPC(i) do { u32 t_ = (u32)__builtin_amdgcn_s_memtime(); if (lane == 0) ST32(L_CT + 64 + (w * 24 + (i)) * 4, t_); } while (0)
#define STAMPDUMP() do { if (lane < 24) ((u32*)(ws + WS_DBG))[(blockIdx.x * 8 + w) * 32 + lane] = LD32(L_CT + 64 + (w * 24 + lane) * 4); } while (0)
#define FSTAMP(i)
#define LSTAMP(i) STAMPC(i)

__device__ __forceinline__ u32 wave_max_u32(u32 v) {
    v = max(v, (u32)__builtin_amdgcn_update_dpp(0, (int)v, 0x111, 0xf, 0xf, false));
    v = max(v, (u32)__builtin_amdgcn_update_dpp(0, (int)v, 0x112, 0xf, 0xf, false));
    v = max(v, (u32)__builtin_amdgcn_update_dpp(0, (int)v, 0x114, 0xf, 0xf, false));
    v = max(v, (u32)__builtin_amdgcn_update_dpp(0, (int)v, 0x118, 0xf, 0xf, false));
    v = max(v, (u32)__builtin_amdgcn_update_dpp(0, (int)v, 0x142, 0xa, 0xf, false));
    v = max(v, (u32)__builtin_amdgcn_update_dpp(0, (int)v, 0x143, 0xc, 0xf, false));
    return (u32)__builtin_amdgcn_readlane((int)v, 63);
}
#define ATT_A \
    "s_mov_b32 %[cnt], 2\n" \
    "v_mov_b32 v232, -1\n" \
    "v_mov_b32 v233, -1\n" \
    "v_mov_b32 v234, -1\n" \
    "v_mov_b32 v235, -1\n" \
    "1:\n" \
    "v_exp_f32 v64, v64\n" \
    "v_exp_f32 v65, v65\n" \
    "v_smfmac_f32_32x32x32_bf16 v[144:159], v[188:191], v[120:127], v192\n" \
    "global_store_dwordx4 v220, v[232:235], %[wsb] sc1\n" \
    "ds_read_b128 v[184:187], v194 offset:1024\n" \
    "v_exp_f32 v66, v66\n" \
    "v_exp_f32 v67, v67\n" \
    "v_exp_f32 v68, v68\n" \
    "v_exp_f32 v69, v69\n" \
    "s_waitcnt lgkmcnt(1)\n" \
    "v_mfma_f32_32x32x16_bf16 v[80:95], v[172:175], v[160:163], 0\n" \
    "ds_read_b128 v[188:191], v194 offset:1536\n" \
    "v_exp_f32 v70, v70\n" \
    "v_exp_f32 v71, v71\n" \
    "v_exp_f32 v72, v72\n" \
    "v_exp_f32 v73, v73\n" \
    "v_smfmac_f32_32x32x32_bf16 v[128:143], v[176:179], v[64:71], v192\n" \
    "v_exp_f32 v74, v74\n" \
    "s_mov_b32 m0, %[skv]\n" \
    "v_exp_f32 v75, v75\n" \
    "v_exp_f32 v76, v76\n" \
    "global_load_lds_dwordx4 v196, %[kvb] offset:0 sc1\n" \
    "v_exp_f32 v77, v77\n" \
    "v_exp_f32 v78, v78\n" \
    "v_exp_f32 v79, v79\n" \
    "v_exp_f32 v96, v96\n" \
    "v_exp_f32 v97, v97\n" \
    "v_smfmac_f32_32x32x32_bf16 v[128:143], v[180:183], v[72:79], v192\n" \
    "v_exp_f32 v98, v98\n" \
    "v_exp_f32 v99, v99\n" \
    "v_exp_f32 v100, v100\n" \
    "v_exp_f32 v101, v101\n" \
    "v_mfma_f32_32x32x16_bf16 v[112:127], v[172:175], v[164:167], 0\n" \
    "ds_read_b128 v[168:171], v193 offset:2048\n" \
    "v_exp_f32 v102, v102\n" \
    "v_exp_f32 v103, v103\n" \
    "v_exp_f32 v104, v104\n" \
    "v_exp_f32 v105, v105\n" \
    "v_smfmac_f32_32x32x32_bf16 v[144:159], v[176:179], v[96:103], v192\n" \
    "v_exp_f32 v106, v106\n" \
    "v_exp_f32 v107, v107\n" \
    "v_exp_f32 v108, v108\n" \
    "v_exp_f32 v109, v109\n" \
    "v_exp_f32 v110, v110\n" \
    "v_exp_f32 v111, v111\n" \
    "v_exp_f32 v80, v80\n" \
    "v_exp_f32 v81, v81\n" \
    "v_smfmac_f32_32x32x32_bf16 v[144:159], v[180:183], v[104:111], v192\n" \
    "ds_read_b128 v[176:179], v194 offset:2048\n" \
    "v_exp_f32 v82, v82\n" \
    "v_exp_f32 v83, v83\n" \
    "v_exp_f32 v84, v84\n" \
    "v_exp_f32 v85, v85\n" \
    "s_waitcnt lgkmcnt(1)\n" \
    "v_mfma_f32_32x32x16_bf16 v[64:79], v[168:171], v[160:163], 0\n" \
    "ds_read_b128 v[180:183], v194 offset:2560\n" \
    "v_exp_f32 v86, v86\n" \
    "v_exp_f32 v87, v87\n" \
    "v_exp_f32 v88, v88\n" \
    "v_exp_f32 v89, v89\n" \
    "v_smfmac_f32_32x32x32_bf16 v[128:143], v[184:187], v[80:87], v192\n" \
    "v_exp_f32 v90, v90\n" \
    "s_mov_b32 m0, %[skv]\n" \
    "v_exp_f32 v91, v91\n" \
    "v_exp_f32 v92, v92\n" \
    "global_load_lds_dwordx4 v196, %[kvb] offset:1024 sc1\n" \
    "v_exp_f32 v93, v93\n" \
    "v_exp_f32 v94, v94\n" \
    "v_exp_f32 v95, v95\n" \
    "v_exp_f32 v112, v112\n" \
    "v_exp_f32 v113, v113\n" \
    "v_smfmac_f32_32x32x32_bf16 v[128:143], v[188:191], v[88:95], v192\n" \
    "v_exp_f32 v114, v114\n" \
    "v_exp_f32 v115, v115\n" \
    "v_exp_f32 v116, v116\n" \
    "v_exp_f32 v117, v117\n" \
    "v_mfma_f32_32x32x16_bf16 v[96:111], v[168:171], v[164:167], 0\n" \
    "ds_read_b128 v[172:175], v193 offset:3072\n" \
    "v_exp_f32 v118, v118\n" \
    "v_exp_f32 v119, v119\n" \
    "v_exp_f32 v120, v120\n" \
    "v_exp_f32 v121, v121\n" \
    "v_smfmac_f32_32x32x32_bf16 v[144:159], v[184:187], v[112:119], v192\n" \
    "v_exp_f32 v122, v122\n" \
    "s_mov_b32 m0, %[sg]\n" \
    "v_exp_f32 v123, v123\n" \
    "v_exp_f32 v124, v124\n" \
    "global_load_lds_dwordx4 v[198:199], off\n" \
    "v_exp_f32 v125, v125\n" \
    "v_exp_f32 v126, v126\n" \
    "v_exp_f32 v127, v127\n" \
    "s_cmp_eq_u32 %[cnt], 1\n" \
    "s_cbranch_scc0 8f\n" \
    "s_setprio 0\n" \
    "8:\n" \
    "v_exp_f32 v64, v64\n" \
    "v_exp_f32 v65, v65\n" \
    "v_smfmac_f32_32x32x32_bf16 v[144:159], v[188:191], v[120:127], v192\n" \
    "ds_read_b128 v[184:187], v194 offset:3072\n" \
    "v_exp_f32 v66, v66\n" \
    "v_exp_f32 v67, v67\n" \
    "v_exp_f32 v68, v68\n" \
    "v_exp_f32 v69, v69\n" \
    "s_waitcnt lgkmcnt(1)\n" \
    "v_mfma_f32_32x32x16_bf16 v[80:95], v[172:175], v[160:163], 0\n" \
    "ds_read_b128 v[188:191], v194 offset:3584\n" \
    "v_exp_f32 v70, v70\n" \
    "v_exp_f32 v71, v71\n" \
    "v_exp_f32 v72, v72\n" \
    "v_exp_f32 v73, v73\n" \
    "v_smfmac_f32_32x32x32_bf16 v[128:143], v[176:179], v[64:71], v192\n" \
    "v_exp_f32 v74, v74\n" \
    "s_mov_b32 m0, %[skv]\n" \
    "v_exp_f32 v75, v75\n" \
    "v_exp_f32 v76, v76\n" \
    "global_load_lds_dwordx4 v196, %[kvb] offset:2048 sc1\n" \
    "v_exp_f32 v77, v77\n" \
    "v_exp_f32 v78, v78\n" \
    "v_exp_f32 v79, v79\n" \
    "v_exp_f32 v96, v96\n" \
    "v_exp_f32 v97, v97\n" \
    "v_smfmac_f32_32x32x32_bf16 v[128:143], v[180:183], v[72:79], v192\n" \
    "v_exp_f32 v98, v98\n" \
    "v_exp_f32 v99, v99\n" \
    "v_exp_f32 v100, v100\n" \
    "v_exp_f32 v101, v101\n" \
    "v_mfma_f32_32x32x16_bf16 v[112:127], v[172:175], v[164:167], 0\n" \
    "ds_read_b128 v[168:171], v193 offset:4096\n" \
    "v_exp_f32 v102, v102\n" \
    "v_exp_f32 v103, v103\n" \
    "v_exp_f32 v104, v104\n" \
    "v_exp_f32 v105, v105\n" \
    "v_smfmac_f32_32x32x32_bf16 v[144:159], v[176:179], v[96:103], v192\n" \
    "v_exp_f32 v106, v106\n" \
    "v_exp_f32 v107, v107\n" \
    "v_exp_f32 v108, v108\n" \
    "v_exp_f32 v109, v109\n" \
    "v_exp_f32 v110, v110\n" \
    "v_exp_f32 v111, v111\n" \
    "v_exp_f32 v80, v80\n" \
    "v_exp_f32 v81, v81\n" \
    "v_smfmac_f32_32x32x32_bf16 v[144:159], v[180:183], v[104:111], v192\n" \
    "ds_read_b128 v[176:179], v194 offset:4096\n" \
    "v_exp_f32 v82, v82\n" \
    "v_exp_f32 v83, v83\n" \
    "v_exp_f32 v84, v84\n" \
    "v_exp_f32 v85, v85\n" \
    "s_waitcnt lgkmcnt(1)\n" \
    "v_mfma_f32_32x32x16_bf16 v[64:79], v[168:171], v[160:163], 0\n" \
    "ds_read_b128 v[180:183], v194 offset:4608\n" \
    "v_exp_f32 v86, v86\n" \
    "v_exp_f32 v87, v87\n" \
    "v_exp_f32 v88, v88\n" \
    "v_exp_f32 v89, v89\n" \
    "v_smfmac_f32_32x32x32_bf16 v[128:143], v[184:187], v[80:87], v192\n" \
    "v_exp_f32 v90, v90\n" \
    "s_mov_b32 m0, %[skv]\n" \
    "v_exp_f32 v91, v91\n" \
    "v_exp_f32 v92, v92\n" \
    "global_load_lds_dwordx4 v196, %[kvb] offset:3072 sc1\n" \
    "v_exp_f32 v93, v93\n" \
    "v_exp_f32 v94, v94\n" \
    "v_exp_f32 v95, v95\n" \
    "v_exp_f32 v112, v112\n" \
    "v_exp_f32 v113, v113\n" \
    "v_smfmac_f32_32x32x32_bf16 v[128:143], v[188:191], v[88:95], v192\n" \
    "v_exp_f32 v114, v114\n" \
    "v_exp_f32 v115, v115\n" \
    "v_exp_f32 v116, v116\n" \
    "v_exp_f32 v117, v117\n" \
    "v_mfma_f32_32x32x16_bf16 v[96:111], v[168:171], v[164:167], 0\n" \
    "ds_read_b128 v[172:175], v193 offset:5120\n" \
    "v_exp_f32 v118, v118\n" \
    "v_exp_f32 v119, v119\n" \
    "v_exp_f32 v120, v120\n" \
    "v_exp_f32 v121, v121\n" \
    "v_smfmac_f32_32x32x32_bf16 v[144:159], v[184:187], v[112:119], v192\n" \
    "v_exp_f32 v122, v122\n" \
    "v_exp_f32 v123, v123\n" \
    "v_exp_f32 v124, v124\n" \
    "v_exp_f32 v125, v125\n" \
    "v_exp_f32 v126, v126\n" \
    "v_exp_f32 v127, v127\n" \
    "v_add_u32 v196, 0x2000, v196\n" \
    "v_mov_b32 v198, v200\n" \
    "v_mov_b32 v199, v201\n" \
    "s_add_u32 %[skv], %[skv], 0x2000\n" \
    "s_add_u32 %[sg], %[sg], 0x400\n" \
    "v_mov_b32 v220, v221\n" \
    "v_add_u32 v193, 4096, v193\n" \
    "v_add_u32 v194, v195, v194\n" \
    "s_sub_u32 %[cnt], %[cnt], 1\n" \
    "s_cmp_lg_u32 %[cnt], 0\n" \
    "s_cbranch_scc1 1b\n" \
    "s_waitcnt vmcnt(5)\n"

#define ATT_A1 \
    "s_mov_b32 %[cnt], 2\n" \
    "v_mov_b32 v232, -1\n" \
    "v_mov_b32 v233, -1\n" \
    "v_mov_b32 v234, -1\n" \
    "v_mov_b32 v235, -1\n" \
    "v_exp_f32 v64, v64\n" \
    "v_exp_f32 v65, v65\n" \
    "v_smfmac_f32_32x32x32_bf16 v[144:159], v[188:191], v[120:127], v192\n" \
    "global_store_dwordx4 v220, v[232:235], %[wsb] sc1\n" \
    "ds_read_b128 v[184:187], v194 offset:1024\n" \
    "v_exp_f32 v66, v66\n" \
    "v_exp_f32 v67, v67\n" \
    "v_exp_f32 v68, v68\n" \
    "v_exp_f32 v69, v69\n" \
    "s_waitcnt lgkmcnt(1)\n" \
    "v_mfma_f32_32x32x16_bf16 v[80:95], v[172:175], v[160:163], 0\n" \
    "ds_read_b128 v[188:191], v194 offset:1536\n" \
    "v_exp_f32 v70, v70\n" \
    "v_exp_f32 v71, v71\n" \
    "v_exp_f32 v72, v72\n" \
    "v_exp_f32 v73, v73\n" \
    "v_smfmac_f32_32x32x32_bf16 v[128:143], v[176:179], v[64:71], v192\n" \
    "v_exp_f32 v74, v74\n" \
    "s_mov_b32 m0, %[skv]\n" \
    "v_exp_f32 v75, v75\n" \
    "v_exp_f32 v76, v76\n" \
    "global_load_lds_dwordx4 v196, %[kvb] offset:0 sc1\n" \
    "v_exp_f32 v77, v77\n" \
    "v_exp_f32 v78, v78\n" \
    "v_exp_f32 v79, v79\n" \
    "v_exp_f32 v96, v96\n" \
    "v_exp_f32 v97, v97\n" \
    "v_smfmac_f32_32x32x32_bf16 v[128:143], v[180:183], v[72:79], v192\n" \
    "v_exp_f32 v98, v98\n" \
    "v_exp_f32 v99, v99\n" \
    "v_exp_f32 v100, v100\n" \
    "v_exp_f32 v101, v101\n" \
    "v_mfma_f32_32x32x16_bf16 v[112:127], v[172:175], v[164:167], 0\n" \
    "ds_read_b128 v[168:171], v193 offset:2048\n" \
    "v_exp_f32 v102, v102\n" \
    "v_exp_f32 v103, v103\n" \
    "v_exp_f32 v104, v104\n" \
    "v_exp_f32 v105, v105\n" \
    "v_smfmac_f32_32x32x32_bf16 v[144:159], v[176:179], v[96:103], v192\n" \
    "v_exp_f32 v106, v106\n" \
    "v_exp_f32 v107, v107\n" \
    "v_exp_f32 v108, v108\n" \
    "v_exp_f32 v109, v109\n" \
    "v_exp_f32 v110, v110\n" \
    "v_exp_f32 v111, v111\n" \
    "v_exp_f32 v80, v80\n" \
    "v_exp_f32 v81, v81\n" \
    "v_smfmac_f32_32x32x32_bf16 v[144:159], v[180:183], v[104:111], v192\n" \
    "ds_read_b128 v[176:179], v194 offset:2048\n" \
    "v_exp_f32 v82, v82\n" \
    "v_exp_f32 v83, v83\n" \
    "v_exp_f32 v84, v84\n" \
    "v_exp_f32 v85, v85\n" \
    "s_waitcnt lgkmcnt(1)\n" \
    "v_mfma_f32_32x32x16_bf16 v[64:79], v[168:171], v[160:163], 0\n" \
    "ds_read_b128 v[180:183], v194 offset:2560\n" \
    "v_exp_f32 v86, v86\n" \
    "v_exp_f32 v87, v87\n" \
    "v_exp_f32 v88, v88\n" \
    "v_exp_f32 v89, v89\n" \
    "v_smfmac_f32_32x32x32_bf16 v[128:143], v[184:187], v[80:87], v192\n" \
    "v_exp_f32 v90, v90\n" \
    "s_mov_b32 m0, %[skv]\n" \
    "v_exp_f32 v91, v91\n" \
    "v_exp_f32 v92, v92\n" \
    "global_load_lds_dwordx4 v196, %[kvb] offset:1024 sc1\n" \
    "v_exp_f32 v93, v93\n" \
    "v_exp_f32 v94, v94\n" \
    "v_exp_f32 v95, v95\n" \
    "v_exp_f32 v112, v112\n" \
    "v_exp_f32 v113, v113\n" \
    "v_smfmac_f32_32x32x32_bf16 v[128:143], v[188:191], v[88:95], v192\n" \
    "v_exp_f32 v114, v114\n" \
    "v_exp_f32 v115, v115\n" \
    "v_exp_f32 v116, v116\n" \
    "v_exp_f32 v117, v117\n" \
    "v_mfma_f32_32x32x16_bf16 v[96:111], v[168:171], v[164:167], 0\n" \
    "ds_read_b128 v[172:175], v193 offset:3072\n" \
    "v_exp_f32 v118, v118\n" \
    "v_exp_f32 v119, v119\n" \
    "v_exp_f32 v120, v120\n" \
    "v_exp_f32 v121, v121\n" \
    "v_smfmac_f32_32x32x32_bf16 v[144:159], v[184:187], v[112:119], v192\n" \
    "v_exp_f32 v122, v122\n" \
    "s_mov_b32 m0, %[sg]\n" \
    "v_exp_f32 v123, v123\n" \
    "v_exp_f32 v124, v124\n" \
    "global_load_lds_dwordx4 v[198:199], off\n" \
    "v_exp_f32 v125, v125\n" \
    "v_exp_f32 v126, v126\n" \
    "v_exp_f32 v127, v127\n" \
    "s_cmp_eq_u32 %[cnt], 1\n" \
    "s_cbranch_scc0 8f\n" \
    "s_setprio 0\n" \
    "8:\n" \
    "v_exp_f32 v64, v64\n" \
    "v_exp_f32 v65, v65\n" \
    "v_smfmac_f32_32x32x32_bf16 v[144:159], v[188:191], v[120:127], v192\n" \
    "ds_read_b128 v[184:187], v194 offset:3072\n" \
    "v_exp_f32 v66, v66\n" \
    "v_exp_f32 v67, v67\n" \
    "v_exp_f32 v68, v68\n" \
    "v_exp_f32 v69, v69\n" \
    "s_waitcnt lgkmcnt(1)\n" \
    "v_mfma_f32_32x32x16_bf16 v[80:95], v[172:175], v[160:163], 0\n" \
    "ds_read_b128 v[188:191], v194 offset:3584\n" \
    "v_exp_f32 v70, v70\n" \
    "v_exp_f32 v71, v71\n" \
    "v_exp_f32 v72, v72\n" \
    "v_exp_f32 v73, v73\n" \
    "v_smfmac_f32_32x32x32_bf16 v[128:143], v[176:179], v[64:71], v192\n" \
    "v_exp_f32 v74, v74\n" \
    "s_mov_b32 m0, %[skv]\n" \
    "v_exp_f32 v75, v75\n" \
    "v_exp_f32 v76, v76\n" \
    "global_load_lds_dwordx4 v196, %[kvb] offset:2048 sc1\n" \
    "v_exp_f32 v77, v77\n" \
    "v_exp_f32 v78, v78\n" \
    "v_exp_f32 v79, v79\n" \
    "v_exp_f32 v96, v96\n" \
    "v_exp_f32 v97, v97\n" \
    "v_smfmac_f32_32x32x32_bf16 v[128:143], v[180:183], v[72:79], v192\n" \
    "v_exp_f32 v98, v98\n" \
    "v_exp_f32 v99, v99\n" \
    "v_exp_f32 v100, v100\n" \
    "v_exp_f32 v101, v101\n" \
    "v_mfma_f32_32x32x16_bf16 v[112:127], v[172:175], v[164:167], 0\n" \
    "ds_read_b128 v[168:171], v193 offset:4096\n" \
    "v_exp_f32 v102, v102\n" \
    "v_exp_f32 v103, v103\n" \
    "v_exp_f32 v104, v104\n" \
    "v_exp_f32 v105, v105\n" \
    "v_smfmac_f32_32x32x32_bf16 v[144:159], v[176:179], v[96:103], v192\n" \
    "v_exp_f32 v106, v106\n" \
    "v_exp_f32 v107, v107\n" \
    "v_exp_f32 v108, v108\n" \
    "v_exp_f32 v109, v109\n" \
    "v_exp_f32 v110, v110\n" \
    "v_exp_f32 v111, v111\n" \
    "v_exp_f32 v80, v80\n" \
    "v_exp_f32 v81, v81\n" \
    "v_smfmac_f32_32x32x32_bf16 v[144:159], v[180:183], v[104:111], v192\n" \
    "ds_read_b128 v[176:179], v194 offset:4096\n" \
    "v_exp_f32 v82, v82\n" \
    "v_exp_f32 v83, v83\n" \
    "v_exp_f32 v84, v84\n" \
    "v_exp_f32 v85, v85\n" \
    "s_waitcnt lgkmcnt(1)\n" \
    "v_mfma_f32_32x32x16_bf16 v[64:79], v[168:171], v[160:163], 0\n" \
    "ds_read_b128 v[180:183], v194 offset:4608\n" \
    "v_exp_f32 v86, v86\n" \
    "v_exp_f32 v87, v87\n" \
    "v_exp_f32 v88, v88\n" \
    "v_exp_f32 v89, v89\n" \
    "v_smfmac_f32_32x32x32_bf16 v[128:143], v[184:187], v[80:87], v192\n" \
    "v_exp_f32 v90, v90\n" \
    "s_mov_b32 m0, %[skv]\n" \
    "v_exp_f32 v91, v91\n" \
    "v_exp_f32 v92, v92\n" \
    "global_load_lds_dwordx4 v196, %[kvb] offset:3072 sc1\n" \
    "v_exp_f32 v93, v93\n" \
    "v_exp_f32 v94, v94\n" \
    "v_exp_f32 v95, v95\n" \
    "v_exp_f32 v112, v112\n" \
    "v_exp_f32 v113, v113\n" \
    "v_smfmac_f32_32x32x32_bf16 v[128:143], v[188:191], v[88:95], v192\n" \
    "v_exp_f32 v114, v114\n" \
    "v_exp_f32 v115, v115\n" \
    "v_exp_f32 v116, v116\n" \
    "v_exp_f32 v117, v117\n" \
    "v_mfma_f32_32x32x16_bf16 v[96:111], v[168:171], v[164:167], 0\n" \
    "ds_read_b128 v[172:175], v193 offset:5120\n" \
    "v_exp_f32 v118, v118\n" \
    "v_exp_f32 v119, v119\n" \
    "v_exp_f32 v120, v120\n" \
    "v_exp_f32 v121, v121\n" \
    "v_smfmac_f32_32x32x32_bf16 v[144:159], v[184:187], v[112:119], v192\n" \
    "v_exp_f32 v122, v122\n" \
    "v_exp_f32 v123, v123\n" \
    "v_exp_f32 v124, v124\n" \
    "v_exp_f32 v125, v125\n" \
    "v_exp_f32 v126, v126\n" \
    "v_exp_f32 v127, v127\n" \
    "v_add_u32 v196, 0x2000, v196\n" \
    "v_mov_b32 v198, v200\n" \
    "v_mov_b32 v199, v201\n" \
    "s_add_u32 %[skv], %[skv], 0x2000\n" \
    "s_add_u32 %[sg], %[sg], 0x400\n" \
    "v_mov_b32 v220, v221\n" \
    "v_add_u32 v193, 4096, v193\n" \
    "v_add_u32 v194, v195, v194\n"

#define ATT_A2 \
    "s_mov_b32 %[cnt], 1\n" \
    "v_exp_f32 v64, v64\n" \
    "v_exp_f32 v65, v65\n" \
    "v_smfmac_f32_32x32x32_bf16 v[144:159], v[188:191], v[120:127], v192\n" \
    "global_store_dwordx4 v220, v[232:235], %[wsb] sc1\n" \
    "ds_read_b128 v[184:187], v194 offset:1024\n" \
    "v_exp_f32 v66, v66\n" \
    "v_exp_f32 v67, v67\n" \
    "v_exp_f32 v68, v68\n" \
    "v_exp_f32 v69, v69\n" \
    "s_waitcnt lgkmcnt(1)\n" \
    "v_mfma_f32_32x32x16_bf16 v[80:95], v[172:175], v[160:163], 0\n" \
    "ds_read_b128 v[188:191], v194 offset:1536\n" \
    "v_exp_f32 v70, v70\n" \
    "v_exp_f32 v71, v71\n" \
    "v_exp_f32 v72, v72\n" \
    "v_exp_f32 v73, v73\n" \
    "v_smfmac_f32_32x32x32_bf16 v[128:143], v[176:179], v[64:71], v192\n" \
    "v_exp_f32 v74, v74\n" \
    "s_mov_b32 m0, %[skv]\n" \
    "v_exp_f32 v75, v75\n" \
    "v_exp_f32 v76, v76\n" \
    "global_load_lds_dwordx4 v196, %[kvb] offset:0 sc1\n" \
    "v_exp_f32 v77, v77\n" \
    "v_exp_f32 v78, v78\n" \
    "v_exp_f32 v79, v79\n" \
    "v_exp_f32 v96, v96\n" \
    "v_exp_f32 v97, v97\n" \
    "v_smfmac_f32_32x32x32_bf16 v[128:143], v[180:183], v[72:79], v192\n" \
    "v_exp_f32 v98, v98\n" \
    "v_exp_f32 v99, v99\n" \
    "v_exp_f32 v100, v100\n" \
    "v_exp_f32 v101, v101\n" \
    "v_mfma_f32_32x32x16_bf16 v[112:127], v[172:175], v[164:167], 0\n" \
    "ds_read_b128 v[168:171], v193 offset:2048\n" \
    "v_exp_f32 v102, v102\n" \
    "v_exp_f32 v103, v103\n" \
    "v_exp_f32 v104, v104\n" \
    "v_exp_f32 v105, v105\n" \
    "v_smfmac_f32_32x32x32_bf16 v[144:159], v[176:179], v[96:103], v192\n" \
    "v_exp_f32 v106, v106\n" \
    "v_exp_f32 v107, v107\n" \
    "v_exp_f32 v108, v108\n" \
    "v_exp_f32 v109, v109\n" \
    "v_exp_f32 v110, v110\n" \
    "v_exp_f32 v111, v111\n" \
    "v_exp_f32 v80, v80\n" \
    "v_exp_f32 v81, v81\n" \
    "v_smfmac_f32_32x32x32_bf16 v[144:159], v[180:183], v[104:111], v192\n" \
    "ds_read_b128 v[176:179], v194 offset:2048\n" \
    "v_exp_f32 v82, v82\n" \
    "v_exp_f32 v83, v83\n" \
    "v_exp_f32 v84, v84\n" \
    "v_exp_f32 v85, v85\n" \
    "s_waitcnt lgkmcnt(1)\n" \
    "v_mfma_f32_32x32x16_bf16 v[64:79], v[168:171], v[160:163], 0\n" \
    "ds_read_b128 v[180:183], v194 offset:2560\n" \
    "v_exp_f32 v86, v86\n" \
    "v_exp_f32 v87, v87\n" \
    "v_exp_f32 v88, v88\n" \
    "v_exp_f32 v89, v89\n" \
    "v_smfmac_f32_32x32x32_bf16 v[128:143], v[184:187], v[80:87], v192\n" \
    "v_exp_f32 v90, v90\n" \
    "s_mov_b32 m0, %[skv]\n" \
    "v_exp_f32 v91, v91\n" \
    "v_exp_f32 v92, v92\n" \
    "global_load_lds_dwordx4 v196, %[kvb] offset:1024 sc1\n" \
    "v_exp_f32 v93, v93\n" \
    "v_exp_f32 v94, v94\n" \
    "v_exp_f32 v95, v95\n" \
    "v_exp_f32 v112, v112\n" \
    "v_exp_f32 v113, v113\n" \
    "v_smfmac_f32_32x32x32_bf16 v[128:143], v[188:191], v[88:95], v192\n" \
    "v_exp_f32 v114, v114\n" \
    "v_exp_f32 v115, v115\n" \
    "v_exp_f32 v116, v116\n" \
    "v_exp_f32 v117, v117\n" \
    "v_mfma_f32_32x32x16_bf16 v[96:111], v[168:171], v[164:167], 0\n" \
    "ds_read_b128 v[172:175], v193 offset:3072\n" \
    "v_exp_f32 v118, v118\n" \
    "v_exp_f32 v119, v119\n" \
    "v_exp_f32 v120, v120\n" \
    "v_exp_f32 v121, v121\n" \
    "v_smfmac_f32_32x32x32_bf16 v[144:159], v[184:187], v[112:119], v192\n" \
    "v_exp_f32 v122, v122\n" \
    "s_mov_b32 m0, %[sg]\n" \
    "v_exp_f32 v123, v123\n" \
    "v_exp_f32 v124, v124\n" \
    "global_load_lds_dwordx4 v[198:199], off\n" \
    "v_exp_f32 v125, v125\n" \
    "v_exp_f32 v126, v126\n" \
    "v_exp_f32 v127, v127\n" \
    "s_cmp_eq_u32 %[cnt], 1\n" \
    "s_cbranch_scc0 8f\n" \
    "s_setprio 0\n" \
    "8:\n" \
    "v_exp_f32 v64, v64\n" \
    "v_exp_f32 v65, v65\n" \
    "v_smfmac_f32_32x32x32_bf16 v[144:159], v[188:191], v[120:127], v192\n" \
    "ds_read_b128 v[184:187], v194 offset:3072\n" \
    "v_exp_f32 v66, v66\n" \
    "v_exp_f32 v67, v67\n" \
    "v_exp_f32 v68, v68\n" \
    "v_exp_f32 v69, v69\n" \
    "s_waitcnt lgkmcnt(1)\n" \
    "v_mfma_f32_32x32x16_bf16 v[80:95], v[172:175], v[160:163], 0\n" \
    "ds_read_b128 v[188:191], v194 offset:3584\n" \
    "v_exp_f32 v70, v70\n" \
    "v_exp_f32 v71, v71\n" \
    "v_exp_f32 v72, v72\n" \
    "v_exp_f32 v73, v73\n" \
    "v_smfmac_f32_32x32x32_bf16 v[128:143], v[176:179], v[64:71], v192\n" \
    "v_exp_f32 v74, v74\n" \
    "s_mov_b32 m0, %[skv]\n" \
    "v_exp_f32 v75, v75\n" \
    "v_exp_f32 v76, v76\n" \
    "global_load_lds_dwordx4 v196, %[kvb] offset:2048 sc1\n" \
    "v_exp_f32 v77, v77\n" \
    "v_exp_f32 v78, v78\n" \
    "v_exp_f32 v79, v79\n" \
    "v_exp_f32 v96, v96\n" \
    "v_exp_f32 v97, v97\n" \
    "v_smfmac_f32_32x32x32_bf16 v[128:143], v[180:183], v[72:79], v192\n" \
    "v_exp_f32 v98, v98\n" \
    "v_exp_f32 v99, v99\n" \
    "v_exp_f32 v100, v100\n" \
    "v_exp_f32 v101, v101\n" \
    "v_mfma_f32_32x32x16_bf16 v[112:127], v[172:175], v[164:167], 0\n" \
    "ds_read_b128 v[168:171], v193 offset:4096\n" \
    "v_exp_f32 v102, v102\n" \
    "v_exp_f32 v103, v103\n" \
    "v_exp_f32 v104, v104\n" \
    "v_exp_f32 v105, v105\n" \
    "v_smfmac_f32_32x32x32_bf16 v[144:159], v[176:179], v[96:103], v192\n" \
    "v_exp_f32 v106, v106\n" \
    "v_exp_f32 v107, v107\n" \
    "v_exp_f32 v108, v108\n" \
    "v_exp_f32 v109, v109\n" \
    "v_exp_f32 v110, v110\n" \
    "v_exp_f32 v111, v111\n" \
    "v_exp_f32 v80, v80\n" \
    "v_exp_f32 v81, v81\n" \
    "v_smfmac_f32_32x32x32_bf16 v[144:159], v[180:183], v[104:111], v192\n" \
    "ds_read_b128 v[176:179], v194 offset:4096\n" \
    "v_exp_f32 v82, v82\n" \
    "v_exp_f32 v83, v83\n" \
    "v_exp_f32 v84, v84\n" \
    "v_exp_f32 v85, v85\n" \
    "s_waitcnt lgkmcnt(1)\n" \
    "v_mfma_f32_32x32x16_bf16 v[64:79], v[168:171], v[160:163], 0\n" \
    "ds_read_b128 v[180:183], v194 offset:4608\n" \
    "v_exp_f32 v86, v86\n" \
    "v_exp_f32 v87, v87\n" \
    "v_exp_f32 v88, v88\n" \
    "v_exp_f32 v89, v89\n" \
    "v_smfmac_f32_32x32x32_bf16 v[128:143], v[184:187], v[80:87], v192\n" \
    "v_exp_f32 v90, v90\n" \
    "s_mov_b32 m0, %[skv]\n" \
    "v_exp_f32 v91, v91\n" \
    "v_exp_f32 v92, v92\n" \
    "global_load_lds_dwordx4 v196, %[kvb] offset:3072 sc1\n" \
    "v_exp_f32 v93, v93\n" \
    "v_exp_f32 v94, v94\n" \
    "v_exp_f32 v95, v95\n" \
    "v_exp_f32 v112, v112\n" \
    "v_exp_f32 v113, v113\n" \
    "v_smfmac_f32_32x32x32_bf16 v[128:143], v[188:191], v[88:95], v192\n" \
    "v_exp_f32 v114, v114\n" \
    "v_exp_f32 v115, v115\n" \
    "v_exp_f32 v116, v116\n" \
    "v_exp_f32 v117, v117\n" \
    "v_mfma_f32_32x32x16_bf16 v[96:111], v[168:171], v[164:167], 0\n" \
    "ds_read_b128 v[172:175], v193 offset:5120\n" \
    "v_exp_f32 v118, v118\n" \
    "v_exp_f32 v119, v119\n" \
    "v_exp_f32 v120, v120\n" \
    "v_exp_f32 v121, v121\n" \
    "v_smfmac_f32_32x32x32_bf16 v[144:159], v[184:187], v[112:119], v192\n" \
    "v_exp_f32 v122, v122\n" \
    "v_exp_f32 v123, v123\n" \
    "v_exp_f32 v124, v124\n" \
    "v_exp_f32 v125, v125\n" \
    "v_exp_f32 v126, v126\n" \
    "v_exp_f32 v127, v127\n" \
    "v_add_u32 v196, 0x2000, v196\n" \
    "v_mov_b32 v198, v200\n" \
    "v_mov_b32 v199, v201\n" \
    "s_add_u32 %[skv], %[skv], 0x2000\n" \
    "s_add_u32 %[sg], %[sg], 0x400\n" \
    "v_mov_b32 v220, v221\n" \
    "v_add_u32 v193, 4096, v193\n" \
    "v_add_u32 v194, v195, v194\n"

#define ATT_B \
    "v_exp_f32 v64, v64\n" \
    "v_exp_f32 v65, v65\n" \
    "v_smfmac_f32_32x32x32_bf16 v[144:159], v[188:191], v[120:127], v192\n" \
    "ds_read_b128 v[184:187], v194 offset:1024\n" \
    "v_exp_f32 v66, v66\n" \
    "v_exp_f32 v67, v67\n" \
    "v_exp_f32 v68, v68\n" \
    "v_exp_f32 v69, v69\n" \
    "s_waitcnt lgkmcnt(1)\n" \
    "v_mfma_f32_32x32x16_bf16 v[80:95], v[172:175], v[160:163], 0\n" \
    "ds_read_b128 v[188:191], v194 offset:1536\n" \
    "v_exp_f32 v70, v70\n" \
    "v_exp_f32 v71, v71\n" \
    "v_exp_f32 v72, v72\n" \
    "v_exp_f32 v73, v73\n" \
    "v_smfmac_f32_32x32x32_bf16 v[128:143], v[176:179], v[64:71], v192\n" \
    "v_exp_f32 v74, v74\n" \
    "s_mov_b32 m0, %[skv3]\n" \
    "v_exp_f32 v75, v75\n" \
    "v_exp_f32 v76, v76\n" \
    "global_load_dwordx4 v[204:207], v202, %[xb]\n" \
    "v_exp_f32 v77, v77\n" \
    "v_exp_f32 v78, v78\n" \
    "v_exp_f32 v79, v79\n" \
    "v_exp_f32 v96, v96\n" \
    "v_exp_f32 v97, v97\n" \
    "v_smfmac_f32_32x32x32_bf16 v[128:143], v[180:183], v[72:79], v192\n" \
    "v_exp_f32 v98, v98\n" \
    "v_exp_f32 v99, v99\n" \
    "v_exp_f32 v100, v100\n" \
    "v_exp_f32 v101, v101\n" \
    "v_mfma_f32_32x32x16_bf16 v[112:127], v[172:175], v[164:167], 0\n" \
    "ds_read_b128 v[168:171], v193 offset:2048\n" \
    "v_exp_f32 v102, v102\n" \
    "v_exp_f32 v103, v103\n" \
    "v_exp_f32 v104, v104\n" \
    "v_exp_f32 v105, v105\n" \
    "v_smfmac_f32_32x32x32_bf16 v[144:159], v[176:179], v[96:103], v192\n" \
    "v_exp_f32 v106, v106\n" \
    "v_exp_f32 v107, v107\n" \
    "v_exp_f32 v108, v108\n" \
    "global_load_dwordx4 v[208:211], v202, %[xb] offset:16\n" \
    "v_exp_f32 v109, v109\n" \
    "v_exp_f32 v110, v110\n" \
    "v_exp_f32 v111, v111\n" \
    "v_exp_f32 v80, v80\n" \
    "v_exp_f32 v81, v81\n" \
    "v_smfmac_f32_32x32x32_bf16 v[144:159], v[180:183], v[104:111], v192\n" \
    "ds_read_b128 v[176:179], v194 offset:2048\n" \
    "v_exp_f32 v82, v82\n" \
    "v_exp_f32 v83, v83\n" \
    "v_exp_f32 v84, v84\n" \
    "v_exp_f32 v85, v85\n" \
    "s_waitcnt lgkmcnt(1)\n" \
    "v_mfma_f32_32x32x16_bf16 v[64:79], v[168:171], v[160:163], 0\n" \
    "ds_read_b128 v[180:183], v194 offset:2560\n" \
    "v_exp_f32 v86, v86\n" \
    "v_exp_f32 v87, v87\n" \
    "v_exp_f32 v88, v88\n" \
    "v_exp_f32 v89, v89\n" \
    "v_smfmac_f32_32x32x32_bf16 v[128:143], v[184:187], v[80:87], v192\n" \
    "v_exp_f32 v90, v90\n" \
    "v_exp_f32 v91, v91\n" \
    "v_exp_f32 v92, v92\n" \
    "global_load_lds_dwordx4 v197, %[kvb] offset:0 sc1\n" \
    "v_exp_f32 v93, v93\n" \
    "v_exp_f32 v94, v94\n" \
    "v_exp_f32 v95, v95\n" \
    "v_exp_f32 v112, v112\n" \
    "v_exp_f32 v113, v113\n" \
    "v_smfmac_f32_32x32x32_bf16 v[128:143], v[188:191], v[88:95], v192\n" \
    "v_exp_f32 v114, v114\n" \
    "v_exp_f32 v115, v115\n" \
    "v_exp_f32 v116, v116\n" \
    "v_exp_f32 v117, v117\n" \
    "v_mfma_f32_32x32x16_bf16 v[96:111], v[168:171], v[164:167], 0\n" \
    "ds_read_b128 v[172:175], v193 offset:3072\n" \
    "v_exp_f32 v118, v118\n" \
    "v_exp_f32 v119, v119\n" \
    "v_exp_f32 v120, v120\n" \
    "v_exp_f32 v121, v121\n" \
    "v_smfmac_f32_32x32x32_bf16 v[144:159], v[184:187], v[112:119], v192\n" \
    "v_exp_f32 v122, v122\n" \
    "v_exp_f32 v123, v123\n" \
    "v_exp_f32 v124, v124\n" \
    "global_load_lds_dwordx4 v197, %[kvb] offset:1024 sc1\n" \
    "v_exp_f32 v125, v125\n" \
    "v_exp_f32 v126, v126\n" \
    "v_exp_f32 v127, v127\n" \
    "v_exp_f32 v64, v64\n" \
    "v_exp_f32 v65, v65\n" \
    "v_smfmac_f32_32x32x32_bf16 v[144:159], v[188:191], v[120:127], v192\n" \
    "ds_read_b128 v[184:187], v194 offset:3072\n" \
    "v_exp_f32 v66, v66\n" \
    "v_exp_f32 v67, v67\n" \
    "v_exp_f32 v68, v68\n" \
    "v_exp_f32 v69, v69\n" \
    "s_waitcnt lgkmcnt(1)\n" \
    "v_mfma_f32_32x32x16_bf16 v[80:95], v[172:175], v[160:163], 0\n" \
    "ds_read_b128 v[188:191], v194 offset:3584\n" \
    "v_exp_f32 v70, v70\n" \
    "v_exp_f32 v71, v71\n" \
    "v_exp_f32 v72, v72\n" \
    "v_exp_f32 v73, v73\n" \
    "v_smfmac_f32_32x32x32_bf16 v[128:143], v[176:179], v[64:71], v192\n" \
    "v_exp_f32 v74, v74\n" \
    "v_exp_f32 v75, v75\n" \
    "v_exp_f32 v76, v76\n" \
    "global_load_lds_dwordx4 v197, %[kvb] offset:2048 sc1\n" \
    "v_exp_f32 v77, v77\n" \
    "v_exp_f32 v78, v78\n" \
    "v_exp_f32 v79, v79\n" \
    "v_exp_f32 v96, v96\n" \
    "v_exp_f32 v97, v97\n" \
    "v_smfmac_f32_32x32x32_bf16 v[128:143], v[180:183], v[72:79], v192\n" \
    "v_exp_f32 v98, v98\n" \
    "v_exp_f32 v99, v99\n" \
    "v_exp_f32 v100, v100\n" \
    "v_exp_f32 v101, v101\n" \
    "v_mfma_f32_32x32x16_bf16 v[112:127], v[172:175], v[164:167], 0\n" \
    "ds_read_b128 v[168:171], v193 offset:4096\n" \
    "v_exp_f32 v102, v102\n" \
    "v_exp_f32 v103, v103\n" \
    "v_exp_f32 v104, v104\n" \
    "v_exp_f32 v105, v105\n" \
    "v_smfmac_f32_32x32x32_bf16 v[144:159], v[176:179], v[96:103], v192\n" \
    "v_exp_f32 v106, v106\n" \
    "v_exp_f32 v107, v107\n" \
    "v_exp_f32 v108, v108\n" \
    "global_load_lds_dwordx4 v197, %[kvb] offset:3072 sc1\n" \
    "v_exp_f32 v109, v109\n" \
    "v_exp_f32 v110, v110\n" \
    "v_exp_f32 v111, v111\n" \
    "v_exp_f32 v80, v80\n" \
    "v_exp_f32 v81, v81\n" \
    "v_smfmac_f32_32x32x32_bf16 v[144:159], v[180:183], v[104:111], v192\n" \
    "ds_read_b128 v[176:179], v194 offset:4096\n" \
    "v_exp_f32 v82, v82\n" \
    "v_exp_f32 v83, v83\n" \
    "v_exp_f32 v84, v84\n" \
    "v_exp_f32 v85, v85\n" \
    "s_waitcnt lgkmcnt(1)\n" \
    "v_mfma_f32_32x32x16_bf16 v[64:79], v[168:171], v[160:163], 0\n" \
    "ds_read_b128 v[180:183], v194 offset:4608\n" \
    "v_exp_f32 v86, v86\n" \
    "v_exp_f32 v87, v87\n" \
    "v_exp_f32 v88, v88\n" \
    "v_exp_f32 v89, v89\n" \
    "v_smfmac_f32_32x32x32_bf16 v[128:143], v[184:187], v[80:87], v192\n" \
    "v_exp_f32 v90, v90\n" \
    "v_exp_f32 v91, v91\n" \
    "v_exp_f32 v92, v92\n" \
    "global_load_dword v212, v203, %[wdb] offset:0\n" \
    "global_load_dword v213, v203, %[wdb] offset:1024\n" \
    "v_exp_f32 v93, v93\n" \
    "v_exp_f32 v94, v94\n" \
    "v_exp_f32 v95, v95\n" \
    "v_exp_f32 v112, v112\n" \
    "v_exp_f32 v113, v113\n" \
    "v_smfmac_f32_32x32x32_bf16 v[128:143], v[188:191], v[88:95], v192\n" \
    "v_exp_f32 v114, v114\n" \
    "v_exp_f32 v115, v115\n" \
    "v_exp_f32 v116, v116\n" \
    "v_exp_f32 v117, v117\n" \
    "v_mfma_f32_32x32x16_bf16 v[96:111], v[168:171], v[164:167], 0\n" \
    "ds_read_b128 v[172:175], v193 offset:5120\n" \
    "v_exp_f32 v118, v118\n" \
    "v_exp_f32 v119, v119\n" \
    "v_exp_f32 v120, v120\n" \
    "v_exp_f32 v121, v121\n" \
    "v_smfmac_f32_32x32x32_bf16 v[144:159], v[184:187], v[112:119], v192\n" \
    "v_exp_f32 v122, v122\n" \
    "v_exp_f32 v123, v123\n" \
    "v_exp_f32 v124, v124\n" \
    "global_load_dword v214, v203, %[wdb] offset:2048\n" \
    "global_load_dword v215, v203, %[wdb] offset:3072\n" \
    "v_exp_f32 v125, v125\n" \
    "v_exp_f32 v126, v126\n" \
    "v_exp_f32 v127, v127\n" \
    "v_add_u32 v203, 0x1000, v203\n" \
    "v_add_u32 v193, 4096, v193\n" \
    "v_add_u32 v194, v195, v194\n" \
    "v_exp_f32 v64, v64\n" \
    "v_exp_f32 v65, v65\n" \
    "v_smfmac_f32_32x32x32_bf16 v[144:159], v[188:191], v[120:127], v192\n" \
    "ds_read_b128 v[184:187], v194 offset:1024\n" \
    "v_exp_f32 v66, v66\n" \
    "v_exp_f32 v67, v67\n" \
    "v_exp_f32 v68, v68\n" \
    "v_exp_f32 v69, v69\n" \
    "s_waitcnt lgkmcnt(1)\n" \
    "v_mfma_f32_32x32x16_bf16 v[80:95], v[172:175], v[160:163], 0\n" \
    "ds_read_b128 v[188:191], v194 offset:1536\n" \
    "v_exp_f32 v70, v70\n" \
    "v_exp_f32 v71, v71\n" \
    "v_exp_f32 v72, v72\n" \
    "v_exp_f32 v73, v73\n" \
    "v_smfmac_f32_32x32x32_bf16 v[128:143], v[176:179], v[64:71], v192\n" \
    "v_exp_f32 v74, v74\n" \
    "v_exp_f32 v75, v75\n" \
    "v_exp_f32 v76, v76\n" \
    "global_load_dword v216, v203, %[wdb] offset:0\n" \
    "global_load_dword v217, v203, %[wdb] offset:1024\n" \
    "v_exp_f32 v77, v77\n" \
    "v_exp_f32 v78, v78\n" \
    "v_exp_f32 v79, v79\n" \
    "v_exp_f32 v96, v96\n" \
    "v_exp_f32 v97, v97\n" \
    "v_smfmac_f32_32x32x32_bf16 v[128:143], v[180:183], v[72:79], v192\n" \
    "v_exp_f32 v98, v98\n" \
    "v_exp_f32 v99, v99\n" \
    "v_exp_f32 v100, v100\n" \
    "v_exp_f32 v101, v101\n" \
    "v_mfma_f32_32x32x16_bf16 v[112:127], v[172:175], v[164:167], 0\n" \
    "ds_read_b128 v[168:171], v193 offset:2048\n" \
    "v_exp_f32 v102, v102\n" \
    "v_exp_f32 v103, v103\n" \
    "v_exp_f32 v104, v104\n" \
    "v_exp_f32 v105, v105\n" \
    "v_smfmac_f32_32x32x32_bf16 v[144:159], v[176:179], v[96:103], v192\n" \
    "v_exp_f32 v106, v106\n" \
    "v_exp_f32 v107, v107\n" \
    "v_exp_f32 v108, v108\n" \
    "global_load_dword v218, v203, %[wdb] offset:2048\n" \
    "global_load_dword v219, v203, %[wdb] offset:3072\n" \
    "v_exp_f32 v109, v109\n" \
    "v_exp_f32 v110, v110\n" \
    "v_exp_f32 v111, v111\n" \
    "v_exp_f32 v80, v80\n" \
    "v_exp_f32 v81, v81\n" \
    "v_smfmac_f32_32x32x32_bf16 v[144:159], v[180:183], v[104:111], v192\n" \
    "s_waitcnt vmcnt(12)\n" \
    "ds_read_b64_tr_b16 v[220:221], v228\n" \
    "ds_read_b64_tr_b16 v[222:223], v229\n" \
    "ds_read_b64_tr_b16 v[224:225], v228 offset:256\n" \
    "ds_read_b64_tr_b16 v[226:227], v229 offset:256\n" \
    "ds_read_b128 v[176:179], v194 offset:2048\n" \
    "v_exp_f32 v82, v82\n" \
    "v_exp_f32 v83, v83\n" \
    "v_exp_f32 v84, v84\n" \
    "v_exp_f32 v85, v85\n" \
    "s_waitcnt lgkmcnt(1)\n" \
    "v_mfma_f32_32x32x16_bf16 v[64:79], v[168:171], v[160:163], 0\n" \
    "ds_read_b128 v[180:183], v194 offset:2560\n" \
    "v_exp_f32 v86, v86\n" \
    "v_exp_f32 v87, v87\n" \
    "v_exp_f32 v88, v88\n" \
    "v_exp_f32 v89, v89\n" \
    "v_smfmac_f32_32x32x32_bf16 v[128:143], v[184:187], v[80:87], v192\n" \
    "v_exp_f32 v90, v90\n" \
    "v_exp_f32 v91, v91\n" \
    "v_exp_f32 v92, v92\n" \
    "v_mfma_f32_16x16x32_bf16 v[232:235], v[220:223], v[204:207], 0\n" \
    "v_exp_f32 v93, v93\n" \
    "v_exp_f32 v94, v94\n" \
    "v_exp_f32 v95, v95\n" \
    "v_exp_f32 v112, v112\n" \
    "v_exp_f32 v113, v113\n" \
    "v_smfmac_f32_32x32x32_bf16 v[128:143], v[188:191], v[88:95], v192\n" \
    "v_exp_f32 v114, v114\n" \
    "v_exp_f32 v115, v115\n" \
    "v_exp_f32 v116, v116\n" \
    "v_exp_f32 v117, v117\n" \
    "v_mfma_f32_32x32x16_bf16 v[96:111], v[168:171], v[164:167], 0\n" \
    "ds_read_b128 v[172:175], v193 offset:3072\n" \
    "v_exp_f32 v118, v118\n" \
    "v_exp_f32 v119, v119\n" \
    "v_exp_f32 v120, v120\n" \
    "v_exp_f32 v121, v121\n" \
    "v_smfmac_f32_32x32x32_bf16 v[144:159], v[184:187], v[112:119], v192\n" \
    "v_exp_f32 v122, v122\n" \
    "v_exp_f32 v123, v123\n" \
    "v_exp_f32 v124, v124\n" \
    "v_exp_f32 v125, v125\n" \
    "v_exp_f32 v126, v126\n" \
    "v_exp_f32 v127, v127\n" \
    "s_setprio 0\n" \
    "v_exp_f32 v64, v64\n" \
    "v_exp_f32 v65, v65\n" \
    "v_smfmac_f32_32x32x32_bf16 v[144:159], v[188:191], v[120:127], v192\n" \
    "ds_read_b128 v[184:187], v194 offset:3072\n" \
    "v_exp_f32 v66, v66\n" \
    "v_exp_f32 v67, v67\n" \
    "v_exp_f32 v68, v68\n" \
    "v_exp_f32 v69, v69\n" \
    "s_waitcnt lgkmcnt(1)\n" \
    "v_mfma_f32_32x32x16_bf16 v[80:95], v[172:175], v[160:163], 0\n" \
    "ds_read_b128 v[188:191], v194 offset:3584\n" \
    "v_exp_f32 v70, v70\n" \
    "v_exp_f32 v71, v71\n" \
    "v_exp_f32 v72, v72\n" \
    "v_exp_f32 v73, v73\n" \
    "v_smfmac_f32_32x32x32_bf16 v[128:143], v[176:179], v[64:71], v192\n" \
    "v_exp_f32 v74, v74\n" \
    "v_exp_f32 v75, v75\n" \
    "v_exp_f32 v76, v76\n" \
    "v_mfma_f32_16x16x32_bf16 v[232:235], v[224:227], v[208:211], v[232:235]\n" \
    "v_exp_f32 v77, v77\n" \
    "v_exp_f32 v78, v78\n" \
    "v_exp_f32 v79, v79\n" \
    "v_exp_f32 v96, v96\n" \
    "v_exp_f32 v97, v97\n" \
    "v_smfmac_f32_32x32x32_bf16 v[128:143], v[180:183], v[72:79], v192\n" \
    "v_exp_f32 v98, v98\n" \
    "v_exp_f32 v99, v99\n" \
    "v_exp_f32 v100, v100\n" \
    "v_exp_f32 v101, v101\n" \
    "v_mfma_f32_32x32x16_bf16 v[112:127], v[172:175], v[164:167], 0\n" \
    "ds_read_b128 v[168:171], v193 offset:4096\n" \
    "v_exp_f32 v102, v102\n" \
    "v_exp_f32 v103, v103\n" \
    "v_exp_f32 v104, v104\n" \
    "v_exp_f32 v105, v105\n" \
    "v_smfmac_f32_32x32x32_bf16 v[144:159], v[176:179], v[96:103], v192\n" \
    "v_exp_f32 v106, v106\n" \
    "v_exp_f32 v107, v107\n" \
    "v_exp_f32 v108, v108\n" \
    "v_exp_f32 v109, v109\n" \
    "v_exp_f32 v110, v110\n" \
    "v_exp_f32 v111, v111\n" \
    "v_exp_f32 v80, v80\n" \
    "v_exp_f32 v81, v81\n" \
    "v_smfmac_f32_32x32x32_bf16 v[144:159], v[180:183], v[104:111], v192\n" \
    "ds_write_b128 v230, v[232:235]\n" \
    "ds_read_b128 v[176:179], v194 offset:4096\n" \
    "v_exp_f32 v82, v82\n" \
    "v_exp_f32 v83, v83\n" \
    "v_exp_f32 v84, v84\n" \
    "v_exp_f32 v85, v85\n" \
    "s_waitcnt lgkmcnt(1)\n" \
    "v_mfma_f32_32x32x16_bf16 v[64:79], v[168:171], v[160:163], 0\n" \
    "ds_read_b128 v[180:183], v194 offset:4608\n" \
    "v_exp_f32 v86, v86\n" \
    "v_exp_f32 v87, v87\n" \
    "v_exp_f32 v88, v88\n" \
    "v_exp_f32 v89, v89\n" \
    "v_smfmac_f32_32x32x32_bf16 v[128:143], v[184:187], v[80:87], v192\n" \
    "v_exp_f32 v90, v90\n" \
    "v_exp_f32 v91, v91\n" \
    "v_exp_f32 v92, v92\n" \
    "v_exp_f32 v93, v93\n" \
    "v_exp_f32 v94, v94\n" \
    "v_exp_f32 v95, v95\n" \
    "v_exp_f32 v112, v112\n" \
    "v_exp_f32 v113, v113\n" \
    "v_smfmac_f32_32x32x32_bf16 v[128:143], v[188:191], v[88:95], v192\n" \
    "v_exp_f32 v114, v114\n" \
    "v_exp_f32 v115, v115\n" \
    "v_exp_f32 v116, v116\n" \
    "v_exp_f32 v117, v117\n" \
    "v_mfma_f32_32x32x16_bf16 v[96:111], v[168:171], v[164:167], 0\n" \
    "ds_read_b128 v[172:175], v193 offset:5120\n" \
    "v_exp_f32 v118, v118\n" \
    "v_exp_f32 v119, v119\n" \
    "v_exp_f32 v120, v120\n" \
    "v_exp_f32 v121, v121\n" \
    "v_smfmac_f32_32x32x32_bf16 v[144:159], v[184:187], v[112:119], v192\n" \
    "v_exp_f32 v122, v122\n" \
    "v_exp_f32 v123, v123\n" \
    "v_exp_f32 v124, v124\n" \
    "v_exp_f32 v125, v125\n" \
    "v_exp_f32 v126, v126\n" \
    "v_exp_f32 v127, v127\n" \
    "v_add_u32 v193, 4096, v193\n" \
    "v_add_u32 v194, v195, v194\n" \
    "s_waitcnt vmcnt(0)\n"

#define ATT_C \
    "s_mov_b32 %[cnt], 2\n" \
    "1:\n" \
    "v_exp_f32 v64, v64\n" \
    "v_exp_f32 v65, v65\n" \
    "v_smfmac_f32_32x32x32_bf16 v[144:159], v[188:191], v[120:127], v192\n" \
    "ds_read_b128 v[184:187], v194 offset:1024\n" \
    "v_exp_f32 v66, v66\n" \
    "v_exp_f32 v67, v67\n" \
    "v_exp_f32 v68, v68\n" \
    "v_exp_f32 v69, v69\n" \
    "s_waitcnt lgkmcnt(1)\n" \
    "v_mfma_f32_32x32x16_bf16 v[80:95], v[172:175], v[160:163], 0\n" \
    "ds_read_b128 v[188:191], v194 offset:1536\n" \
    "v_exp_f32 v70, v70\n" \
    "v_exp_f32 v71, v71\n" \
    "v_exp_f32 v72, v72\n" \
    "v_exp_f32 v73, v73\n" \
    "v_smfmac_f32_32x32x32_bf16 v[128:143], v[176:179], v[64:71], v192\n" \
    "v_exp_f32 v74, v74\n" \
    "s_mov_b32 m0, %[swo]\n" \
    "v_exp_f32 v75, v75\n" \
    "v_exp_f32 v76, v76\n" \
    "global_load_lds_dwordx4 v236, %[wob] offset:0\n" \
    "v_exp_f32 v77, v77\n" \
    "v_exp_f32 v78, v78\n" \
    "v_exp_f32 v79, v79\n" \
    "v_exp_f32 v96, v96\n" \
    "v_exp_f32 v97, v97\n" \
    "v_smfmac_f32_32x32x32_bf16 v[128:143], v[180:183], v[72:79], v192\n" \
    "v_exp_f32 v98, v98\n" \
    "v_exp_f32 v99, v99\n" \
    "v_exp_f32 v100, v100\n" \
    "v_exp_f32 v101, v101\n" \
    "v_mfma_f32_32x32x16_bf16 v[112:127], v[172:175], v[164:167], 0\n" \
    "ds_read_b128 v[168:171], v193 offset:2048\n" \
    "v_exp_f32 v102, v102\n" \
    "v_exp_f32 v103, v103\n" \
    "v_exp_f32 v104, v104\n" \
    "v_exp_f32 v105, v105\n" \
    "v_smfmac_f32_32x32x32_bf16 v[144:159], v[176:179], v[96:103], v192\n" \
    "v_exp_f32 v106, v106\n" \
    "v_exp_f32 v107, v107\n" \
    "v_exp_f32 v108, v108\n" \
    "v_exp_f32 v109, v109\n" \
    "v_exp_f32 v110, v110\n" \
    "v_exp_f32 v111, v111\n" \
    "v_exp_f32 v80, v80\n" \
    "v_exp_f32 v81, v81\n" \
    "v_smfmac_f32_32x32x32_bf16 v[144:159], v[180:183], v[104:111], v192\n" \
    "ds_read_b128 v[176:179], v194 offset:2048\n" \
    "v_exp_f32 v82, v82\n" \
    "v_exp_f32 v83, v83\n" \
    "v_exp_f32 v84, v84\n" \
    "v_exp_f32 v85, v85\n" \
    "s_waitcnt lgkmcnt(1)\n" \
    "v_mfma_f32_32x32x16_bf16 v[64:79], v[168:171], v[160:163], 0\n" \
    "ds_read_b128 v[180:183], v194 offset:2560\n" \
    "v_exp_f32 v86, v86\n" \
    "v_exp_f32 v87, v87\n" \
    "v_exp_f32 v88, v88\n" \
    "v_exp_f32 v89, v89\n" \
    "v_smfmac_f32_32x32x32_bf16 v[128:143], v[184:187], v[80:87], v192\n" \
    "v_exp_f32 v90, v90\n" \
    "v_exp_f32 v91, v91\n" \
    "v_exp_f32 v92, v92\n" \
    "global_load_lds_dwordx4 v237, %[wob] offset:1024\n" \
    "v_exp_f32 v93, v93\n" \
    "v_exp_f32 v94, v94\n" \
    "v_exp_f32 v95, v95\n" \
    "v_exp_f32 v112, v112\n" \
    "v_exp_f32 v113, v113\n" \
    "v_smfmac_f32_32x32x32_bf16 v[128:143], v[188:191], v[88:95], v192\n" \
    "v_exp_f32 v114, v114\n" \
    "v_exp_f32 v115, v115\n" \
    "v_exp_f32 v116, v116\n" \
    "v_exp_f32 v117, v117\n" \
    "v_mfma_f32_32x32x16_bf16 v[96:111], v[168:171], v[164:167], 0\n" \
    "ds_read_b128 v[172:175], v193 offset:3072\n" \
    "v_exp_f32 v118, v118\n" \
    "v_exp_f32 v119, v119\n" \
    "v_exp_f32 v120, v120\n" \
    "v_exp_f32 v121, v121\n" \
    "v_smfmac_f32_32x32x32_bf16 v[144:159], v[184:187], v[112:119], v192\n" \
    "v_exp_f32 v122, v122\n" \
    "v_exp_f32 v123, v123\n" \
    "v_exp_f32 v124, v124\n" \
    "v_exp_f32 v125, v125\n" \
    "v_exp_f32 v126, v126\n" \
    "v_exp_f32 v127, v127\n" \
    "v_exp_f32 v64, v64\n" \
    "v_exp_f32 v65, v65\n" \
    "v_smfmac_f32_32x32x32_bf16 v[144:159], v[188:191], v[120:127], v192\n" \
    "ds_read_b128 v[184:187], v194 offset:3072\n" \
    "v_exp_f32 v66, v66\n" \
    "v_exp_f32 v67, v67\n" \
    "v_exp_f32 v68, v68\n" \
    "v_exp_f32 v69, v69\n" \
    "s_waitcnt lgkmcnt(1)\n" \
    "v_mfma_f32_32x32x16_bf16 v[80:95], v[172:175], v[160:163], 0\n" \
    "ds_read_b128 v[188:191], v194 offset:3584\n" \
    "v_exp_f32 v70, v70\n" \
    "v_exp_f32 v71, v71\n" \
    "v_exp_f32 v72, v72\n" \
    "v_exp_f32 v73, v73\n" \
    "v_smfmac_f32_32x32x32_bf16 v[128:143], v[176:179], v[64:71], v192\n" \
    "v_exp_f32 v74, v74\n" \
    "v_exp_f32 v75, v75\n" \
    "v_exp_f32 v76, v76\n" \
    "global_load_lds_dwordx4 v238, %[wob] offset:2048\n" \
    "v_exp_f32 v77, v77\n" \
    "v_exp_f32 v78, v78\n" \
    "v_exp_f32 v79, v79\n" \
    "v_exp_f32 v96, v96\n" \
    "v_exp_f32 v97, v97\n" \
    "v_smfmac_f32_32x32x32_bf16 v[128:143], v[180:183], v[72:79], v192\n" \
    "v_exp_f32 v98, v98\n" \
    "v_exp_f32 v99, v99\n" \
    "v_exp_f32 v100, v100\n" \
    "v_exp_f32 v101, v101\n" \
    "v_mfma_f32_32x32x16_bf16 v[112:127], v[172:175], v[164:167], 0\n" \
    "ds_read_b128 v[168:171], v193 offset:4096\n" \
    "v_exp_f32 v102, v102\n" \
    "v_exp_f32 v103, v103\n" \
    "v_exp_f32 v104, v104\n" \
    "v_exp_f32 v105, v105\n" \
    "v_smfmac_f32_32x32x32_bf16 v[144:159], v[176:179], v[96:103], v192\n" \
    "v_exp_f32 v106, v106\n" \
    "v_exp_f32 v107, v107\n" \
    "v_exp_f32 v108, v108\n" \
    "v_exp_f32 v109, v109\n" \
    "v_exp_f32 v110, v110\n" \
    "v_exp_f32 v111, v111\n" \
    "v_exp_f32 v80, v80\n" \
    "v_exp_f32 v81, v81\n" \
    "v_smfmac_f32_32x32x32_bf16 v[144:159], v[180:183], v[104:111], v192\n" \
    "ds_read_b128 v[176:179], v194 offset:4096\n" \
    "v_exp_f32 v82, v82\n" \
    "v_exp_f32 v83, v83\n" \
    "v_exp_f32 v84, v84\n" \
    "v_exp_f32 v85, v85\n" \
    "s_waitcnt lgkmcnt(1)\n" \
    "v_mfma_f32_32x32x16_bf16 v[64:79], v[168:171], v[160:163], 0\n" \
    "ds_read_b128 v[180:183], v194 offset:4608\n" \
    "v_exp_f32 v86, v86\n" \
    "v_exp_f32 v87, v87\n" \
    "v_exp_f32 v88, v88\n" \
    "v_exp_f32 v89, v89\n" \
    "v_smfmac_f32_32x32x32_bf16 v[128:143], v[184:187], v[80:87], v192\n" \
    "v_exp_f32 v90, v90\n" \
    "v_exp_f32 v91, v91\n" \
    "v_exp_f32 v92, v92\n" \
    "global_load_lds_dwordx4 v239, %[wob] offset:3072\n" \
    "v_exp_f32 v93, v93\n" \
    "v_exp_f32 v94, v94\n" \
    "v_exp_f32 v95, v95\n" \
    "v_exp_f32 v112, v112\n" \
    "v_exp_f32 v113, v113\n" \
    "v_smfmac_f32_32x32x32_bf16 v[128:143], v[188:191], v[88:95], v192\n" \
    "v_exp_f32 v114, v114\n" \
    "v_exp_f32 v115, v115\n" \
    "v_exp_f32 v116, v116\n" \
    "v_exp_f32 v117, v117\n" \
    "v_mfma_f32_32x32x16_bf16 v[96:111], v[168:171], v[164:167], 0\n" \
    "ds_read_b128 v[172:175], v193 offset:5120\n" \
    "v_exp_f32 v118, v118\n" \
    "v_exp_f32 v119, v119\n" \
    "v_exp_f32 v120, v120\n" \
    "v_exp_f32 v121, v121\n" \
    "v_smfmac_f32_32x32x32_bf16 v[144:159], v[184:187], v[112:119], v192\n" \
    "v_exp_f32 v122, v122\n" \
    "v_exp_f32 v123, v123\n" \
    "v_exp_f32 v124, v124\n" \
    "v_exp_f32 v125, v125\n" \
    "v_exp_f32 v126, v126\n" \
    "v_exp_f32 v127, v127\n" \
    "v_add_u32 v236, 0x8000, v236\n" \
    "v_add_u32 v237, 0x8000, v237\n" \
    "v_add_u32 v238, 0x8000, v238\n" \
    "v_add_u32 v239, 0x8000, v239\n" \
    "s_add_u32 %[swo], %[swo], 0x1000\n" \
    "v_add_u32 v193, 4096, v193\n" \
    "v_add_u32 v194, v195, v194\n" \
    "s_sub_u32 %[cnt], %[cnt], 1\n" \
    "s_cmp_lg_u32 %[cnt], 0\n" \
    "s_cbranch_scc1 1b\n" \
    "global_load_dword v220, v221, %[wsb] sc1\n" \
    "s_mov_b32 %[cnt], 2\n" \
    "2:\n" \
    "v_exp_f32 v64, v64\n" \
    "v_exp_f32 v65, v65\n" \
    "v_smfmac_f32_32x32x32_bf16 v[144:159], v[188:191], v[120:127], v192\n" \
    "ds_read_b128 v[184:187], v194 offset:1024\n" \
    "v_exp_f32 v66, v66\n" \
    "v_exp_f32 v67, v67\n" \
    "v_exp_f32 v68, v68\n" \
    "v_exp_f32 v69, v69\n" \
    "s_waitcnt lgkmcnt(1)\n" \
    "v_mfma_f32_32x32x16_bf16 v[80:95], v[172:175], v[160:163], 0\n" \
    "ds_read_b128 v[188:191], v194 offset:1536\n" \
    "v_exp_f32 v70, v70\n" \
    "v_exp_f32 v71, v71\n" \
    "v_exp_f32 v72, v72\n" \
    "v_exp_f32 v73, v73\n" \
    "v_smfmac_f32_32x32x32_bf16 v[128:143], v[176:179], v[64:71], v192\n" \
    "v_exp_f32 v74, v74\n" \
    "v_exp_f32 v75, v75\n" \
    "v_exp_f32 v76, v76\n" \
    "v_exp_f32 v77, v77\n" \
    "v_exp_f32 v78, v78\n" \
    "v_exp_f32 v79, v79\n" \
    "v_exp_f32 v96, v96\n" \
    "v_exp_f32 v97, v97\n" \
    "v_smfmac_f32_32x32x32_bf16 v[128:143], v[180:183], v[72:79], v192\n" \
    "v_exp_f32 v98, v98\n" \
    "v_exp_f32 v99, v99\n" \
    "v_exp_f32 v100, v100\n" \
    "v_exp_f32 v101, v101\n" \
    "v_mfma_f32_32x32x16_bf16 v[112:127], v[172:175], v[164:167], 0\n" \
    "ds_read_b128 v[168:171], v193 offset:2048\n" \
    "v_exp_f32 v102, v102\n" \
    "v_exp_f32 v103, v103\n" \
    "v_exp_f32 v104, v104\n" \
    "v_exp_f32 v105, v105\n" \
    "v_smfmac_f32_32x32x32_bf16 v[144:159], v[176:179], v[96:103], v192\n" \
    "v_exp_f32 v106, v106\n" \
    "v_exp_f32 v107, v107\n" \
    "v_exp_f32 v108, v108\n" \
    "v_exp_f32 v109, v109\n" \
    "v_exp_f32 v110, v110\n" \
    "v_exp_f32 v111, v111\n" \
    "v_exp_f32 v80, v80\n" \
    "v_exp_f32 v81, v81\n" \
    "v_smfmac_f32_32x32x32_bf16 v[144:159], v[180:183], v[104:111], v192\n" \
    "ds_read_b128 v[176:179], v194 offset:2048\n" \
    "v_exp_f32 v82, v82\n" \
    "v_exp_f32 v83, v83\n" \
    "v_exp_f32 v84, v84\n" \
    "v_exp_f32 v85, v85\n" \
    "s_waitcnt lgkmcnt(1)\n" \
    "v_mfma_f32_32x32x16_bf16 v[64:79], v[168:171], v[160:163], 0\n" \
    "ds_read_b128 v[180:183], v194 offset:2560\n" \
    "v_exp_f32 v86, v86\n" \
    "v_exp_f32 v87, v87\n" \
    "v_exp_f32 v88, v88\n" \
    "v_exp_f32 v89, v89\n" \
    "v_smfmac_f32_32x32x32_bf16 v[128:143], v[184:187], v[80:87], v192\n" \
    "v_exp_f32 v90, v90\n" \
    "v_exp_f32 v91, v91\n" \
    "v_exp_f32 v92, v92\n" \
    "v_exp_f32 v93, v93\n" \
    "v_exp_f32 v94, v94\n" \
    "v_exp_f32 v95, v95\n" \
    "v_exp_f32 v112, v112\n" \
    "v_exp_f32 v113, v113\n" \
    "v_smfmac_f32_32x32x32_bf16 v[128:143], v[188:191], v[88:95], v192\n" \
    "v_exp_f32 v114, v114\n" \
    "v_exp_f32 v115, v115\n" \
    "v_exp_f32 v116, v116\n" \
    "v_exp_f32 v117, v117\n" \
    "v_mfma_f32_32x32x16_bf16 v[96:111], v[168:171], v[164:167], 0\n" \
    "ds_read_b128 v[172:175], v193 offset:3072\n" \
    "v_exp_f32 v118, v118\n" \
    "v_exp_f32 v119, v119\n" \
    "v_exp_f32 v120, v120\n" \
    "v_exp_f32 v121, v121\n" \
    "v_smfmac_f32_32x32x32_bf16 v[144:159], v[184:187], v[112:119], v192\n" \
    "v_exp_f32 v122, v122\n" \
    "v_exp_f32 v123, v123\n" \
    "v_exp_f32 v124, v124\n" \
    "v_exp_f32 v125, v125\n" \
    "v_exp_f32 v126, v126\n" \
    "v_exp_f32 v127, v127\n" \
    "v_exp_f32 v64, v64\n" \
    "v_exp_f32 v65, v65\n" \
    "v_smfmac_f32_32x32x32_bf16 v[144:159], v[188:191], v[120:127], v192\n" \
    "ds_read_b128 v[184:187], v194 offset:3072\n" \
    "v_exp_f32 v66, v66\n" \
    "v_exp_f32 v67, v67\n" \
    "v_exp_f32 v68, v68\n" \
    "v_exp_f32 v69, v69\n" \
    "s_waitcnt lgkmcnt(1)\n" \
    "v_mfma_f32_32x32x16_bf16 v[80:95], v[172:175], v[160:163], 0\n" \
    "ds_read_b128 v[188:191], v194 offset:3584\n" \
    "v_exp_f32 v70, v70\n" \
    "v_exp_f32 v71, v71\n" \
    "v_exp_f32 v72, v72\n" \
    "v_exp_f32 v73, v73\n" \
    "v_smfmac_f32_32x32x32_bf16 v[128:143], v[176:179], v[64:71], v192\n" \
    "v_exp_f32 v74, v74\n" \
    "v_exp_f32 v75, v75\n" \
    "v_exp_f32 v76, v76\n" \
    "v_exp_f32 v77, v77\n" \
    "v_exp_f32 v78, v78\n" \
    "v_exp_f32 v79, v79\n" \
    "v_exp_f32 v96, v96\n" \
    "v_exp_f32 v97, v97\n" \
    "v_smfmac_f32_32x32x32_bf16 v[128:143], v[180:183], v[72:79], v192\n" \
    "v_exp_f32 v98, v98\n" \
    "v_exp_f32 v99, v99\n" \
    "v_exp_f32 v100, v100\n" \
    "v_exp_f32 v101, v101\n" \
    "v_mfma_f32_32x32x16_bf16 v[112:127], v[172:175], v[164:167], 0\n" \
    "ds_read_b128 v[168:171], v193 offset:4096\n" \
    "v_exp_f32 v102, v102\n" \
    "v_exp_f32 v103, v103\n" \
    "v_exp_f32 v104, v104\n" \
    "v_exp_f32 v105, v105\n" \
    "v_smfmac_f32_32x32x32_bf16 v[144:159], v[176:179], v[96:103], v192\n" \
    "v_exp_f32 v106, v106\n" \
    "v_exp_f32 v107, v107\n" \
    "v_exp_f32 v108, v108\n" \
    "v_exp_f32 v109, v109\n" \
    "v_exp_f32 v110, v110\n" \
    "v_exp_f32 v111, v111\n" \
    "v_exp_f32 v80, v80\n" \
    "v_exp_f32 v81, v81\n" \
    "v_smfmac_f32_32x32x32_bf16 v[144:159], v[180:183], v[104:111], v192\n" \
    "ds_read_b128 v[176:179], v194 offset:4096\n" \
    "v_exp_f32 v82, v82\n" \
    "v_exp_f32 v83, v83\n" \
    "v_exp_f32 v84, v84\n" \
    "v_exp_f32 v85, v85\n" \
    "s_waitcnt lgkmcnt(1)\n" \
    "v_mfma_f32_32x32x16_bf16 v[64:79], v[168:171], v[160:163], 0\n" \
    "ds_read_b128 v[180:183], v194 offset:4608\n" \
    "v_exp_f32 v86, v86\n" \
    "v_exp_f32 v87, v87\n" \
    "v_exp_f32 v88, v88\n" \
    "v_exp_f32 v89, v89\n" \
    "v_smfmac_f32_32x32x32_bf16 v[128:143], v[184:187], v[80:87], v192\n" \
    "v_exp_f32 v90, v90\n" \
    "v_exp_f32 v91, v91\n" \
    "v_exp_f32 v92, v92\n" \
    "v_exp_f32 v93, v93\n" \
    "v_exp_f32 v94, v94\n" \
    "v_exp_f32 v95, v95\n" \
    "v_exp_f32 v112, v112\n" \
    "v_exp_f32 v113, v113\n" \
    "v_smfmac_f32_32x32x32_bf16 v[128:143], v[188:191], v[88:95], v192\n" \
    "v_exp_f32 v114, v114\n" \
    "v_exp_f32 v115, v115\n" \
    "v_exp_f32 v116, v116\n" \
    "v_exp_f32 v117, v117\n" \
    "v_mfma_f32_32x32x16_bf16 v[96:111], v[168:171], v[164:167], 0\n" \
    "ds_read_b128 v[172:175], v193 offset:5120\n" \
    "v_exp_f32 v118, v118\n" \
    "v_exp_f32 v119, v119\n" \
    "v_exp_f32 v120, v120\n" \
    "v_exp_f32 v121, v121\n" \
    "v_smfmac_f32_32x32x32_bf16 v[144:159], v[184:187], v[112:119], v192\n" \
    "v_exp_f32 v122, v122\n" \
    "v_exp_f32 v123, v123\n" \
    "v_exp_f32 v124, v124\n" \
    "v_exp_f32 v125, v125\n" \
    "v_exp_f32 v126, v126\n" \
    "v_exp_f32 v127, v127\n" \
    "s_cmp_eq_u32 %[cnt], 2\n" \
    "s_cbranch_scc0 8f\n" \
    "s_setprio 0\n" \
    "8:\n" \
    "v_add_u32 v193, 4096, v193\n" \
    "v_add_u32 v194, v195, v194\n" \
    "s_sub_u32 %[cnt], %[cnt], 1\n" \
    "s_cmp_lg_u32 %[cnt], 0\n" \
    "s_cbranch_scc1 2b\n" \
    "s_nop 1\n" \
    "v_smfmac_f32_32x32x32_bf16 v[144:159], v[188:191], v[120:127], v192\n" \
    "s_setprio 0\n" \
    "s_waitcnt vmcnt(0)\n" \
    "s_nop 7\n" \
    "s_nop 7\n" \
    "s_nop 3\n" \
    "s_waitcnt lgkmcnt(0)\n"

__device__ __forceinline__ void slow_block(const char* ws, u32x4 q, int lane, float* o, float& lsum) {
    const u32 koff = WS_K + lane * 16;
    const u32 r32 = lane & 31;
    float m = -3.0e38f;
    for (int t = 0; t < 64; ++t) {
        u32x4 kf;
        GLD128C(kf, ws, koff + t * 1024, 0);
        asm volatile("s_waitcnt vmcnt(0)" : "+v"(kf) :: "memory");
        f32x16 s = {0};
        s = __builtin_amdgcn_mfma_f32_32x32x16_bf16(asbf(kf), asbf(q), s, 0, 0, 0);
#pragma unroll
        for (int i = 0; i < 16; ++i) m = fmaxf(m, s[i]);
    }
    {
        auto sw = __builtin_amdgcn_permlane32_swap(__float_as_uint(m), __float_as_uint(m), false, false);
        m = fmaxf(__uint_as_float(sw[0]), __uint_as_float(sw[1]));
    }
    f32x16 acc = {0};
    const u32 voff = WS_V + (lane >> 5) * 256 + (lane & 15) * 16;
    for (int t = 0; t < 64; ++t) {
        u32x4 kf, v0, v1;
        GLD128C(kf, ws, koff + t * 1024, 0);
        GLD128C(v0, ws, voff + t * 1024, 0);
        GLD128C(v1, ws, voff + t * 1024, 512);
        asm volatile("s_waitcnt vmcnt(0)" : "+v"(kf), "+v"(v0), "+v"(v1) :: "memory");
        if (r32 >= 16) { v0 = (u32x4){0x3f803f80u, 0x3f803f80u, 0x3f803f80u, 0x3f803f80u}; v1 = v0; }
        f32x16 s = {0};
        s = __builtin_amdgcn_mfma_f32_32x32x16_bf16(asbf(kf), asbf(q), s, 0, 0, 0);
        u32 p[8];
#pragma unroll
        for (int i = 0; i < 8; ++i)
            p[i] = pk(__builtin_amdgcn_exp2f(s[2 * i] - m), __builtin_amdgcn_exp2f(s[2 * i + 1] - m));
#pragma unroll
        for (int st = 0; st < 2; ++st) {
            u32x4 vv = st ? v1 : v0;
            auto a = __builtin_amdgcn_permlane32_swap(vv[0], vv[2], false, false);
            auto c = __builtin_amdgcn_permlane32_swap(vv[1], vv[3], false, false);
            u32x4 af = {a[0], c[0], a[1], c[1]};
            u32x4 bf = {p[4 * st], p[4 * st + 1], p[4 * st + 2], p[4 * st + 3]};
            acc = __builtin_amdgcn_mfma_f32_32x32x16_bf16(asbf(af), asbf(bf), acc, 0, 0, 0);
        }
    }
#pragma unroll
    for (int i = 0; i < 8; ++i) o[i] = acc[i];
    lsum = acc[8];
}

#define PRIO_MODE 1
#define PRIO_START() do { if (w >= 4) __builtin_amdgcn_s_setprio(1); } while (0)
#define PRIO_END() __builtin_amdgcn_s_setprio(0)
__global__ void __launch_bounds__(512, 2)
fused_kernel(const u16* x_, const u16* wq_, const u16* wk_, const u16* wv_, const u16* wo_, const u16* wr_, const u16* wg_,
             const u16* wu_, const u16* wd_, const u16* gw_, u16* out_, char* ws_)
{
    u32x16 ka0; u32x8 ka1;
    {
        const void* kp = (const void*)__builtin_amdgcn_kernarg_segment_ptr();
        asm volatile("s_load_dwordx16 %0, %2, 0\n\ts_load_dwordx8 %1, %2, 64\n\ts_waitcnt lgkmcnt(0)"
                     : "=&s"(ka0), "=&s"(ka1) : "s"(kp) : "memory");
    }
#define KP(v, i) ((u64)v[2 * (i)] | ((u64)v[2 * (i) + 1] << 32))
    const u16* x  = (const u16*)KP(ka0, 0); const u16* wq = (const u16*)KP(ka0, 1);
    const u16* wk = (const u16*)KP(ka0, 2); const u16* wv = (const u16*)KP(ka0, 3);
    const u16* wo = (const u16*)KP(ka0, 4); const u16* wr = (const u16*)KP(ka0, 5);
    const u16* wg = (const u16*)KP(ka0, 6); const u16* wu = (const u16*)KP(ka0, 7);
    const u16* wd = (const u16*)KP(ka1, 0); const u16* gw = (const u16*)KP(ka1, 1);
    u16* out = (u16*)KP(ka1, 2); char* ws = (char*)KP(ka1, 3);

    __shared__ __attribute__((aligned(1024))) char smem[L_END];
    LDSP char* lds = (LDSP char*)smem;
    const u32 lb = (u32)(size_t)lds;
    const int tid = threadIdx.x;
    const int lane = tid & 63;
    const int w = __builtin_amdgcn_readfirstlane(tid >> 6);
    const int b = blockIdx.x;
    const int xcd = b & 7, jj = b >> 3;
    const int tb = xcd * 4 + (jj >> 3), hg = jj & 7;
    const int g = lane >> 4, l15 = lane & 15, h = lane >> 5, l31 = lane & 31;
    const bool isK = hg < 4;
    const bool ffn = jj < 28;
    const int fj = xcd * 28 + min(jj, 27);
    STAMP(1); STAMPC(18);

    const int th = w & 1, hp = (w >> 1) & 1, blk = w >> 2;
    const int T0 = tb * 64 + (hg & 3) * 16;
    u32x4 xf0, xf1, vf0, vf1;
    {
        const u16* wsrc = isK ? wk : wv;
#pragma unroll
        for (int i = 0; i < 2; ++i) {
            u32 slot = 64 * i + lane, pos = slot >> 3, B = pos ^ ((pos >> 2) & 1);
            u32 row = 4 * B + ((slot >> 1) & 3);
            dma16(wsrc, (64 * w + row) * 32 + (slot & 1) * 16, lb + L_W + w * 2048 + i * 1024);
        }
        u32 xo = (T0 + l15) * 1024 + (64 * w + 16 * g) * 2;
        GLD128(xf0, x, xo, 0);
        GLD128(xf1, x, xo, 16);
        const u16* vec = isK ? wr : gw;
        u32 vo = (64 * w + 16 * g) * 2;
        GLD128(vf0, vec, vo, 0);
        GLD128(vf1, vec, vo, 16);
    }
    u32 epo;
    { u32 z = 0; GLD32C(epo, ws, z, 0); }
    asm volatile("s_barrier" ::: "memory");
    if (w < 7) {
#pragma unroll
        for (int i = 0; i < 19; ++i) {
            const int p = i < 18 ? w + 7 * i : __builtin_amdgcn_readfirstlane(min(w + 7 * i, 127));
            if (i < 9 || (i == 9 && w == 0)) {
                dma16(x, (tb * 64 + p) * 1024 + ((lane ^ (p & 15)) << 4), lb + L_K + p * 1024);
            } else {
                const int G = p - 64;
                dma16(wq, (8 * G + ((lane >> 2) & 7)) * 1024 + hg * 128 + (lane >> 5) * 64 + (lane & 3) * 16, lb + L_V + G * 1024);
            }
        }
    }
    if (tid < 256) { u32 o1 = (tid & 15) == 0 ? 0x3f803f80u : 0u; ST128(L_ONE + tid * 16, ((u32x4){o1, o1, o1, o1})); }
    asm volatile("s_cmp_eq_u32 %[w], 7\n\ts_cbranch_scc1 1f\n\ts_waitcnt vmcnt(20)\n\ts_branch 2f\n1:\n\ts_waitcnt vmcnt(1)\n2:"
                 : "+v"(xf0), "+v"(xf1), "+v"(vf0), "+v"(vf1) : [w] "s"(w) : "memory", "scc");
    FSTAMP(2);
    {
        const u32 ta = L_W + w * 2048 + l15 * 8;
        const u32 gx = g & 1;
        u32x4 wf0 = cat(trread(lds, ta + ((4 * g + 0) ^ gx) * 128), trread(lds, ta + ((4 * g + 1) ^ gx) * 128));
        u32x4 wf1 = cat(trread(lds, ta + ((4 * g + 2) ^ gx) * 128), trread(lds, ta + ((4 * g + 3) ^ gx) * 128));
        f32x4 acc = {0, 0, 0, 0}, dacc = {0, 0, 0, 0};
        if (isK) {
            acc = __builtin_amdgcn_mfma_f32_16x16x32_bf16(asbf(wf0), asbf(xf0), acc, 0, 0, 0);
            acc = __builtin_amdgcn_mfma_f32_16x16x32_bf16(asbf(wf1), asbf(xf1), acc, 0, 0, 0);
        } else {
            acc = __builtin_amdgcn_mfma_f32_16x16x32_bf16(asbf(xf0), asbf(wf0), acc, 0, 0, 0);
            acc = __builtin_amdgcn_mfma_f32_16x16x32_bf16(asbf(xf1), asbf(wf1), acc, 0, 0, 0);
        }
        dacc = __builtin_amdgcn_mfma_f32_16x16x32_bf16(asbf(vf0), asbf(xf0), dacc, 0, 0, 0);
        dacc = __builtin_amdgcn_mfma_f32_16x16x32_bf16(asbf(vf1), asbf(xf1), dacc, 0, 0, 0);
        ST128(L_P1 + w * 1024 + lane * 16, __builtin_bit_cast(u32x4, acc));
        if (g == 0) STF(L_PD + w * 64 + l15 * 4, dacc[0]);
    }
    BAR();
    FSTAMP(3);
    asm volatile("s_cmp_eq_u32 %[w], 7\n\ts_cbranch_scc1 1f\n\ts_waitcnt vmcnt(19)\n\ts_branch 2f\n1:\n\ts_waitcnt vmcnt(0)\n2:"
                 : "+v"(epo) : [w] "s"(w) : "memory", "scc");
    const u32 epoch = (u32)__builtin_amdgcn_readfirstlane((int)epo) + 1u;
    if (w == 7) {
        f32x4 ps[8]; float pd[8];
#pragma unroll
        for (int i = 0; i < 8; ++i) {
            ps[i] = __builtin_bit_cast(f32x4, LD128(L_P1 + i * 1024 + lane * 16));
            pd[i] = LDF(L_PD + i * 64 + l15 * 4);
        }
        __builtin_amdgcn_sched_barrier(0);
        const f32x4 s = ((ps[0] + ps[1]) + (ps[2] + ps[3])) + ((ps[4] + ps[5]) + (ps[6] + ps[7]));
        const float d = ((pd[0] + pd[1]) + (pd[2] + pd[3])) + ((pd[4] + pd[5]) + (pd[6] + pd[7]));
        u32x2 pv = {pk(s[0], s[1]), pk(s[2], s[3])};
        const int tile = T0 >> 5, sub = (T0 >> 4) & 1;
        u32 key = 0;
        if (isK) {
            u32 off = WS_K + tile * 1024 + ((g & 1) * 32 + sub * 16 + l15) * 16 + (g >> 1) * 8;
            GST64C(ws, off, pv);
            u32 rb = pk(d, d) & 0xffffu;
            u32 ord = (rb & 0x8000u) ? (~rb & 0xffffu) : (rb | 0x8000u);
            key = wave_max_u32((ord << 16) | (0xffffu - (u32)(T0 + l15)));
        } else {
            u32 off = WS_V + tile * 1024 + sub * 512 + (g >> 1) * 256 + l15 * 16 + (g & 1) * 8;
            GST64C(ws, off, pv);
            if (g == 0) { u32 go = WS_G + (T0 + l15) * 4; u32 dv = __float_as_uint(d); GST32C(ws, go, dv); }
        }
        VMWAIT(0);
        if (lane < 8) { u32x2 fl = {key, epoch}; u32 fo = WS_F1 + lane * 32768 + (tile * 4 + (isK ? 0 : 2) + sub) * 128; GST64C(ws, fo, fl); }
        FSTAMP(4);
    } else {
        VMWAIT(0);
    }
    BAR();
    FSTAMP(5);

    f32x16 qacc = {0};
    if (w < 4) {
        const int qh = w >> 1;
        const int tok = th * 32 + l31;
        u32 abase = L_V + h * 1024 + qh * 512 + ((l15 >> 2) * 64) + ((lane >> 4) & 1) * 32 + (l15 & 3) * 8;
        asm volatile("" : "+v"(abase));
        u32 bb[8];
#pragma unroll
        for (int j = 0; j < 8; ++j) { bb[j] = L_K + tok * 1024 + (((2 * j + h) ^ (tok & 15)) << 4); asm volatile("" : "+v"(bb[j])); }
#pragma unroll
        for (int s = 0; s < 32; ++s) {
            u32x4 af = cat(trread(lds, abase + s * 2048), trread(lds, abase + s * 2048 + 256));
            u32x4 bf = LD128(bb[s & 7] + (s >> 3) * 256);
            qacc = __builtin_amdgcn_mfma_f32_32x32x16_bf16(asbf(af), asbf(bf), qacc, 0, 0, 0);
        }
        FSTAMP(6);
    } else {
        const int j = w - 4;
        const u32 fo = WS_F1 + xcd * 32768 + (j * 64 + lane) * 128;
        u32x2 f;
        u32 spins = 0;
        do {
            asm volatile("global_load_dwordx2 %0, %1, %2 sc1\n\ts_waitcnt vmcnt(0)" : "=&v"(f) : "v"(fo), "s"(ws) : "memory");
        } while ((f[1] != epoch) && ++spins <= SPIN_MAX);
        const u32 key = wave_max_u32(f[0]);
        if (lane == 0) ST32(L_CT + j * 4, key);
        FSTAMP(7);
    }
    BAR();
    FSTAMP(9);
    u32 kvo = (w & 1) * 65536 + ((w >> 1) & 1) * 32768;
    if (w >= 4) {
        dma16c4(ws + WS_K, kvo + lane * 16, lb + L_K + kvo);
        dma16c4(ws + WS_K, kvo + 4096 + lane * 16, lb + L_K + kvo + 4096);
        dma16c2(ws + WS_K, kvo + 8192 + lane * 16, lb + L_K + kvo + 8192);
    } else {
        const float qs = 0.25f * 1.4426950408889634f;
        u32x4 qx, qy;
#pragma unroll
        for (int i = 0; i < 4; ++i) {
            u32 a = pk(qacc[2 * i], qacc[2 * i + 1]), c = pk(qacc[8 + 2 * i], qacc[8 + 2 * i + 1]);
            qx[i] = pk(lo16(a) * qs, hi16(a) * qs);
            qy[i] = pk(lo16(c) * qs, hi16(c) * qs);
        }
        ST128(L_P1 + w * 2048 + lane * 16, qx);
        ST128(L_P1 + w * 2048 + 1024 + lane * 16, qy);
    }
    const u32 key = max(max(LD32(L_CT), LD32(L_CT + 4)), max(LD32(L_CT + 8), LD32(L_CT + 12)));
    const int sel = __builtin_amdgcn_readfirstlane(0xffff - (key & 0xffff)) & 2047;
    u32x8 EXA; u32x4 EXB, WOA;
    u32 skv = lb + L_K + kvo + (10 + 4 * (w >> 2)) * 1024, sg = lb + L_W + w * 2048;
    const u32 skv3 = lb + L_K + kvo + (26 + 2 * (w >> 2)) * 1024;
    u32 swo = lb + ((w & 4) ? L_V : L_K) + ((w >> 1) & 1) * 32768 + (w & 1) * 8192;
    {
        EXA[0] = kvo + (10 + 4 * (w >> 2)) * 1024 + lane * 16;
        EXA[1] = kvo + (26 + 2 * (w >> 2)) * 1024 + lane * 16;
        const u32 pos = lane >> 3, B = pos ^ ((pos >> 2) & 1);
        const u32 row = 64 * w + 4 * B + ((lane >> 1) & 3);
        const u64 g0 = (u64)((lane & 1) ? wu : wg) + (u64)(row * 3584 + fj * 16), g1 = g0 + 32 * 3584;
        EXA[2] = (u32)g0; EXA[3] = (u32)(g0 >> 32); EXA[4] = (u32)g1; EXA[5] = (u32)(g1 >> 32);
        EXA[6] = sel * 1024 + (64 * w + 16 * g) * 2;
        EXA[7] = fj * 8192 + (128 * (w & 3) + 2 * lane) * 2;
        const u32 ta = lb + L_W + w * 2048 + l15 * 8, gx = g & 1;
        EXB[0] = ta + (4 * g + gx) * 128;
        EXB[1] = ta + (4 * g + (gx ^ 1)) * 128;
        EXB[2] = lb + (l15 == 0 ? L_FP + w * 64 + g * 16 : L_P1 + w * 1024 + lane * 16);
        EXB[3] = 0;
#pragma unroll
        for (int i = 0; i < 4; ++i) {
            const u32 r = 64 * w + 8 * i + (lane >> 3), slot = lane & 7;
            const u32 f = ((r >> 1) & 1) | ((i & 1) << 1);
            const u32 c = (((slot >> 1) ^ f) << 1) | (slot & 1);
            WOA[i] = r * 1024 + hg * 128 + c * 16 - i * 1024;
        }
    }
    u32x2 PZA;
    {
        const int head = hg * 4 + hp * 2 + blk, tl = th * 32 + l31;
        PZA[0] = WS_O + ((tb * 4 + (tl >> 4)) * 16 + (head >> 1)) * 1024 + (((head & 1) * 2 + h) * 16 + (tl & 15)) * 16;
        PZA[1] = (ffn && w < 4) ? WS_Y + fj * 2048 + w * 512 + l31 * 16 : PZA[0];
    }
    const int hf = w >> 2;
    u32 ka = lb + L_K + hf * 32768 + lane * 16;
    const bool vlane = l31 < 16;
    u32 va = vlane ? (lb + L_V + hf * 32768 + h * 256 + l15 * 16) : (lb + L_ONE - 1024 + h * 256 + l15 * 16);
    u32 vinc = vlane ? 4096u : 0u;
    u32 va0 = vlane ? va : va + 1024;
    u32 qa = lb + L_P1 + (w & 3) * 2048 + lane * 16;
    asm volatile("" : "+{v[196:203]}"(EXA), "+{v[228:231]}"(EXB), "+{v[236:239]}"(WOA), "+{v[220:221]}"(PZA), "+{v193}"(ka), "+{v194}"(va), "+{v195}"(vinc), "+v"(va0), "+v"(qa),
                 "+s"(skv), "+s"(sg), "+s"(swo));
    if (w >= 4) VMWAIT(0);
    BAR();
    STAMP(10); STAMPC(19);
    if (b == 0 && tid == 256) { u32 z = 0; GST32C(ws, z, epoch); }

    u32x4 qx, qy;
    f32x16 SX0, SX1, SY0, SY1, OX, OY;
    u32x4 K0, K1, V00, V01, V10, V11;
    u32 idx = 0xDDDDu;
    {
        asm volatile(
            "ds_read_b128 v[160:163], %[qa]\n"
            "ds_read_b128 v[164:167], %[qa] offset:1024\n"
            "ds_read_b128 v[168:171], v193\n"
            "ds_read_b128 v[172:175], v193 offset:1024\n"
            "ds_read_b128 v[176:179], %[va0]\n"
            "ds_read_b128 v[180:183], %[va0] offset:512\n"
            "v_mov_b32 v128, 0\n v_mov_b32 v129, 0\n v_mov_b32 v130, 0\n v_mov_b32 v131, 0\n"
            "v_mov_b32 v132, 0\n v_mov_b32 v133, 0\n v_mov_b32 v134, 0\n v_mov_b32 v135, 0\n"
            "v_mov_b32 v136, 0\n v_mov_b32 v137, 0\n v_mov_b32 v138, 0\n v_mov_b32 v139, 0\n"
            "v_mov_b32 v140, 0\n v_mov_b32 v141, 0\n v_mov_b32 v142, 0\n v_mov_b32 v143, 0\n"
            "v_mov_b32 v144, 0\n v_mov_b32 v145, 0\n v_mov_b32 v146, 0\n v_mov_b32 v147, 0\n"
            "v_mov_b32 v148, 0\n v_mov_b32 v149, 0\n v_mov_b32 v150, 0\n v_mov_b32 v151, 0\n"
            "v_mov_b32 v152, 0\n v_mov_b32 v153, 0\n v_mov_b32 v154, 0\n v_mov_b32 v155, 0\n"
            "v_mov_b32 v156, 0\n v_mov_b32 v157, 0\n v_mov_b32 v158, 0\n v_mov_b32 v159, 0\n"
            "v_mov_b32 v120, 0\n v_mov_b32 v121, 0\n v_mov_b32 v122, 0\n v_mov_b32 v123, 0\n"
            "v_mov_b32 v124, 0\n v_mov_b32 v125, 0\n v_mov_b32 v126, 0\n v_mov_b32 v127, 0\n"
            "v_mov_b32 v188, 0\n v_mov_b32 v189, 0\n v_mov_b32 v190, 0\n v_mov_b32 v191, 0\n"
            "s_waitcnt lgkmcnt(0)\n"
            "v_mfma_f32_32x32x16_bf16 v[64:79], v[168:171], v[160:163], 0\n"
            "v_mfma_f32_32x32x16_bf16 v[96:111], v[168:171], v[164:167], 0\n"
            "s_nop 7\n s_nop 7\n"
            : "={v[64:79]}"(SX0), "={v[80:95]}"(SX1), "={v[96:111]}"(SY0), "={v[112:127]}"(SY1),
              "={v[128:143]}"(OX), "={v[144:159]}"(OY),
              "={v[168:171]}"(K0), "={v[172:175]}"(K1), "={v[176:179]}"(V00), "={v[180:183]}"(V01),
              "={v[184:187]}"(V10), "={v[188:191]}"(V11), "=&{v[160:163]}"(qx), "=&{v[164:167]}"(qy)
            : "{v193}"(ka), [va0] "v"(va0), [qa] "v"(qa)
            : "memory");
    }
#define ATT_OPS \
            : "+{v[64:79]}"(SX0), "+{v[80:95]}"(SX1), "+{v[96:111]}"(SY0), "+{v[112:127]}"(SY1), \
              "+{v[128:143]}"(OX), "+{v[144:159]}"(OY), \
              "+{v[168:171]}"(K0), "+{v[172:175]}"(K1), "+{v[176:179]}"(V00), "+{v[180:183]}"(V01), \
              "+{v[184:187]}"(V10), "+{v[188:191]}"(V11), "+{v193}"(ka), "+{v194}"(va), [cnt] "=&s"(cnt_)
#define ATT_INS : "{v[160:163]}"(qx), "{v[164:167]}"(qy), "{v192}"(idx), "{v195}"(vinc)
    u32 cnt_;
    const char* kvb = ws + WS_K;
    PRIO_START();
    asm volatile(ATT_A1 ATT_OPS, "+{v[196:203]}"(EXA), "+{v[228:231]}"(EXB), "+{v[236:239]}"(WOA), "+{v[220:221]}"(PZA), [skv] "+s"(skv), [sg] "+s"(sg)
                 ATT_INS, [kvb] "s"(kvb), [wsb] "s"(ws) : "memory", "scc", "v232", "v233", "v234", "v235");
    LSTAMP(2);
    asm volatile(ATT_A2 ATT_OPS, "+{v[196:203]}"(EXA), "+{v[228:231]}"(EXB), "+{v[236:239]}"(WOA), "+{v[220:221]}"(PZA), [skv] "+s"(skv), [sg] "+s"(sg)
                 ATT_INS, [kvb] "s"(kvb), [wsb] "s"(ws) : "memory", "scc", "v232", "v233", "v234", "v235");
    LSTAMP(3);
    VMWAIT(5);
    LSTAMP(4);
    BAR();
    LSTAMP(8);
    if (tid == 448) { u32 fo = WS_F2 + b * 4; GST32C(ws, fo, epoch); }
    PRIO_START();
    u32x16 EXD;
    asm volatile(ATT_B ATT_OPS, "+{v[196:203]}"(EXA), "=&{v[204:219]}"(EXD), "+{v[236:239]}"(WOA)
                 ATT_INS, "{v[228:231]}"(EXB), [kvb] "s"(kvb), [skv3] "s"(skv3), [xb] "s"(x), [wdb] "s"(wd)
                 : "memory", "scc", "v220", "v221", "v222", "v223", "v224", "v225", "v226", "v227", "v232", "v233", "v234", "v235");
    LSTAMP(9);
    BAR();
    LSTAMP(11);
    if (ffn && w < 4) {
        float sum = LDF(L_FP + l15 * 4);
#pragma unroll
        for (int i = 1; i < 8; ++i) sum += LDF(L_FP + i * 64 + l15 * 4);
        sum = bfr(sum);
        float up = __shfl(sum, (lane & 7) + 8);
        float gt = __shfl(sum, lane & 7);
        float hv = bfr(bfr(gt * sigm(gt)) * up);
        u32 hb = __float_as_uint(hv);
        float y0 = 0.f, y1 = 0.f;
#pragma unroll
        for (int i = 0; i < 8; ++i) {
            const float hs = __uint_as_float((u32)__builtin_amdgcn_readlane(hb, i));
            y0 += hs * lo16(EXD[8 + i]);
            y1 += hs * hi16(EXD[8 + i]);
        }
        u32x2 gr = {__float_as_uint(y0), __float_as_uint(y1)};
        gr[0] = gr[0] == POISON ? 0x7fc00000u : gr[0];
        gr[1] = gr[1] == POISON ? 0x7fc00000u : gr[1];
        u32 yo = WS_Y + fj * 2048 + (128 * w + 2 * lane) * 4;
        GST64C(ws, yo, gr);
    }
    LSTAMP(22);
    PRIO_START();
    u32 f2v; const u32 f2a = WS_F2 + (32 * w + l31) * 4;
    asm volatile(ATT_C ATT_OPS, "+{v[236:239]}"(WOA), "=&{v220}"(f2v), [swo] "+s"(swo) ATT_INS, "{v221}"(f2a), [wob] "s"(wo), [wsb] "s"(ws) : "memory", "scc");
    const int tq = w & 3, kh = w >> 2;
    const bool owner = (sel >> 6) == tb;
    int lane_b = lane;
    asm volatile("" : "+v"(lane_b));
    {
    const int lane = lane_b;
    const int g = lane >> 4, l15 = lane & 15, h = lane >> 5, l31 = lane & 31;
    const int tokg = tb * 64 + tq * 16 + l15;
    const int ncol = hg * 64 + kh * 32 + (g & 1) * 16 + (g >> 1) * 8;
    u32 oslot;
    {
        const int head = hg * 4 + hp * 2 + blk, tl = th * 32 + l31;
        oslot = WS_O + ((tb * 4 + (tl >> 4)) * 16 + (head >> 1)) * 1024 + (((head & 1) * 2 + h) * 16 + (tl & 15)) * 16;
    }
    STAMP(12); STAMPC(20);
    {
        u32 spins = 0;
        while (!__all(f2v == epoch) && ++spins <= SPIN_MAX) {
            const u32 a = WS_F2 + (32 * w + l31) * 4;
            asm volatile("global_load_dword %0, %1, %2 sc1\n\ts_waitcnt vmcnt(0)" : "=&v"(f2v) : "v"(a), "s"(ws) : "memory");
        }
    }
    u32x4 xe, Y0, Y1, Y2, Y3, Y4, Y5, Y6; u32 gl;
    const u32 yo0 = WS_Y + (28 * w + g) * 2048 + hg * 256 + l15 * 16;
    {
        u32 xo = tokg * 1024 + ncol * 2;
        GLD128(xe, x, xo, 0);
        u32 go = WS_G + tokg * 4;
        GLD32C(gl, ws, go, 0);
    }

    {
        f32x16& mine = blk ? OY : OX;
        f32x16& give = blk ? OX : OY;
        const u32 cw = L_W + w * 2304, cr = L_W + (w ^ 4) * 2304;
        ST128(cw + lane * 16, ((u32x4){__float_as_uint(give[0]), __float_as_uint(give[1]), __float_as_uint(give[2]), __float_as_uint(give[3])}));
        ST128(cw + 1024 + lane * 16, ((u32x4){__float_as_uint(give[4]), __float_as_uint(give[5]), __float_as_uint(give[6]), __float_as_uint(give[7])}));
        STF(cw + 2048 + lane * 4, give[8]);
        BAR();
        f32x4 p0 = __builtin_bit_cast(f32x4, LD128(cr + lane * 16));
        f32x4 p1 = __builtin_bit_cast(f32x4, LD128(cr + 1024 + lane * 16));
        float l = LDF(cr + 2048 + lane * 4) + mine[8];
        float o[8];
#pragma unroll
        for (int i = 0; i < 4; ++i) { o[i] = mine[i] + p0[i]; o[4 + i] = mine[4 + i] + p1[i]; }
        {
            auto sw = __builtin_amdgcn_permlane32_swap(__float_as_uint(l), __float_as_uint(l), false, false);
            l = __uint_as_float(sw[0]);
        }
        float amax = fmaxf(fmaxf(fmaxf(fabsf(o[0]), fabsf(o[1])), fmaxf(fabsf(o[2]), fabsf(o[3]))),
                           fmaxf(fmaxf(fabsf(o[4]), fabsf(o[5])), fmaxf(fabsf(o[6]), fabsf(o[7]))));
        bool safe = (l >= 8.6736174e-19f) && (l <= 1.2676506e30f) && (amax <= 3.0e38f);
        if (!__all(safe)) {
            slow_block(ws, blk ? qy : qx, lane, o, l);
            auto sw = __builtin_amdgcn_permlane32_swap(__float_as_uint(l), __float_as_uint(l), false, false);
            l = __uint_as_float(sw[0]);
        }
        const float ri = __builtin_amdgcn_rcpf(l);
        u32 A0 = pk(o[0] * ri, o[1] * ri), A1 = pk(o[2] * ri, o[3] * ri);
        u32 B0 = pk(o[4] * ri, o[5] * ri), B1 = pk(o[6] * ri, o[7] * ri);
        auto s0 = __builtin_amdgcn_permlane32_swap(A0, B0, false, false);
        auto s1 = __builtin_amdgcn_permlane32_swap(A1, B1, false, false);
        u32x4 ov = {s0[0], s1[0], s0[1], s1[1]};
#pragma unroll
        for (int i = 0; i < 4; ++i) ov[i] = ov[i] == POISON ? 0x7fc07fc0u : ov[i];
        GST128C(ws, oslot, ov);
    }
    {
        u32 yo = yo0;
        asm volatile("s_cmp_lg_u32 %[stb], %[tb]\n\ts_cbranch_scc1 3f\n"
                     "global_load_dwordx4 %0, %[yo], %[ws] sc1\n v_add_u32 %[yo], 0x2000, %[yo]\n"
                     "global_load_dwordx4 %1, %[yo], %[ws] sc1\n v_add_u32 %[yo], 0x2000, %[yo]\n"
                     "global_load_dwordx4 %2, %[yo], %[ws] sc1\n v_add_u32 %[yo], 0x2000, %[yo]\n"
                     "global_load_dwordx4 %3, %[yo], %[ws] sc1\n v_add_u32 %[yo], 0x2000, %[yo]\n"
                     "global_load_dwordx4 %4, %[yo], %[ws] sc1\n v_add_u32 %[yo], 0x2000, %[yo]\n"
                     "global_load_dwordx4 %5, %[yo], %[ws] sc1\n v_add_u32 %[yo], 0x2000, %[yo]\n"
                     "global_load_dwordx4 %6, %[yo], %[ws] sc1\n3:"
                     : "=&v"(Y0), "=&v"(Y1), "=&v"(Y2), "=&v"(Y3), "=&v"(Y4), "=&v"(Y5), "=&v"(Y6), [yo] "+v"(yo)
                     : [stb] "s"(sel >> 6), [tb] "s"(tb), [ws] "s"(ws) : "memory", "scc");
    }
    STAMP(13);

    u32x4 af[4][8];
    {
        const u32 f = ((l15 >> 3) & 1) | ((g & 1) << 1);
        const u32 rb = (kh ? L_V : L_K) + (8 * g + (l15 >> 2)) * 128 + (l15 & 3) * 8;
#pragma unroll
        for (int nb = 0; nb < 4; ++nb) {
            u32 a = rb + ((nb ^ f) * 32);
            asm volatile("" : "+v"(a));
#pragma unroll
            for (int st = 0; st < 8; ++st) {
                const u32 o_ = (st >> 2) * 32768 + (st & 3) * 4096;
                af[nb][st] = cat(trread(lds, a + o_), trread(lds, a + o_ + 512));
            }
        }
    }
    STAMP(14);
    asm volatile("s_cmp_lg_u32 %[stb], %[tb]\n\ts_cbranch_scc1 3f\n\ts_waitcnt vmcnt(0)\n3:\n\ts_waitcnt vmcnt(1)"
                 : "+v"(xe), "+v"(gl), "+v"(Y0), "+v"(Y1), "+v"(Y2), "+v"(Y3), "+v"(Y4), "+v"(Y5), "+v"(Y6)
                 : [stb] "s"(sel >> 6), [tb] "s"(tb) : "memory", "scc");
    if (owner) {
        u32 spins = 0;
#define YMX(v) max(max(v[0], v[1]), max(v[2], v[3]))
        while (!__all(max(max(max(YMX(Y0), YMX(Y1)), max(YMX(Y2), YMX(Y3))), max(max(YMX(Y4), YMX(Y5)), YMX(Y6))) != POISON) && ++spins <= SPIN_MAX) {
            u32 yo = yo0;
            asm volatile("global_load_dwordx4 %0, %[yo], %[ws] sc1\n v_add_u32 %[yo], 0x2000, %[yo]\n"
                         "global_load_dwordx4 %1, %[yo], %[ws] sc1\n v_add_u32 %[yo], 0x2000, %[yo]\n"
                         "global_load_dwordx4 %2, %[yo], %[ws] sc1\n v_add_u32 %[yo], 0x2000, %[yo]\n"
                         "global_load_dwordx4 %3, %[yo], %[ws] sc1\n v_add_u32 %[yo], 0x2000, %[yo]\n"
                         "global_load_dwordx4 %4, %[yo], %[ws] sc1\n v_add_u32 %[yo], 0x2000, %[yo]\n"
                         "global_load_dwordx4 %5, %[yo], %[ws] sc1\n v_add_u32 %[yo], 0x2000, %[yo]\n"
                         "global_load_dwordx4 %6, %[yo], %[ws] sc1\n s_waitcnt vmcnt(0)"
                         : "=&v"(Y0), "=&v"(Y1), "=&v"(Y2), "=&v"(Y3), "=&v"(Y4), "=&v"(Y5), "=&v"(Y6), [yo] "+v"(yo)
                         : [ws] "s"(ws) : "memory");
        }
        f32x4 ys;
#pragma unroll
        for (int i = 0; i < 4; ++i) {
            float v = ((__uint_as_float(Y0[i]) + __uint_as_float(Y1[i])) + (__uint_as_float(Y2[i]) + __uint_as_float(Y3[i])))
                    + ((__uint_as_float(Y4[i]) + __uint_as_float(Y5[i])) + __uint_as_float(Y6[i]));
            auto a = __builtin_amdgcn_permlane32_swap(__float_as_uint(v), __float_as_uint(v), false, false);
            v = __uint_as_float(a[0]) + __uint_as_float(a[1]);
            auto c = __builtin_amdgcn_permlane16_swap(__float_as_uint(v), __float_as_uint(v), false, false);
            ys[i] = __uint_as_float(c[0]) + __uint_as_float(c[1]);
        }
        if (g == 0) ST128(L_YP + w * 256 + l15 * 16, __builtin_bit_cast(u32x4, ys));
    }
    u32x4 of0, of1, of2, of3, of4, of5, of6, of7;
    {
        const u32 oo = WS_O + ((tb * 4 + tq) * 16 + 8 * kh) * 1024 + lane * 16, o2 = oo + 4096;
        u32 spins = 0;
        while (true) {
            asm volatile("global_load_dwordx4 %0, %8, %10 sc1\n\tglobal_load_dwordx4 %1, %8, %10 offset:1024 sc1\n\t"
                         "global_load_dwordx4 %2, %8, %10 offset:2048 sc1\n\tglobal_load_dwordx4 %3, %8, %10 offset:3072 sc1\n\t"
                         "global_load_dwordx4 %4, %9, %10 sc1\n\tglobal_load_dwordx4 %5, %9, %10 offset:1024 sc1\n\t"
                         "global_load_dwordx4 %6, %9, %10 offset:2048 sc1\n\tglobal_load_dwordx4 %7, %9, %10 offset:3072 sc1\n\t"
                         "s_waitcnt vmcnt(0)"
                         : "=&v"(of0), "=&v"(of1), "=&v"(of2), "=&v"(of3), "=&v"(of4), "=&v"(of5), "=&v"(of6), "=&v"(of7)
                         : "v"(oo), "v"(o2), "s"(ws) : "memory");
            u32 mx = 0;
#define OMX(v) mx = max(max(mx, max(v[0], v[1])), max(v[2], v[3]));
            OMX(of0) OMX(of1) OMX(of2) OMX(of3) OMX(of4) OMX(of5) OMX(of6) OMX(of7)
            if (__all(mx != POISON) || ++spins > SPIN_MAX) break;
        }
    }
    STAMP(15);
    f32x4 acc[4] = {{0, 0, 0, 0}, {0, 0, 0, 0}, {0, 0, 0, 0}, {0, 0, 0, 0}};
#define OSTEP(OF, ST) \
    _Pragma("unroll") for (int nb = 0; nb < 4; ++nb) acc[nb] = __builtin_amdgcn_mfma_f32_16x16x32_bf16(asbf(af[nb][ST]), asbf(OF), acc[nb], 0, 0, 0);
    OSTEP(of0, 0) OSTEP(of1, 1) OSTEP(of2, 2) OSTEP(of3, 3) OSTEP(of4, 4) OSTEP(of5, 5) OSTEP(of6, 6) OSTEP(of7, 7)
    STAMP(16);
    {
        f32x4 k0 = kh ? acc[2] : acc[0], k1 = kh ? acc[3] : acc[1];
        f32x4 g0 = kh ? acc[0] : acc[2], g1 = kh ? acc[1] : acc[3];
        ST128(L_K + 16384 + w * 2048 + lane * 16, __builtin_bit_cast(u32x4, g0));
        ST128(L_K + 16384 + w * 2048 + 1024 + lane * 16, __builtin_bit_cast(u32x4, g1));
        BAR();
        k0 += __builtin_bit_cast(f32x4, LD128(L_K + 16384 + (w ^ 4) * 2048 + lane * 16));
        k1 += __builtin_bit_cast(f32x4, LD128(L_K + 16384 + (w ^ 4) * 2048 + 1024 + lane * 16));
        float a[8];
#pragma unroll
        for (int i = 0; i < 4; ++i) {
            auto sw = __builtin_amdgcn_permlane16_swap(__float_as_uint(k0[i]), __float_as_uint(k1[i]), false, false);
            a[i] = __uint_as_float(sw[0]); a[4 + i] = __uint_as_float(sw[1]);
        }
        const float gt = bfr(sigm(bfr(__uint_as_float(gl))));
        const float og = bfr(1.0f - gt);
        float bb[8];
#pragma unroll
        for (int i = 0; i < 4; ++i) { bb[2 * i] = lo16(xe[i]); bb[2 * i + 1] = hi16(xe[i]); }
        if (owner && tokg == sel) {
            const u32 rord = key >> 16;
            const float rsel = __uint_as_float(((rord & 0x8000u) ? (rord & 0x7fffu) : (~rord & 0xffffu)) << 16);
            const float sg = bfr(sigm(rsel));
            const int c0 = kh * 32 + (g & 1) * 16 + (g >> 1) * 8;
#pragma unroll
            for (int i = 0; i < 8; ++i) {
                float y = 0.f;
#pragma unroll
                for (int p = 0; p < 8; ++p) y += LDF(L_YP + p * 256 + (c0 + i) * 4);
                bb[i] = bfr(bb[i] + bfr(bfr(y) * sg));
            }
        }
        u32x4 ov;
#pragma unroll
        for (int i = 0; i < 4; ++i)
            ov[i] = pk(bfr(gt * bfr(a[2 * i])) + bfr(og * bb[2 * i]), bfr(gt * bfr(a[2 * i + 1])) + bfr(og * bb[2 * i + 1]));
        u32 oo_ = tokg * 1024 + ncol * 2;
        GST128(out, oo_, ov);
    }
    STAMP(17); STAMPC(21);
    BAR();
    if (tid == 0 && (b >> 3) == 5) {
#define RD(wv, i) LD32(L_CT + 64 + ((wv) * 24 + (i)) * 4)
        u32 t1 = RD(0, 1), c0 = RD(0, 19), c4 = RD(4, 19);
        printf("STMP b %d t0 %u att0 %u att %u/%u end %u | g0e %u/%u g1p %u/%u g1e %u/%u B1 %u/%u g3e %u/%u B2 %u/%u 1b %u/%u att %u/%u\n", b, t1 % 100000, RD(0,10) - t1, RD(0,12) - t1, RD(4,12) - t1, RD(4,17) - t1,
 RD(0,2) - c0, RD(4,2) - c4, RD(0,3) - c0, RD(4,3) - c4,
               RD(0,4) - c0, RD(4,4) - c4, RD(0,8) - c0, RD(4,8) - c4, RD(0,9) - c0, RD(4,9) - c4, RD(0,11) - c0, RD(4,11) - c4, RD(0,22) - c0, RD(4,22) - c4, RD(0,20) - c0, RD(4,20) - c4);
    }
    }
}

extern "C" void kernel_launch(void* const* d_in, const int* in_sizes, int n_in, void* d_out, int out_size,
                              void* d_ws, size_t ws_size, hipStream_t stream) {
    (void)in_sizes; (void)n_in; (void)out_size; (void)ws_size;
    fused_kernel<<<256, 512, 0, stream>>>((const u16*)d_in[0], (const u16*)d_in[1], (const u16*)d_in[2], (const u16*)d_in[3],
                                          (const u16*)d_in[4], (const u16*)d_in[6], (const u16*)d_in[7], (const u16*)d_in[8],
                                          (const u16*)d_in[9], (const u16*)d_in[10], (u16*)d_out, (char*)d_ws);
}
```

```cpp
#include <hip/hip_runtime.h>
#include <stdint.h>
#include <stdio.h>
#define DBG_STAMP
#define V2N

typedef unsigned int u32;
typedef unsigned long long u64;
typedef unsigned short u16;
typedef u32 u32x2 __attribute__((ext_vector_type(2)));
typedef u32 u32x4 __attribute__((ext_vector_type(4)));
typedef u32 u32x8 __attribute__((ext_vector_type(8)));
typedef u32 u32x16 __attribute__((ext_vector_type(16)));
typedef float f32x2 __attribute__((ext_vector_type(2)));
typedef float f32x4 __attribute__((ext_vector_type(4)));
typedef float f32x16 __attribute__((ext_vector_type(16)));
typedef __bf16 bf16x2 __attribute__((ext_vector_type(2)));
typedef __bf16 bf16x8 __attribute__((ext_vector_type(8)));
#define LDSP __attribute__((address_space(3)))

constexpr u32 WS_E   = 0;
constexpr u32 WS_F1  = 4096;
constexpr u32 WS_G   = 24576;
constexpr u32 WS_K   = 65536;
constexpr u32 WS_V   = 131072;
constexpr u32 WS_O   = 262144;
constexpr u32 WS_Y   = 2621440;
constexpr u32 WS_X   = 3145728;
constexpr u32 WS_DBG = 4194304;

constexpr u32 L_K   = 0;
constexpr u32 L_V   = 65536;
constexpr u32 L_ONE = 131072;
constexpr u32 L_W   = 135168;
constexpr u32 L_M   = 151552;
constexpr u32 L_P1  = L_M;
constexpr u32 L_PD  = L_M + 8192;
constexpr u32 L_FP  = L_M + 8704;
constexpr u32 L_YP  = L_M + 9216;
constexpr u32 L_CT  = L_M + 11264;
constexpr u32 L_END = 163840;

constexpr u32 SPIN_MAX = 1u << 22;
constexpr u32 POISON = 0xffffffffu;

#define LD128(a) (*(const LDSP u32x4*)(lds + (a)))
#define ST128(a, v) (*(LDSP u32x4*)(lds + (a)) = (v))
#define LD32(a) (*(const LDSP u32*)(lds + (a)))
#define ST32(a, v) (*(LDSP u32*)(lds + (a)) = (v))
#define LDF(a) (*(const LDSP float*)(lds + (a)))
#define STF(a, v) (*(LDSP float*)(lds + (a)) = (v))

__device__ __forceinline__ u32x2 trread(LDSP char* lds, u32 a) {
    typedef short v4s __attribute__((ext_vector_type(4)));
    v4s r = __builtin_amdgcn_ds_read_tr16_b64_v4i16((LDSP v4s*)(lds + a));
    return __builtin_bit_cast(u32x2, r);
}
__device__ __forceinline__ bf16x8 asbf(u32x4 v) { return __builtin_bit_cast(bf16x8, v); }
__device__ __forceinline__ u32x4 cat(u32x2 a, u32x2 b) { u32x4 r = {a[0], a[1], b[0], b[1]}; return r; }
__device__ __forceinline__ u32 pk(float a, float b) {
    f32x2 f = {a, b};
    return __builtin_bit_cast(u32, __builtin_convertvector(f, bf16x2));
}
__device__ __forceinline__ float bfr(float a) {
    return __uint_as_float(pk(a, a) & 0xffff0000u);
}
__device__ __forceinline__ float lo16(u32 v) { return __uint_as_float(v << 16); }
__device__ __forceinline__ float hi16(u32 v) { return __uint_as_float(v & 0xffff0000u); }
__device__ __forceinline__ float sigm(float z) { return __builtin_amdgcn_rcpf(1.0f + __builtin_amdgcn_exp2f(-1.4426950408889634f * z)); }

__device__ __forceinline__ void dma16(const void* base, u32 voff, u32 ldsaddr) {
    asm volatile("s_mov_b32 m0, %0\n\ts_nop 0\n\tglobal_load_lds_dwordx4 %1, %2" :: "s"(ldsaddr), "v"(voff), "s"(base) : "memory");
}
__device__ __forceinline__ void dma16c4(const void* base, u32 voff, u32 ldsaddr) {
    asm volatile("s_mov_b32 m0, %0\n\ts_nop 0\n\tglobal_load_lds_dwordx4 %1, %2 sc1\n\tglobal_load_lds_dwordx4 %1, %2 offset:1024 sc1\n\t"
                 "global_load_lds_dwordx4 %1, %2 offset:2048 sc1\n\tglobal_load_lds_dwordx4 %1, %2 offset:3072 sc1"
                 :: "s"(ldsaddr), "v"(voff), "s"(base) : "memory");
}
__device__ __forceinline__ void dma16c3(const void* base, u32 voff, u32 ldsaddr) {
    asm volatile("s_mov_b32 m0, %0\n\ts_nop 0\n\tglobal_load_lds_dwordx4 %1, %2 sc1\n\tglobal_load_lds_dwordx4 %1, %2 offset:1024 sc1\n\t"
                 "global_load_lds_dwordx4 %1, %2 offset:2048 sc1"
                 :: "s"(ldsaddr), "v"(voff), "s"(base) : "memory");
}
__device__ __forceinline__ void dma16c2(const void* base, u32 voff, u32 ldsaddr) {
    asm volatile("s_mov_b32 m0, %0\n\ts_nop 0\n\tglobal_load_lds_dwordx4 %1, %2 sc1\n\tglobal_load_lds_dwordx4 %1, %2 offset:1024 sc1"
                 :: "s"(ldsaddr), "v"(voff), "s"(base) : "memory");
}
__device__ __forceinline__ void dma16c(const void* base, u32 voff, u32 ldsaddr) {
    asm volatile("s_mov_b32 m0, %0\n\ts_nop 0\n\tglobal_load_lds_dwordx4 %1, %2 sc1" :: "s"(ldsaddr), "v"(voff), "s"(base) : "memory");
}
#define GLD128(dst, base, voff, OFF)  asm volatile("global_load_dwordx4 %0, %1, %2 offset:" #OFF : "=v"(dst) : "v"(voff), "s"(base) : "memory")
#define GLD128C(dst, base, voff, OFF) asm volatile("global_load_dwordx4 %0, %1, %2 offset:" #OFF " sc1" : "=v"(dst) : "v"(voff), "s"(base) : "memory")
#define GLD64C(dst, base, voff, OFF)  asm volatile("global_load_dwordx2 %0, %1, %2 offset:" #OFF " sc1" : "=v"(dst) : "v"(voff), "s"(base) : "memory")
#define GLD32C(dst, base, voff, OFF)  asm volatile("global_load_dword %0, %1, %2 offset:" #OFF " sc1" : "=v"(dst) : "v"(voff), "s"(base) : "memory")
#define GLD32(dst, base, voff, OFF)   asm volatile("global_load_dword %0, %1, %2 offset:" #OFF : "=v"(dst) : "v"(voff), "s"(base) : "memory")
#define GLD16(dst, base, voff, OFF)   asm volatile("global_load_ushort %0, %1, %2 offset:" #OFF : "=v"(dst) : "v"(voff), "s"(base) : "memory")
#define GST128(base, voff, val)  asm volatile("global_store_dwordx4 %0, %1, %2\n\ts_nop 1" :: "v"(voff), "v"(val), "s"(base) : "memory")
#define GST128C(base, voff, val) asm volatile("global_store_dwordx4 %0, %1, %2 sc1\n\ts_nop 1" :: "v"(voff), "v"(val), "s"(base) : "memory")
#define GST64C(base, voff, val)  asm volatile("global_store_dwordx2 %0, %1, %2 sc1\n\ts_nop 0" :: "v"(voff), "v"(val), "s"(base) : "memory")
#define GST32C(base, voff, val)  asm volatile("global_store_dword %0, %1, %2 sc1" :: "v"(voff), "v"(val), "s"(base) : "memory")
#define VMWAIT(N) asm volatile("s_waitcnt vmcnt(" #N ")" ::: "memory")
#define BAR() asm volatile("s_waitcnt lgkmcnt(0)\n\ts_barrier" ::: "memory")

#define STAMP(i) do { u32 t_ = (u32)__builtin_amdgcn_s_memrealtime(); if (lane == 0) ST32(L_CT + 64 + (w * 24 + (i)) * 4, t_); } while (0)
#define STAMPC(i) do { u32 t_ = (u32)__builtin_amdgcn_s_memtime(); if (lane == 0) ST32(L_CT + 64 + (w * 24 + (i)) * 4, t_); } while (0)
#define STAMPDUMP() do { if (lane < 24) ((u32*)(ws + WS_DBG))[(blockIdx.x * 8 + w) * 32 + lane] = LD32(L_CT + 64 + (w * 24 + lane) * 4); } while (0)
#define FSTAMP(i) STAMP(i)
#define LSTAMP(i)

__device__ __forceinline__ u32 wave_max_u32(u32 v) {
    v = max(v, (u32)__builtin_amdgcn_update_dpp(0, (int)v, 0x111, 0xf, 0xf, false));
    v = max(v, (u32)__builtin_amdgcn_update_dpp(0, (int)v, 0x112, 0xf, 0xf, false));
    v = max(v, (u32)__builtin_amdgcn_update_dpp(0, (int)v, 0x114, 0xf, 0xf, false));
    v = max(v, (u32)__builtin_amdgcn_update_dpp(0, (int)v, 0x118, 0xf, 0xf, false));
    v = max(v, (u32)__builtin_amdgcn_update_dpp(0, (int)v, 0x142, 0xa, 0xf, false));
    v = max(v, (u32)__builtin_amdgcn_update_dpp(0, (int)v, 0x143, 0xc, 0xf, false));
    return (u32)__builtin_amdgcn_readlane((int)v, 63);
}
#define ATT_A \
    "s_mov_b32 %[cnt], 2\n" \
    "1:\n" \
    "v_exp_f32 v64, v64\n" \
    "v_exp_f32 v65, v65\n" \
    "v_smfmac_f32_32x32x32_bf16 v[144:159], v[188:191], v[120:127], v192\n" \
    "ds_read_b128 v[184:187], v194 offset:1024\n" \
    "v_exp_f32 v66, v66\n" \
    "v_exp_f32 v67, v67\n" \
    "v_exp_f32 v68, v68\n" \
    "v_exp_f32 v69, v69\n" \
    "s_waitcnt lgkmcnt(1)\n" \
    "v_mfma_f32_32x32x16_bf16 v[80:95], v[172:175], v[160:163], 0\n" \
    "ds_read_b128 v[188:191], v194 offset:1536\n" \
    "v_exp_f32 v70, v70\n" \
    "v_exp_f32 v71, v71\n" \
    "v_exp_f32 v72, v72\n" \
    "v_exp_f32 v73, v73\n" \
    "v_smfmac_f32_32x32x32_bf16 v[128:143], v[176:179], v[64:71], v192\n" \
    "v_exp_f32 v74, v74\n" \
    "s_mov_b32 m0, %[skv]\n" \
    "v_exp_f32 v75, v75\n" \
    "v_exp_f32 v76, v76\n" \
    "global_load_lds_dwordx4 v196, %[kvb] offset:0 sc1\n" \
    "v_exp_f32 v77, v77\n" \
    "v_exp_f32 v78, v78\n" \
    "v_exp_f32 v79, v79\n" \
    "v_exp_f32 v96, v96\n" \
    "v_exp_f32 v97, v97\n" \
    "v_smfmac_f32_32x32x32_bf16 v[128:143], v[180:183], v[72:79], v192\n" \
    "v_exp_f32 v98, v98\n" \
    "v_exp_f32 v99, v99\n" \
    "v_exp_f32 v100, v100\n" \
    "v_exp_f32 v101, v101\n" \
    "v_mfma_f32_32x32x16_bf16 v[112:127], v[172:175], v[164:167], 0\n" \
    "ds_read_b128 v[168:171], v193 offset:2048\n" \
    "v_exp_f32 v102, v102\n" \
    "v_exp_f32 v103, v103\n" \
    "v_exp_f32 v104, v104\n" \
    "v_exp_f32 v105, v105\n" \
    "v_smfmac_f32_32x32x32_bf16 v[144:159], v[176:179], v[96:103], v192\n" \
    "v_exp_f32 v106, v106\n" \
    "v_exp_f32 v107, v107\n" \
    "v_exp_f32 v108, v108\n" \
    "v_exp_f32 v109, v109\n" \
    "v_exp_f32 v110, v110\n" \
    "v_exp_f32 v111, v111\n" \
    "v_exp_f32 v80, v80\n" \
    "v_exp_f32 v81, v81\n" \
    "v_smfmac_f32_32x32x32_bf16 v[144:159], v[180:183], v[104:111], v192\n" \
    "ds_read_b128 v[176:179], v194 offset:2048\n" \
    "v_exp_f32 v82, v82\n" \
    "v_exp_f32 v83, v83\n" \
    "v_exp_f32 v84, v84\n" \
    "v_exp_f32 v85, v85\n" \
    "s_waitcnt lgkmcnt(1)\n" \
    "v_mfma_f32_32x32x16_bf16 v[64:79], v[168:171], v[160:163], 0\n" \
    "ds_read_b128 v[180:183], v194 offset:2560\n" \
    "v_exp_f32 v86, v86\n" \
    "v_exp_f32 v87, v87\n" \
    "v_exp_f32 v88, v88\n" \
    "v_exp_f32 v89, v89\n" \
    "v_smfmac_f32_32x32x32_bf16 v[128:143], v[184:187], v[80:87], v192\n" \
    "v_exp_f32 v90, v90\n" \
    "s_mov_b32 m0, %[skv]\n" \
    "v_exp_f32 v91, v91\n" \
    "v_exp_f32 v92, v92\n" \
    "global_load_lds_dwordx4 v196, %[kvb] offset:1024 sc1\n" \
    "v_exp_f32 v93, v93\n" \
    "v_exp_f32 v94, v94\n" \
    "v_exp_f32 v95, v95\n" \
    "v_exp_f32 v112, v112\n" \
    "v_exp_f32 v113, v113\n" \
    "v_smfmac_f32_32x32x32_bf16 v[128:143], v[188:191], v[88:95], v192\n" \
    "v_exp_f32 v114, v114\n" \
    "v_exp_f32 v115, v115\n" \
    "v_exp_f32 v116, v116\n" \
    "v_exp_f32 v117, v117\n" \
    "v_mfma_f32_32x32x16_bf16 v[96:111], v[168:171], v[164:167], 0\n" \
    "ds_read_b128 v[172:175], v193 offset:3072\n" \
    "v_exp_f32 v118, v118\n" \
    "v_exp_f32 v119, v119\n" \
    "v_exp_f32 v120, v120\n" \
    "v_exp_f32 v121, v121\n" \
    "v_smfmac_f32_32x32x32_bf16 v[144:159], v[184:187], v[112:119], v192\n" \
    "v_exp_f32 v122, v122\n" \
    "s_mov_b32 m0, %[sg]\n" \
    "v_exp_f32 v123, v123\n" \
    "v_exp_f32 v124, v124\n" \
    "global_load_lds_dwordx4 v[198:199], off\n" \
    "v_exp_f32 v125, v125\n" \
    "v_exp_f32 v126, v126\n" \
    "v_exp_f32 v127, v127\n" \
    "s_cmp_eq_u32 %[cnt], 1\n" \
    "s_cbranch_scc0 8f\n" \
    "s_setprio 0\n" \
    "8:\n" \
    "v_exp_f32 v64, v64\n" \
    "v_exp_f32 v65, v65\n" \
    "v_smfmac_f32_32x32x32_bf16 v[144:159], v[188:191], v[120:127], v192\n" \
    "ds_read_b128 v[184:187], v194 offset:3072\n" \
    "v_exp_f32 v66, v66\n" \
    "v_exp_f32 v67, v67\n" \
    "v_exp_f32 v68, v68\n" \
    "v_exp_f32 v69, v69\n" \
    "s_waitcnt lgkmcnt(1)\n" \
    "v_mfma_f32_32x32x16_bf16 v[80:95], v[172:175], v[160:163], 0\n" \
    "ds_read_b128 v[188:191], v194 offset:3584\n" \
    "v_exp_f32 v70, v70\n" \
    "v_exp_f32 v71, v71\n" \
    "v_exp_f32 v72, v72\n" \
    "v_exp_f32 v73, v73\n" \
    "v_smfmac_f32_32x32x32_bf16 v[128:143], v[176:179], v[64:71], v192\n" \
    "v_exp_f32 v74, v74\n" \
    "s_mov_b32 m0, %[skv]\n" \
    "v_exp_f32 v75, v75\n" \
    "v_exp_f32 v76, v76\n" \
    "global_load_lds_dwordx4 v196, %[kvb] offset:2048 sc1\n" \
    "v_exp_f32 v77, v77\n" \
    "v_exp_f32 v78, v78\n" \
    "v_exp_f32 v79, v79\n" \
    "v_exp_f32 v96, v96\n" \
    "v_exp_f32 v97, v97\n" \
    "v_smfmac_f32_32x32x32_bf16 v[128:143], v[180:183], v[72:79], v192\n" \
    "v_exp_f32 v98, v98\n" \
    "v_exp_f32 v99, v99\n" \
    "v_exp_f32 v100, v100\n" \
    "v_exp_f32 v101, v101\n" \
    "v_mfma_f32_32x32x16_bf16 v[112:127], v[172:175], v[164:167], 0\n" \
    "ds_read_b128 v[168:171], v193 offset:4096\n" \
    "v_exp_f32 v102, v102\n" \
    "v_exp_f32 v103, v103\n" \
    "v_exp_f32 v104, v104\n" \
    "v_exp_f32 v105, v105\n" \
    "v_smfmac_f32_32x32x32_bf16 v[144:159], v[176:179], v[96:103], v192\n" \
    "v_exp_f32 v106, v106\n" \
    "v_exp_f32 v107, v107\n" \
    "v_exp_f32 v108, v108\n" \
    "v_exp_f32 v109, v109\n" \
    "v_exp_f32 v110, v110\n" \
    "v_exp_f32 v111, v111\n" \
    "v_exp_f32 v80, v80\n" \
    "v_exp_f32 v81, v81\n" \
    "v_smfmac_f32_32x32x32_bf16 v[144:159], v[180:183], v[104:111], v192\n" \
    "ds_read_b128 v[176:179], v194 offset:4096\n" \
    "v_exp_f32 v82, v82\n" \
    "v_exp_f32 v83, v83\n" \
    "v_exp_f32 v84, v84\n" \
    "v_exp_f32 v85, v85\n" \
    "s_waitcnt lgkmcnt(1)\n" \
    "v_mfma_f32_32x32x16_bf16 v[64:79], v[168:171], v[160:163], 0\n" \
    "ds_read_b128 v[180:183], v194 offset:4608\n" \
    "v_exp_f32 v86, v86\n" \
    "v_exp_f32 v87, v87\n" \
    "v_exp_f32 v88, v88\n" \
    "v_exp_f32 v89, v89\n" \
    "v_smfmac_f32_32x32x32_bf16 v[128:143], v[184:187], v[80:87], v192\n" \
    "v_exp_f32 v90, v90\n" \
    "s_mov_b32 m0, %[skv]\n" \
    "v_exp_f32 v91, v91\n" \
    "v_exp_f32 v92, v92\n" \
    "global_load_lds_dwordx4 v196, %[kvb] offset:3072 sc1\n" \
    "v_exp_f32 v93, v93\n" \
    "v_exp_f32 v94, v94\n" \
    "v_exp_f32 v95, v95\n" \
    "v_exp_f32 v112, v112\n" \
    "v_exp_f32 v113, v113\n" \
    "v_smfmac_f32_32x32x32_bf16 v[128:143], v[188:191], v[88:95], v192\n" \
    "v_exp_f32 v114, v114\n" \
    "v_exp_f32 v115, v115\n" \
    "v_exp_f32 v116, v116\n" \
    "v_exp_f32 v117, v117\n" \
    "v_mfma_f32_32x32x16_bf16 v[96:111], v[168:171], v[164:167], 0\n" \
    "ds_read_b128 v[172:175], v193 offset:5120\n" \
    "v_exp_f32 v118, v118\n" \
    "v_exp_f32 v119, v119\n" \
    "v_exp_f32 v120, v120\n" \
    "v_exp_f32 v121, v121\n" \
    "v_smfmac_f32_32x32x32_bf16 v[144:159], v[184:187], v[112:119], v192\n" \
    "v_exp_f32 v122, v122\n" \
    "v_exp_f32 v123, v123\n" \
    "v_exp_f32 v124, v124\n" \
    "v_exp_f32 v125, v125\n" \
    "v_exp_f32 v126, v126\n" \
    "v_exp_f32 v127, v127\n" \
    "v_add_u32 v196, 0x2000, v196\n" \
    "v_mov_b32 v198, v200\n" \
    "v_mov_b32 v199, v201\n" \
    "s_add_u32 %[skv], %[skv], 0x2000\n" \
    "s_add_u32 %[sg], %[sg], 0x400\n" \
    "v_add_u32 v193, 4096, v193\n" \
    "v_add_u32 v194, v195, v194\n" \
    "s_sub_u32 %[cnt], %[cnt], 1\n" \
    "s_cmp_lg_u32 %[cnt], 0\n" \
    "s_cbranch_scc1 1b\n" \
    "s_waitcnt vmcnt(5)\n"

#define ATT_B \
    "v_exp_f32 v64, v64\n" \
    "v_exp_f32 v65, v65\n" \
    "v_smfmac_f32_32x32x32_bf16 v[144:159], v[188:191], v[120:127], v192\n" \
    "ds_read_b128 v[184:187], v194 offset:1024\n" \
    "v_exp_f32 v66, v66\n" \
    "v_exp_f32 v67, v67\n" \
    "v_exp_f32 v68, v68\n" \
    "v_exp_f32 v69, v69\n" \
    "s_waitcnt lgkmcnt(1)\n" \
    "v_mfma_f32_32x32x16_bf16 v[80:95], v[172:175], v[160:163], 0\n" \
    "ds_read_b128 v[188:191], v194 offset:1536\n" \
    "v_exp_f32 v70, v70\n" \
    "v_exp_f32 v71, v71\n" \
    "v_exp_f32 v72, v72\n" \
    "v_exp_f32 v73, v73\n" \
    "v_smfmac_f32_32x32x32_bf16 v[128:143], v[176:179], v[64:71], v192\n" \
    "v_exp_f32 v74, v74\n" \
    "s_mov_b32 m0, %[skv3]\n" \
    "v_exp_f32 v75, v75\n" \
    "v_exp_f32 v76, v76\n" \
    "global_load_dwordx4 v[204:207], v202, %[xb]\n" \
    "v_exp_f32 v77, v77\n" \
    "v_exp_f32 v78, v78\n" \
    "v_exp_f32 v79, v79\n" \
    "v_exp_f32 v96, v96\n" \
    "v_exp_f32 v97, v97\n" \
    "v_smfmac_f32_32x32x32_bf16 v[128:143], v[180:183], v[72:79], v192\n" \
    "v_exp_f32 v98, v98\n" \
    "v_exp_f32 v99, v99\n" \
    "v_exp_f32 v100, v100\n" \
    "v_exp_f32 v101, v101\n" \
    "v_mfma_f32_32x32x16_bf16 v[112:127], v[172:175], v[164:167], 0\n" \
    "ds_read_b128 v[168:171], v193 offset:2048\n" \
    "v_exp_f32 v102, v102\n" \
    "v_exp_f32 v103, v103\n" \
    "v_exp_f32 v104, v104\n" \
    "v_exp_f32 v105, v105\n" \
    "v_smfmac_f32_32x32x32_bf16 v[144:159], v[176:179], v[96:103], v192\n" \
    "v_exp_f32 v106, v106\n" \
    "v_exp_f32 v107, v107\n" \
    "v_exp_f32 v108, v108\n" \
    "global_load_dwordx4 v[208:211], v202, %[xb] offset:16\n" \
    "v_exp_f32 v109, v109\n" \
    "v_exp_f32 v110, v110\n" \
    "v_exp_f32 v111, v111\n" \
    "v_exp_f32 v80, v80\n" \
    "v_exp_f32 v81, v81\n" \
    "v_smfmac_f32_32x32x32_bf16 v[144:159], v[180:183], v[104:111], v192\n" \
    "ds_read_b128 v[176:179], v194 offset:2048\n" \
    "v_exp_f32 v82, v82\n" \
    "v_exp_f32 v83, v83\n" \
    "v_exp_f32 v84, v84\n" \
    "v_exp_f32 v85, v85\n" \
    "s_waitcnt lgkmcnt(1)\n" \
    "v_mfma_f32_32x32x16_bf16 v[64:79], v[168:171], v[160:163], 0\n" \
    "ds_read_b128 v[180:183], v194 offset:2560\n" \
    "v_exp_f32 v86, v86\n" \
    "v_exp_f32 v87, v87\n" \
    "v_exp_f32 v88, v88\n" \
    "v_exp_f32 v89, v89\n" \
    "v_smfmac_f32_32x32x32_bf16 v[128:143], v[184:187], v[80:87], v192\n" \
    "v_exp_f32 v90, v90\n" \
    "v_exp_f32 v91, v91\n" \
    "v_exp_f32 v92, v92\n" \
    "global_load_lds_dwordx4 v197, %[kvb] offset:0 sc1\n" \
    "v_exp_f32 v93, v93\n" \
    "v_exp_f32 v94, v94\n" \
    "v_exp_f32 v95, v95\n" \
    "v_exp_f32 v112, v112\n" \
    "v_exp_f32 v113, v113\n" \
    "v_smfmac_f32_32x32x32_bf16 v[128:143], v[188:191], v[88:95], v192\n" \
    "v_exp_f32 v114, v114\n" \
    "v_exp_f32 v115, v115\n" \
    "v_exp_f32 v116, v116\n" \
    "v_exp_f32 v117, v117\n" \
    "v_mfma_f32_32x32x16_bf16 v[96:111], v[168:171], v[164:167], 0\n" \
    "ds_read_b128 v[172:175], v193 offset:3072\n" \
    "v_exp_f32 v118, v118\n" \
    "v_exp_f32 v119, v119\n" \
    "v_exp_f32 v120, v120\n" \
    "v_exp_f32 v121, v121\n" \
    "v_smfmac_f32_32x32x32_bf16 v[144:159], v[184:187], v[112:119], v192\n" \
    "v_exp_f32 v122, v122\n" \
    "v_exp_f32 v123, v123\n" \
    "v_exp_f32 v124, v124\n" \
    "global_load_lds_dwordx4 v197, %[kvb] offset:1024 sc1\n" \
    "v_exp_f32 v125, v125\n" \
    "v_exp_f32 v126, v126\n" \
    "v_exp_f32 v127, v127\n" \
    "v_exp_f32 v64, v64\n" \
    "v_exp_f32 v65, v65\n" \
    "v_smfmac_f32_32x32x32_bf16 v[144:159], v[188:191], v[120:127], v192\n" \
    "ds_read_b128 v[184:187], v194 offset:3072\n" \
    "v_exp_f32 v66, v66\n" \
    "v_exp_f32 v67, v67\n" \
    "v_exp_f32 v68, v68\n" \
    "v_exp_f32 v69, v69\n" \
    "s_waitcnt lgkmcnt(1)\n" \
    "v_mfma_f32_32x32x16_bf16 v[80:95], v[172:175], v[160:163], 0\n" \
    "ds_read_b128 v[188:191], v194 offset:3584\n" \
    "v_exp_f32 v70, v70\n" \
    "v_exp_f32 v71, v71\n" \
    "v_exp_f32 v72, v72\n" \
    "v_exp_f32 v73, v73\n" \
    "v_smfmac_f32_32x32x32_bf16 v[128:143], v[176:179], v[64:71], v192\n" \
    "v_exp_f32 v74, v74\n" \
    "v_exp_f32 v75, v75\n" \
    "v_exp_f32 v76, v76\n" \
    "global_load_lds_dwordx4 v197, %[kvb] offset:2048 sc1\n" \
    "v_exp_f32 v77, v77\n" \
    "v_exp_f32 v78, v78\n" \
    "v_exp_f32 v79, v79\n" \
    "v_exp_f32 v96, v96\n" \
    "v_exp_f32 v97, v97\n" \
    "v_smfmac_f32_32x32x32_bf16 v[128:143], v[180:183], v[72:79], v192\n" \
    "v_exp_f32 v98, v98\n" \
    "v_exp_f32 v99, v99\n" \
    "v_exp_f32 v100, v100\n" \
    "v_exp_f32 v101, v101\n" \
    "v_mfma_f32_32x32x16_bf16 v[112:127], v[172:175], v[164:167], 0\n" \
    "ds_read_b128 v[168:171], v193 offset:4096\n" \
    "v_exp_f32 v102, v102\n" \
    "v_exp_f32 v103, v103\n" \
    "v_exp_f32 v104, v104\n" \
    "v_exp_f32 v105, v105\n" \
    "v_smfmac_f32_32x32x32_bf16 v[144:159], v[176:179], v[96:103], v192\n" \
    "v_exp_f32 v106, v106\n" \
    "v_exp_f32 v107, v107\n" \
    "v_exp_f32 v108, v108\n" \
    "global_load_lds_dwordx4 v197, %[kvb] offset:3072 sc1\n" \
    "v_exp_f32 v109, v109\n" \
    "v_exp_f32 v110, v110\n" \
    "v_exp_f32 v111, v111\n" \
    "v_exp_f32 v80, v80\n" \
    "v_exp_f32 v81, v81\n" \
    "v_smfmac_f32_32x32x32_bf16 v[144:159], v[180:183], v[104:111], v192\n" \
    "ds_read_b128 v[176:179], v194 offset:4096\n" \
    "v_exp_f32 v82, v82\n" \
    "v_exp_f32 v83, v83\n" \
    "v_exp_f32 v84, v84\n" \
    "v_exp_f32 v85, v85\n" \
    "s_waitcnt lgkmcnt(1)\n" \
    "v_mfma_f32_32x32x16_bf16 v[64:79], v[168:171], v[160:163], 0\n" \
    "ds_read_b128 v[180:183], v194 offset:4608\n" \
    "v_exp_f32 v86, v86\n" \
    "v_exp_f32 v87, v87\n" \
    "v_exp_f32 v88, v88\n" \
    "v_exp_f32 v89, v89\n" \
    "v_smfmac_f32_32x32x32_bf16 v[128:143], v[184:187], v[80:87], v192\n" \
    "v_exp_f32 v90, v90\n" \
    "v_exp_f32 v91, v91\n" \
    "v_exp_f32 v92, v92\n" \
    "global_load_dword v212, v203, %[wdb] offset:0\n" \
    "global_load_dword v213, v203, %[wdb] offset:1024\n" \
    "v_exp_f32 v93, v93\n" \
    "v_exp_f32 v94, v94\n" \
    "v_exp_f32 v95, v95\n" \
    "v_exp_f32 v112, v112\n" \
    "v_exp_f32 v113, v113\n" \
    "v_smfmac_f32_32x32x32_bf16 v[128:143], v[188:191], v[88:95], v192\n" \
    "v_exp_f32 v114, v114\n" \
    "v_exp_f32 v115, v115\n" \
    "v_exp_f32 v116, v116\n" \
    "v_exp_f32 v117, v117\n" \
    "v_mfma_f32_32x32x16_bf16 v[96:111], v[168:171], v[164:167], 0\n" \
    "ds_read_b128 v[172:175], v193 offset:5120\n" \
    "v_exp_f32 v118, v118\n" \
    "v_exp_f32 v119, v119\n" \
    "v_exp_f32 v120, v120\n" \
    "v_exp_f32 v121, v121\n" \
    "v_smfmac_f32_32x32x32_bf16 v[144:159], v[184:187], v[112:119], v192\n" \
    "v_exp_f32 v122, v122\n" \
    "v_exp_f32 v123, v123\n" \
    "v_exp_f32 v124, v124\n" \
    "global_load_dword v214, v203, %[wdb] offset:2048\n" \
    "global_load_dword v215, v203, %[wdb] offset:3072\n" \
    "v_exp_f32 v125, v125\n" \
    "v_exp_f32 v126, v126\n" \
    "v_exp_f32 v127, v127\n" \
    "v_add_u32 v203, 0x1000, v203\n" \
    "v_add_u32 v193, 4096, v193\n" \
    "v_add_u32 v194, v195, v194\n" \
    "v_exp_f32 v64, v64\n" \
    "v_exp_f32 v65, v65\n" \
    "v_smfmac_f32_32x32x32_bf16 v[144:159], v[188:191], v[120:127], v192\n" \
    "ds_read_b128 v[184:187], v194 offset:1024\n" \
    "v_exp_f32 v66, v66\n" \
    "v_exp_f32 v67, v67\n" \
    "v_exp_f32 v68, v68\n" \
    "v_exp_f32 v69, v69\n" \
    "s_waitcnt lgkmcnt(1)\n" \
    "v_mfma_f32_32x32x16_bf16 v[80:95], v[172:175], v[160:163], 0\n" \
    "ds_read_b128 v[188:191], v194 offset:1536\n" \
    "v_exp_f32 v70, v70\n" \
    "v_exp_f32 v71, v71\n" \
    "v_exp_f32 v72, v72\n" \
    "v_exp_f32 v73, v73\n" \
    "v_smfmac_f32_32x32x32_bf16 v[128:143], v[176:179], v[64:71], v192\n" \
    "v_exp_f32 v74, v74\n" \
    "v_exp_f32 v75, v75\n" \
    "v_exp_f32 v76, v76\n" \
    "global_load_dword v216, v203, %[wdb] offset:0\n" \
    "global_load_dword v217, v203, %[wdb] offset:1024\n" \
    "v_exp_f32 v77, v77\n" \
    "v_exp_f32 v78, v78\n" \
    "v_exp_f32 v79, v79\n" \
    "v_exp_f32 v96, v96\n" \
    "v_exp_f32 v97, v97\n" \
    "v_smfmac_f32_32x32x32_bf16 v[128:143], v[180:183], v[72:79], v192\n" \
    "v_exp_f32 v98, v98\n" \
    "v_exp_f32 v99, v99\n" \
    "v_exp_f32 v100, v100\n" \
    "v_exp_f32 v101, v101\n" \
    "v_mfma_f32_32x32x16_bf16 v[112:127], v[172:175], v[164:167], 0\n" \
    "ds_read_b128 v[168:171], v193 offset:2048\n" \
    "v_exp_f32 v102, v102\n" \
    "v_exp_f32 v103, v103\n" \
    "v_exp_f32 v104, v104\n" \
    "v_exp_f32 v105, v105\n" \
    "v_smfmac_f32_32x32x32_bf16 v[144:159], v[176:179], v[96:103], v192\n" \
    "v_exp_f32 v106, v106\n" \
    "v_exp_f32 v107, v107\n" \
    "v_exp_f32 v108, v108\n" \
    "global_load_dword v218, v203, %[wdb] offset:2048\n" \
    "global_load_dword v219, v203, %[wdb] offset:3072\n" \
    "v_exp_f32 v109, v109\n" \
    "v_exp_f32 v110, v110\n" \
    "v_exp_f32 v111, v111\n" \
    "v_exp_f32 v80, v80\n" \
    "v_exp_f32 v81, v81\n" \
    "v_smfmac_f32_32x32x32_bf16 v[144:159], v[180:183], v[104:111], v192\n" \
    "s_waitcnt vmcnt(12)\n" \
    "ds_read_b64_tr_b16 v[220:221], v228\n" \
    "ds_read_b64_tr_b16 v[222:223], v229\n" \
    "ds_read_b64_tr_b16 v[224:225], v228 offset:256\n" \
    "ds_read_b64_tr_b16 v[226:227], v229 offset:256\n" \
    "ds_read_b128 v[176:179], v194 offset:2048\n" \
    "v_exp_f32 v82, v82\n" \
    "v_exp_f32 v83, v83\n" \
    "v_exp_f32 v84, v84\n" \
    "v_exp_f32 v85, v85\n" \
    "s_waitcnt lgkmcnt(1)\n" \
    "v_mfma_f32_32x32x16_bf16 v[64:79], v[168:171], v[160:163], 0\n" \
    "ds_read_b128 v[180:183], v194 offset:2560\n" \
    "v_exp_f32 v86, v86\n" \
    "v_exp_f32 v87, v87\n" \
    "v_exp_f32 v88, v88\n" \
    "v_exp_f32 v89, v89\n" \
    "v_smfmac_f32_32x32x32_bf16 v[128:143], v[184:187], v[80:87], v192\n" \
    "v_exp_f32 v90, v90\n" \
    "v_exp_f32 v91, v91\n" \
    "v_exp_f32 v92, v92\n" \
    "v_mfma_f32_16x16x32_bf16 v[232:235], v[220:223], v[204:207], 0\n" \
    "v_exp_f32 v93, v93\n" \
    "v_exp_f32 v94, v94\n" \
    "v_exp_f32 v95, v95\n" \
    "v_exp_f32 v112, v112\n" \
    "v_exp_f32 v113, v113\n" \
    "v_smfmac_f32_32x32x32_bf16 v[128:143], v[188:191], v[88:95], v192\n" \
    "v_exp_f32 v114, v114\n" \
    "v_exp_f32 v115, v115\n" \
    "v_exp_f32 v116, v116\n" \
    "v_exp_f32 v117, v117\n" \
    "v_mfma_f32_32x32x16_bf16 v[96:111], v[168:171], v[164:167], 0\n" \
    "ds_read_b128 v[172:175], v193 offset:3072\n" \
    "v_exp_f32 v118, v118\n" \
    "v_exp_f32 v119, v119\n" \
    "v_exp_f32 v120, v120\n" \
    "v_exp_f32 v121, v121\n" \
    "v_smfmac_f32_32x32x32_bf16 v[144:159], v[184:187], v[112:119], v192\n" \
    "v_exp_f32 v122, v122\n" \
    "v_exp_f32 v123, v123\n" \
    "v_exp_f32 v124, v124\n" \
    "v_exp_f32 v125, v125\n" \
    "v_exp_f32 v126, v126\n" \
    "v_exp_f32 v127, v127\n" \
    "s_setprio 0\n" \
    "v_exp_f32 v64, v64\n" \
    "v_exp_f32 v65, v65\n" \
    "v_smfmac_f32_32x32x32_bf16 v[144:159], v[188:191], v[120:127], v192\n" \
    "ds_read_b128 v[184:187], v194 offset:3072\n" \
    "v_exp_f32 v66, v66\n" \
    "v_exp_f32 v67, v67\n" \
    "v_exp_f32 v68, v68\n" \
    "v_exp_f32 v69, v69\n" \
    "s_waitcnt lgkmcnt(1)\n" \
    "v_mfma_f32_32x32x16_bf16 v[80:95], v[172:175], v[160:163], 0\n" \
    "ds_read_b128 v[188:191], v194 offset:3584\n" \
    "v_exp_f32 v70, v70\n" \
    "v_exp_f32 v71, v71\n" \
    "v_exp_f32 v72, v72\n" \
    "v_exp_f32 v73, v73\n" \
    "v_smfmac_f32_32x32x32_bf16 v[128:143], v[176:179], v[64:71], v192\n" \
    "v_exp_f32 v74, v74\n" \
    "v_exp_f32 v75, v75\n" \
    "v_exp_f32 v76, v76\n" \
    "v_mfma_f32_16x16x32_bf16 v[232:235], v[224:227], v[208:211], v[232:235]\n" \
    "v_exp_f32 v77, v77\n" \
    "v_exp_f32 v78, v78\n" \
    "v_exp_f32 v79, v79\n" \
    "v_exp_f32 v96, v96\n" \
    "v_exp_f32 v97, v97\n" \
    "v_smfmac_f32_32x32x32_bf16 v[128:143], v[180:183], v[72:79], v192\n" \
    "v_exp_f32 v98, v98\n" \
    "v_exp_f32 v99, v99\n" \
    "v_exp_f32 v100, v100\n" \
    "v_exp_f32 v101, v101\n" \
    "v_mfma_f32_32x32x16_bf16 v[112:127], v[172:175], v[164:167], 0\n" \
    "ds_read_b128 v[168:171], v193 offset:4096\n" \
    "v_exp_f32 v102, v102\n" \
    "v_exp_f32 v103, v103\n" \
    "v_exp_f32 v104, v104\n" \
    "v_exp_f32 v105, v105\n" \
    "v_smfmac_f32_32x32x32_bf16 v[144:159], v[176:179], v[96:103], v192\n" \
    "v_exp_f32 v106, v106\n" \
    "v_exp_f32 v107, v107\n" \
    "v_exp_f32 v108, v108\n" \
    "v_exp_f32 v109, v109\n" \
    "v_exp_f32 v110, v110\n" \
    "v_exp_f32 v111, v111\n" \
    "v_exp_f32 v80, v80\n" \
    "v_exp_f32 v81, v81\n" \
    "v_smfmac_f32_32x32x32_bf16 v[144:159], v[180:183], v[104:111], v192\n" \
    "ds_write_b128 v230, v[232:235]\n" \
    "ds_read_b128 v[176:179], v194 offset:4096\n" \
    "v_exp_f32 v82, v82\n" \
    "v_exp_f32 v83, v83\n" \
    "v_exp_f32 v84, v84\n" \
    "v_exp_f32 v85, v85\n" \
    "s_waitcnt lgkmcnt(1)\n" \
    "v_mfma_f32_32x32x16_bf16 v[64:79], v[168:171], v[160:163], 0\n" \
    "ds_read_b128 v[180:183], v194 offset:4608\n" \
    "v_exp_f32 v86, v86\n" \
    "v_exp_f32 v87, v87\n" \
    "v_exp_f32 v88, v88\n" \
    "v_exp_f32 v89, v89\n" \
    "v_smfmac_f32_32x32x32_bf16 v[128:143], v[184:187], v[80:87], v192\n" \
    "v_exp_f32 v90, v90\n" \
    "v_exp_f32 v91, v91\n" \
    "v_exp_f32 v92, v92\n" \
    "v_exp_f32 v93, v93\n" \
    "v_exp_f32 v94, v94\n" \
    "v_exp_f32 v95, v95\n" \
    "v_exp_f32 v112, v112\n" \
    "v_exp_f32 v113, v113\n" \
    "v_smfmac_f32_32x32x32_bf16 v[128:143], v[188:191], v[88:95], v192\n" \
    "v_exp_f32 v114, v114\n" \
    "v_exp_f32 v115, v115\n" \
    "v_exp_f32 v116, v116\n" \
    "v_exp_f32 v117, v117\n" \
    "v_mfma_f32_32x32x16_bf16 v[96:111], v[168:171], v[164:167], 0\n" \
    "ds_read_b128 v[172:175], v193 offset:5120\n" \
    "v_exp_f32 v118, v118\n" \
    "v_exp_f32 v119, v119\n" \
    "v_exp_f32 v120, v120\n" \
    "v_exp_f32 v121, v121\n" \
    "v_smfmac_f32_32x32x32_bf16 v[144:159], v[184:187], v[112:119], v192\n" \
    "v_exp_f32 v122, v122\n" \
    "v_exp_f32 v123, v123\n" \
    "v_exp_f32 v124, v124\n" \
    "v_exp_f32 v125, v125\n" \
    "v_exp_f32 v126, v126\n" \
    "v_exp_f32 v127, v127\n" \
    "v_add_u32 v193, 4096, v193\n" \
    "v_add_u32 v194, v195, v194\n" \
    "s_waitcnt vmcnt(0)\n"

#define ATT_C \
    "s_mov_b32 %[cnt], 2\n" \
    "1:\n" \
    "v_exp_f32 v64, v64\n" \
    "v_exp_f32 v65, v65\n" \
    "v_smfmac_f32_32x32x32_bf16 v[144:159], v[188:191], v[120:127], v192\n" \
    "ds_read_b128 v[184:187], v194 offset:1024\n" \
    "v_exp_f32 v66, v66\n" \
    "v_exp_f32 v67, v67\n" \
    "v_exp_f32 v68, v68\n" \
    "v_exp_f32 v69, v69\n" \
    "s_waitcnt lgkmcnt(1)\n" \
    "v_mfma_f32_32x32x16_bf16 v[80:95], v[172:175], v[160:163], 0\n" \
    "ds_read_b128 v[188:191], v194 offset:1536\n" \
    "v_exp_f32 v70, v70\n" \
    "v_exp_f32 v71, v71\n" \
    "v_exp_f32 v72, v72\n" \
    "v_exp_f32 v73, v73\n" \
    "v_smfmac_f32_32x32x32_bf16 v[128:143], v[176:179], v[64:71], v192\n" \
    "v_exp_f32 v74, v74\n" \
    "s_mov_b32 m0, %[swo]\n" \
    "v_exp_f32 v75, v75\n" \
    "v_exp_f32 v76, v76\n" \
    "global_load_lds_dwordx4 v236, %[wob] offset:0\n" \
    "v_exp_f32 v77, v77\n" \
    "v_exp_f32 v78, v78\n" \
    "v_exp_f32 v79, v79\n" \
    "v_exp_f32 v96, v96\n" \
    "v_exp_f32 v97, v97\n" \
    "v_smfmac_f32_32x32x32_bf16 v[128:143], v[180:183], v[72:79], v192\n" \
    "v_exp_f32 v98, v98\n" \
    "v_exp_f32 v99, v99\n" \
    "v_exp_f32 v100, v100\n" \
    "v_exp_f32 v101, v101\n" \
    "v_mfma_f32_32x32x16_bf16 v[112:127], v[172:175], v[164:167], 0\n" \
    "ds_read_b128 v[168:171], v193 offset:2048\n" \
    "v_exp_f32 v102, v102\n" \
    "v_exp_f32 v103, v103\n" \
    "v_exp_f32 v104, v104\n" \
    "v_exp_f32 v105, v105\n" \
    "v_smfmac_f32_32x32x32_bf16 v[144:159], v[176:179], v[96:103], v192\n" \
    "v_exp_f32 v106, v106\n" \
    "v_exp_f32 v107, v107\n" \
    "v_exp_f32 v108, v108\n" \
    "v_exp_f32 v109, v109\n" \
    "v_exp_f32 v110, v110\n" \
    "v_exp_f32 v111, v111\n" \
    "v_exp_f32 v80, v80\n" \
    "v_exp_f32 v81, v81\n" \
    "v_smfmac_f32_32x32x32_bf16 v[144:159], v[180:183], v[104:111], v192\n" \
    "ds_read_b128 v[176:179], v194 offset:2048\n" \
    "v_exp_f32 v82, v82\n" \
    "v_exp_f32 v83, v83\n" \
    "v_exp_f32 v84, v84\n" \
    "v_exp_f32 v85, v85\n" \
    "s_waitcnt lgkmcnt(1)\n" \
    "v_mfma_f32_32x32x16_bf16 v[64:79], v[168:171], v[160:163], 0\n" \
    "ds_read_b128 v[180:183], v194 offset:2560\n" \
    "v_exp_f32 v86, v86\n" \
    "v_exp_f32 v87, v87\n" \
    "v_exp_f32 v88, v88\n" \
    "v_exp_f32 v89, v89\n" \
    "v_smfmac_f32_32x32x32_bf16 v[128:143], v[184:187], v[80:87], v192\n" \
    "v_exp_f32 v90, v90\n" \
    "v_exp_f32 v91, v91\n" \
    "v_exp_f32 v92, v92\n" \
    "global_load_lds_dwordx4 v237, %[wob] offset:1024\n" \
    "v_exp_f32 v93, v93\n" \
    "v_exp_f32 v94, v94\n" \
    "v_exp_f32 v95, v95\n" \
    "v_exp_f32 v112, v112\n" \
    "v_exp_f32 v113, v113\n" \
    "v_smfmac_f32_32x32x32_bf16 v[128:143], v[188:191], v[88:95], v192\n" \
    "v_exp_f32 v114, v114\n" \
    "v_exp_f32 v115, v115\n" \
    "v_exp_f32 v116, v116\n" \
    "v_exp_f32 v117, v117\n" \
    "v_mfma_f32_32x32x16_bf16 v[96:111], v[168:171], v[164:167], 0\n" \
    "ds_read_b128 v[172:175], v193 offset:3072\n" \
    "v_exp_f32 v118, v118\n" \
    "v_exp_f32 v119, v119\n" \
    "v_exp_f32 v120, v120\n" \
    "v_exp_f32 v121, v121\n" \
    "v_smfmac_f32_32x32x32_bf16 v[144:159], v[184:187], v[112:119], v192\n" \
    "v_exp_f32 v122, v122\n" \
    "v_exp_f32 v123, v123\n" \
    "v_exp_f32 v124, v124\n" \
    "v_exp_f32 v125, v125\n" \
    "v_exp_f32 v126, v126\n" \
    "v_exp_f32 v127, v127\n" \
    "v_exp_f32 v64, v64\n" \
    "v_exp_f32 v65, v65\n" \
    "v_smfmac_f32_32x32x32_bf16 v[144:159], v[188:191], v[120:127], v192\n" \
    "ds_read_b128 v[184:187], v194 offset:3072\n" \
    "v_exp_f32 v66, v66\n" \
    "v_exp_f32 v67, v67\n" \
    "v_exp_f32 v68, v68\n" \
    "v_exp_f32 v69, v69\n" \
    "s_waitcnt lgkmcnt(1)\n" \
    "v_mfma_f32_32x32x16_bf16 v[80:95], v[172:175], v[160:163], 0\n" \
    "ds_read_b128 v[188:191], v194 offset:3584\n" \
    "v_exp_f32 v70, v70\n" \
    "v_exp_f32 v71, v71\n" \
    "v_exp_f32 v72, v72\n" \
    "v_exp_f32 v73, v73\n" \
    "v_smfmac_f32_32x32x32_bf16 v[128:143], v[176:179], v[64:71], v192\n" \
    "v_exp_f32 v74, v74\n" \
    "v_exp_f32 v75, v75\n" \
    "v_exp_f32 v76, v76\n" \
    "global_load_lds_dwordx4 v238, %[wob] offset:2048\n" \
    "v_exp_f32 v77, v77\n" \
    "v_exp_f32 v78, v78\n" \
    "v_exp_f32 v79, v79\n" \
    "v_exp_f32 v96, v96\n" \
    "v_exp_f32 v97, v97\n" \
    "v_smfmac_f32_32x32x32_bf16 v[128:143], v[180:183], v[72:79], v192\n" \
    "v_exp_f32 v98, v98\n" \
    "v_exp_f32 v99, v99\n" \
    "v_exp_f32 v100, v100\n" \
    "v_exp_f32 v101, v101\n" \
    "v_mfma_f32_32x32x16_bf16 v[112:127], v[172:175], v[164:167], 0\n" \
    "ds_read_b128 v[168:171], v193 offset:4096\n" \
    "v_exp_f32 v102, v102\n" \
    "v_exp_f32 v103, v103\n" \
    "v_exp_f32 v104, v104\n" \
    "v_exp_f32 v105, v105\n" \
    "v_smfmac_f32_32x32x32_bf16 v[144:159], v[176:179], v[96:103], v192\n" \
    "v_exp_f32 v106, v106\n" \
    "v_exp_f32 v107, v107\n" \
    "v_exp_f32 v108, v108\n" \
    "v_exp_f32 v109, v109\n" \
    "v_exp_f32 v110, v110\n" \
    "v_exp_f32 v111, v111\n" \
    "v_exp_f32 v80, v80\n" \
    "v_exp_f32 v81, v81\n" \
    "v_smfmac_f32_32x32x32_bf16 v[144:159], v[180:183], v[104:111], v192\n" \
    "ds_read_b128 v[176:179], v194 offset:4096\n" \
    "v_exp_f32 v82, v82\n" \
    "v_exp_f32 v83, v83\n" \
    "v_exp_f32 v84, v84\n" \
    "v_exp_f32 v85, v85\n" \
    "s_waitcnt lgkmcnt(1)\n" \
    "v_mfma_f32_32x32x16_bf16 v[64:79], v[168:171], v[160:163], 0\n" \
    "ds_read_b128 v[180:183], v194 offset:4608\n" \
    "v_exp_f32 v86, v86\n" \
    "v_exp_f32 v87, v87\n" \
    "v_exp_f32 v88, v88\n" \
    "v_exp_f32 v89, v89\n" \
    "v_smfmac_f32_32x32x32_bf16 v[128:143], v[184:187], v[80:87], v192\n" \
    "v_exp_f32 v90, v90\n" \
    "v_exp_f32 v91, v91\n" \
    "v_exp_f32 v92, v92\n" \
    "global_load_lds_dwordx4 v239, %[wob] offset:3072\n" \
    "v_exp_f32 v93, v93\n" \
    "v_exp_f32 v94, v94\n" \
    "v_exp_f32 v95, v95\n" \
    "v_exp_f32 v112, v112\n" \
    "v_exp_f32 v113, v113\n" \
    "v_smfmac_f32_32x32x32_bf16 v[128:143], v[188:191], v[88:95], v192\n" \
    "v_exp_f32 v114, v114\n" \
    "v_exp_f32 v115, v115\n" \
    "v_exp_f32 v116, v116\n" \
    "v_exp_f32 v117, v117\n" \
    "v_mfma_f32_32x32x16_bf16 v[96:111], v[168:171], v[164:167], 0\n" \
    "ds_read_b128 v[172:175], v193 offset:5120\n" \
    "v_exp_f32 v118, v118\n" \
    "v_exp_f32 v119, v119\n" \
    "v_exp_f32 v120, v120\n" \
    "v_exp_f32 v121, v121\n" \
    "v_smfmac_f32_32x32x32_bf16 v[144:159], v[184:187], v[112:119], v192\n" \
    "v_exp_f32 v122, v122\n" \
    "v_exp_f32 v123, v123\n" \
    "v_exp_f32 v124, v124\n" \
    "v_exp_f32 v125, v125\n" \
    "v_exp_f32 v126, v126\n" \
    "v_exp_f32 v127, v127\n" \
    "v_add_u32 v236, 0x8000, v236\n" \
    "v_add_u32 v237, 0x8000, v237\n" \
    "v_add_u32 v238, 0x8000, v238\n" \
    "v_add_u32 v239, 0x8000, v239\n" \
    "s_add_u32 %[swo], %[swo], 0x1000\n" \
    "v_add_u32 v193, 4096, v193\n" \
    "v_add_u32 v194, v195, v194\n" \
    "s_sub_u32 %[cnt], %[cnt], 1\n" \
    "s_cmp_lg_u32 %[cnt], 0\n" \
    "s_cbranch_scc1 1b\n" \
    "s_mov_b32 %[cnt], 2\n" \
    "2:\n" \
    "v_exp_f32 v64, v64\n" \
    "v_exp_f32 v65, v65\n" \
    "v_smfmac_f32_32x32x32_bf16 v[144:159], v[188:191], v[120:127], v192\n" \
    "ds_read_b128 v[184:187], v194 offset:1024\n" \
    "v_exp_f32 v66, v66\n" \
    "v_exp_f32 v67, v67\n" \
    "v_exp_f32 v68, v68\n" \
    "v_exp_f32 v69, v69\n" \
    "s_waitcnt lgkmcnt(1)\n" \
    "v_mfma_f32_32x32x16_bf16 v[80:95], v[172:175], v[160:163], 0\n" \
    "ds_read_b128 v[188:191], v194 offset:1536\n" \
    "v_exp_f32 v70, v70\n" \
    "v_exp_f32 v71, v71\n" \
    "v_exp_f32 v72, v72\n" \
    "v_exp_f32 v73, v73\n" \
    "v_smfmac_f32_32x32x32_bf16 v[128:143], v[176:179], v[64:71], v192\n" \
    "v_exp_f32 v74, v74\n" \
    "v_exp_f32 v75, v75\n" \
    "v_exp_f32 v76, v76\n" \
    "v_exp_f32 v77, v77\n" \
    "v_exp_f32 v78, v78\n" \
    "v_exp_f32 v79, v79\n" \
    "v_exp_f32 v96, v96\n" \
    "v_exp_f32 v97, v97\n" \
    "v_smfmac_f32_32x32x32_bf16 v[128:143], v[180:183], v[72:79], v192\n" \
    "v_exp_f32 v98, v98\n" \
    "v_exp_f32 v99, v99\n" \
    "v_exp_f32 v100, v100\n" \
    "v_exp_f32 v101, v101\n" \
    "v_mfma_f32_32x32x16_bf16 v[112:127], v[172:175], v[164:167], 0\n" \
    "ds_read_b128 v[168:171], v193 offset:2048\n" \
    "v_exp_f32 v102, v102\n" \
    "v_exp_f32 v103, v103\n" \
    "v_exp_f32 v104, v104\n" \
    "v_exp_f32 v105, v105\n" \
    "v_smfmac_f32_32x32x32_bf16 v[144:159], v[176:179], v[96:103], v192\n" \
    "v_exp_f32 v106, v106\n" \
    "v_exp_f32 v107, v107\n" \
    "v_exp_f32 v108, v108\n" \
    "v_exp_f32 v109, v109\n" \
    "v_exp_f32 v110, v110\n" \
    "v_exp_f32 v111, v111\n" \
    "v_exp_f32 v80, v80\n" \
    "v_exp_f32 v81, v81\n" \
    "v_smfmac_f32_32x32x32_bf16 v[144:159], v[180:183], v[104:111], v192\n" \
    "ds_read_b128 v[176:179], v194 offset:2048\n" \
    "v_exp_f32 v82, v82\n" \
    "v_exp_f32 v83, v83\n" \
    "v_exp_f32 v84, v84\n" \
    "v_exp_f32 v85, v85\n" \
    "s_waitcnt lgkmcnt(1)\n" \
    "v_mfma_f32_32x32x16_bf16 v[64:79], v[168:171], v[160:163], 0\n" \
    "ds_read_b128 v[180:183], v194 offset:2560\n" \
    "v_exp_f32 v86, v86\n" \
    "v_exp_f32 v87, v87\n" \
    "v_exp_f32 v88, v88\n" \
    "v_exp_f32 v89, v89\n" \
    "v_smfmac_f32_32x32x32_bf16 v[128:143], v[184:187], v[80:87], v192\n" \
    "v_exp_f32 v90, v90\n" \
    "v_exp_f32 v91, v91\n" \
    "v_exp_f32 v92, v92\n" \
    "v_exp_f32 v93, v93\n" \
    "v_exp_f32 v94, v94\n" \
    "v_exp_f32 v95, v95\n" \
    "v_exp_f32 v112, v112\n" \
    "v_exp_f32 v113, v113\n" \
    "v_smfmac_f32_32x32x32_bf16 v[128:143], v[188:191], v[88:95], v192\n" \
    "v_exp_f32 v114, v114\n" \
    "v_exp_f32 v115, v115\n" \
    "v_exp_f32 v116, v116\n" \
    "v_exp_f32 v117, v117\n" \
    "v_mfma_f32_32x32x16_bf16 v[96:111], v[168:171], v[164:167], 0\n" \
    "ds_read_b128 v[172:175], v193 offset:3072\n" \
    "v_exp_f32 v118, v118\n" \
    "v_exp_f32 v119, v119\n" \
    "v_exp_f32 v120, v120\n" \
    "v_exp_f32 v121, v121\n" \
    "v_smfmac_f32_32x32x32_bf16 v[144:159], v[184:187], v[112:119], v192\n" \
    "v_exp_f32 v122, v122\n" \
    "v_exp_f32 v123, v123\n" \
    "v_exp_f32 v124, v124\n" \
    "v_exp_f32 v125, v125\n" \
    "v_exp_f32 v126, v126\n" \
    "v_exp_f32 v127, v127\n" \
    "v_exp_f32 v64, v64\n" \
    "v_exp_f32 v65, v65\n" \
    "v_smfmac_f32_32x32x32_bf16 v[144:159], v[188:191], v[120:127], v192\n" \
    "ds_read_b128 v[184:187], v194 offset:3072\n" \
    "v_exp_f32 v66, v66\n" \
    "v_exp_f32 v67, v67\n" \
    "v_exp_f32 v68, v68\n" \
    "v_exp_f32 v69, v69\n" \
    "s_waitcnt lgkmcnt(1)\n" \
    "v_mfma_f32_32x32x16_bf16 v[80:95], v[172:175], v[160:163], 0\n" \
    "ds_read_b128 v[188:191], v194 offset:3584\n" \
    "v_exp_f32 v70, v70\n" \
    "v_exp_f32 v71, v71\n" \
    "v_exp_f32 v72, v72\n" \
    "v_exp_f32 v73, v73\n" \
    "v_smfmac_f32_32x32x32_bf16 v[128:143], v[176:179], v[64:71], v192\n" \
    "v_exp_f32 v74, v74\n" \
    "v_exp_f32 v75, v75\n" \
    "v_exp_f32 v76, v76\n" \
    "v_exp_f32 v77, v77\n" \
    "v_exp_f32 v78, v78\n" \
    "v_exp_f32 v79, v79\n" \
    "v_exp_f32 v96, v96\n" \
    "v_exp_f32 v97, v97\n" \
    "v_smfmac_f32_32x32x32_bf16 v[128:143], v[180:183], v[72:79], v192\n" \
    "v_exp_f32 v98, v98\n" \
    "v_exp_f32 v99, v99\n" \
    "v_exp_f32 v100, v100\n" \
    "v_exp_f32 v101, v101\n" \
    "v_mfma_f32_32x32x16_bf16 v[112:127], v[172:175], v[164:167], 0\n" \
    "ds_read_b128 v[168:171], v193 offset:4096\n" \
    "v_exp_f32 v102, v102\n" \
    "v_exp_f32 v103, v103\n" \
    "v_exp_f32 v104, v104\n" \
    "v_exp_f32 v105, v105\n" \
    "v_smfmac_f32_32x32x32_bf16 v[144:159], v[176:179], v[96:103], v192\n" \
    "v_exp_f32 v106, v106\n" \
    "v_exp_f32 v107, v107\n" \
    "v_exp_f32 v108, v108\n" \
    "v_exp_f32 v109, v109\n" \
    "v_exp_f32 v110, v110\n" \
    "v_exp_f32 v111, v111\n" \
    "v_exp_f32 v80, v80\n" \
    "v_exp_f32 v81, v81\n" \
    "v_smfmac_f32_32x32x32_bf16 v[144:159], v[180:183], v[104:111], v192\n" \
    "ds_read_b128 v[176:179], v194 offset:4096\n" \
    "v_exp_f32 v82, v82\n" \
    "v_exp_f32 v83, v83\n" \
    "v_exp_f32 v84, v84\n" \
    "v_exp_f32 v85, v85\n" \
    "s_waitcnt lgkmcnt(1)\n" \
    "v_mfma_f32_32x32x16_bf16 v[64:79], v[168:171], v[160:163], 0\n" \
    "ds_read_b128 v[180:183], v194 offset:4608\n" \
    "v_exp_f32 v86, v86\n" \
    "v_exp_f32 v87, v87\n" \
    "v_exp_f32 v88, v88\n" \
    "v_exp_f32 v89, v89\n" \
    "v_smfmac_f32_32x32x32_bf16 v[128:143], v[184:187], v[80:87], v192\n" \
    "v_exp_f32 v90, v90\n" \
    "v_exp_f32 v91, v91\n" \
    "v_exp_f32 v92, v92\n" \
    "v_exp_f32 v93, v93\n" \
    "v_exp_f32 v94, v94\n" \
    "v_exp_f32 v95, v95\n" \
    "v_exp_f32 v112, v112\n" \
    "v_exp_f32 v113, v113\n" \
    "v_smfmac_f32_32x32x32_bf16 v[128:143], v[188:191], v[88:95], v192\n" \
    "v_exp_f32 v114, v114\n" \
    "v_exp_f32 v115, v115\n" \
    "v_exp_f32 v116, v116\n" \
    "v_exp_f32 v117, v117\n" \
    "v_mfma_f32_32x32x16_bf16 v[96:111], v[168:171], v[164:167], 0\n" \
    "ds_read_b128 v[172:175], v193 offset:5120\n" \
    "v_exp_f32 v118, v118\n" \
    "v_exp_f32 v119, v119\n" \
    "v_exp_f32 v120, v120\n" \
    "v_exp_f32 v121, v121\n" \
    "v_smfmac_f32_32x32x32_bf16 v[144:159], v[184:187], v[112:119], v192\n" \
    "v_exp_f32 v122, v122\n" \
    "v_exp_f32 v123, v123\n" \
    "v_exp_f32 v124, v124\n" \
    "v_exp_f32 v125, v125\n" \
    "v_exp_f32 v126, v126\n" \
    "v_exp_f32 v127, v127\n" \
    "s_cmp_eq_u32 %[cnt], 2\n" \
    "s_cbranch_scc0 8f\n" \
    "s_setprio 0\n" \
    "8:\n" \
    "v_add_u32 v193, 4096, v193\n" \
    "v_add_u32 v194, v195, v194\n" \
    "s_sub_u32 %[cnt], %[cnt], 1\n" \
    "s_cmp_lg_u32 %[cnt], 0\n" \
    "s_cbranch_scc1 2b\n" \
    "s_nop 1\n" \
    "v_smfmac_f32_32x32x32_bf16 v[144:159], v[188:191], v[120:127], v192\n" \
    "s_setprio 0\n" \
    "s_waitcnt vmcnt(0)\n" \
    "s_nop 7\n" \
    "s_nop 7\n" \
    "s_nop 3\n" \
    "s_waitcnt lgkmcnt(0)\n"

__device__ __forceinline__ void slow_block(const char* ws, u32x4 q, int lane, float* o, float& lsum) {
    const u32 koff = WS_K + lane * 16;
    const u32 r32 = lane & 31;
    float m = -3.0e38f;
    for (int t = 0; t < 64; ++t) {
        u32x4 kf;
        GLD128C(kf, ws, koff + t * 1024, 0);
        asm volatile("s_waitcnt vmcnt(0)" : "+v"(kf) :: "memory");
        f32x16 s = {0};
        s = __builtin_amdgcn_mfma_f32_32x32x16_bf16(asbf(kf), asbf(q), s, 0, 0, 0);
#pragma unroll
        for (int i = 0; i < 16; ++i) m = fmaxf(m, s[i]);
    }
    {
        auto sw = __builtin_amdgcn_permlane32_swap(__float_as_uint(m), __float_as_uint(m), false, false);
        m = fmaxf(__uint_as_float(sw[0]), __uint_as_float(sw[1]));
    }
    f32x16 acc = {0};
    const u32 voff = WS_V + (lane >> 5) * 256 + (lane & 15) * 16;
    for (int t = 0; t < 64; ++t) {
        u32x4 kf, v0, v1;
        GLD128C(kf, ws, koff + t * 1024, 0);
        GLD128C(v0, ws, voff + t * 1024, 0);
        GLD128C(v1, ws, voff + t * 1024, 512);
        asm volatile("s_waitcnt vmcnt(0)" : "+v"(kf), "+v"(v0), "+v"(v1) :: "memory");
        if (r32 >= 16) { v0 = (u32x4){0x3f803f80u, 0x3f803f80u, 0x3f803f80u, 0x3f803f80u}; v1 = v0; }
        f32x16 s = {0};
        s = __builtin_amdgcn_mfma_f32_32x32x16_bf16(asbf(kf), asbf(q), s, 0, 0, 0);
        u32 p[8];
#pragma unroll
        for (int i = 0; i < 8; ++i)
            p[i] = pk(__builtin_amdgcn_exp2f(s[2 * i] - m), __builtin_amdgcn_exp2f(s[2 * i + 1] - m));
#pragma unroll
        for (int st = 0; st < 2; ++st) {
            u32x4 vv = st ? v1 : v0;
            auto a = __builtin_amdgcn_permlane32_swap(vv[0], vv[2], false, false);
            auto c = __builtin_amdgcn_permlane32_swap(vv[1], vv[3], false, false);
            u32x4 af = {a[0], c[0], a[1], c[1]};
            u32x4 bf = {p[4 * st], p[4 * st + 1], p[4 * st + 2], p[4 * st + 3]};
            acc = __builtin_amdgcn_mfma_f32_32x32x16_bf16(asbf(af), asbf(bf), acc, 0, 0, 0);
        }
    }
#pragma unroll
    for (int i = 0; i < 8; ++i) o[i] = acc[i];
    lsum = acc[8];
}

#define PRIO_MODE 1
#define PRIO_START() do { if (w >= 4) __builtin_amdgcn_s_setprio(1); } while (0)
#define PRIO_END() __builtin_amdgcn_s_setprio(0)
__global__ void __launch_bounds__(512, 2)
fused_kernel(const u16* x_, const u16* wq_, const u16* wk_, const u16* wv_, const u16* wo_, const u16* wr_, const u16* wg_,
             const u16* wu_, const u16* wd_, const u16* gw_, u16* out_, char* ws_)
{
    u32x16 ka0; u32x8 ka1;
    {
        const void* kp = (const void*)__builtin_amdgcn_kernarg_segment_ptr();
        asm volatile("s_load_dwordx16 %0, %2, 0\n\ts_load_dwordx8 %1, %2, 64\n\ts_waitcnt lgkmcnt(0)"
                     : "=&s"(ka0), "=&s"(ka1) : "s"(kp) : "memory");
    }
#define KP(v, i) ((u64)v[2 * (i)] | ((u64)v[2 * (i) + 1] << 32))
    const u16* x  = (const u16*)KP(ka0, 0); const u16* wq = (const u16*)KP(ka0, 1);
    const u16* wk = (const u16*)KP(ka0, 2); const u16* wv = (const u16*)KP(ka0, 3);
    const u16* wo = (const u16*)KP(ka0, 4); const u16* wr = (const u16*)KP(ka0, 5);
    const u16* wg = (const u16*)KP(ka0, 6); const u16* wu = (const u16*)KP(ka0, 7);
    const u16* wd = (const u16*)KP(ka1, 0); const u16* gw = (const u16*)KP(ka1, 1);
    u16* out = (u16*)KP(ka1, 2); char* ws = (char*)KP(ka1, 3);

    __shared__ __attribute__((aligned(1024))) char smem[L_END];
    LDSP char* lds = (LDSP char*)smem;
    const u32 lb = (u32)(size_t)lds;
    const int tid = threadIdx.x;
    const int lane = tid & 63;
    const int w = __builtin_amdgcn_readfirstlane(tid >> 6);
    const int b = blockIdx.x;
    const int xcd = b & 7, jj = b >> 3;
    const int tb = xcd * 4 + (jj >> 3), hg = jj & 7;
    const int g = lane >> 4, l15 = lane & 15, h = lane >> 5, l31 = lane & 31;
    const bool isK = hg < 4;
    const bool ffn = jj < 28;
    const int fj = xcd * 28 + min(jj, 27);
    STAMP(1); STAMPC(18);

    const int th = w & 1, hp = (w >> 1) & 1, blk = w >> 2;
    const int T0 = tb * 64 + (hg & 3) * 16;
    u32x4 xf0, xf1, vf0, vf1;
    {
        const u16* wsrc = isK ? wk : wv;
#pragma unroll
        for (int i = 0; i < 2; ++i) {
            u32 slot = 64 * i + lane, pos = slot >> 3, B = pos ^ ((pos >> 2) & 1);
            u32 row = 4 * B + ((slot >> 1) & 3);
            dma16(wsrc, (64 * w + row) * 32 + (slot & 1) * 16, lb + L_W + w * 2048 + i * 1024);
        }
        u32 xo = (T0 + l15) * 1024 + (64 * w + 16 * g) * 2;
        GLD128(xf0, x, xo, 0);
        GLD128(xf1, x, xo, 16);
        const u16* vec = isK ? wr : gw;
        u32 vo = (64 * w + 16 * g) * 2;
        GLD128(vf0, vec, vo, 0);
        GLD128(vf1, vec, vo, 16);
    }
    u32 epo;
    { u32 z = 0; GLD32C(epo, ws, z, 0); }
    asm volatile("s_barrier" ::: "memory");
    if (w < 7) {
#pragma unroll
        for (int i = 0; i < 19; ++i) {
            const int p = i < 18 ? w + 7 * i : __builtin_amdgcn_readfirstlane(min(w + 7 * i, 127));
            if (i < 9 || (i == 9 && w == 0)) {
                dma16(x, (tb * 64 + p) * 1024 + ((lane ^ (p & 15)) << 4), lb + L_K + p * 1024);
            } else {
                const int G = p - 64;
                dma16(wq, (8 * G + ((lane >> 2) & 7)) * 1024 + hg * 128 + (lane >> 5) * 64 + (lane & 3) * 16, lb + L_V + G * 1024);
            }
        }
    } else {
        const u32x4 pz = {POISON, POISON, POISON, POISON};
#pragma unroll
        for (int i = 0; i < 8; ++i) {
            u32 po = WS_O + ((tb * 4 + (i >> 1)) * 16 + hg * 2 + (i & 1)) * 1024 + lane * 16;
            GST128C(ws, po, pz);
        }
        u32 yo = (ffn ? WS_Y + fj * 2048 : WS_X + b * 2048) + lane * 16;
        GST128C(ws, yo, pz);
        yo += 1024;
        GST128C(ws, yo, pz);
    }
    if (tid < 256) { u32 o1 = (tid & 15) == 0 ? 0x3f803f80u : 0u; ST128(L_ONE + tid * 16, ((u32x4){o1, o1, o1, o1})); }
    asm volatile("s_cmp_eq_u32 %[w], 7\n\ts_cbranch_scc1 1f\n\ts_waitcnt vmcnt(20)\n\ts_branch 2f\n1:\n\ts_waitcnt vmcnt(11)\n2:"
                 : "+v"(xf0), "+v"(xf1), "+v"(vf0), "+v"(vf1) : [w] "s"(w) : "memory", "scc");
    FSTAMP(2);
    {
        const u32 ta = L_W + w * 2048 + l15 * 8;
        const u32 gx = g & 1;
        u32x4 wf0 = cat(trread(lds, ta + ((4 * g + 0) ^ gx) * 128), trread(lds, ta + ((4 * g + 1) ^ gx) * 128));
        u32x4 wf1 = cat(trread(lds, ta + ((4 * g + 2) ^ gx) * 128), trread(lds, ta + ((4 * g + 3) ^ gx) * 128));
        f32x4 acc = {0, 0, 0, 0}, dacc = {0, 0, 0, 0};
        if (isK) {
            acc = __builtin_amdgcn_mfma_f32_16x16x32_bf16(asbf(wf0), asbf(xf0), acc, 0, 0, 0);
            acc = __builtin_amdgcn_mfma_f32_16x16x32_bf16(asbf(wf1), asbf(xf1), acc, 0, 0, 0);
        } else {
            acc = __builtin_amdgcn_mfma_f32_16x16x32_bf16(asbf(xf0), asbf(wf0), acc, 0, 0, 0);
            acc = __builtin_amdgcn_mfma_f32_16x16x32_bf16(asbf(xf1), asbf(wf1), acc, 0, 0, 0);
        }
        dacc = __builtin_amdgcn_mfma_f32_16x16x32_bf16(asbf(vf0), asbf(xf0), dacc, 0, 0, 0);
        dacc = __builtin_amdgcn_mfma_f32_16x16x32_bf16(asbf(vf1), asbf(xf1), dacc, 0, 0, 0);
        ST128(L_P1 + w * 1024 + lane * 16, __builtin_bit_cast(u32x4, acc));
        if (g == 0) STF(L_PD + w * 64 + l15 * 4, dacc[0]);
    }
    BAR();
    FSTAMP(3);
    asm volatile("s_cmp_eq_u32 %[w], 7\n\ts_cbranch_scc1 1f\n\ts_waitcnt vmcnt(19)\n\ts_branch 2f\n1:\n\ts_waitcnt vmcnt(10)\n2:"
                 : "+v"(epo) : [w] "s"(w) : "memory", "scc");
    const u32 epoch = (u32)__builtin_amdgcn_readfirstlane((int)epo) + 1u;
    if (w == 7) {
        f32x4 ps[8]; float pd[8];
#pragma unroll
        for (int i = 0; i < 8; ++i) {
            ps[i] = __builtin_bit_cast(f32x4, LD128(L_P1 + i * 1024 + lane * 16));
            pd[i] = LDF(L_PD + i * 64 + l15 * 4);
        }
        __builtin_amdgcn_sched_barrier(0);
        const f32x4 s = ((ps[0] + ps[1]) + (ps[2] + ps[3])) + ((ps[4] + ps[5]) + (ps[6] + ps[7]));
        const float d = ((pd[0] + pd[1]) + (pd[2] + pd[3])) + ((pd[4] + pd[5]) + (pd[6] + pd[7]));
        u32x2 pv = {pk(s[0], s[1]), pk(s[2], s[3])};
        const int tile = T0 >> 5, sub = (T0 >> 4) & 1;
        u32 key = 0;
        if (isK) {
            u32 off = WS_K + tile * 1024 + ((g & 1) * 32 + sub * 16 + l15) * 16 + (g >> 1) * 8;
            GST64C(ws, off, pv);
            u32 rb = pk(d, d) & 0xffffu;
            u32 ord = (rb & 0x8000u) ? (~rb & 0xffffu) : (rb | 0x8000u);
            key = wave_max_u32((ord << 16) | (0xffffu - (u32)(T0 + l15)));
        } else {
            u32 off = WS_V + tile * 1024 + sub * 512 + (g >> 1) * 256 + l15 * 16 + (g & 1) * 8;
            GST64C(ws, off, pv);
            if (g == 0) { u32 go = WS_G + (T0 + l15) * 4; u32 dv = __float_as_uint(d); GST32C(ws, go, dv); }
        }
        VMWAIT(0);
        if (lane < 8) { u32x2 fl = {key, epoch}; u32 fo = WS_F1 + lane * 2048 + (tile * 4 + (isK ? 0 : 2) + sub) * 8; GST64C(ws, fo, fl); }
        FSTAMP(4);
    } else {
        VMWAIT(0);
    }
    BAR();
    FSTAMP(5);

    f32x16 qacc = {0};
    if (w < 4) {
        const int qh = w >> 1;
        const int tok = th * 32 + l31;
        u32 abase = L_V + h * 1024 + qh * 512 + ((l15 >> 2) * 64) + ((lane >> 4) & 1) * 32 + (l15 & 3) * 8;
        asm volatile("" : "+v"(abase));
        u32 bb[8];
#pragma unroll
        for (int j = 0; j < 8; ++j) { bb[j] = L_K + tok * 1024 + (((2 * j + h) ^ (tok & 15)) << 4); asm volatile("" : "+v"(bb[j])); }
#pragma unroll
        for (int s = 0; s < 32; ++s) {
            u32x4 af = cat(trread(lds, abase + s * 2048), trread(lds, abase + s * 2048 + 256));
            u32x4 bf = LD128(bb[s & 7] + (s >> 3) * 256);
            qacc = __builtin_amdgcn_mfma_f32_32x32x16_bf16(asbf(af), asbf(bf), qacc, 0, 0, 0);
        }
        FSTAMP(6);
    } else {
        const int j = w - 4;
        const u32 fo = WS_F1 + xcd * 2048 + (j * 64 + l31 * 2) * 8;
        u32x4 f;
        u32 spins = 0;
        while (true) {
            asm volatile("global_load_dwordx4 %0, %1, %2 sc1\n\ts_waitcnt vmcnt(0)" : "=&v"(f) : "v"(fo), "s"(ws) : "memory");
            if (__all((f[1] == epoch) & (f[3] == epoch)) || ++spins > SPIN_MAX) break;
        }
        const u32 key = wave_max_u32(max(f[0], f[2]));
        if (lane == 0) ST32(L_CT + j * 4, key);
        FSTAMP(7);
    }
    BAR();
    FSTAMP(9);
    u32 kvo = (w & 1) * 65536 + ((w >> 1) & 1) * 32768;
    if (w >= 4) {
        dma16c4(ws + WS_K, kvo + lane * 16, lb + L_K + kvo);
        dma16c4(ws + WS_K, kvo + 4096 + lane * 16, lb + L_K + kvo + 4096);
        dma16c2(ws + WS_K, kvo + 8192 + lane * 16, lb + L_K + kvo + 8192);
    } else {
        const float qs = 0.25f * 1.4426950408889634f;
        u32x4 qx, qy;
#pragma unroll
        for (int i = 0; i < 4; ++i) {
            u32 a = pk(qacc[2 * i], qacc[2 * i + 1]), c = pk(qacc[8 + 2 * i], qacc[8 + 2 * i + 1]);
            qx[i] = pk(lo16(a) * qs, hi16(a) * qs);
            qy[i] = pk(lo16(c) * qs, hi16(c) * qs);
        }
        ST128(L_P1 + w * 2048 + lane * 16, qx);
        ST128(L_P1 + w * 2048 + 1024 + lane * 16, qy);
    }
    const u32 key = max(max(LD32(L_CT), LD32(L_CT + 4)), max(LD32(L_CT + 8), LD32(L_CT + 12)));
    const int sel = __builtin_amdgcn_readfirstlane(0xffff - (key & 0xffff)) & 2047;
    u32x8 EXA; u32x4 EXB, WOA;
    u32 skv = lb + L_K + kvo + (10 + 4 * (w >> 2)) * 1024, sg = lb + L_W + w * 2048;
    const u32 skv3 = lb + L_K + kvo + (26 + 2 * (w >> 2)) * 1024;
    u32 swo = lb + ((w & 4) ? L_V : L_K) + ((w >> 1) & 1) * 32768 + (w & 1) * 8192;
    {
        EXA[0] = kvo + (10 + 4 * (w >> 2)) * 1024 + lane * 16;
        EXA[1] = kvo + (26 + 2 * (w >> 2)) * 1024 + lane * 16;
        const u32 pos = lane >> 3, B = pos ^ ((pos >> 2) & 1);
        const u32 row = 64 * w + 4 * B + ((lane >> 1) & 3);
        const u64 g0 = (u64)((lane & 1) ? wu : wg) + (u64)(row * 3584 + fj * 16), g1 = g0 + 32 * 3584;
        EXA[2] = (u32)g0; EXA[3] = (u32)(g0 >> 32); EXA[4] = (u32)g1; EXA[5] = (u32)(g1 >> 32);
        EXA[6] = sel * 1024 + (64 * w + 16 * g) * 2;
        EXA[7] = fj * 8192 + (128 * (w & 3) + 2 * lane) * 2;
        const u32 ta = lb + L_W + w * 2048 + l15 * 8, gx = g & 1;
        EXB[0] = ta + (4 * g + gx) * 128;
        EXB[1] = ta + (4 * g + (gx ^ 1)) * 128;
        EXB[2] = lb + (l15 == 0 ? L_FP + w * 64 + g * 16 : L_P1 + w * 1024 + lane * 16);
        EXB[3] = 0;
#pragma unroll
        for (int i = 0; i < 4; ++i) {
            const u32 r = 64 * w + 8 * i + (lane >> 3), slot = lane & 7;
            const u32 f = ((r >> 1) & 1) | ((i & 1) << 1);
            const u32 c = (((slot >> 1) ^ f) << 1) | (slot & 1);
            WOA[i] = r * 1024 + hg * 128 + c * 16 - i * 1024;
        }
    }
    const int hf = w >> 2;
    u32 ka = lb + L_K + hf * 32768 + lane * 16;
    const bool vlane = l31 < 16;
    u32 va = vlane ? (lb + L_V + hf * 32768 + h * 256 + l15 * 16) : (lb + L_ONE - 1024 + h * 256 + l15 * 16);
    u32 vinc = vlane ? 4096u : 0u;
    u32 va0 = vlane ? va : va + 1024;
    u32 qa = lb + L_P1 + (w & 3) * 2048 + lane * 16;
    asm volatile("" : "+{v[196:203]}"(EXA), "+{v[228:231]}"(EXB), "+{v[236:239]}"(WOA), "+{v193}"(ka), "+{v194}"(va), "+{v195}"(vinc), "+v"(va0), "+v"(qa),
                 "+s"(skv), "+s"(sg), "+s"(swo));
    if (w >= 4) VMWAIT(0);
    BAR();
    STAMP(10); STAMPC(19);
    if (b == 0 && tid == 256) { u32 z = 0; GST32C(ws, z, epoch); }

    u32x4 qx, qy;
    f32x16 SX0, SX1, SY0, SY1, OX, OY;
    u32x4 K0, K1, V00, V01, V10, V11;
    u32 idx = 0xDDDDu;
    {
        asm volatile(
            "ds_read_b128 v[160:163], %[qa]\n"
            "ds_read_b128 v[164:167], %[qa] offset:1024\n"
            "ds_read_b128 v[168:171], v193\n"
            "ds_read_b128 v[172:175], v193 offset:1024\n"
            "ds_read_b128 v[176:179], %[va0]\n"
            "ds_read_b128 v[180:183], %[va0] offset:512\n"
            "v_mov_b32 v128, 0\n v_mov_b32 v129, 0\n v_mov_b32 v130, 0\n v_mov_b32 v131, 0\n"
            "v_mov_b32 v132, 0\n v_mov_b32 v133, 0\n v_mov_b32 v134, 0\n v_mov_b32 v135, 0\n"
            "v_mov_b32 v136, 0\n v_mov_b32 v137, 0\n v_mov_b32 v138, 0\n v_mov_b32 v139, 0\n"
            "v_mov_b32 v140, 0\n v_mov_b32 v141, 0\n v_mov_b32 v142, 0\n v_mov_b32 v143, 0\n"
            "v_mov_b32 v144, 0\n v_mov_b32 v145, 0\n v_mov_b32 v146, 0\n v_mov_b32 v147, 0\n"
            "v_mov_b32 v148, 0\n v_mov_b32 v149, 0\n v_mov_b32 v150, 0\n v_mov_b32 v151, 0\n"
            "v_mov_b32 v152, 0\n v_mov_b32 v153, 0\n v_mov_b32 v154, 0\n v_mov_b32 v155, 0\n"
            "v_mov_b32 v156, 0\n v_mov_b32 v157, 0\n v_mov_b32 v158, 0\n v_mov_b32 v159, 0\n"
            "v_mov_b32 v120, 0\n v_mov_b32 v121, 0\n v_mov_b32 v122, 0\n v_mov_b32 v123, 0\n"
            "v_mov_b32 v124, 0\n v_mov_b32 v125, 0\n v_mov_b32 v126, 0\n v_mov_b32 v127, 0\n"
            "v_mov_b32 v188, 0\n v_mov_b32 v189, 0\n v_mov_b32 v190, 0\n v_mov_b32 v191, 0\n"
            "s_waitcnt lgkmcnt(0)\n"
            "v_mfma_f32_32x32x16_bf16 v[64:79], v[168:171], v[160:163], 0\n"
            "v_mfma_f32_32x32x16_bf16 v[96:111], v[168:171], v[164:167], 0\n"
            "s_nop 7\n s_nop 7\n"
            : "={v[64:79]}"(SX0), "={v[80:95]}"(SX1), "={v[96:111]}"(SY0), "={v[112:127]}"(SY1),
              "={v[128:143]}"(OX), "={v[144:159]}"(OY),
              "={v[168:171]}"(K0), "={v[172:175]}"(K1), "={v[176:179]}"(V00), "={v[180:183]}"(V01),
              "={v[184:187]}"(V10), "={v[188:191]}"(V11), "=&{v[160:163]}"(qx), "=&{v[164:167]}"(qy)
            : "{v193}"(ka), [va0] "v"(va0), [qa] "v"(qa)
            : "memory");
    }
#define ATT_OPS \
            : "+{v[64:79]}"(SX0), "+{v[80:95]}"(SX1), "+{v[96:111]}"(SY0), "+{v[112:127]}"(SY1), \
              "+{v[128:143]}"(OX), "+{v[144:159]}"(OY), \
              "+{v[168:171]}"(K0), "+{v[172:175]}"(K1), "+{v[176:179]}"(V00), "+{v[180:183]}"(V01), \
              "+{v[184:187]}"(V10), "+{v[188:191]}"(V11), "+{v193}"(ka), "+{v194}"(va), [cnt] "=&s"(cnt_)
#define ATT_INS : "{v[160:163]}"(qx), "{v[164:167]}"(qy), "{v192}"(idx), "{v195}"(vinc)
    u32 cnt_;
    const char* kvb = ws + WS_K;
    PRIO_START();
    asm volatile(ATT_A ATT_OPS, "+{v[196:203]}"(EXA), "+{v[228:231]}"(EXB), "+{v[236:239]}"(WOA), [skv] "+s"(skv), [sg] "+s"(sg)
                 ATT_INS, [kvb] "s"(kvb) : "memory", "scc");
    LSTAMP(4);
    BAR();
    LSTAMP(8);
    PRIO_START();
    u32x16 EXD;
    asm volatile(ATT_B ATT_OPS, "+{v[196:203]}"(EXA), "=&{v[204:219]}"(EXD), "+{v[236:239]}"(WOA)
                 ATT_INS, "{v[228:231]}"(EXB), [kvb] "s"(kvb), [skv3] "s"(skv3), [xb] "s"(x), [wdb] "s"(wd)
                 : "memory", "scc", "v220", "v221", "v222", "v223", "v224", "v225", "v226", "v227", "v232", "v233", "v234", "v235");
    LSTAMP(9);
    BAR();
    LSTAMP(11);
    if (ffn && w < 4) {
        float sum = LDF(L_FP + l15 * 4);
#pragma unroll
        for (int i = 1; i < 8; ++i) sum += LDF(L_FP + i * 64 + l15 * 4);
        sum = bfr(sum);
        float up = __shfl(sum, (lane & 7) + 8);
        float gt = __shfl(sum, lane & 7);
        float hv = bfr(bfr(gt * sigm(gt)) * up);
        u32 hb = __float_as_uint(hv);
        float y0 = 0.f, y1 = 0.f;
#pragma unroll
        for (int i = 0; i < 8; ++i) {
            const float hs = __uint_as_float((u32)__builtin_amdgcn_readlane(hb, i));
            y0 += hs * lo16(EXD[8 + i]);
            y1 += hs * hi16(EXD[8 + i]);
        }
        u32x2 gr = {__float_as_uint(y0), __float_as_uint(y1)};
        gr[0] = gr[0] == POISON ? 0x7fc00000u : gr[0];
        gr[1] = gr[1] == POISON ? 0x7fc00000u : gr[1];
        u32 yo = WS_Y + fj * 2048 + (128 * w + 2 * lane) * 4;
        GST64C(ws, yo, gr);
    }
    LSTAMP(22);
    PRIO_START();
    asm volatile(ATT_C ATT_OPS, "+{v[236:239]}"(WOA), [swo] "+s"(swo) ATT_INS, [wob] "s"(wo) : "memory", "scc");
    const int tq = w & 3, kh = w >> 2;
    const bool owner = (sel >> 6) == tb;
    int lane_b = lane;
    asm volatile("" : "+v"(lane_b));
    {
    const int lane = lane_b;
    const int g = lane >> 4, l15 = lane & 15, h = lane >> 5, l31 = lane & 31;
    const int tokg = tb * 64 + tq * 16 + l15;
    const int ncol = hg * 64 + kh * 32 + (g & 1) * 16 + (g >> 1) * 8;
    u32 oslot;
    {
        const int head = hg * 4 + hp * 2 + blk, tl = th * 32 + l31;
        oslot = WS_O + ((tb * 4 + (tl >> 4)) * 16 + (head >> 1)) * 1024 + (((head & 1) * 2 + h) * 16 + (tl & 15)) * 16;
    }
    STAMP(12); STAMPC(20);
    u32x4 xe, Y0, Y1, Y2, Y3, Y4, Y5, Y6; u32 gl;
    const u32 yo0 = WS_Y + (28 * w + g) * 2048 + hg * 256 + l15 * 16;
    {
        u32 xo = tokg * 1024 + ncol * 2;
        GLD128(xe, x, xo, 0);
        u32 go = WS_G + tokg * 4;
        GLD32C(gl, ws, go, 0);
        u32 yo = yo0;
        asm volatile("s_cmp_lg_u32 %[stb], %[tb]\n\ts_cbranch_scc1 3f\n"
                     "global_load_dwordx4 %0, %[yo], %[ws] sc1\n v_add_u32 %[yo], 0x2000, %[yo]\n"
                     "global_load_dwordx4 %1, %[yo], %[ws] sc1\n v_add_u32 %[yo], 0x2000, %[yo]\n"
                     "global_load_dwordx4 %2, %[yo], %[ws] sc1\n v_add_u32 %[yo], 0x2000, %[yo]\n"
                     "global_load_dwordx4 %3, %[yo], %[ws] sc1\n v_add_u32 %[yo], 0x2000, %[yo]\n"
                     "global_load_dwordx4 %4, %[yo], %[ws] sc1\n v_add_u32 %[yo], 0x2000, %[yo]\n"
                     "global_load_dwordx4 %5, %[yo], %[ws] sc1\n v_add_u32 %[yo], 0x2000, %[yo]\n"
                     "global_load_dwordx4 %6, %[yo], %[ws] sc1\n3:"
                     : "=&v"(Y0), "=&v"(Y1), "=&v"(Y2), "=&v"(Y3), "=&v"(Y4), "=&v"(Y5), "=&v"(Y6), [yo] "+v"(yo)
                     : [stb] "s"(sel >> 6), [tb] "s"(tb), [ws] "s"(ws) : "memory", "scc");
    }

    {
        f32x16& mine = blk ? OY : OX;
        f32x16& give = blk ? OX : OY;
        const u32 cw = L_W + w * 2304, cr = L_W + (w ^ 4) * 2304;
        ST128(cw + lane * 16, ((u32x4){__float_as_uint(give[0]), __float_as_uint(give[1]), __float_as_uint(give[2]), __float_as_uint(give[3])}));
        ST128(cw + 1024 + lane * 16, ((u32x4){__float_as_uint(give[4]), __float_as_uint(give[5]), __float_as_uint(give[6]), __float_as_uint(give[7])}));
        STF(cw + 2048 + lane * 4, give[8]);
        BAR();
        f32x4 p0 = __builtin_bit_cast(f32x4, LD128(cr + lane * 16));
        f32x4 p1 = __builtin_bit_cast(f32x4, LD128(cr + 1024 + lane * 16));
        float l = LDF(cr + 2048 + lane * 4) + mine[8];
        float o[8];
#pragma unroll
        for (int i = 0; i < 4; ++i) { o[i] = mine[i] + p0[i]; o[4 + i] = mine[4 + i] + p1[i]; }
        {
            auto sw = __builtin_amdgcn_permlane32_swap(__float_as_uint(l), __float_as_uint(l), false, false);
            l = __uint_as_float(sw[0]);
        }
        float amax = fmaxf(fmaxf(fmaxf(fabsf(o[0]), fabsf(o[1])), fmaxf(fabsf(o[2]), fabsf(o[3]))),
                           fmaxf(fmaxf(fabsf(o[4]), fabsf(o[5])), fmaxf(fabsf(o[6]), fabsf(o[7]))));
        bool safe = (l >= 8.6736174e-19f) && (l <= 1.2676506e30f) && (amax <= 3.0e38f);
        if (!__all(safe)) {
            slow_block(ws, blk ? qy : qx, lane, o, l);
            auto sw = __builtin_amdgcn_permlane32_swap(__float_as_uint(l), __float_as_uint(l), false, false);
            l = __uint_as_float(sw[0]);
        }
        const float ri = __builtin_amdgcn_rcpf(l);
        u32 A0 = pk(o[0] * ri, o[1] * ri), A1 = pk(o[2] * ri, o[3] * ri);
        u32 B0 = pk(o[4] * ri, o[5] * ri), B1 = pk(o[6] * ri, o[7] * ri);
        auto s0 = __builtin_amdgcn_permlane32_swap(A0, B0, false, false);
        auto s1 = __builtin_amdgcn_permlane32_swap(A1, B1, false, false);
        u32x4 ov = {s0[0], s1[0], s0[1], s1[1]};
#pragma unroll
        for (int i = 0; i < 4; ++i) ov[i] = ov[i] == POISON ? 0x7fc07fc0u : ov[i];
        GST128C(ws, oslot, ov);
    }
    STAMP(13);

    u32x4 af[4][8];
    {
        const u32 f = ((l15 >> 3) & 1) | ((g & 1) << 1);
        const u32 rb = (kh ? L_V : L_K) + (8 * g + (l15 >> 2)) * 128 + (l15 & 3) * 8;
#pragma unroll
        for (int nb = 0; nb < 4; ++nb) {
            u32 a = rb + ((nb ^ f) * 32);
            asm volatile("" : "+v"(a));
#pragma unroll
            for (int st = 0; st < 8; ++st) {
                const u32 o_ = (st >> 2) * 32768 + (st & 3) * 4096;
                af[nb][st] = cat(trread(lds, a + o_), trread(lds, a + o_ + 512));
            }
        }
    }
    STAMP(14);
    asm volatile("s_waitcnt vmcnt(1)" : "+v"(xe), "+v"(gl), "+v"(Y0), "+v"(Y1), "+v"(Y2), "+v"(Y3), "+v"(Y4), "+v"(Y5), "+v"(Y6) :: "memory");
    if (owner) {
        u32 spins = 0;
#define YMX(v) max(max(v[0], v[1]), max(v[2], v[3]))
        while (!__all(max(max(max(YMX(Y0), YMX(Y1)), max(YMX(Y2), YMX(Y3))), max(max(YMX(Y4), YMX(Y5)), YMX(Y6))) != POISON) && ++spins <= SPIN_MAX) {
            u32 yo = yo0;
            asm volatile("global_load_dwordx4 %0, %[yo], %[ws] sc1\n v_add_u32 %[yo], 0x2000, %[yo]\n"
                         "global_load_dwordx4 %1, %[yo], %[ws] sc1\n v_add_u32 %[yo], 0x2000, %[yo]\n"
                         "global_load_dwordx4 %2, %[yo], %[ws] sc1\n v_add_u32 %[yo], 0x2000, %[yo]\n"
                         "global_load_dwordx4 %3, %[yo], %[ws] sc1\n v_add_u32 %[yo], 0x2000, %[yo]\n"
                         "global_load_dwordx4 %4, %[yo], %[ws] sc1\n v_add_u32 %[yo], 0x2000, %[yo]\n"
                         "global_load_dwordx4 %5, %[yo], %[ws] sc1\n v_add_u32 %[yo], 0x2000, %[yo]\n"
                         "global_load_dwordx4 %6, %[yo], %[ws] sc1\n s_waitcnt vmcnt(0)"
                         : "=&v"(Y0), "=&v"(Y1), "=&v"(Y2), "=&v"(Y3), "=&v"(Y4), "=&v"(Y5), "=&v"(Y6), [yo] "+v"(yo)
                         : [ws] "s"(ws) : "memory");
        }
        f32x4 ys;
#pragma unroll
        for (int i = 0; i < 4; ++i) {
            float v = ((__uint_as_float(Y0[i]) + __uint_as_float(Y1[i])) + (__uint_as_float(Y2[i]) + __uint_as_float(Y3[i])))
                    + ((__uint_as_float(Y4[i]) + __uint_as_float(Y5[i])) + __uint_as_float(Y6[i]));
            auto a = __builtin_amdgcn_permlane32_swap(__float_as_uint(v), __float_as_uint(v), false, false);
            v = __uint_as_float(a[0]) + __uint_as_float(a[1]);
            auto c = __builtin_amdgcn_permlane16_swap(__float_as_uint(v), __float_as_uint(v), false, false);
            ys[i] = __uint_as_float(c[0]) + __uint_as_float(c[1]);
        }
        if (g == 0) ST128(L_YP + w * 256 + l15 * 16, __builtin_bit_cast(u32x4, ys));
    }
    u32x4 of0, of1, of2, of3, of4, of5, of6, of7;
    {
        const u32 oo = WS_O + ((tb * 4 + tq) * 16 + 8 * kh) * 1024 + lane * 16, o2 = oo + 4096;
        u32 spins = 0;
        while (true) {
            asm volatile("global_load_dwordx4 %0, %8, %10 sc1\n\tglobal_load_dwordx4 %1, %8, %10 offset:1024 sc1\n\t"
                         "global_load_dwordx4 %2, %8, %10 offset:2048 sc1\n\tglobal_load_dwordx4 %3, %8, %10 offset:3072 sc1\n\t"
                         "global_load_dwordx4 %4, %9, %10 sc1\n\tglobal_load_dwordx4 %5, %9, %10 offset:1024 sc1\n\t"
                         "global_load_dwordx4 %6, %9, %10 offset:2048 sc1\n\tglobal_load_dwordx4 %7, %9, %10 offset:3072 sc1\n\t"
                         "s_waitcnt vmcnt(0)"
                         : "=&v"(of0), "=&v"(of1), "=&v"(of2), "=&v"(of3), "=&v"(of4), "=&v"(of5), "=&v"(of6), "=&v"(of7)
                         : "v"(oo), "v"(o2), "s"(ws) : "memory");
            u32 mx = 0;
#define OMX(v) mx = max(max(mx, max(v[0], v[1])), max(v[2], v[3]));
            OMX(of0) OMX(of1) OMX(of2) OMX(of3) OMX(of4) OMX(of5) OMX(of6) OMX(of7)
            if (__all(mx != POISON) || ++spins > SPIN_MAX) break;
        }
    }
    STAMP(15);
    f32x4 acc[4] = {{0, 0, 0, 0}, {0, 0, 0, 0}, {0, 0, 0, 0}, {0, 0, 0, 0}};
#define OSTEP(OF, ST) \
    _Pragma("unroll") for (int nb = 0; nb < 4; ++nb) acc[nb] = __builtin_amdgcn_mfma_f32_16x16x32_bf16(asbf(af[nb][ST]), asbf(OF), acc[nb], 0, 0, 0);
    OSTEP(of0, 0) OSTEP(of1, 1) OSTEP(of2, 2) OSTEP(of3, 3) OSTEP(of4, 4) OSTEP(of5, 5) OSTEP(of6, 6) OSTEP(of7, 7)
    STAMP(16);
    {
        f32x4 k0 = kh ? acc[2] : acc[0], k1 = kh ? acc[3] : acc[1];
        f32x4 g0 = kh ? acc[0] : acc[2], g1 = kh ? acc[1] : acc[3];
        ST128(L_K + 16384 + w * 2048 + lane * 16, __builtin_bit_cast(u32x4, g0));
        ST128(L_K + 16384 + w * 2048 + 1024 + lane * 16, __builtin_bit_cast(u32x4, g1));
        BAR();
        k0 += __builtin_bit_cast(f32x4, LD128(L_K + 16384 + (w ^ 4) * 2048 + lane * 16));
        k1 += __builtin_bit_cast(f32x4, LD128(L_K + 16384 + (w ^ 4) * 2048 + 1024 + lane * 16));
        float a[8];
#pragma unroll
        for (int i = 0; i < 4; ++i) {
            auto sw = __builtin_amdgcn_permlane16_swap(__float_as_uint(k0[i]), __float_as_uint(k1[i]), false, false);
            a[i] = __uint_as_float(sw[0]); a[4 + i] = __uint_as_float(sw[1]);
        }
        const float gt = bfr(sigm(bfr(__uint_as_float(gl))));
        const float og = bfr(1.0f - gt);
        float bb[8];
#pragma unroll
        for (int i = 0; i < 4; ++i) { bb[2 * i] = lo16(xe[i]); bb[2 * i + 1] = hi16(xe[i]); }
        if (owner && tokg == sel) {
            const u32 rord = key >> 16;
            const float rsel = __uint_as_float(((rord & 0x8000u) ? (rord & 0x7fffu) : (~rord & 0xffffu)) << 16);
            const float sg = bfr(sigm(rsel));
            const int c0 = kh * 32 + (g & 1) * 16 + (g >> 1) * 8;
#pragma unroll
            for (int i = 0; i < 8; ++i) {
                float y = 0.f;
#pragma unroll
                for (int p = 0; p < 8; ++p) y += LDF(L_YP + p * 256 + (c0 + i) * 4);
                bb[i] = bfr(bb[i] + bfr(bfr(y) * sg));
            }
        }
        u32x4 ov;
#pragma unroll
        for (int i = 0; i < 4; ++i)
            ov[i] = pk(bfr(gt * bfr(a[2 * i])) + bfr(og * bb[2 * i]), bfr(gt * bfr(a[2 * i + 1])) + bfr(og * bb[2 * i + 1]));
        u32 oo_ = tokg * 1024 + ncol * 2;
        GST128(out, oo_, ov);
    }
    STAMP(17); STAMPC(21);
    BAR();
    if (tid == 0 && (b >> 3) == 5) {
#define RD(wv, i) LD32(L_CT + 64 + ((wv) * 24 + (i)) * 4)
        u32 t1 = RD(0, 1);
        printf("STMP b %d t0 %u prio %u/%u bar1 %u pub %u bar2 %u qp %u poll %u b2b %u att0 %u att %u/%u ost %u oseen %u/%u end %u/%u cyc %u %u %u\n", b, t1 % 100000, RD(0,2) - t1, RD(7,2) - t1, RD(0,3) - t1, RD(7,4) - t1, RD(0,5) - t1, RD(0,6) - t1, RD(4,7) - t1,
               RD(0,9) - t1, RD(0,10) - t1, RD(0,12) - t1, RD(4,12) - t1, RD(0,13) - t1, RD(0,15) - t1, RD(4,15) - t1, RD(0,17) - t1, RD(4,17) - t1, RD(0,19) - RD(0,18), RD(0,20) - RD(0,19), RD(0,21) - RD(0,20));
    }
    }
}

extern "C" void kernel_launch(void* const* d_in, const int* in_sizes, int n_in, void* d_out, int out_size,
                              void* d_ws, size_t ws_size, hipStream_t stream) {
    (void)in_sizes; (void)n_in; (void)out_size; (void)ws_size;
    fused_kernel<<<256, 512, 0, stream>>>((const u16*)d_in[0], (const u16*)d_in[1], (const u16*)d_in[2], (const u16*)d_in[3],
                                          (const u16*)d_in[4], (const u16*)d_in[6], (const u16*)d_in[7], (const u16*)d_in[8],
                                          (const u16*)d_in[9], (const u16*)d_in[10], (u16*)d_out, (char*)d_ws);
}
```

```cpp
#include <hip/hip_runtime.h>
#include <stdint.h>
#include <stdio.h>
#define DBG_STAMP
#define V2N

typedef unsigned int u32;
typedef unsigned long long u64;
typedef unsigned short u16;
typedef u32 u32x2 __attribute__((ext_vector_type(2)));
typedef u32 u32x4 __attribute__((ext_vector_type(4)));
typedef u32 u32x8 __attribute__((ext_vector_type(8)));
typedef u32 u32x16 __attribute__((ext_vector_type(16)));
typedef float f32x2 __attribute__((ext_vector_type(2)));
typedef float f32x4 __attribute__((ext_vector_type(4)));
typedef float f32x16 __attribute__((ext_vector_type(16)));
typedef __bf16 bf16x2 __attribute__((ext_vector_type(2)));
typedef __bf16 bf16x8 __attribute__((ext_vector_type(8)));
#define LDSP __attribute__((address_space(3)))

constexpr u32 WS_E   = 0;
constexpr u32 WS_F2  = 4096;
constexpr u32 WS_G   = 24576;
constexpr u32 WS_K   = 65536;
constexpr u32 WS_V   = 131072;
constexpr u32 WS_F1  = 262144;
constexpr u32 WS_O   = 524288;
constexpr u32 WS_Y   = 2621440;
constexpr u32 WS_DBG = 4194304;

constexpr u32 L_K   = 0;
constexpr u32 L_V   = 65536;
constexpr u32 L_ONE = 131072;
constexpr u32 L_W   = 135168;
constexpr u32 L_M   = 151552;
constexpr u32 L_P1  = L_M;
constexpr u32 L_PD  = L_M + 8192;
constexpr u32 L_FP  = L_M + 8704;
constexpr u32 L_YP  = L_M + 9216;
constexpr u32 L_CT  = L_M + 11264;
constexpr u32 L_END = 163840;

constexpr u32 SPIN_MAX = 1u << 22;
constexpr u32 POISON = 0xffffffffu;

#define LD128(a) (*(const LDSP u32x4*)(lds + (a)))
#define ST128(a, v) (*(LDSP u32x4*)(lds + (a)) = (v))
#define LD32(a) (*(const LDSP u32*)(lds + (a)))
#define ST32(a, v) (*(LDSP u32*)(lds + (a)) = (v))
#define LDF(a) (*(const LDSP float*)(lds + (a)))
#define STF(a, v) (*(LDSP float*)(lds + (a)) = (v))

__device__ __forceinline__ u32x2 trread(LDSP char* lds, u32 a) {
    typedef short v4s __attribute__((ext_vector_type(4)));
    v4s r = __builtin_amdgcn_ds_read_tr16_b64_v4i16((LDSP v4s*)(lds + a));
    return __builtin_bit_cast(u32x2, r);
}
__device__ __forceinline__ bf16x8 asbf(u32x4 v) { return __builtin_bit_cast(bf16x8, v); }
__device__ __forceinline__ u32x4 cat(u32x2 a, u32x2 b) { u32x4 r = {a[0], a[1], b[0], b[1]}; return r; }
__device__ __forceinline__ u32 pk(float a, float b) {
    f32x2 f = {a, b};
    return __builtin_bit_cast(u32, __builtin_convertvector(f, bf16x2));
}
__device__ __forceinline__ float bfr(float a) {
    return __uint_as_float(pk(a, a) & 0xffff0000u);
}
__device__ __forceinline__ float lo16(u32 v) { return __uint_as_float(v << 16); }
__device__ __forceinline__ float hi16(u32 v) { return __uint_as_float(v & 0xffff0000u); }
__device__ __forceinline__ float sigm(float z) { return __builtin_amdgcn_rcpf(1.0f + __builtin_amdgcn_exp2f(-1.4426950408889634f * z)); }

__device__ __forceinline__ void dma16(const void* base, u32 voff, u32 ldsaddr) {
    asm volatile("s_mov_b32 m0, %0\n\ts_nop 0\n\tglobal_load_lds_dwordx4 %1, %2" :: "s"(ldsaddr), "v"(voff), "s"(base) : "memory");
}
__device__ __forceinline__ void dma16c4(const void* base, u32 voff, u32 ldsaddr) {
    asm volatile("s_mov_b32 m0, %0\n\ts_nop 0\n\tglobal_load_lds_dwordx4 %1, %2 sc1\n\tglobal_load_lds_dwordx4 %1, %2 offset:1024 sc1\n\t"
                 "global_load_lds_dwordx4 %1, %2 offset:2048 sc1\n\tglobal_load_lds_dwordx4 %1, %2 offset:3072 sc1"
                 :: "s"(ldsaddr), "v"(voff), "s"(base) : "memory");
}
__device__ __forceinline__ void dma16c3(const void* base, u32 voff, u32 ldsaddr) {
    asm volatile("s_mov_b32 m0, %0\n\ts_nop 0\n\tglobal_load_lds_dwordx4 %1, %2 sc1\n\tglobal_load_lds_dwordx4 %1, %2 offset:1024 sc1\n\t"
                 "global_load_lds_dwordx4 %1, %2 offset:2048 sc1"
                 :: "s"(ldsaddr), "v"(voff), "s"(base) : "memory");
}
__device__ __forceinline__ void dma16c2(const void* base, u32 voff, u32 ldsaddr) {
    asm volatile("s_mov_b32 m0, %0\n\ts_nop 0\n\tglobal_load_lds_dwordx4 %1, %2 sc1\n\tglobal_load_lds_dwordx4 %1, %2 offset:1024 sc1"
                 :: "s"(ldsaddr), "v"(voff), "s"(base) : "memory");
}
__device__ __forceinline__ void dma16c(const void* base, u32 voff, u32 ldsaddr) {
    asm volatile("s_mov_b32 m0, %0\n\ts_nop 0\n\tglobal_load_lds_dwordx4 %1, %2 sc1" :: "s"(ldsaddr), "v"(voff), "s"(base) : "memory");
}
#define GLD128(dst, base, voff, OFF)  asm volatile("global_load_dwordx4 %0, %1, %2 offset:" #OFF : "=v"(dst) : "v"(voff), "s"(base) : "memory")
#define GLD128C(dst, base, voff, OFF) asm volatile("global_load_dwordx4 %0, %1, %2 offset:" #OFF " sc1" : "=v"(dst) : "v"(voff), "s"(base) : "memory")
#define GLD64C(dst, base, voff, OFF)  asm volatile("global_load_dwordx2 %0, %1, %2 offset:" #OFF " sc1" : "=v"(dst) : "v"(voff), "s"(base) : "memory")
#define GLD32C(dst, base, voff, OFF)  asm volatile("global_load_dword %0, %1, %2 offset:" #OFF " sc1" : "=v"(dst) : "v"(voff), "s"(base) : "memory")
#define GLD32(dst, base, voff, OFF)   asm volatile("global_load_dword %0, %1, %2 offset:" #OFF : "=v"(dst) : "v"(voff), "s"(base) : "memory")
#define GLD16(dst, base, voff, OFF)   asm volatile("global_load_ushort %0, %1, %2 offset:" #OFF : "=v"(dst) : "v"(voff), "s"(base) : "memory")
#define GST128(base, voff, val)  asm volatile("global_store_dwordx4 %0, %1, %2\n\ts_nop 1" :: "v"(voff), "v"(val), "s"(base) : "memory")
#define GST128C(base, voff, val) asm volatile("global_store_dwordx4 %0, %1, %2 sc1\n\ts_nop 1" :: "v"(voff), "v"(val), "s"(base) : "memory")
#define GST64C(base, voff, val)  asm volatile("global_store_dwordx2 %0, %1, %2 sc1\n\ts_nop 0" :: "v"(voff), "v"(val), "s"(base) : "memory")
#define GST32C(base, voff, val)  asm volatile("global_store_dword %0, %1, %2 sc1" :: "v"(voff), "v"(val), "s"(base) : "memory")
#define VMWAIT(N) asm volatile("s_waitcnt vmcnt(" #N ")" ::: "memory")
#define BAR() asm volatile("s_waitcnt lgkmcnt(0)\n\ts_barrier" ::: "memory")

#define STAMP(i) do { u32 t_ = (u32)__builtin_amdgcn_s_memrealtime(); if (lane == 0) ST32(L_CT + 64 + (w * 24 + (i)) * 4, t_); } while (0)
#define STAMPC(i) do { u32 t_ = (u32)__builtin_amdgcn_s_memtime(); if (lane == 0) ST32(L_CT + 64 + (w * 24 + (i)) * 4, t_); } while (0)
#define STAMPDUMP() do { if (lane < 24) ((u32*)(ws + WS_DBG))[(blockIdx.x * 8 + w) * 32 + lane] = LD32(L_CT + 64 + (w * 24 + lane) * 4); } while (0)
#define FSTAMP(i) STAMP(i)
#define LSTAMP(i)

__device__ __forceinline__ u32 wave_max_u32(u32 v) {
    v = max(v, (u32)__builtin_amdgcn_update_dpp(0, (int)v, 0x111, 0xf, 0xf, false));
    v = max(v, (u32)__builtin_amdgcn_update_dpp(0, (int)v, 0x112, 0xf, 0xf, false));
    v = max(v, (u32)__builtin_amdgcn_update_dpp(0, (int)v, 0x114, 0xf, 0xf, false));
    v = max(v, (u32)__builtin_amdgcn_update_dpp(0, (int)v, 0x118, 0xf, 0xf, false));
    v = max(v, (u32)__builtin_amdgcn_update_dpp(0, (int)v, 0x142, 0xa, 0xf, false));
    v = max(v, (u32)__builtin_amdgcn_update_dpp(0, (int)v, 0x143, 0xc, 0xf, false));
    return (u32)__builtin_amdgcn_readlane((int)v, 63);
}
#define ATT_A \
    "s_mov_b32 %[cnt], 2\n" \
    "v_mov_b32 v232, -1\n" \
    "v_mov_b32 v233, -1\n" \
    "v_mov_b32 v234, -1\n" \
    "v_mov_b32 v235, -1\n" \
    "1:\n" \
    "v_exp_f32 v64, v64\n" \
    "v_exp_f32 v65, v65\n" \
    "v_smfmac_f32_32x32x32_bf16 v[144:159], v[188:191], v[120:127], v192\n" \
    "global_store_dwordx4 v220, v[232:235], %[wsb] sc1\n" \
    "ds_read_b128 v[184:187], v194 offset:1024\n" \
    "v_exp_f32 v66, v66\n" \
    "v_exp_f32 v67, v67\n" \
    "v_exp_f32 v68, v68\n" \
    "v_exp_f32 v69, v69\n" \
    "s_waitcnt lgkmcnt(1)\n" \
    "v_mfma_f32_32x32x16_bf16 v[80:95], v[172:175], v[160:163], 0\n" \
    "ds_read_b128 v[188:191], v194 offset:1536\n" \
    "v_exp_f32 v70, v70\n" \
    "v_exp_f32 v71, v71\n" \
    "v_exp_f32 v72, v72\n" \
    "v_exp_f32 v73, v73\n" \
    "v_smfmac_f32_32x32x32_bf16 v[128:143], v[176:179], v[64:71], v192\n" \
    "v_exp_f32 v74, v74\n" \
    "s_mov_b32 m0, %[skv]\n" \
    "v_exp_f32 v75, v75\n" \
    "v_exp_f32 v76, v76\n" \
    "global_load_lds_dwordx4 v196, %[kvb] offset:0 sc1\n" \
    "v_exp_f32 v77, v77\n" \
    "v_exp_f32 v78, v78\n" \
    "v_exp_f32 v79, v79\n" \
    "v_exp_f32 v96, v96\n" \
    "v_exp_f32 v97, v97\n" \
    "v_smfmac_f32_32x32x32_bf16 v[128:143], v[180:183], v[72:79], v192\n" \
    "v_exp_f32 v98, v98\n" \
    "v_exp_f32 v99, v99\n" \
    "v_exp_f32 v100, v100\n" \
    "v_exp_f32 v101, v101\n" \
    "v_mfma_f32_32x32x16_bf16 v[112:127], v[172:175], v[164:167], 0\n" \
    "ds_read_b128 v[168:171], v193 offset:2048\n" \
    "v_exp_f32 v102, v102\n" \
    "v_exp_f32 v103, v103\n" \
    "v_exp_f32 v104, v104\n" \
    "v_exp_f32 v105, v105\n" \
    "v_smfmac_f32_32x32x32_bf16 v[144:159], v[176:179], v[96:103], v192\n" \
    "v_exp_f32 v106, v106\n" \
    "v_exp_f32 v107, v107\n" \
    "v_exp_f32 v108, v108\n" \
    "v_exp_f32 v109, v109\n" \
    "v_exp_f32 v110, v110\n" \
    "v_exp_f32 v111, v111\n" \
    "v_exp_f32 v80, v80\n" \
    "v_exp_f32 v81, v81\n" \
    "v_smfmac_f32_32x32x32_bf16 v[144:159], v[180:183], v[104:111], v192\n" \
    "ds_read_b128 v[176:179], v194 offset:2048\n" \
    "v_exp_f32 v82, v82\n" \
    "v_exp_f32 v83, v83\n" \
    "v_exp_f32 v84, v84\n" \
    "v_exp_f32 v85, v85\n" \
    "s_waitcnt lgkmcnt(1)\n" \
    "v_mfma_f32_32x32x16_bf16 v[64:79], v[168:171], v[160:163], 0\n" \
    "ds_read_b128 v[180:183], v194 offset:2560\n" \
    "v_exp_f32 v86, v86\n" \
    "v_exp_f32 v87, v87\n" \
    "v_exp_f32 v88, v88\n" \
    "v_exp_f32 v89, v89\n" \
    "v_smfmac_f32_32x32x32_bf16 v[128:143], v[184:187], v[80:87], v192\n" \
    "v_exp_f32 v90, v90\n" \
    "s_mov_b32 m0, %[skv]\n" \
    "v_exp_f32 v91, v91\n" \
    "v_exp_f32 v92, v92\n" \
    "global_load_lds_dwordx4 v196, %[kvb] offset:1024 sc1\n" \
    "v_exp_f32 v93, v93\n" \
    "v_exp_f32 v94, v94\n" \
    "v_exp_f32 v95, v95\n" \
    "v_exp_f32 v112, v112\n" \
    "v_exp_f32 v113, v113\n" \
    "v_smfmac_f32_32x32x32_bf16 v[128:143], v[188:191], v[88:95], v192\n" \
    "v_exp_f32 v114, v114\n" \
    "v_exp_f32 v115, v115\n" \
    "v_exp_f32 v116, v116\n" \
    "v_exp_f32 v117, v117\n" \
    "v_mfma_f32_32x32x16_bf16 v[96:111], v[168:171], v[164:167], 0\n" \
    "ds_read_b128 v[172:175], v193 offset:3072\n" \
    "v_exp_f32 v118, v118\n" \
    "v_exp_f32 v119, v119\n" \
    "v_exp_f32 v120, v120\n" \
    "v_exp_f32 v121, v121\n" \
    "v_smfmac_f32_32x32x32_bf16 v[144:159], v[184:187], v[112:119], v192\n" \
    "v_exp_f32 v122, v122\n" \
    "s_mov_b32 m0, %[sg]\n" \
    "v_exp_f32 v123, v123\n" \
    "v_exp_f32 v124, v124\n" \
    "global_load_lds_dwordx4 v[198:199], off\n" \
    "v_exp_f32 v125, v125\n" \
    "v_exp_f32 v126, v126\n" \
    "v_exp_f32 v127, v127\n" \
    "s_cmp_eq_u32 %[cnt], 1\n" \
    "s_cbranch_scc0 8f\n" \
    "s_setprio 0\n" \
    "8:\n" \
    "v_exp_f32 v64, v64\n" \
    "v_exp_f32 v65, v65\n" \
    "v_smfmac_f32_32x32x32_bf16 v[144:159], v[188:191], v[120:127], v192\n" \
    "ds_read_b128 v[184:187], v194 offset:3072\n" \
    "v_exp_f32 v66, v66\n" \
    "v_exp_f32 v67, v67\n" \
    "v_exp_f32 v68, v68\n" \
    "v_exp_f32 v69, v69\n" \
    "s_waitcnt lgkmcnt(1)\n" \
    "v_mfma_f32_32x32x16_bf16 v[80:95], v[172:175], v[160:163], 0\n" \
    "ds_read_b128 v[188:191], v194 offset:3584\n" \
    "v_exp_f32 v70, v70\n" \
    "v_exp_f32 v71, v71\n" \
    "v_exp_f32 v72, v72\n" \
    "v_exp_f32 v73, v73\n" \
    "v_smfmac_f32_32x32x32_bf16 v[128:143], v[176:179], v[64:71], v192\n" \
    "v_exp_f32 v74, v74\n" \
    "s_mov_b32 m0, %[skv]\n" \
    "v_exp_f32 v75, v75\n" \
    "v_exp_f32 v76, v76\n" \
    "global_load_lds_dwordx4 v196, %[kvb] offset:2048 sc1\n" \
    "v_exp_f32 v77, v77\n" \
    "v_exp_f32 v78, v78\n" \
    "v_exp_f32 v79, v79\n" \
    "v_exp_f32 v96, v96\n" \
    "v_exp_f32 v97, v97\n" \
    "v_smfmac_f32_32x32x32_bf16 v[128:143], v[180:183], v[72:79], v192\n" \
    "v_exp_f32 v98, v98\n" \
    "v_exp_f32 v99, v99\n" \
    "v_exp_f32 v100, v100\n" \
    "v_exp_f32 v101, v101\n" \
    "v_mfma_f32_32x32x16_bf16 v[112:127], v[172:175], v[164:167], 0\n" \
    "ds_read_b128 v[168:171], v193 offset:4096\n" \
    "v_exp_f32 v102, v102\n" \
    "v_exp_f32 v103, v103\n" \
    "v_exp_f32 v104, v104\n" \
    "v_exp_f32 v105, v105\n" \
    "v_smfmac_f32_32x32x32_bf16 v[144:159], v[176:179], v[96:103], v192\n" \
    "v_exp_f32 v106, v106\n" \
    "v_exp_f32 v107, v107\n" \
    "v_exp_f32 v108, v108\n" \
    "v_exp_f32 v109, v109\n" \
    "v_exp_f32 v110, v110\n" \
    "v_exp_f32 v111, v111\n" \
    "v_exp_f32 v80, v80\n" \
    "v_exp_f32 v81, v81\n" \
    "v_smfmac_f32_32x32x32_bf16 v[144:159], v[180:183], v[104:111], v192\n" \
    "ds_read_b128 v[176:179], v194 offset:4096\n" \
    "v_exp_f32 v82, v82\n" \
    "v_exp_f32 v83, v83\n" \
    "v_exp_f32 v84, v84\n" \
    "v_exp_f32 v85, v85\n" \
    "s_waitcnt lgkmcnt(1)\n" \
    "v_mfma_f32_32x32x16_bf16 v[64:79], v[168:171], v[160:163], 0\n" \
    "ds_read_b128 v[180:183], v194 offset:4608\n" \
    "v_exp_f32 v86, v86\n" \
    "v_exp_f32 v87, v87\n" \
    "v_exp_f32 v88, v88\n" \
    "v_exp_f32 v89, v89\n" \
    "v_smfmac_f32_32x32x32_bf16 v[128:143], v[184:187], v[80:87], v192\n" \
    "v_exp_f32 v90, v90\n" \
    "s_mov_b32 m0, %[skv]\n" \
    "v_exp_f32 v91, v91\n" \
    "v_exp_f32 v92, v92\n" \
    "global_load_lds_dwordx4 v196, %[kvb] offset:3072 sc1\n" \
    "v_exp_f32 v93, v93\n" \
    "v_exp_f32 v94, v94\n" \
    "v_exp_f32 v95, v95\n" \
    "v_exp_f32 v112, v112\n" \
    "v_exp_f32 v113, v113\n" \
    "v_smfmac_f32_32x32x32_bf16 v[128:143], v[188:191], v[88:95], v192\n" \
    "v_exp_f32 v114, v114\n" \
    "v_exp_f32 v115, v115\n" \
    "v_exp_f32 v116, v116\n" \
    "v_exp_f32 v117, v117\n" \
    "v_mfma_f32_32x32x16_bf16 v[96:111], v[168:171], v[164:167], 0\n" \
    "ds_read_b128 v[172:175], v193 offset:5120\n" \
    "v_exp_f32 v118, v118\n" \
    "v_exp_f32 v119, v119\n" \
    "v_exp_f32 v120, v120\n" \
    "v_exp_f32 v121, v121\n" \
    "v_smfmac_f32_32x32x32_bf16 v[144:159], v[184:187], v[112:119], v192\n" \
    "v_exp_f32 v122, v122\n" \
    "v_exp_f32 v123, v123\n" \
    "v_exp_f32 v124, v124\n" \
    "v_exp_f32 v125, v125\n" \
    "v_exp_f32 v126, v126\n" \
    "v_exp_f32 v127, v127\n" \
    "v_add_u32 v196, 0x2000, v196\n" \
    "v_mov_b32 v198, v200\n" \
    "v_mov_b32 v199, v201\n" \
    "s_add_u32 %[skv], %[skv], 0x2000\n" \
    "s_add_u32 %[sg], %[sg], 0x400\n" \
    "v_mov_b32 v220, v221\n" \
    "v_add_u32 v193, 4096, v193\n" \
    "v_add_u32 v194, v195, v194\n" \
    "s_sub_u32 %[cnt], %[cnt], 1\n" \
    "s_cmp_lg_u32 %[cnt], 0\n" \
    "s_cbranch_scc1 1b\n" \
    "s_waitcnt vmcnt(5)\n"

#define ATT_A1 \
    "s_mov_b32 %[cnt], 2\n" \
    "v_mov_b32 v232, -1\n" \
    "v_mov_b32 v233, -1\n" \
    "v_mov_b32 v234, -1\n" \
    "v_mov_b32 v235, -1\n" \
    "v_exp_f32 v64, v64\n" \
    "v_exp_f32 v65, v65\n" \
    "v_smfmac_f32_32x32x32_bf16 v[144:159], v[188:191], v[120:127], v192\n" \
    "global_store_dwordx4 v220, v[232:235], %[wsb] sc1\n" \
    "ds_read_b128 v[184:187], v194 offset:1024\n" \
    "v_exp_f32 v66, v66\n" \
    "v_exp_f32 v67, v67\n" \
    "v_exp_f32 v68, v68\n" \
    "v_exp_f32 v69, v69\n" \
    "s_waitcnt lgkmcnt(1)\n" \
    "v_mfma_f32_32x32x16_bf16 v[80:95], v[172:175], v[160:163], 0\n" \
    "ds_read_b128 v[188:191], v194 offset:1536\n" \
    "v_exp_f32 v70, v70\n" \
    "v_exp_f32 v71, v71\n" \
    "v_exp_f32 v72, v72\n" \
    "v_exp_f32 v73, v73\n" \
    "v_smfmac_f32_32x32x32_bf16 v[128:143], v[176:179], v[64:71], v192\n" \
    "v_exp_f32 v74, v74\n" \
    "s_mov_b32 m0, %[skv]\n" \
    "v_exp_f32 v75, v75\n" \
    "v_exp_f32 v76, v76\n" \
    "global_load_lds_dwordx4 v196, %[kvb] offset:0 sc1\n" \
    "v_exp_f32 v77, v77\n" \
    "v_exp_f32 v78, v78\n" \
    "v_exp_f32 v79, v79\n" \
    "v_exp_f32 v96, v96\n" \
    "v_exp_f32 v97, v97\n" \
    "v_smfmac_f32_32x32x32_bf16 v[128:143], v[180:183], v[72:79], v192\n" \
    "v_exp_f32 v98, v98\n" \
    "v_exp_f32 v99, v99\n" \
    "v_exp_f32 v100, v100\n" \
    "v_exp_f32 v101, v101\n" \
    "v_mfma_f32_32x32x16_bf16 v[112:127], v[172:175], v[164:167], 0\n" \
    "ds_read_b128 v[168:171], v193 offset:2048\n" \
    "v_exp_f32 v102, v102\n" \
    "v_exp_f32 v103, v103\n" \
    "v_exp_f32 v104, v104\n" \
    "v_exp_f32 v105, v105\n" \
    "v_smfmac_f32_32x32x32_bf16 v[144:159], v[176:179], v[96:103], v192\n" \
    "v_exp_f32 v106, v106\n" \
    "v_exp_f32 v107, v107\n" \
    "v_exp_f32 v108, v108\n" \
    "v_exp_f32 v109, v109\n" \
    "v_exp_f32 v110, v110\n" \
    "v_exp_f32 v111, v111\n" \
    "v_exp_f32 v80, v80\n" \
    "v_exp_f32 v81, v81\n" \
    "v_smfmac_f32_32x32x32_bf16 v[144:159], v[180:183], v[104:111], v192\n" \
    "ds_read_b128 v[176:179], v194 offset:2048\n" \
    "v_exp_f32 v82, v82\n" \
    "v_exp_f32 v83, v83\n" \
    "v_exp_f32 v84, v84\n" \
    "v_exp_f32 v85, v85\n" \
    "s_waitcnt lgkmcnt(1)\n" \
    "v_mfma_f32_32x32x16_bf16 v[64:79], v[168:171], v[160:163], 0\n" \
    "ds_read_b128 v[180:183], v194 offset:2560\n" \
    "v_exp_f32 v86, v86\n" \
    "v_exp_f32 v87, v87\n" \
    "v_exp_f32 v88, v88\n" \
    "v_exp_f32 v89, v89\n" \
    "v_smfmac_f32_32x32x32_bf16 v[128:143], v[184:187], v[80:87], v192\n" \
    "v_exp_f32 v90, v90\n" \
    "s_mov_b32 m0, %[skv]\n" \
    "v_exp_f32 v91, v91\n" \
    "v_exp_f32 v92, v92\n" \
    "global_load_lds_dwordx4 v196, %[kvb] offset:1024 sc1\n" \
    "v_exp_f32 v93, v93\n" \
    "v_exp_f32 v94, v94\n" \
    "v_exp_f32 v95, v95\n" \
    "v_exp_f32 v112, v112\n" \
    "v_exp_f32 v113, v113\n" \
    "v_smfmac_f32_32x32x32_bf16 v[128:143], v[188:191], v[88:95], v192\n" \
    "v_exp_f32 v114, v114\n" \
    "v_exp_f32 v115, v115\n" \
    "v_exp_f32 v116, v116\n" \
    "v_exp_f32 v117, v117\n" \
    "v_mfma_f32_32x32x16_bf16 v[96:111], v[168:171], v[164:167], 0\n" \
    "ds_read_b128 v[172:175], v193 offset:3072\n" \
    "v_exp_f32 v118, v118\n" \
    "v_exp_f32 v119, v119\n" \
    "v_exp_f32 v120, v120\n" \
    "v_exp_f32 v121, v121\n" \
    "v_smfmac_f32_32x32x32_bf16 v[144:159], v[184:187], v[112:119], v192\n" \
    "v_exp_f32 v122, v122\n" \
    "s_mov_b32 m0, %[sg]\n" \
    "v_exp_f32 v123, v123\n" \
    "v_exp_f32 v124, v124\n" \
    "global_load_lds_dwordx4 v[198:199], off\n" \
    "v_exp_f32 v125, v125\n" \
    "v_exp_f32 v126, v126\n" \
    "v_exp_f32 v127, v127\n" \
    "s_cmp_eq_u32 %[cnt], 1\n" \
    "s_cbranch_scc0 8f\n" \
    "s_setprio 0\n" \
    "8:\n" \
    "v_exp_f32 v64, v64\n" \
    "v_exp_f32 v65, v65\n" \
    "v_smfmac_f32_32x32x32_bf16 v[144:159], v[188:191], v[120:127], v192\n" \
    "ds_read_b128 v[184:187], v194 offset:3072\n" \
    "v_exp_f32 v66, v66\n" \
    "v_exp_f32 v67, v67\n" \
    "v_exp_f32 v68, v68\n" \
    "v_exp_f32 v69, v69\n" \
    "s_waitcnt lgkmcnt(1)\n" \
    "v_mfma_f32_32x32x16_bf16 v[80:95], v[172:175], v[160:163], 0\n" \
    "ds_read_b128 v[188:191], v194 offset:3584\n" \
    "v_exp_f32 v70, v70\n" \
    "v_exp_f32 v71, v71\n" \
    "v_exp_f32 v72, v72\n" \
    "v_exp_f32 v73, v73\n" \
    "v_smfmac_f32_32x32x32_bf16 v[128:143], v[176:179], v[64:71], v192\n" \
    "v_exp_f32 v74, v74\n" \
    "s_mov_b32 m0, %[skv]\n" \
    "v_exp_f32 v75, v75\n" \
    "v_exp_f32 v76, v76\n" \
    "global_load_lds_dwordx4 v196, %[kvb] offset:2048 sc1\n" \
    "v_exp_f32 v77, v77\n" \
    "v_exp_f32 v78, v78\n" \
    "v_exp_f32 v79, v79\n" \
    "v_exp_f32 v96, v96\n" \
    "v_exp_f32 v97, v97\n" \
    "v_smfmac_f32_32x32x32_bf16 v[128:143], v[180:183], v[72:79], v192\n" \
    "v_exp_f32 v98, v98\n" \
    "v_exp_f32 v99, v99\n" \
    "v_exp_f32 v100, v100\n" \
    "v_exp_f32 v101, v101\n" \
    "v_mfma_f32_32x32x16_bf16 v[112:127], v[172:175], v[164:167], 0\n" \
    "ds_read_b128 v[168:171], v193 offset:4096\n" \
    "v_exp_f32 v102, v102\n" \
    "v_exp_f32 v103, v103\n" \
    "v_exp_f32 v104, v104\n" \
    "v_exp_f32 v105, v105\n" \
    "v_smfmac_f32_32x32x32_bf16 v[144:159], v[176:179], v[96:103], v192\n" \
    "v_exp_f32 v106, v106\n" \
    "v_exp_f32 v107, v107\n" \
    "v_exp_f32 v108, v108\n" \
    "v_exp_f32 v109, v109\n" \
    "v_exp_f32 v110, v110\n" \
    "v_exp_f32 v111, v111\n" \
    "v_exp_f32 v80, v80\n" \
    "v_exp_f32 v81, v81\n" \
    "v_smfmac_f32_32x32x32_bf16 v[144:159], v[180:183], v[104:111], v192\n" \
    "ds_read_b128 v[176:179], v194 offset:4096\n" \
    "v_exp_f32 v82, v82\n" \
    "v_exp_f32 v83, v83\n" \
    "v_exp_f32 v84, v84\n" \
    "v_exp_f32 v85, v85\n" \
    "s_waitcnt lgkmcnt(1)\n" \
    "v_mfma_f32_32x32x16_bf16 v[64:79], v[168:171], v[160:163], 0\n" \
    "ds_read_b128 v[180:183], v194 offset:4608\n" \
    "v_exp_f32 v86, v86\n" \
    "v_exp_f32 v87, v87\n" \
    "v_exp_f32 v88, v88\n" \
    "v_exp_f32 v89, v89\n" \
    "v_smfmac_f32_32x32x32_bf16 v[128:143], v[184:187], v[80:87], v192\n" \
    "v_exp_f32 v90, v90\n" \
    "s_mov_b32 m0, %[skv]\n" \
    "v_exp_f32 v91, v91\n" \
    "v_exp_f32 v92, v92\n" \
    "global_load_lds_dwordx4 v196, %[kvb] offset:3072 sc1\n" \
    "v_exp_f32 v93, v93\n" \
    "v_exp_f32 v94, v94\n" \
    "v_exp_f32 v95, v95\n" \
    "v_exp_f32 v112, v112\n" \
    "v_exp_f32 v113, v113\n" \
    "v_smfmac_f32_32x32x32_bf16 v[128:143], v[188:191], v[88:95], v192\n" \
    "v_exp_f32 v114, v114\n" \
    "v_exp_f32 v115, v115\n" \
    "v_exp_f32 v116, v116\n" \
    "v_exp_f32 v117, v117\n" \
    "v_mfma_f32_32x32x16_bf16 v[96:111], v[168:171], v[164:167], 0\n" \
    "ds_read_b128 v[172:175], v193 offset:5120\n" \
    "v_exp_f32 v118, v118\n" \
    "v_exp_f32 v119, v119\n" \
    "v_exp_f32 v120, v120\n" \
    "v_exp_f32 v121, v121\n" \
    "v_smfmac_f32_32x32x32_bf16 v[144:159], v[184:187], v[112:119], v192\n" \
    "v_exp_f32 v122, v122\n" \
    "v_exp_f32 v123, v123\n" \
    "v_exp_f32 v124, v124\n" \
    "v_exp_f32 v125, v125\n" \
    "v_exp_f32 v126, v126\n" \
    "v_exp_f32 v127, v127\n" \
    "v_add_u32 v196, 0x2000, v196\n" \
    "v_mov_b32 v198, v200\n" \
    "v_mov_b32 v199, v201\n" \
    "s_add_u32 %[skv], %[skv], 0x2000\n" \
    "s_add_u32 %[sg], %[sg], 0x400\n" \
    "v_mov_b32 v220, v221\n" \
    "v_add_u32 v193, 4096, v193\n" \
    "v_add_u32 v194, v195, v194\n"

#define ATT_A2 \
    "s_mov_b32 %[cnt], 1\n" \
    "v_exp_f32 v64, v64\n" \
    "v_exp_f32 v65, v65\n" \
    "v_smfmac_f32_32x32x32_bf16 v[144:159], v[188:191], v[120:127], v192\n" \
    "global_store_dwordx4 v220, v[232:235], %[wsb] sc1\n" \
    "ds_read_b128 v[184:187], v194 offset:1024\n" \
    "v_exp_f32 v66, v66\n" \
    "v_exp_f32 v67, v67\n" \
    "v_exp_f32 v68, v68\n" \
    "v_exp_f32 v69, v69\n" \
    "s_waitcnt lgkmcnt(1)\n" \
    "v_mfma_f32_32x32x16_bf16 v[80:95], v[172:175], v[160:163], 0\n" \
    "ds_read_b128 v[188:191], v194 offset:1536\n" \
    "v_exp_f32 v70, v70\n" \
    "v_exp_f32 v71, v71\n" \
    "v_exp_f32 v72, v72\n" \
    "v_exp_f32 v73, v73\n" \
    "v_smfmac_f32_32x32x32_bf16 v[128:143], v[176:179], v[64:71], v192\n" \
    "v_exp_f32 v74, v74\n" \
    "s_mov_b32 m0, %[skv]\n" \
    "v_exp_f32 v75, v75\n" \
    "v_exp_f32 v76, v76\n" \
    "global_load_lds_dwordx4 v196, %[kvb] offset:0 sc1\n" \
    "v_exp_f32 v77, v77\n" \
    "v_exp_f32 v78, v78\n" \
    "v_exp_f32 v79, v79\n" \
    "v_exp_f32 v96, v96\n" \
    "v_exp_f32 v97, v97\n" \
    "v_smfmac_f32_32x32x32_bf16 v[128:143], v[180:183], v[72:79], v192\n" \
    "v_exp_f32 v98, v98\n" \
    "v_exp_f32 v99, v99\n" \
    "v_exp_f32 v100, v100\n" \
    "v_exp_f32 v101, v101\n" \
    "v_mfma_f32_32x32x16_bf16 v[112:127], v[172:175], v[164:167], 0\n" \
    "ds_read_b128 v[168:171], v193 offset:2048\n" \
    "v_exp_f32 v102, v102\n" \
    "v_exp_f32 v103, v103\n" \
    "v_exp_f32 v104, v104\n" \
    "v_exp_f32 v105, v105\n" \
    "v_smfmac_f32_32x32x32_bf16 v[144:159], v[176:179], v[96:103], v192\n" \
    "v_exp_f32 v106, v106\n" \
    "v_exp_f32 v107, v107\n" \
    "v_exp_f32 v108, v108\n" \
    "v_exp_f32 v109, v109\n" \
    "v_exp_f32 v110, v110\n" \
    "v_exp_f32 v111, v111\n" \
    "v_exp_f32 v80, v80\n" \
    "v_exp_f32 v81, v81\n" \
    "v_smfmac_f32_32x32x32_bf16 v[144:159], v[180:183], v[104:111], v192\n" \
    "ds_read_b128 v[176:179], v194 offset:2048\n" \
    "v_exp_f32 v82, v82\n" \
    "v_exp_f32 v83, v83\n" \
    "v_exp_f32 v84, v84\n" \
    "v_exp_f32 v85, v85\n" \
    "s_waitcnt lgkmcnt(1)\n" \
    "v_mfma_f32_32x32x16_bf16 v[64:79], v[168:171], v[160:163], 0\n" \
    "ds_read_b128 v[180:183], v194 offset:2560\n" \
    "v_exp_f32 v86, v86\n" \
    "v_exp_f32 v87, v87\n" \
    "v_exp_f32 v88, v88\n" \
    "v_exp_f32 v89, v89\n" \
    "v_smfmac_f32_32x32x32_bf16 v[128:143], v[184:187], v[80:87], v192\n" \
    "v_exp_f32 v90, v90\n" \
    "s_mov_b32 m0, %[skv]\n" \
    "v_exp_f32 v91, v91\n" \
    "v_exp_f32 v92, v92\n" \
    "global_load_lds_dwordx4 v196, %[kvb] offset:1024 sc1\n" \
    "v_exp_f32 v93, v93\n" \
    "v_exp_f32 v94, v94\n" \
    "v_exp_f32 v95, v95\n" \
    "v_exp_f32 v112, v112\n" \
    "v_exp_f32 v113, v113\n" \
    "v_smfmac_f32_32x32x32_bf16 v[128:143], v[188:191], v[88:95], v192\n" \
    "v_exp_f32 v114, v114\n" \
    "v_exp_f32 v115, v115\n" \
    "v_exp_f32 v116, v116\n" \
    "v_exp_f32 v117, v117\n" \
    "v_mfma_f32_32x32x16_bf16 v[96:111], v[168:171], v[164:167], 0\n" \
    "ds_read_b128 v[172:175], v193 offset:3072\n" \
    "v_exp_f32 v118, v118\n" \
    "v_exp_f32 v119, v119\n" \
    "v_exp_f32 v120, v120\n" \
    "v_exp_f32 v121, v121\n" \
    "v_smfmac_f32_32x32x32_bf16 v[144:159], v[184:187], v[112:119], v192\n" \
    "v_exp_f32 v122, v122\n" \
    "s_mov_b32 m0, %[sg]\n" \
    "v_exp_f32 v123, v123\n" \
    "v_exp_f32 v124, v124\n" \
    "global_load_lds_dwordx4 v[198:199], off\n" \
    "v_exp_f32 v125, v125\n" \
    "v_exp_f32 v126, v126\n" \
    "v_exp_f32 v127, v127\n" \
    "s_cmp_eq_u32 %[cnt], 1\n" \
    "s_cbranch_scc0 8f\n" \
    "s_setprio 0\n" \
    "8:\n" \
    "v_exp_f32 v64, v64\n" \
    "v_exp_f32 v65, v65\n" \
    "v_smfmac_f32_32x32x32_bf16 v[144:159], v[188:191], v[120:127], v192\n" \
    "ds_read_b128 v[184:187], v194 offset:3072\n" \
    "v_exp_f32 v66, v66\n" \
    "v_exp_f32 v67, v67\n" \
    "v_exp_f32 v68, v68\n" \
    "v_exp_f32 v69, v69\n" \
    "s_waitcnt lgkmcnt(1)\n" \
    "v_mfma_f32_32x32x16_bf16 v[80:95], v[172:175], v[160:163], 0\n" \
    "ds_read_b128 v[188:191], v194 offset:3584\n" \
    "v_exp_f32 v70, v70\n" \
    "v_exp_f32 v71, v71\n" \
    "v_exp_f32 v72, v72\n" \
    "v_exp_f32 v73, v73\n" \
    "v_smfmac_f32_32x32x32_bf16 v[128:143], v[176:179], v[64:71], v192\n" \
    "v_exp_f32 v74, v74\n" \
    "s_mov_b32 m0, %[skv]\n" \
    "v_exp_f32 v75, v75\n" \
    "v_exp_f32 v76, v76\n" \
    "global_load_lds_dwordx4 v196, %[kvb] offset:2048 sc1\n" \
    "v_exp_f32 v77, v77\n" \
    "v_exp_f32 v78, v78\n" \
    "v_exp_f32 v79, v79\n" \
    "v_exp_f32 v96, v96\n" \
    "v_exp_f32 v97, v97\n" \
    "v_smfmac_f32_32x32x32_bf16 v[128:143], v[180:183], v[72:79], v192\n" \
    "v_exp_f32 v98, v98\n" \
    "v_exp_f32 v99, v99\n" \
    "v_exp_f32 v100, v100\n" \
    "v_exp_f32 v101, v101\n" \
    "v_mfma_f32_32x32x16_bf16 v[112:127], v[172:175], v[164:167], 0\n" \
    "ds_read_b128 v[168:171], v193 offset:4096\n" \
    "v_exp_f32 v102, v102\n" \
    "v_exp_f32 v103, v103\n" \
    "v_exp_f32 v104, v104\n" \
    "v_exp_f32 v105, v105\n" \
    "v_smfmac_f32_32x32x32_bf16 v[144:159], v[176:179], v[96:103], v192\n" \
    "v_exp_f32 v106, v106\n" \
    "v_exp_f32 v107, v107\n" \
    "v_exp_f32 v108, v108\n" \
    "v_exp_f32 v109, v109\n" \
    "v_exp_f32 v110, v110\n" \
    "v_exp_f32 v111, v111\n" \
    "v_exp_f32 v80, v80\n" \
    "v_exp_f32 v81, v81\n" \
    "v_smfmac_f32_32x32x32_bf16 v[144:159], v[180:183], v[104:111], v192\n" \
    "ds_read_b128 v[176:179], v194 offset:4096\n" \
    "v_exp_f32 v82, v82\n" \
    "v_exp_f32 v83, v83\n" \
    "v_exp_f32 v84, v84\n" \
    "v_exp_f32 v85, v85\n" \
    "s_waitcnt lgkmcnt(1)\n" \
    "v_mfma_f32_32x32x16_bf16 v[64:79], v[168:171], v[160:163], 0\n" \
    "ds_read_b128 v[180:183], v194 offset:4608\n" \
    "v_exp_f32 v86, v86\n" \
    "v_exp_f32 v87, v87\n" \
    "v_exp_f32 v88, v88\n" \
    "v_exp_f32 v89, v89\n" \
    "v_smfmac_f32_32x32x32_bf16 v[128:143], v[184:187], v[80:87], v192\n" \
    "v_exp_f32 v90, v90\n" \
    "s_mov_b32 m0, %[skv]\n" \
    "v_exp_f32 v91, v91\n" \
    "v_exp_f32 v92, v92\n" \
    "global_load_lds_dwordx4 v196, %[kvb] offset:3072 sc1\n" \
    "v_exp_f32 v93, v93\n" \
    "v_exp_f32 v94, v94\n" \
    "v_exp_f32 v95, v95\n" \
    "v_exp_f32 v112, v112\n" \
    "v_exp_f32 v113, v113\n" \
    "v_smfmac_f32_32x32x32_bf16 v[128:143], v[188:191], v[88:95], v192\n" \
    "v_exp_f32 v114, v114\n" \
    "v_exp_f32 v115, v115\n" \
    "v_exp_f32 v116, v116\n" \
    "v_exp_f32 v117, v117\n" \
    "v_mfma_f32_32x32x16_bf16 v[96:111], v[168:171], v[164:167], 0\n" \
    "ds_read_b128 v[172:175], v193 offset:5120\n" \
    "v_exp_f32 v118, v118\n" \
    "v_exp_f32 v119, v119\n" \
    "v_exp_f32 v120, v120\n" \
    "v_exp_f32 v121, v121\n" \
    "v_smfmac_f32_32x32x32_bf16 v[144:159], v[184:187], v[112:119], v192\n" \
    "v_exp_f32 v122, v122\n" \
    "v_exp_f32 v123, v123\n" \
    "v_exp_f32 v124, v124\n" \
    "v_exp_f32 v125, v125\n" \
    "v_exp_f32 v126, v126\n" \
    "v_exp_f32 v127, v127\n" \
    "v_add_u32 v196, 0x2000, v196\n" \
    "v_mov_b32 v198, v200\n" \
    "v_mov_b32 v199, v201\n" \
    "s_add_u32 %[skv], %[skv], 0x2000\n" \
    "s_add_u32 %[sg], %[sg], 0x400\n" \
    "v_mov_b32 v220, v221\n" \
    "v_add_u32 v193, 4096, v193\n" \
    "v_add_u32 v194, v195, v194\n"

#define ATT_B \
    "v_exp_f32 v64, v64\n" \
    "v_exp_f32 v65, v65\n" \
    "v_smfmac_f32_32x32x32_bf16 v[144:159], v[188:191], v[120:127], v192\n" \
    "ds_read_b128 v[184:187], v194 offset:1024\n" \
    "v_exp_f32 v66, v66\n" \
    "v_exp_f32 v67, v67\n" \
    "v_exp_f32 v68, v68\n" \
    "v_exp_f32 v69, v69\n" \
    "s_waitcnt lgkmcnt(1)\n" \
    "v_mfma_f32_32x32x16_bf16 v[80:95], v[172:175], v[160:163], 0\n" \
    "ds_read_b128 v[188:191], v194 offset:1536\n" \
    "v_exp_f32 v70, v70\n" \
    "v_exp_f32 v71, v71\n" \
    "v_exp_f32 v72, v72\n" \
    "v_exp_f32 v73, v73\n" \
    "v_smfmac_f32_32x32x32_bf16 v[128:143], v[176:179], v[64:71], v192\n" \
    "v_exp_f32 v74, v74\n" \
    "s_mov_b32 m0, %[skv3]\n" \
    "v_exp_f32 v75, v75\n" \
    "v_exp_f32 v76, v76\n" \
    "global_load_dwordx4 v[204:207], v202, %[xb]\n" \
    "v_exp_f32 v77, v77\n" \
    "v_exp_f32 v78, v78\n" \
    "v_exp_f32 v79, v79\n" \
    "v_exp_f32 v96, v96\n" \
    "v_exp_f32 v97, v97\n" \
    "v_smfmac_f32_32x32x32_bf16 v[128:143], v[180:183], v[72:79], v192\n" \
    "v_exp_f32 v98, v98\n" \
    "v_exp_f32 v99, v99\n" \
    "v_exp_f32 v100, v100\n" \
    "v_exp_f32 v101, v101\n" \
    "v_mfma_f32_32x32x16_bf16 v[112:127], v[172:175], v[164:167], 0\n" \
    "ds_read_b128 v[168:171], v193 offset:2048\n" \
    "v_exp_f32 v102, v102\n" \
    "v_exp_f32 v103, v103\n" \
    "v_exp_f32 v104, v104\n" \
    "v_exp_f32 v105, v105\n" \
    "v_smfmac_f32_32x32x32_bf16 v[144:159], v[176:179], v[96:103], v192\n" \
    "v_exp_f32 v106, v106\n" \
    "v_exp_f32 v107, v107\n" \
    "v_exp_f32 v108, v108\n" \
    "global_load_dwordx4 v[208:211], v202, %[xb] offset:16\n" \
    "v_exp_f32 v109, v109\n" \
    "v_exp_f32 v110, v110\n" \
    "v_exp_f32 v111, v111\n" \
    "v_exp_f32 v80, v80\n" \
    "v_exp_f32 v81, v81\n" \
    "v_smfmac_f32_32x32x32_bf16 v[144:159], v[180:183], v[104:111], v192\n" \
    "ds_read_b128 v[176:179], v194 offset:2048\n" \
    "v_exp_f32 v82, v82\n" \
    "v_exp_f32 v83, v83\n" \
    "v_exp_f32 v84, v84\n" \
    "v_exp_f32 v85, v85\n" \
    "s_waitcnt lgkmcnt(1)\n" \
    "v_mfma_f32_32x32x16_bf16 v[64:79], v[168:171], v[160:163], 0\n" \
    "ds_read_b128 v[180:183], v194 offset:2560\n" \
    "v_exp_f32 v86, v86\n" \
    "v_exp_f32 v87, v87\n" \
    "v_exp_f32 v88, v88\n" \
    "v_exp_f32 v89, v89\n" \
    "v_smfmac_f32_32x32x32_bf16 v[128:143], v[184:187], v[80:87], v192\n" \
    "v_exp_f32 v90, v90\n" \
    "v_exp_f32 v91, v91\n" \
    "v_exp_f32 v92, v92\n" \
    "global_load_lds_dwordx4 v197, %[kvb] offset:0 sc1\n" \
    "v_exp_f32 v93, v93\n" \
    "v_exp_f32 v94, v94\n" \
    "v_exp_f32 v95, v95\n" \
    "v_exp_f32 v112, v112\n" \
    "v_exp_f32 v113, v113\n" \
    "v_smfmac_f32_32x32x32_bf16 v[128:143], v[188:191], v[88:95], v192\n" \
    "v_exp_f32 v114, v114\n" \
    "v_exp_f32 v115, v115\n" \
    "v_exp_f32 v116, v116\n" \
    "v_exp_f32 v117, v117\n" \
    "v_mfma_f32_32x32x16_bf16 v[96:111], v[168:171], v[164:167], 0\n" \
    "ds_read_b128 v[172:175], v193 offset:3072\n" \
    "v_exp_f32 v118, v118\n" \
    "v_exp_f32 v119, v119\n" \
    "v_exp_f32 v120, v120\n" \
    "v_exp_f32 v121, v121\n" \
    "v_smfmac_f32_32x32x32_bf16 v[144:159], v[184:187], v[112:119], v192\n" \
    "v_exp_f32 v122, v122\n" \
    "v_exp_f32 v123, v123\n" \
    "v_exp_f32 v124, v124\n" \
    "global_load_lds_dwordx4 v197, %[kvb] offset:1024 sc1\n" \
    "v_exp_f32 v125, v125\n" \
    "v_exp_f32 v126, v126\n" \
    "v_exp_f32 v127, v127\n" \
    "v_exp_f32 v64, v64\n" \
    "v_exp_f32 v65, v65\n" \
    "v_smfmac_f32_32x32x32_bf16 v[144:159], v[188:191], v[120:127], v192\n" \
    "ds_read_b128 v[184:187], v194 offset:3072\n" \
    "v_exp_f32 v66, v66\n" \
    "v_exp_f32 v67, v67\n" \
    "v_exp_f32 v68, v68\n" \
    "v_exp_f32 v69, v69\n" \
    "s_waitcnt lgkmcnt(1)\n" \
    "v_mfma_f32_32x32x16_bf16 v[80:95], v[172:175], v[160:163], 0\n" \
    "ds_read_b128 v[188:191], v194 offset:3584\n" \
    "v_exp_f32 v70, v70\n" \
    "v_exp_f32 v71, v71\n" \
    "v_exp_f32 v72, v72\n" \
    "v_exp_f32 v73, v73\n" \
    "v_smfmac_f32_32x32x32_bf16 v[128:143], v[176:179], v[64:71], v192\n" \
    "v_exp_f32 v74, v74\n" \
    "v_exp_f32 v75, v75\n" \
    "v_exp_f32 v76, v76\n" \
    "global_load_lds_dwordx4 v197, %[kvb] offset:2048 sc1\n" \
    "v_exp_f32 v77, v77\n" \
    "v_exp_f32 v78, v78\n" \
    "v_exp_f32 v79, v79\n" \
    "v_exp_f32 v96, v96\n" \
    "v_exp_f32 v97, v97\n" \
    "v_smfmac_f32_32x32x32_bf16 v[128:143], v[180:183], v[72:79], v192\n" \
    "v_exp_f32 v98, v98\n" \
    "v_exp_f32 v99, v99\n" \
    "v_exp_f32 v100, v100\n" \
    "v_exp_f32 v101, v101\n" \
    "v_mfma_f32_32x32x16_bf16 v[112:127], v[172:175], v[164:167], 0\n" \
    "ds_read_b128 v[168:171], v193 offset:4096\n" \
    "v_exp_f32 v102, v102\n" \
    "v_exp_f32 v103, v103\n" \
    "v_exp_f32 v104, v104\n" \
    "v_exp_f32 v105, v105\n" \
    "v_smfmac_f32_32x32x32_bf16 v[144:159], v[176:179], v[96:103], v192\n" \
    "v_exp_f32 v106, v106\n" \
    "v_exp_f32 v107, v107\n" \
    "v_exp_f32 v108, v108\n" \
    "global_load_lds_dwordx4 v197, %[kvb] offset:3072 sc1\n" \
    "v_exp_f32 v109, v109\n" \
    "v_exp_f32 v110, v110\n" \
    "v_exp_f32 v111, v111\n" \
    "v_exp_f32 v80, v80\n" \
    "v_exp_f32 v81, v81\n" \
    "v_smfmac_f32_32x32x32_bf16 v[144:159], v[180:183], v[104:111], v192\n" \
    "ds_read_b128 v[176:179], v194 offset:4096\n" \
    "v_exp_f32 v82, v82\n" \
    "v_exp_f32 v83, v83\n" \
    "v_exp_f32 v84, v84\n" \
    "v_exp_f32 v85, v85\n" \
    "s_waitcnt lgkmcnt(1)\n" \
    "v_mfma_f32_32x32x16_bf16 v[64:79], v[168:171], v[160:163], 0\n" \
    "ds_read_b128 v[180:183], v194 offset:4608\n" \
    "v_exp_f32 v86, v86\n" \
    "v_exp_f32 v87, v87\n" \
    "v_exp_f32 v88, v88\n" \
    "v_exp_f32 v89, v89\n" \
    "v_smfmac_f32_32x32x32_bf16 v[128:143], v[184:187], v[80:87], v192\n" \
    "v_exp_f32 v90, v90\n" \
    "v_exp_f32 v91, v91\n" \
    "v_exp_f32 v92, v92\n" \
    "global_load_dword v212, v203, %[wdb] offset:0\n" \
    "global_load_dword v213, v203, %[wdb] offset:1024\n" \
    "v_exp_f32 v93, v93\n" \
    "v_exp_f32 v94, v94\n" \
    "v_exp_f32 v95, v95\n" \
    "v_exp_f32 v112, v112\n" \
    "v_exp_f32 v113, v113\n" \
    "v_smfmac_f32_32x32x32_bf16 v[128:143], v[188:191], v[88:95], v192\n" \
    "v_exp_f32 v114, v114\n" \
    "v_exp_f32 v115, v115\n" \
    "v_exp_f32 v116, v116\n" \
    "v_exp_f32 v117, v117\n" \
    "v_mfma_f32_32x32x16_bf16 v[96:111], v[168:171], v[164:167], 0\n" \
    "ds_read_b128 v[172:175], v193 offset:5120\n" \
    "v_exp_f32 v118, v118\n" \
    "v_exp_f32 v119, v119\n" \
    "v_exp_f32 v120, v120\n" \
    "v_exp_f32 v121, v121\n" \
    "v_smfmac_f32_32x32x32_bf16 v[144:159], v[184:187], v[112:119], v192\n" \
    "v_exp_f32 v122, v122\n" \
    "v_exp_f32 v123, v123\n" \
    "v_exp_f32 v124, v124\n" \
    "global_load_dword v214, v203, %[wdb] offset:2048\n" \
    "global_load_dword v215, v203, %[wdb] offset:3072\n" \
    "v_exp_f32 v125, v125\n" \
    "v_exp_f32 v126, v126\n" \
    "v_exp_f32 v127, v127\n" \
    "v_add_u32 v203, 0x1000, v203\n" \
    "v_add_u32 v193, 4096, v193\n" \
    "v_add_u32 v194, v195, v194\n" \
    "v_exp_f32 v64, v64\n" \
    "v_exp_f32 v65, v65\n" \
    "v_smfmac_f32_32x32x32_bf16 v[144:159], v[188:191], v[120:127], v192\n" \
    "ds_read_b128 v[184:187], v194 offset:1024\n" \
    "v_exp_f32 v66, v66\n" \
    "v_exp_f32 v67, v67\n" \
    "v_exp_f32 v68, v68\n" \
    "v_exp_f32 v69, v69\n" \
    "s_waitcnt lgkmcnt(1)\n" \
    "v_mfma_f32_32x32x16_bf16 v[80:95], v[172:175], v[160:163], 0\n" \
    "ds_read_b128 v[188:191], v194 offset:1536\n" \
    "v_exp_f32 v70, v70\n" \
    "v_exp_f32 v71, v71\n" \
    "v_exp_f32 v72, v72\n" \
    "v_exp_f32 v73, v73\n" \
    "v_smfmac_f32_32x32x32_bf16 v[128:143], v[176:179], v[64:71], v192\n" \
    "v_exp_f32 v74, v74\n" \
    "v_exp_f32 v75, v75\n" \
    "v_exp_f32 v76, v76\n" \
    "global_load_dword v216, v203, %[wdb] offset:0\n" \
    "global_load_dword v217, v203, %[wdb] offset:1024\n" \
    "v_exp_f32 v77, v77\n" \
    "v_exp_f32 v78, v78\n" \
    "v_exp_f32 v79, v79\n" \
    "v_exp_f32 v96, v96\n" \
    "v_exp_f32 v97, v97\n" \
    "v_smfmac_f32_32x32x32_bf16 v[128:143], v[180:183], v[72:79], v192\n" \
    "v_exp_f32 v98, v98\n" \
    "v_exp_f32 v99, v99\n" \
    "v_exp_f32 v100, v100\n" \
    "v_exp_f32 v101, v101\n" \
    "v_mfma_f32_32x32x16_bf16 v[112:127], v[172:175], v[164:167], 0\n" \
    "ds_read_b128 v[168:171], v193 offset:2048\n" \
    "v_exp_f32 v102, v102\n" \
    "v_exp_f32 v103, v103\n" \
    "v_exp_f32 v104, v104\n" \
    "v_exp_f32 v105, v105\n" \
    "v_smfmac_f32_32x32x32_bf16 v[144:159], v[176:179], v[96:103], v192\n" \
    "v_exp_f32 v106, v106\n" \
    "v_exp_f32 v107, v107\n" \
    "v_exp_f32 v108, v108\n" \
    "global_load_dword v218, v203, %[wdb] offset:2048\n" \
    "global_load_dword v219, v203, %[wdb] offset:3072\n" \
    "v_exp_f32 v109, v109\n" \
    "v_exp_f32 v110, v110\n" \
    "v_exp_f32 v111, v111\n" \
    "v_exp_f32 v80, v80\n" \
    "v_exp_f32 v81, v81\n" \
    "v_smfmac_f32_32x32x32_bf16 v[144:159], v[180:183], v[104:111], v192\n" \
    "s_waitcnt vmcnt(12)\n" \
    "ds_read_b64_tr_b16 v[220:221], v228\n" \
    "ds_read_b64_tr_b16 v[222:223], v229\n" \
    "ds_read_b64_tr_b16 v[224:225], v228 offset:256\n" \
    "ds_read_b64_tr_b16 v[226:227], v229 offset:256\n" \
    "ds_read_b128 v[176:179], v194 offset:2048\n" \
    "v_exp_f32 v82, v82\n" \
    "v_exp_f32 v83, v83\n" \
    "v_exp_f32 v84, v84\n" \
    "v_exp_f32 v85, v85\n" \
    "s_waitcnt lgkmcnt(1)\n" \
    "v_mfma_f32_32x32x16_bf16 v[64:79], v[168:171], v[160:163], 0\n" \
    "ds_read_b128 v[180:183], v194 offset:2560\n" \
    "v_exp_f32 v86, v86\n" \
    "v_exp_f32 v87, v87\n" \
    "v_exp_f32 v88, v88\n" \
    "v_exp_f32 v89, v89\n" \
    "v_smfmac_f32_32x32x32_bf16 v[128:143], v[184:187], v[80:87], v192\n" \
    "v_exp_f32 v90, v90\n" \
    "v_exp_f32 v91, v91\n" \
    "v_exp_f32 v92, v92\n" \
    "v_mfma_f32_16x16x32_bf16 v[232:235], v[220:223], v[204:207], 0\n" \
    "v_exp_f32 v93, v93\n" \
    "v_exp_f32 v94, v94\n" \
    "v_exp_f32 v95, v95\n" \
    "v_exp_f32 v112, v112\n" \
    "v_exp_f32 v113, v113\n" \
    "v_smfmac_f32_32x32x32_bf16 v[128:143], v[188:191], v[88:95], v192\n" \
    "v_exp_f32 v114, v114\n" \
    "v_exp_f32 v115, v115\n" \
    "v_exp_f32 v116, v116\n" \
    "v_exp_f32 v117, v117\n" \
    "v_mfma_f32_32x32x16_bf16 v[96:111], v[168:171], v[164:167], 0\n" \
    "ds_read_b128 v[172:175], v193 offset:3072\n" \
    "v_exp_f32 v118, v118\n" \
    "v_exp_f32 v119, v119\n" \
    "v_exp_f32 v120, v120\n" \
    "v_exp_f32 v121, v121\n" \
    "v_smfmac_f32_32x32x32_bf16 v[144:159], v[184:187], v[112:119], v192\n" \
    "v_exp_f32 v122, v122\n" \
    "v_exp_f32 v123, v123\n" \
    "v_exp_f32 v124, v124\n" \
    "v_exp_f32 v125, v125\n" \
    "v_exp_f32 v126, v126\n" \
    "v_exp_f32 v127, v127\n" \
    "s_setprio 0\n" \
    "v_exp_f32 v64, v64\n" \
    "v_exp_f32 v65, v65\n" \
    "v_smfmac_f32_32x32x32_bf16 v[144:159], v[188:191], v[120:127], v192\n" \
    "ds_read_b128 v[184:187], v194 offset:3072\n" \
    "v_exp_f32 v66, v66\n" \
    "v_exp_f32 v67, v67\n" \
    "v_exp_f32 v68, v68\n" \
    "v_exp_f32 v69, v69\n" \
    "s_waitcnt lgkmcnt(1)\n" \
    "v_mfma_f32_32x32x16_bf16 v[80:95], v[172:175], v[160:163], 0\n" \
    "ds_read_b128 v[188:191], v194 offset:3584\n" \
    "v_exp_f32 v70, v70\n" \
    "v_exp_f32 v71, v71\n" \
    "v_exp_f32 v72, v72\n" \
    "v_exp_f32 v73, v73\n" \
    "v_smfmac_f32_32x32x32_bf16 v[128:143], v[176:179], v[64:71], v192\n" \
    "v_exp_f32 v74, v74\n" \
    "v_exp_f32 v75, v75\n" \
    "v_exp_f32 v76, v76\n" \
    "v_mfma_f32_16x16x32_bf16 v[232:235], v[224:227], v[208:211], v[232:235]\n" \
    "v_exp_f32 v77, v77\n" \
    "v_exp_f32 v78, v78\n" \
    "v_exp_f32 v79, v79\n" \
    "v_exp_f32 v96, v96\n" \
    "v_exp_f32 v97, v97\n" \
    "v_smfmac_f32_32x32x32_bf16 v[128:143], v[180:183], v[72:79], v192\n" \
    "v_exp_f32 v98, v98\n" \
    "v_exp_f32 v99, v99\n" \
    "v_exp_f32 v100, v100\n" \
    "v_exp_f32 v101, v101\n" \
    "v_mfma_f32_32x32x16_bf16 v[112:127], v[172:175], v[164:167], 0\n" \
    "ds_read_b128 v[168:171], v193 offset:4096\n" \
    "v_exp_f32 v102, v102\n" \
    "v_exp_f32 v103, v103\n" \
    "v_exp_f32 v104, v104\n" \
    "v_exp_f32 v105, v105\n" \
    "v_smfmac_f32_32x32x32_bf16 v[144:159], v[176:179], v[96:103], v192\n" \
    "v_exp_f32 v106, v106\n" \
    "v_exp_f32 v107, v107\n" \
    "v_exp_f32 v108, v108\n" \
    "v_exp_f32 v109, v109\n" \
    "v_exp_f32 v110, v110\n" \
    "v_exp_f32 v111, v111\n" \
    "v_exp_f32 v80, v80\n" \
    "v_exp_f32 v81, v81\n" \
    "v_smfmac_f32_32x32x32_bf16 v[144:159], v[180:183], v[104:111], v192\n" \
    "ds_write_b128 v230, v[232:235]\n" \
    "ds_read_b128 v[176:179], v194 offset:4096\n" \
    "v_exp_f32 v82, v82\n" \
    "v_exp_f32 v83, v83\n" \
    "v_exp_f32 v84, v84\n" \
    "v_exp_f32 v85, v85\n" \
    "s_waitcnt lgkmcnt(1)\n" \
    "v_mfma_f32_32x32x16_bf16 v[64:79], v[168:171], v[160:163], 0\n" \
    "ds_read_b128 v[180:183], v194 offset:4608\n" \
    "v_exp_f32 v86, v86\n" \
    "v_exp_f32 v87, v87\n" \
    "v_exp_f32 v88, v88\n" \
    "v_exp_f32 v89, v89\n" \
    "v_smfmac_f32_32x32x32_bf16 v[128:143], v[184:187], v[80:87], v192\n" \
    "v_exp_f32 v90, v90\n" \
    "v_exp_f32 v91, v91\n" \
    "v_exp_f32 v92, v92\n" \
    "v_exp_f32 v93, v93\n" \
    "v_exp_f32 v94, v94\n" \
    "v_exp_f32 v95, v95\n" \
    "v_exp_f32 v112, v112\n" \
    "v_exp_f32 v113, v113\n" \
    "v_smfmac_f32_32x32x32_bf16 v[128:143], v[188:191], v[88:95], v192\n" \
    "v_exp_f32 v114, v114\n" \
    "v_exp_f32 v115, v115\n" \
    "v_exp_f32 v116, v116\n" \
    "v_exp_f32 v117, v117\n" \
    "v_mfma_f32_32x32x16_bf16 v[96:111], v[168:171], v[164:167], 0\n" \
    "ds_read_b128 v[172:175], v193 offset:5120\n" \
    "v_exp_f32 v118, v118\n" \
    "v_exp_f32 v119, v119\n" \
    "v_exp_f32 v120, v120\n" \
    "v_exp_f32 v121, v121\n" \
    "v_smfmac_f32_32x32x32_bf16 v[144:159], v[184:187], v[112:119], v192\n" \
    "v_exp_f32 v122, v122\n" \
    "v_exp_f32 v123, v123\n" \
    "v_exp_f32 v124, v124\n" \
    "v_exp_f32 v125, v125\n" \
    "v_exp_f32 v126, v126\n" \
    "v_exp_f32 v127, v127\n" \
    "v_add_u32 v193, 4096, v193\n" \
    "v_add_u32 v194, v195, v194\n" \
    "s_waitcnt vmcnt(0)\n"

#define ATT_C \
    "s_mov_b32 %[cnt], 2\n" \
    "1:\n" \
    "v_exp_f32 v64, v64\n" \
    "v_exp_f32 v65, v65\n" \
    "v_smfmac_f32_32x32x32_bf16 v[144:159], v[188:191], v[120:127], v192\n" \
    "ds_read_b128 v[184:187], v194 offset:1024\n" \
    "v_exp_f32 v66, v66\n" \
    "v_exp_f32 v67, v67\n" \
    "v_exp_f32 v68, v68\n" \
    "v_exp_f32 v69, v69\n" \
    "s_waitcnt lgkmcnt(1)\n" \
    "v_mfma_f32_32x32x16_bf16 v[80:95], v[172:175], v[160:163], 0\n" \
    "ds_read_b128 v[188:191], v194 offset:1536\n" \
    "v_exp_f32 v70, v70\n" \
    "v_exp_f32 v71, v71\n" \
    "v_exp_f32 v72, v72\n" \
    "v_exp_f32 v73, v73\n" \
    "v_smfmac_f32_32x32x32_bf16 v[128:143], v[176:179], v[64:71], v192\n" \
    "v_exp_f32 v74, v74\n" \
    "s_mov_b32 m0, %[swo]\n" \
    "v_exp_f32 v75, v75\n" \
    "v_exp_f32 v76, v76\n" \
    "global_load_lds_dwordx4 v236, %[wob] offset:0\n" \
    "v_exp_f32 v77, v77\n" \
    "v_exp_f32 v78, v78\n" \
    "v_exp_f32 v79, v79\n" \
    "v_exp_f32 v96, v96\n" \
    "v_exp_f32 v97, v97\n" \
    "v_smfmac_f32_32x32x32_bf16 v[128:143], v[180:183], v[72:79], v192\n" \
    "v_exp_f32 v98, v98\n" \
    "v_exp_f32 v99, v99\n" \
    "v_exp_f32 v100, v100\n" \
    "v_exp_f32 v101, v101\n" \
    "v_mfma_f32_32x32x16_bf16 v[112:127], v[172:175], v[164:167], 0\n" \
    "ds_read_b128 v[168:171], v193 offset:2048\n" \
    "v_exp_f32 v102, v102\n" \
    "v_exp_f32 v103, v103\n" \
    "v_exp_f32 v104, v104\n" \
    "v_exp_f32 v105, v105\n" \
    "v_smfmac_f32_32x32x32_bf16 v[144:159], v[176:179], v[96:103], v192\n" \
    "v_exp_f32 v106, v106\n" \
    "v_exp_f32 v107, v107\n" \
    "v_exp_f32 v108, v108\n" \
    "v_exp_f32 v109, v109\n" \
    "v_exp_f32 v110, v110\n" \
    "v_exp_f32 v111, v111\n" \
    "v_exp_f32 v80, v80\n" \
    "v_exp_f32 v81, v81\n" \
    "v_smfmac_f32_32x32x32_bf16 v[144:159], v[180:183], v[104:111], v192\n" \
    "ds_read_b128 v[176:179], v194 offset:2048\n" \
    "v_exp_f32 v82, v82\n" \
    "v_exp_f32 v83, v83\n" \
    "v_exp_f32 v84, v84\n" \
    "v_exp_f32 v85, v85\n" \
    "s_waitcnt lgkmcnt(1)\n" \
    "v_mfma_f32_32x32x16_bf16 v[64:79], v[168:171], v[160:163], 0\n" \
    "ds_read_b128 v[180:183], v194 offset:2560\n" \
    "v_exp_f32 v86, v86\n" \
    "v_exp_f32 v87, v87\n" \
    "v_exp_f32 v88, v88\n" \
    "v_exp_f32 v89, v89\n" \
    "v_smfmac_f32_32x32x32_bf16 v[128:143], v[184:187], v[80:87], v192\n" \
    "v_exp_f32 v90, v90\n" \
    "v_exp_f32 v91, v91\n" \
    "v_exp_f32 v92, v92\n" \
    "global_load_lds_dwordx4 v237, %[wob] offset:1024\n" \
    "v_exp_f32 v93, v93\n" \
    "v_exp_f32 v94, v94\n" \
    "v_exp_f32 v95, v95\n" \
    "v_exp_f32 v112, v112\n" \
    "v_exp_f32 v113, v113\n" \
    "v_smfmac_f32_32x32x32_bf16 v[128:143], v[188:191], v[88:95], v192\n" \
    "v_exp_f32 v114, v114\n" \
    "v_exp_f32 v115, v115\n" \
    "v_exp_f32 v116, v116\n" \
    "v_exp_f32 v117, v117\n" \
    "v_mfma_f32_32x32x16_bf16 v[96:111], v[168:171], v[164:167], 0\n" \
    "ds_read_b128 v[172:175], v193 offset:3072\n" \
    "v_exp_f32 v118, v118\n" \
    "v_exp_f32 v119, v119\n" \
    "v_exp_f32 v120, v120\n" \
    "v_exp_f32 v121, v121\n" \
    "v_smfmac_f32_32x32x32_bf16 v[144:159], v[184:187], v[112:119], v192\n" \
    "v_exp_f32 v122, v122\n" \
    "v_exp_f32 v123, v123\n" \
    "v_exp_f32 v124, v124\n" \
    "v_exp_f32 v125, v125\n" \
    "v_exp_f32 v126, v126\n" \
    "v_exp_f32 v127, v127\n" \
    "v_exp_f32 v64, v64\n" \
    "v_exp_f32 v65, v65\n" \
    "v_smfmac_f32_32x32x32_bf16 v[144:159], v[188:191], v[120:127], v192\n" \
    "ds_read_b128 v[184:187], v194 offset:3072\n" \
    "v_exp_f32 v66, v66\n" \
    "v_exp_f32 v67, v67\n" \
    "v_exp_f32 v68, v68\n" \
    "v_exp_f32 v69, v69\n" \
    "s_waitcnt lgkmcnt(1)\n" \
    "v_mfma_f32_32x32x16_bf16 v[80:95], v[172:175], v[160:163], 0\n" \
    "ds_read_b128 v[188:191], v194 offset:3584\n" \
    "v_exp_f32 v70, v70\n" \
    "v_exp_f32 v71, v71\n" \
    "v_exp_f32 v72, v72\n" \
    "v_exp_f32 v73, v73\n" \
    "v_smfmac_f32_32x32x32_bf16 v[128:143], v[176:179], v[64:71], v192\n" \
    "v_exp_f32 v74, v74\n" \
    "v_exp_f32 v75, v75\n" \
    "v_exp_f32 v76, v76\n" \
    "global_load_lds_dwordx4 v238, %[wob] offset:2048\n" \
    "v_exp_f32 v77, v77\n" \
    "v_exp_f32 v78, v78\n" \
    "v_exp_f32 v79, v79\n" \
    "v_exp_f32 v96, v96\n" \
    "v_exp_f32 v97, v97\n" \
    "v_smfmac_f32_32x32x32_bf16 v[128:143], v[180:183], v[72:79], v192\n" \
    "v_exp_f32 v98, v98\n" \
    "v_exp_f32 v99, v99\n" \
    "v_exp_f32 v100, v100\n" \
    "v_exp_f32 v101, v101\n" \
    "v_mfma_f32_32x32x16_bf16 v[112:127], v[172:175], v[164:167], 0\n" \
    "ds_read_b128 v[168:171], v193 offset:4096\n" \
    "v_exp_f32 v102, v102\n" \
    "v_exp_f32 v103, v103\n" \
    "v_exp_f32 v104, v104\n" \
    "v_exp_f32 v105, v105\n" \
    "v_smfmac_f32_32x32x32_bf16 v[144:159], v[176:179], v[96:103], v192\n" \
    "v_exp_f32 v106, v106\n" \
    "v_exp_f32 v107, v107\n" \
    "v_exp_f32 v108, v108\n" \
    "v_exp_f32 v109, v109\n" \
    "v_exp_f32 v110, v110\n" \
    "v_exp_f32 v111, v111\n" \
    "v_exp_f32 v80, v80\n" \
    "v_exp_f32 v81, v81\n" \
    "v_smfmac_f32_32x32x32_bf16 v[144:159], v[180:183], v[104:111], v192\n" \
    "ds_read_b128 v[176:179], v194 offset:4096\n" \
    "v_exp_f32 v82, v82\n" \
    "v_exp_f32 v83, v83\n" \
    "v_exp_f32 v84, v84\n" \
    "v_exp_f32 v85, v85\n" \
    "s_waitcnt lgkmcnt(1)\n" \
    "v_mfma_f32_32x32x16_bf16 v[64:79], v[168:171], v[160:163], 0\n" \
    "ds_read_b128 v[180:183], v194 offset:4608\n" \
    "v_exp_f32 v86, v86\n" \
    "v_exp_f32 v87, v87\n" \
    "v_exp_f32 v88, v88\n" \
    "v_exp_f32 v89, v89\n" \
    "v_smfmac_f32_32x32x32_bf16 v[128:143], v[184:187], v[80:87], v192\n" \
    "v_exp_f32 v90, v90\n" \
    "v_exp_f32 v91, v91\n" \
    "v_exp_f32 v92, v92\n" \
    "global_load_lds_dwordx4 v239, %[wob] offset:3072\n" \
    "v_exp_f32 v93, v93\n" \
    "v_exp_f32 v94, v94\n" \
    "v_exp_f32 v95, v95\n" \
    "v_exp_f32 v112, v112\n" \
    "v_exp_f32 v113, v113\n" \
    "v_smfmac_f32_32x32x32_bf16 v[128:143], v[188:191], v[88:95], v192\n" \
    "v_exp_f32 v114, v114\n" \
    "v_exp_f32 v115, v115\n" \
    "v_exp_f32 v116, v116\n" \
    "v_exp_f32 v117, v117\n" \
    "v_mfma_f32_32x32x16_bf16 v[96:111], v[168:171], v[164:167], 0\n" \
    "ds_read_b128 v[172:175], v193 offset:5120\n" \
    "v_exp_f32 v118, v118\n" \
    "v_exp_f32 v119, v119\n" \
    "v_exp_f32 v120, v120\n" \
    "v_exp_f32 v121, v121\n" \
    "v_smfmac_f32_32x32x32_bf16 v[144:159], v[184:187], v[112:119], v192\n" \
    "v_exp_f32 v122, v122\n" \
    "v_exp_f32 v123, v123\n" \
    "v_exp_f32 v124, v124\n" \
    "v_exp_f32 v125, v125\n" \
    "v_exp_f32 v126, v126\n" \
    "v_exp_f32 v127, v127\n" \
    "v_add_u32 v236, 0x8000, v236\n" \
    "v_add_u32 v237, 0x8000, v237\n" \
    "v_add_u32 v238, 0x8000, v238\n" \
    "v_add_u32 v239, 0x8000, v239\n" \
    "s_add_u32 %[swo], %[swo], 0x1000\n" \
    "v_add_u32 v193, 4096, v193\n" \
    "v_add_u32 v194, v195, v194\n" \
    "s_sub_u32 %[cnt], %[cnt], 1\n" \
    "s_cmp_lg_u32 %[cnt], 0\n" \
    "s_cbranch_scc1 1b\n" \
    "global_load_dword v220, v221, %[wsb] sc1\n" \
    "s_mov_b32 %[cnt], 2\n" \
    "2:\n" \
    "v_exp_f32 v64, v64\n" \
    "v_exp_f32 v65, v65\n" \
    "v_smfmac_f32_32x32x32_bf16 v[144:159], v[188:191], v[120:127], v192\n" \
    "ds_read_b128 v[184:187], v194 offset:1024\n" \
    "v_exp_f32 v66, v66\n" \
    "v_exp_f32 v67, v67\n" \
    "v_exp_f32 v68, v68\n" \
    "v_exp_f32 v69, v69\n" \
    "s_waitcnt lgkmcnt(1)\n" \
    "v_mfma_f32_32x32x16_bf16 v[80:95], v[172:175], v[160:163], 0\n" \
    "ds_read_b128 v[188:191], v194 offset:1536\n" \
    "v_exp_f32 v70, v70\n" \
    "v_exp_f32 v71, v71\n" \
    "v_exp_f32 v72, v72\n" \
    "v_exp_f32 v73, v73\n" \
    "v_smfmac_f32_32x32x32_bf16 v[128:143], v[176:179], v[64:71], v192\n" \
    "v_exp_f32 v74, v74\n" \
    "v_exp_f32 v75, v75\n" \
    "v_exp_f32 v76, v76\n" \
    "v_exp_f32 v77, v77\n" \
    "v_exp_f32 v78, v78\n" \
    "v_exp_f32 v79, v79\n" \
    "v_exp_f32 v96, v96\n" \
    "v_exp_f32 v97, v97\n" \
    "v_smfmac_f32_32x32x32_bf16 v[128:143], v[180:183], v[72:79], v192\n" \
    "v_exp_f32 v98, v98\n" \
    "v_exp_f32 v99, v99\n" \
    "v_exp_f32 v100, v100\n" \
    "v_exp_f32 v101, v101\n" \
    "v_mfma_f32_32x32x16_bf16 v[112:127], v[172:175], v[164:167], 0\n" \
    "ds_read_b128 v[168:171], v193 offset:2048\n" \
    "v_exp_f32 v102, v102\n" \
    "v_exp_f32 v103, v103\n" \
    "v_exp_f32 v104, v104\n" \
    "v_exp_f32 v105, v105\n" \
    "v_smfmac_f32_32x32x32_bf16 v[144:159], v[176:179], v[96:103], v192\n" \
    "v_exp_f32 v106, v106\n" \
    "v_exp_f32 v107, v107\n" \
    "v_exp_f32 v108, v108\n" \
    "v_exp_f32 v109, v109\n" \
    "v_exp_f32 v110, v110\n" \
    "v_exp_f32 v111, v111\n" \
    "v_exp_f32 v80, v80\n" \
    "v_exp_f32 v81, v81\n" \
    "v_smfmac_f32_32x32x32_bf16 v[144:159], v[180:183], v[104:111], v192\n" \
    "ds_read_b128 v[176:179], v194 offset:2048\n" \
    "v_exp_f32 v82, v82\n" \
    "v_exp_f32 v83, v83\n" \
    "v_exp_f32 v84, v84\n" \
    "v_exp_f32 v85, v85\n" \
    "s_waitcnt lgkmcnt(1)\n" \
    "v_mfma_f32_32x32x16_bf16 v[64:79], v[168:171], v[160:163], 0\n" \
    "ds_read_b128 v[180:183], v194 offset:2560\n" \
    "v_exp_f32 v86, v86\n" \
    "v_exp_f32 v87, v87\n" \
    "v_exp_f32 v88, v88\n" \
    "v_exp_f32 v89, v89\n" \
    "v_smfmac_f32_32x32x32_bf16 v[128:143], v[184:187], v[80:87], v192\n" \
    "v_exp_f32 v90, v90\n" \
    "v_exp_f32 v91, v91\n" \
    "v_exp_f32 v92, v92\n" \
    "v_exp_f32 v93, v93\n" \
    "v_exp_f32 v94, v94\n" \
    "v_exp_f32 v95, v95\n" \
    "v_exp_f32 v112, v112\n" \
    "v_exp_f32 v113, v113\n" \
    "v_smfmac_f32_32x32x32_bf16 v[128:143], v[188:191], v[88:95], v192\n" \
    "v_exp_f32 v114, v114\n" \
    "v_exp_f32 v115, v115\n" \
    "v_exp_f32 v116, v116\n" \
    "v_exp_f32 v117, v117\n" \
    "v_mfma_f32_32x32x16_bf16 v[96:111], v[168:171], v[164:167], 0\n" \
    "ds_read_b128 v[172:175], v193 offset:3072\n" \
    "v_exp_f32 v118, v118\n" \
    "v_exp_f32 v119, v119\n" \
    "v_exp_f32 v120, v120\n" \
    "v_exp_f32 v121, v121\n" \
    "v_smfmac_f32_32x32x32_bf16 v[144:159], v[184:187], v[112:119], v192\n" \
    "v_exp_f32 v122, v122\n" \
    "v_exp_f32 v123, v123\n" \
    "v_exp_f32 v124, v124\n" \
    "v_exp_f32 v125, v125\n" \
    "v_exp_f32 v126, v126\n" \
    "v_exp_f32 v127, v127\n" \
    "v_exp_f32 v64, v64\n" \
    "v_exp_f32 v65, v65\n" \
    "v_smfmac_f32_32x32x32_bf16 v[144:159], v[188:191], v[120:127], v192\n" \
    "ds_read_b128 v[184:187], v194 offset:3072\n" \
    "v_exp_f32 v66, v66\n" \
    "v_exp_f32 v67, v67\n" \
    "v_exp_f32 v68, v68\n" \
    "v_exp_f32 v69, v69\n" \
    "s_waitcnt lgkmcnt(1)\n" \
    "v_mfma_f32_32x32x16_bf16 v[80:95], v[172:175], v[160:163], 0\n" \
    "ds_read_b128 v[188:191], v194 offset:3584\n" \
    "v_exp_f32 v70, v70\n" \
    "v_exp_f32 v71, v71\n" \
    "v_exp_f32 v72, v72\n" \
    "v_exp_f32 v73, v73\n" \
    "v_smfmac_f32_32x32x32_bf16 v[128:143], v[176:179], v[64:71], v192\n" \
    "v_exp_f32 v74, v74\n" \
    "v_exp_f32 v75, v75\n" \
    "v_exp_f32 v76, v76\n" \
    "v_exp_f32 v77, v77\n" \
    "v_exp_f32 v78, v78\n" \
    "v_exp_f32 v79, v79\n" \
    "v_exp_f32 v96, v96\n" \
    "v_exp_f32 v97, v97\n" \
    "v_smfmac_f32_32x32x32_bf16 v[128:143], v[180:183], v[72:79], v192\n" \
    "v_exp_f32 v98, v98\n" \
    "v_exp_f32 v99, v99\n" \
    "v_exp_f32 v100, v100\n" \
    "v_exp_f32 v101, v101\n" \
    "v_mfma_f32_32x32x16_bf16 v[112:127], v[172:175], v[164:167], 0\n" \
    "ds_read_b128 v[168:171], v193 offset:4096\n" \
    "v_exp_f32 v102, v102\n" \
    "v_exp_f32 v103, v103\n" \
    "v_exp_f32 v104, v104\n" \
    "v_exp_f32 v105, v105\n" \
    "v_smfmac_f32_32x32x32_bf16 v[144:159], v[176:179], v[96:103], v192\n" \
    "v_exp_f32 v106, v106\n" \
    "v_exp_f32 v107, v107\n" \
    "v_exp_f32 v108, v108\n" \
    "v_exp_f32 v109, v109\n" \
    "v_exp_f32 v110, v110\n" \
    "v_exp_f32 v111, v111\n" \
    "v_exp_f32 v80, v80\n" \
    "v_exp_f32 v81, v81\n" \
    "v_smfmac_f32_32x32x32_bf16 v[144:159], v[180:183], v[104:111], v192\n" \
    "ds_read_b128 v[176:179], v194 offset:4096\n" \
    "v_exp_f32 v82, v82\n" \
    "v_exp_f32 v83, v83\n" \
    "v_exp_f32 v84, v84\n" \
    "v_exp_f32 v85, v85\n" \
    "s_waitcnt lgkmcnt(1)\n" \
    "v_mfma_f32_32x32x16_bf16 v[64:79], v[168:171], v[160:163], 0\n" \
    "ds_read_b128 v[180:183], v194 offset:4608\n" \
    "v_exp_f32 v86, v86\n" \
    "v_exp_f32 v87, v87\n" \
    "v_exp_f32 v88, v88\n" \
    "v_exp_f32 v89, v89\n" \
    "v_smfmac_f32_32x32x32_bf16 v[128:143], v[184:187], v[80:87], v192\n" \
    "v_exp_f32 v90, v90\n" \
    "v_exp_f32 v91, v91\n" \
    "v_exp_f32 v92, v92\n" \
    "v_exp_f32 v93, v93\n" \
    "v_exp_f32 v94, v94\n" \
    "v_exp_f32 v95, v95\n" \
    "v_exp_f32 v112, v112\n" \
    "v_exp_f32 v113, v113\n" \
    "v_smfmac_f32_32x32x32_bf16 v[128:143], v[188:191], v[88:95], v192\n" \
    "v_exp_f32 v114, v114\n" \
    "v_exp_f32 v115, v115\n" \
    "v_exp_f32 v116, v116\n" \
    "v_exp_f32 v117, v117\n" \
    "v_mfma_f32_32x32x16_bf16 v[96:111], v[168:171], v[164:167], 0\n" \
    "ds_read_b128 v[172:175], v193 offset:5120\n" \
    "v_exp_f32 v118, v118\n" \
    "v_exp_f32 v119, v119\n" \
    "v_exp_f32 v120, v120\n" \
    "v_exp_f32 v121, v121\n" \
    "v_smfmac_f32_32x32x32_bf16 v[144:159], v[184:187], v[112:119], v192\n" \
    "v_exp_f32 v122, v122\n" \
    "v_exp_f32 v123, v123\n" \
    "v_exp_f32 v124, v124\n" \
    "v_exp_f32 v125, v125\n" \
    "v_exp_f32 v126, v126\n" \
    "v_exp_f32 v127, v127\n" \
    "s_cmp_eq_u32 %[cnt], 2\n" \
    "s_cbranch_scc0 8f\n" \
    "s_setprio 0\n" \
    "8:\n" \
    "v_add_u32 v193, 4096, v193\n" \
    "v_add_u32 v194, v195, v194\n" \
    "s_sub_u32 %[cnt], %[cnt], 1\n" \
    "s_cmp_lg_u32 %[cnt], 0\n" \
    "s_cbranch_scc1 2b\n" \
    "s_nop 1\n" \
    "v_smfmac_f32_32x32x32_bf16 v[144:159], v[188:191], v[120:127], v192\n" \
    "s_setprio 0\n" \
    "s_waitcnt vmcnt(0)\n" \
    "s_nop 7\n" \
    "s_nop 7\n" \
    "s_nop 3\n" \
    "s_waitcnt lgkmcnt(0)\n"

__device__ __forceinline__ void slow_block(const char* ws, u32x4 q, int lane, float* o, float& lsum) {
    const u32 koff = WS_K + lane * 16;
    const u32 r32 = lane & 31;
    float m = -3.0e38f;
    for (int t = 0; t < 64; ++t) {
        u32x4 kf;
        GLD128C(kf, ws, koff + t * 1024, 0);
        asm volatile("s_waitcnt vmcnt(0)" : "+v"(kf) :: "memory");
        f32x16 s = {0};
        s = __builtin_amdgcn_mfma_f32_32x32x16_bf16(asbf(kf), asbf(q), s, 0, 0, 0);
#pragma unroll
        for (int i = 0; i < 16; ++i) m = fmaxf(m, s[i]);
    }
    {
        auto sw = __builtin_amdgcn_permlane32_swap(__float_as_uint(m), __float_as_uint(m), false, false);
        m = fmaxf(__uint_as_float(sw[0]), __uint_as_float(sw[1]));
    }
    f32x16 acc = {0};
    const u32 voff = WS_V + (lane >> 5) * 256 + (lane & 15) * 16;
    for (int t = 0; t < 64; ++t) {
        u32x4 kf, v0, v1;
        GLD128C(kf, ws, koff + t * 1024, 0);
        GLD128C(v0, ws, voff + t * 1024, 0);
        GLD128C(v1, ws, voff + t * 1024, 512);
        asm volatile("s_waitcnt vmcnt(0)" : "+v"(kf), "+v"(v0), "+v"(v1) :: "memory");
        if (r32 >= 16) { v0 = (u32x4){0x3f803f80u, 0x3f803f80u, 0x3f803f80u, 0x3f803f80u}; v1 = v0; }
        f32x16 s = {0};
        s = __builtin_amdgcn_mfma_f32_32x32x16_bf16(asbf(kf), asbf(q), s, 0, 0, 0);
        u32 p[8];
#pragma unroll
        for (int i = 0; i < 8; ++i)
            p[i] = pk(__builtin_amdgcn_exp2f(s[2 * i] - m), __builtin_amdgcn_exp2f(s[2 * i + 1] - m));
#pragma unroll
        for (int st = 0; st < 2; ++st) {
            u32x4 vv = st ? v1 : v0;
            auto a = __builtin_amdgcn_permlane32_swap(vv[0], vv[2], false, false);
            auto c = __builtin_amdgcn_permlane32_swap(vv[1], vv[3], false, false);
            u32x4 af = {a[0], c[0], a[1], c[1]};
            u32x4 bf = {p[4 * st], p[4 * st + 1], p[4 * st + 2], p[4 * st + 3]};
            acc = __builtin_amdgcn_mfma_f32_32x32x16_bf16(asbf(af), asbf(bf), acc, 0, 0, 0);
        }
    }
#pragma unroll
    for (int i = 0; i < 8; ++i) o[i] = acc[i];
    lsum = acc[8];
}

#define PRIO_MODE 1
#define PRIO_START() do { if (w >= 4) __builtin_amdgcn_s_setprio(1); } while (0)
#define PRIO_END() __builtin_amdgcn_s_setprio(0)
__global__ void __launch_bounds__(512, 2)
fused_kernel(const u16* x_, const u16* wq_, const u16* wk_, const u16* wv_, const u16* wo_, const u16* wr_, const u16* wg_,
             const u16* wu_, const u16* wd_, const u16* gw_, u16* out_, char* ws_)
{
    u32x16 ka0; u32x8 ka1;
    u64 tA_, tB_;
    asm volatile("s_memrealtime %0\n\ts_waitcnt lgkmcnt(0)\n\ts_memrealtime %1\n\ts_waitcnt lgkmcnt(0)" : "=s"(tA_), "=s"(tB_) :: "memory");
    {
        const void* kp = (const void*)__builtin_amdgcn_kernarg_segment_ptr();
        asm volatile("s_load_dwordx16 %0, %2, 0\n\ts_load_dwordx8 %1, %2, 64\n\ts_waitcnt lgkmcnt(0)"
                     : "=&s"(ka0), "=&s"(ka1) : "s"(kp) : "memory");
    }
#define KP(v, i) ((u64)v[2 * (i)] | ((u64)v[2 * (i) + 1] << 32))
    const u16* x  = (const u16*)KP(ka0, 0); const u16* wq = (const u16*)KP(ka0, 1);
    const u16* wk = (const u16*)KP(ka0, 2); const u16* wv = (const u16*)KP(ka0, 3);
    const u16* wo = (const u16*)KP(ka0, 4); const u16* wr = (const u16*)KP(ka0, 5);
    const u16* wg = (const u16*)KP(ka0, 6); const u16* wu = (const u16*)KP(ka0, 7);
    const u16* wd = (const u16*)KP(ka1, 0); const u16* gw = (const u16*)KP(ka1, 1);
    u16* out = (u16*)KP(ka1, 2); char* ws = (char*)KP(ka1, 3);

    __shared__ __attribute__((aligned(1024))) char smem[L_END];
    LDSP char* lds = (LDSP char*)smem;
    const u32 lb = (u32)(size_t)lds;
    const int tid = threadIdx.x;
    const int lane = tid & 63;
    const int w = __builtin_amdgcn_readfirstlane(tid >> 6);
    const int b = blockIdx.x;
    const int xcd = b & 7, jj = b >> 3;
    const int tb = xcd * 4 + (jj >> 3), hg = jj & 7;
    const int g = lane >> 4, l15 = lane & 15, h = lane >> 5, l31 = lane & 31;
    const bool isK = hg < 4;
    const bool ffn = jj < 28;
    const int fj = xcd * 28 + min(jj, 27);
    STAMP(1); STAMPC(18); if (lane == 0) { ST32(L_CT + 64 + (w * 24 + 23) * 4, (u32)tA_); ST32(L_CT + 64 + (w * 24 + 8) * 4, (u32)tB_); }

    const int th = w & 1, hp = (w >> 1) & 1, blk = w >> 2;
    const int T0 = tb * 64 + (hg & 3) * 16;
    u32x4 xf0, xf1, vf0, vf1;
    {
        const u16* wsrc = isK ? wk : wv;
#pragma unroll
        for (int i = 0; i < 2; ++i) {
            u32 slot = 64 * i + lane, pos = slot >> 3, B = pos ^ ((pos >> 2) & 1);
            u32 row = 4 * B + ((slot >> 1) & 3);
            dma16(wsrc, (64 * w + row) * 32 + (slot & 1) * 16, lb + L_W + w * 2048 + i * 1024);
        }
        u32 xo = (T0 + l15) * 1024 + (64 * w + 16 * g) * 2;
        GLD128(xf0, x, xo, 0);
        GLD128(xf1, x, xo, 16);
        const u16* vec = isK ? wr : gw;
        u32 vo = (64 * w + 16 * g) * 2;
        GLD128(vf0, vec, vo, 0);
        GLD128(vf1, vec, vo, 16);
    }
    u32 epo;
    { u32 z = 0; GLD32C(epo, ws, z, 0); }
    asm volatile("s_barrier" ::: "memory");
    if (w < 7) {
#pragma unroll
        for (int i = 0; i < 19; ++i) {
            const int p = i < 18 ? w + 7 * i : __builtin_amdgcn_readfirstlane(min(w + 7 * i, 127));
            if (i < 9 || (i == 9 && w == 0)) {
                dma16(x, (tb * 64 + p) * 1024 + ((lane ^ (p & 15)) << 4), lb + L_K + p * 1024);
            } else {
                const int G = p - 64;
                dma16(wq, (8 * G + ((lane >> 2) & 7)) * 1024 + hg * 128 + (lane >> 5) * 64 + (lane & 3) * 16, lb + L_V + G * 1024);
            }
        }
    }
    if (tid < 256) { u32 o1 = (tid & 15) == 0 ? 0x3f803f80u : 0u; ST128(L_ONE + tid * 16, ((u32x4){o1, o1, o1, o1})); }
    asm volatile("s_cmp_eq_u32 %[w], 7\n\ts_cbranch_scc1 1f\n\ts_waitcnt vmcnt(20)\n\ts_branch 2f\n1:\n\ts_waitcnt vmcnt(1)\n2:"
                 : "+v"(xf0), "+v"(xf1), "+v"(vf0), "+v"(vf1) : [w] "s"(w) : "memory", "scc");
    FSTAMP(2);
    {
        const u32 ta = L_W + w * 2048 + l15 * 8;
        const u32 gx = g & 1;
        u32x4 wf0 = cat(trread(lds, ta + ((4 * g + 0) ^ gx) * 128), trread(lds, ta + ((4 * g + 1) ^ gx) * 128));
        u32x4 wf1 = cat(trread(lds, ta + ((4 * g + 2) ^ gx) * 128), trread(lds, ta + ((4 * g + 3) ^ gx) * 128));
        f32x4 acc = {0, 0, 0, 0}, dacc = {0, 0, 0, 0};
        if (isK) {
            acc = __builtin_amdgcn_mfma_f32_16x16x32_bf16(asbf(wf0), asbf(xf0), acc, 0, 0, 0);
            acc = __builtin_amdgcn_mfma_f32_16x16x32_bf16(asbf(wf1), asbf(xf1), acc, 0, 0, 0);
        } else {
            acc = __builtin_amdgcn_mfma_f32_16x16x32_bf16(asbf(xf0), asbf(wf0), acc, 0, 0, 0);
            acc = __builtin_amdgcn_mfma_f32_16x16x32_bf16(asbf(xf1), asbf(wf1), acc, 0, 0, 0);
        }
        dacc = __builtin_amdgcn_mfma_f32_16x16x32_bf16(asbf(vf0), asbf(xf0), dacc, 0, 0, 0);
        dacc = __builtin_amdgcn_mfma_f32_16x16x32_bf16(asbf(vf1), asbf(xf1), dacc, 0, 0, 0);
        ST128(L_P1 + w * 1024 + lane * 16, __builtin_bit_cast(u32x4, acc));
        if (g == 0) STF(L_PD + w * 64 + l15 * 4, dacc[0]);
    }
    BAR();
    FSTAMP(3);
    asm volatile("s_cmp_eq_u32 %[w], 7\n\ts_cbranch_scc1 1f\n\ts_waitcnt vmcnt(19)\n\ts_branch 2f\n1:\n\ts_waitcnt vmcnt(0)\n2:"
                 : "+v"(epo) : [w] "s"(w) : "memory", "scc");
    const u32 epoch = (u32)__builtin_amdgcn_readfirstlane((int)epo) + 1u;
    if (w == 7) {
        f32x4 ps[8]; float pd[8];
#pragma unroll
        for (int i = 0; i < 8; ++i) {
            ps[i] = __builtin_bit_cast(f32x4, LD128(L_P1 + i * 1024 + lane * 16));
            pd[i] = LDF(L_PD + i * 64 + l15 * 4);
        }
        __builtin_amdgcn_sched_barrier(0);
        const f32x4 s = ((ps[0] + ps[1]) + (ps[2] + ps[3])) + ((ps[4] + ps[5]) + (ps[6] + ps[7]));
        const float d = ((pd[0] + pd[1]) + (pd[2] + pd[3])) + ((pd[4] + pd[5]) + (pd[6] + pd[7]));
        u32x2 pv = {pk(s[0], s[1]), pk(s[2], s[3])};
        const int tile = T0 >> 5, sub = (T0 >> 4) & 1;
        u32 key = 0;
        if (isK) {
            u32 off = WS_K + tile * 1024 + ((g & 1) * 32 + sub * 16 + l15) * 16 + (g >> 1) * 8;
            GST64C(ws, off, pv);
            u32 rb = pk(d, d) & 0xffffu;
            u32 ord = (rb & 0x8000u) ? (~rb & 0xffffu) : (rb | 0x8000u);
            key = wave_max_u32((ord << 16) | (0xffffu - (u32)(T0 + l15)));
        } else {
            u32 off = WS_V + tile * 1024 + sub * 512 + (g >> 1) * 256 + l15 * 16 + (g & 1) * 8;
            GST64C(ws, off, pv);
            if (g == 0) { u32 go = WS_G + (T0 + l15) * 4; u32 dv = __float_as_uint(d); GST32C(ws, go, dv); }
        }
        VMWAIT(0);
        if (lane < 8) { u32x2 fl = {key, epoch}; u32 fo = WS_F1 + lane * 32768 + (tile * 4 + (isK ? 0 : 2) + sub) * 128; GST64C(ws, fo, fl); }
        FSTAMP(4);
    } else {
        VMWAIT(0);
    }
    BAR();
    FSTAMP(5);

    f32x16 qacc = {0};
    if (w < 4) {
        const int qh = w >> 1;
        const int tok = th * 32 + l31;
        u32 abase = L_V + h * 1024 + qh * 512 + ((l15 >> 2) * 64) + ((lane >> 4) & 1) * 32 + (l15 & 3) * 8;
        asm volatile("" : "+v"(abase));
        u32 bb[8];
#pragma unroll
        for (int j = 0; j < 8; ++j) { bb[j] = L_K + tok * 1024 + (((2 * j + h) ^ (tok & 15)) << 4); asm volatile("" : "+v"(bb[j])); }
#pragma unroll
        for (int s = 0; s < 32; ++s) {
            u32x4 af = cat(trread(lds, abase + s * 2048), trread(lds, abase + s * 2048 + 256));
            u32x4 bf = LD128(bb[s & 7] + (s >> 3) * 256);
            qacc = __builtin_amdgcn_mfma_f32_32x32x16_bf16(asbf(af), asbf(bf), qacc, 0, 0, 0);
        }
        FSTAMP(6);
    } else {
        const int j = w - 4;
        const u32 fo = WS_F1 + xcd * 32768 + (j * 64 + lane) * 128;
        u32x2 f;
        u32 spins = 0;
        do {
            asm volatile("global_load_dwordx2 %0, %1, %2 sc1\n\ts_waitcnt vmcnt(0)" : "=&v"(f) : "v"(fo), "s"(ws) : "memory");
        } while ((f[1] != epoch) && ++spins <= SPIN_MAX);
        const u32 key = wave_max_u32(f[0]);
        if (lane == 0) ST32(L_CT + j * 4, key);
        FSTAMP(7);
    }
    BAR();
    FSTAMP(9);
    u32 kvo = (w & 1) * 65536 + ((w >> 1) & 1) * 32768;
    if (w >= 4) {
        dma16c4(ws + WS_K, kvo + lane * 16, lb + L_K + kvo);
        dma16c4(ws + WS_K, kvo + 4096 + lane * 16, lb + L_K + kvo + 4096);
        dma16c2(ws + WS_K, kvo + 8192 + lane * 16, lb + L_K + kvo + 8192);
    } else {
        const float qs = 0.25f * 1.4426950408889634f;
        u32x4 qx, qy;
#pragma unroll
        for (int i = 0; i < 4; ++i) {
            u32 a = pk(qacc[2 * i], qacc[2 * i + 1]), c = pk(qacc[8 + 2 * i], qacc[8 + 2 * i + 1]);
            qx[i] = pk(lo16(a) * qs, hi16(a) * qs);
            qy[i] = pk(lo16(c) * qs, hi16(c) * qs);
        }
        ST128(L_P1 + w * 2048 + lane * 16, qx);
        ST128(L_P1 + w * 2048 + 1024 + lane * 16, qy);
    }
    const u32 key = max(max(LD32(L_CT), LD32(L_CT + 4)), max(LD32(L_CT + 8), LD32(L_CT + 12)));
    const int sel = __builtin_amdgcn_readfirstlane(0xffff - (key & 0xffff)) & 2047;
    u32x8 EXA; u32x4 EXB, WOA;
    u32 skv = lb + L_K + kvo + (10 + 4 * (w >> 2)) * 1024, sg = lb + L_W + w * 2048;
    const u32 skv3 = lb + L_K + kvo + (26 + 2 * (w >> 2)) * 1024;
    u32 swo = lb + ((w & 4) ? L_V : L_K) + ((w >> 1) & 1) * 32768 + (w & 1) * 8192;
    {
        EXA[0] = kvo + (10 + 4 * (w >> 2)) * 1024 + lane * 16;
        EXA[1] = kvo + (26 + 2 * (w >> 2)) * 1024 + lane * 16;
        const u32 pos = lane >> 3, B = pos ^ ((pos >> 2) & 1);
        const u32 row = 64 * w + 4 * B + ((lane >> 1) & 3);
        const u64 g0 = (u64)((lane & 1) ? wu : wg) + (u64)(row * 3584 + fj * 16), g1 = g0 + 32 * 3584;
        EXA[2] = (u32)g0; EXA[3] = (u32)(g0 >> 32); EXA[4] = (u32)g1; EXA[5] = (u32)(g1 >> 32);
        EXA[6] = sel * 1024 + (64 * w + 16 * g) * 2;
        EXA[7] = fj * 8192 + (128 * (w & 3) + 2 * lane) * 2;
        const u32 ta = lb + L_W + w * 2048 + l15 * 8, gx = g & 1;
        EXB[0] = ta + (4 * g + gx) * 128;
        EXB[1] = ta + (4 * g + (gx ^ 1)) * 128;
        EXB[2] = lb + (l15 == 0 ? L_FP + w * 64 + g * 16 : L_P1 + w * 1024 + lane * 16);
        EXB[3] = 0;
#pragma unroll
        for (int i = 0; i < 4; ++i) {
            const u32 r = 64 * w + 8 * i + (lane >> 3), slot = lane & 7;
            const u32 f = ((r >> 1) & 1) | ((i & 1) << 1);
            const u32 c = (((slot >> 1) ^ f) << 1) | (slot & 1);
            WOA[i] = r * 1024 + hg * 128 + c * 16 - i * 1024;
        }
    }
    u32x2 PZA;
    {
        const int head = hg * 4 + hp * 2 + blk, tl = th * 32 + l31;
        PZA[0] = WS_O + ((tb * 4 + (tl >> 4)) * 16 + (head >> 1)) * 1024 + (((head & 1) * 2 + h) * 16 + (tl & 15)) * 16;
        PZA[1] = (ffn && w < 4) ? WS_Y + fj * 2048 + w * 512 + l31 * 16 : PZA[0];
    }
    const int hf = w >> 2;
    u32 ka = lb + L_K + hf * 32768 + lane * 16;
    const bool vlane = l31 < 16;
    u32 va = vlane ? (lb + L_V + hf * 32768 + h * 256 + l15 * 16) : (lb + L_ONE - 1024 + h * 256 + l15 * 16);
    u32 vinc = vlane ? 4096u : 0u;
    u32 va0 = vlane ? va : va + 1024;
    u32 qa = lb + L_P1 + (w & 3) * 2048 + lane * 16;
    asm volatile("" : "+{v[196:203]}"(EXA), "+{v[228:231]}"(EXB), "+{v[236:239]}"(WOA), "+{v[220:221]}"(PZA), "+{v193}"(ka), "+{v194}"(va), "+{v195}"(vinc), "+v"(va0), "+v"(qa),
                 "+s"(skv), "+s"(sg), "+s"(swo));
    if (w >= 4) VMWAIT(0);
    BAR();
    STAMP(10); STAMPC(19);
    if (b == 0 && tid == 256) { u32 z = 0; GST32C(ws, z, epoch); }

    u32x4 qx, qy;
    f32x16 SX0, SX1, SY0, SY1, OX, OY;
    u32x4 K0, K1, V00, V01, V10, V11;
    u32 idx = 0xDDDDu;
    {
        asm volatile(
            "ds_read_b128 v[160:163], %[qa]\n"
            "ds_read_b128 v[164:167], %[qa] offset:1024\n"
            "ds_read_b128 v[168:171], v193\n"
            "ds_read_b128 v[172:175], v193 offset:1024\n"
            "ds_read_b128 v[176:179], %[va0]\n"
            "ds_read_b128 v[180:183], %[va0] offset:512\n"
            "v_mov_b32 v128, 0\n v_mov_b32 v129, 0\n v_mov_b32 v130, 0\n v_mov_b32 v131, 0\n"
            "v_mov_b32 v132, 0\n v_mov_b32 v133, 0\n v_mov_b32 v134, 0\n v_mov_b32 v135, 0\n"
            "v_mov_b32 v136, 0\n v_mov_b32 v137, 0\n v_mov_b32 v138, 0\n v_mov_b32 v139, 0\n"
            "v_mov_b32 v140, 0\n v_mov_b32 v141, 0\n v_mov_b32 v142, 0\n v_mov_b32 v143, 0\n"
            "v_mov_b32 v144, 0\n v_mov_b32 v145, 0\n v_mov_b32 v146, 0\n v_mov_b32 v147, 0\n"
            "v_mov_b32 v148, 0\n v_mov_b32 v149, 0\n v_mov_b32 v150, 0\n v_mov_b32 v151, 0\n"
            "v_mov_b32 v152, 0\n v_mov_b32 v153, 0\n v_mov_b32 v154, 0\n v_mov_b32 v155, 0\n"
            "v_mov_b32 v156, 0\n v_mov_b32 v157, 0\n v_mov_b32 v158, 0\n v_mov_b32 v159, 0\n"
            "v_mov_b32 v120, 0\n v_mov_b32 v121, 0\n v_mov_b32 v122, 0\n v_mov_b32 v123, 0\n"
            "v_mov_b32 v124, 0\n v_mov_b32 v125, 0\n v_mov_b32 v126, 0\n v_mov_b32 v127, 0\n"
            "v_mov_b32 v188, 0\n v_mov_b32 v189, 0\n v_mov_b32 v190, 0\n v_mov_b32 v191, 0\n"
            "s_waitcnt lgkmcnt(0)\n"
            "v_mfma_f32_32x32x16_bf16 v[64:79], v[168:171], v[160:163], 0\n"
            "v_mfma_f32_32x32x16_bf16 v[96:111], v[168:171], v[164:167], 0\n"
            "s_nop 7\n s_nop 7\n"
            : "={v[64:79]}"(SX0), "={v[80:95]}"(SX1), "={v[96:111]}"(SY0), "={v[112:127]}"(SY1),
              "={v[128:143]}"(OX), "={v[144:159]}"(OY),
              "={v[168:171]}"(K0), "={v[172:175]}"(K1), "={v[176:179]}"(V00), "={v[180:183]}"(V01),
              "={v[184:187]}"(V10), "={v[188:191]}"(V11), "=&{v[160:163]}"(qx), "=&{v[164:167]}"(qy)
            : "{v193}"(ka), [va0] "v"(va0), [qa] "v"(qa)
            : "memory");
    }
#define ATT_OPS \
            : "+{v[64:79]}"(SX0), "+{v[80:95]}"(SX1), "+{v[96:111]}"(SY0), "+{v[112:127]}"(SY1), \
              "+{v[128:143]}"(OX), "+{v[144:159]}"(OY), \
              "+{v[168:171]}"(K0), "+{v[172:175]}"(K1), "+{v[176:179]}"(V00), "+{v[180:183]}"(V01), \
              "+{v[184:187]}"(V10), "+{v[188:191]}"(V11), "+{v193}"(ka), "+{v194}"(va), [cnt] "=&s"(cnt_)
#define ATT_INS : "{v[160:163]}"(qx), "{v[164:167]}"(qy), "{v192}"(idx), "{v195}"(vinc)
    u32 cnt_;
    const char* kvb = ws + WS_K;
    PRIO_START();
    asm volatile(ATT_A ATT_OPS, "+{v[196:203]}"(EXA), "+{v[228:231]}"(EXB), "+{v[236:239]}"(WOA), "+{v[220:221]}"(PZA), [skv] "+s"(skv), [sg] "+s"(sg)
                 ATT_INS, [kvb] "s"(kvb), [wsb] "s"(ws) : "memory", "scc", "v232", "v233", "v234", "v235");
    LSTAMP(4);
    BAR();
    LSTAMP(8);
    if (tid == 448) { u32 fo = WS_F2 + b * 4; GST32C(ws, fo, epoch); }
    PRIO_START();
    u32x16 EXD;
    asm volatile(ATT_B ATT_OPS, "+{v[196:203]}"(EXA), "=&{v[204:219]}"(EXD), "+{v[236:239]}"(WOA)
                 ATT_INS, "{v[228:231]}"(EXB), [kvb] "s"(kvb), [skv3] "s"(skv3), [xb] "s"(x), [wdb] "s"(wd)
                 : "memory", "scc", "v220", "v221", "v222", "v223", "v224", "v225", "v226", "v227", "v232", "v233", "v234", "v235");
    LSTAMP(9);
    BAR();
    LSTAMP(11);
    if (ffn && w < 4) {
        float sum = LDF(L_FP + l15 * 4);
#pragma unroll
        for (int i = 1; i < 8; ++i) sum += LDF(L_FP + i * 64 + l15 * 4);
        sum = bfr(sum);
        float up = __shfl(sum, (lane & 7) + 8);
        float gt = __shfl(sum, lane & 7);
        float hv = bfr(bfr(gt * sigm(gt)) * up);
        u32 hb = __float_as_uint(hv);
        float y0 = 0.f, y1 = 0.f;
#pragma unroll
        for (int i = 0; i < 8; ++i) {
            const float hs = __uint_as_float((u32)__builtin_amdgcn_readlane(hb, i));
            y0 += hs * lo16(EXD[8 + i]);
            y1 += hs * hi16(EXD[8 + i]);
        }
        u32x2 gr = {__float_as_uint(y0), __float_as_uint(y1)};
        gr[0] = gr[0] == POISON ? 0x7fc00000u : gr[0];
        gr[1] = gr[1] == POISON ? 0x7fc00000u : gr[1];
        u32 yo = WS_Y + fj * 2048 + (128 * w + 2 * lane) * 4;
        GST64C(ws, yo, gr);
    }
    LSTAMP(22);
    PRIO_START();
    u32 f2v; const u32 f2a = WS_F2 + (32 * w + l31) * 4;
    asm volatile(ATT_C ATT_OPS, "+{v[236:239]}"(WOA), "=&{v220}"(f2v), [swo] "+s"(swo) ATT_INS, "{v221}"(f2a), [wob] "s"(wo), [wsb] "s"(ws) : "memory", "scc");
    const int tq = w & 3, kh = w >> 2;
    const bool owner = (sel >> 6) == tb;
    int lane_b = lane;
    asm volatile("" : "+v"(lane_b));
    {
    const int lane = lane_b;
    const int g = lane >> 4, l15 = lane & 15, h = lane >> 5, l31 = lane & 31;
    const int tokg = tb * 64 + tq * 16 + l15;
    const int ncol = hg * 64 + kh * 32 + (g & 1) * 16 + (g >> 1) * 8;
    u32 oslot;
    {
        const int head = hg * 4 + hp * 2 + blk, tl = th * 32 + l31;
        oslot = WS_O + ((tb * 4 + (tl >> 4)) * 16 + (head >> 1)) * 1024 + (((head & 1) * 2 + h) * 16 + (tl & 15)) * 16;
    }
    STAMP(12); STAMPC(20);
    {
        u32 spins = 0;
        while (!__all(f2v == epoch) && ++spins <= SPIN_MAX) {
            const u32 a = WS_F2 + (32 * w + l31) * 4;
            asm volatile("global_load_dword %0, %1, %2 sc1\n\ts_waitcnt vmcnt(0)" : "=&v"(f2v) : "v"(a), "s"(ws) : "memory");
        }
    }
    u32x4 xe, Y0, Y1, Y2, Y3, Y4, Y5, Y6; u32 gl;
    const u32 yo0 = WS_Y + (28 * w + g) * 2048 + hg * 256 + l15 * 16;
    {
        u32 xo = tokg * 1024 + ncol * 2;
        GLD128(xe, x, xo, 0);
        u32 go = WS_G + tokg * 4;
        GLD32C(gl, ws, go, 0);
    }

    {
        f32x16& mine = blk ? OY : OX;
        f32x16& give = blk ? OX : OY;
        const u32 cw = L_W + w * 2304, cr = L_W + (w ^ 4) * 2304;
        ST128(cw + lane * 16, ((u32x4){__float_as_uint(give[0]), __float_as_uint(give[1]), __float_as_uint(give[2]), __float_as_uint(give[3])}));
        ST128(cw + 1024 + lane * 16, ((u32x4){__float_as_uint(give[4]), __float_as_uint(give[5]), __float_as_uint(give[6]), __float_as_uint(give[7])}));
        STF(cw + 2048 + lane * 4, give[8]);
        BAR();
        f32x4 p0 = __builtin_bit_cast(f32x4, LD128(cr + lane * 16));
        f32x4 p1 = __builtin_bit_cast(f32x4, LD128(cr + 1024 + lane * 16));
        float l = LDF(cr + 2048 + lane * 4) + mine[8];
        float o[8];
#pragma unroll
        for (int i = 0; i < 4; ++i) { o[i] = mine[i] + p0[i]; o[4 + i] = mine[4 + i] + p1[i]; }
        {
            auto sw = __builtin_amdgcn_permlane32_swap(__float_as_uint(l), __float_as_uint(l), false, false);
            l = __uint_as_float(sw[0]);
        }
        float amax = fmaxf(fmaxf(fmaxf(fabsf(o[0]), fabsf(o[1])), fmaxf(fabsf(o[2]), fabsf(o[3]))),
                           fmaxf(fmaxf(fabsf(o[4]), fabsf(o[5])), fmaxf(fabsf(o[6]), fabsf(o[7]))));
        bool safe = (l >= 8.6736174e-19f) && (l <= 1.2676506e30f) && (amax <= 3.0e38f);
        if (!__all(safe)) {
            slow_block(ws, blk ? qy : qx, lane, o, l);
            auto sw = __builtin_amdgcn_permlane32_swap(__float_as_uint(l), __float_as_uint(l), false, false);
            l = __uint_as_float(sw[0]);
        }
        const float ri = __builtin_amdgcn_rcpf(l);
        u32 A0 = pk(o[0] * ri, o[1] * ri), A1 = pk(o[2] * ri, o[3] * ri);
        u32 B0 = pk(o[4] * ri, o[5] * ri), B1 = pk(o[6] * ri, o[7] * ri);
        auto s0 = __builtin_amdgcn_permlane32_swap(A0, B0, false, false);
        auto s1 = __builtin_amdgcn_permlane32_swap(A1, B1, false, false);
        u32x4 ov = {s0[0], s1[0], s0[1], s1[1]};
#pragma unroll
        for (int i = 0; i < 4; ++i) ov[i] = ov[i] == POISON ? 0x7fc07fc0u : ov[i];
        GST128C(ws, oslot, ov);
    }
    {
        u32 yo = yo0;
        asm volatile("s_cmp_lg_u32 %[stb], %[tb]\n\ts_cbranch_scc1 3f\n"
                     "global_load_dwordx4 %0, %[yo], %[ws] sc1\n v_add_u32 %[yo], 0x2000, %[yo]\n"
                     "global_load_dwordx4 %1, %[yo], %[ws] sc1\n v_add_u32 %[yo], 0x2000, %[yo]\n"
                     "global_load_dwordx4 %2, %[yo], %[ws] sc1\n v_add_u32 %[yo], 0x2000, %[yo]\n"
                     "global_load_dwordx4 %3, %[yo], %[ws] sc1\n v_add_u32 %[yo], 0x2000, %[yo]\n"
                     "global_load_dwordx4 %4, %[yo], %[ws] sc1\n v_add_u32 %[yo], 0x2000, %[yo]\n"
                     "global_load_dwordx4 %5, %[yo], %[ws] sc1\n v_add_u32 %[yo], 0x2000, %[yo]\n"
                     "global_load_dwordx4 %6, %[yo], %[ws] sc1\n3:"
                     : "=&v"(Y0), "=&v"(Y1), "=&v"(Y2), "=&v"(Y3), "=&v"(Y4), "=&v"(Y5), "=&v"(Y6), [yo] "+v"(yo)
                     : [stb] "s"(sel >> 6), [tb] "s"(tb), [ws] "s"(ws) : "memory", "scc");
    }
    STAMP(13);

    u32x4 af[4][8];
    {
        const u32 f = ((l15 >> 3) & 1) | ((g & 1) << 1);
        const u32 rb = (kh ? L_V : L_K) + (8 * g + (l15 >> 2)) * 128 + (l15 & 3) * 8;
#pragma unroll
        for (int nb = 0; nb < 4; ++nb) {
            u32 a = rb + ((nb ^ f) * 32);
            asm volatile("" : "+v"(a));
#pragma unroll
            for (int st = 0; st < 8; ++st) {
                const u32 o_ = (st >> 2) * 32768 + (st & 3) * 4096;
                af[nb][st] = cat(trread(lds, a + o_), trread(lds, a + o_ + 512));
            }
        }
    }
    STAMP(14);
    asm volatile("s_cmp_lg_u32 %[stb], %[tb]\n\ts_cbranch_scc1 3f\n\ts_waitcnt vmcnt(0)\n3:\n\ts_waitcnt vmcnt(1)"
                 : "+v"(xe), "+v"(gl), "+v"(Y0), "+v"(Y1), "+v"(Y2), "+v"(Y3), "+v"(Y4), "+v"(Y5), "+v"(Y6)
                 : [stb] "s"(sel >> 6), [tb] "s"(tb) : "memory", "scc");
    if (owner) {
        u32 spins = 0;
#define YMX(v) max(max(v[0], v[1]), max(v[2], v[3]))
        while (!__all(max(max(max(YMX(Y0), YMX(Y1)), max(YMX(Y2), YMX(Y3))), max(max(YMX(Y4), YMX(Y5)), YMX(Y6))) != POISON) && ++spins <= SPIN_MAX) {
            u32 yo = yo0;
            asm volatile("global_load_dwordx4 %0, %[yo], %[ws] sc1\n v_add_u32 %[yo], 0x2000, %[yo]\n"
                         "global_load_dwordx4 %1, %[yo], %[ws] sc1\n v_add_u32 %[yo], 0x2000, %[yo]\n"
                         "global_load_dwordx4 %2, %[yo], %[ws] sc1\n v_add_u32 %[yo], 0x2000, %[yo]\n"
                         "global_load_dwordx4 %3, %[yo], %[ws] sc1\n v_add_u32 %[yo], 0x2000, %[yo]\n"
                         "global_load_dwordx4 %4, %[yo], %[ws] sc1\n v_add_u32 %[yo], 0x2000, %[yo]\n"
                         "global_load_dwordx4 %5, %[yo], %[ws] sc1\n v_add_u32 %[yo], 0x2000, %[yo]\n"
                         "global_load_dwordx4 %6, %[yo], %[ws] sc1\n s_waitcnt vmcnt(0)"
                         : "=&v"(Y0), "=&v"(Y1), "=&v"(Y2), "=&v"(Y3), "=&v"(Y4), "=&v"(Y5), "=&v"(Y6), [yo] "+v"(yo)
                         : [ws] "s"(ws) : "memory");
        }
        f32x4 ys;
#pragma unroll
        for (int i = 0; i < 4; ++i) {
            float v = ((__uint_as_float(Y0[i]) + __uint_as_float(Y1[i])) + (__uint_as_float(Y2[i]) + __uint_as_float(Y3[i])))
                    + ((__uint_as_float(Y4[i]) + __uint_as_float(Y5[i])) + __uint_as_float(Y6[i]));
            auto a = __builtin_amdgcn_permlane32_swap(__float_as_uint(v), __float_as_uint(v), false, false);
            v = __uint_as_float(a[0]) + __uint_as_float(a[1]);
            auto c = __builtin_amdgcn_permlane16_swap(__float_as_uint(v), __float_as_uint(v), false, false);
            ys[i] = __uint_as_float(c[0]) + __uint_as_float(c[1]);
        }
        if (g == 0) ST128(L_YP + w * 256 + l15 * 16, __builtin_bit_cast(u32x4, ys));
    }
    u32x4 of0, of1, of2, of3, of4, of5, of6, of7;
    {
        const u32 oo = WS_O + ((tb * 4 + tq) * 16 + 8 * kh) * 1024 + lane * 16, o2 = oo + 4096;
        u32 spins = 0;
        while (true) {
            asm volatile("global_load_dwordx4 %0, %8, %10 sc1\n\tglobal_load_dwordx4 %1, %8, %10 offset:1024 sc1\n\t"
                         "global_load_dwordx4 %2, %8, %10 offset:2048 sc1\n\tglobal_load_dwordx4 %3, %8, %10 offset:3072 sc1\n\t"
                         "global_load_dwordx4 %4, %9, %10 sc1\n\tglobal_load_dwordx4 %5, %9, %10 offset:1024 sc1\n\t"
                         "global_load_dwordx4 %6, %9, %10 offset:2048 sc1\n\tglobal_load_dwordx4 %7, %9, %10 offset:3072 sc1\n\t"
                         "s_waitcnt vmcnt(0)"
                         : "=&v"(of0), "=&v"(of1), "=&v"(of2), "=&v"(of3), "=&v"(of4), "=&v"(of5), "=&v"(of6), "=&v"(of7)
                         : "v"(oo), "v"(o2), "s"(ws) : "memory");
            u32 mx = 0;
#define OMX(v) mx = max(max(mx, max(v[0], v[1])), max(v[2], v[3]));
            OMX(of0) OMX(of1) OMX(of2) OMX(of3) OMX(of4) OMX(of5) OMX(of6) OMX(of7)
            if (__all(mx != POISON) || ++spins > SPIN_MAX) break;
        }
    }
    STAMP(15);
    f32x4 acc[4] = {{0, 0, 0, 0}, {0, 0, 0, 0}, {0, 0, 0, 0}, {0, 0, 0, 0}};
#define OSTEP(OF, ST) \
    _Pragma("unroll") for (int nb = 0; nb < 4; ++nb) acc[nb] = __builtin_amdgcn_mfma_f32_16x16x32_bf16(asbf(af[nb][ST]), asbf(OF), acc[nb], 0, 0, 0);
    OSTEP(of0, 0) OSTEP(of1, 1) OSTEP(of2, 2) OSTEP(of3, 3) OSTEP(of4, 4) OSTEP(of5, 5) OSTEP(of6, 6) OSTEP(of7, 7)
    STAMP(16);
    {
        f32x4 k0 = kh ? acc[2] : acc[0], k1 = kh ? acc[3] : acc[1];
        f32x4 g0 = kh ? acc[0] : acc[2], g1 = kh ? acc[1] : acc[3];
        ST128(L_K + 16384 + w * 2048 + lane * 16, __builtin_bit_cast(u32x4, g0));
        ST128(L_K + 16384 + w * 2048 + 1024 + lane * 16, __builtin_bit_cast(u32x4, g1));
        BAR();
        k0 += __builtin_bit_cast(f32x4, LD128(L_K + 16384 + (w ^ 4) * 2048 + lane * 16));
        k1 += __builtin_bit_cast(f32x4, LD128(L_K + 16384 + (w ^ 4) * 2048 + 1024 + lane * 16));
        float a[8];
#pragma unroll
        for (int i = 0; i < 4; ++i) {
            auto sw = __builtin_amdgcn_permlane16_swap(__float_as_uint(k0[i]), __float_as_uint(k1[i]), false, false);
            a[i] = __uint_as_float(sw[0]); a[4 + i] = __uint_as_float(sw[1]);
        }
        const float gt = bfr(sigm(bfr(__uint_as_float(gl))));
        const float og = bfr(1.0f - gt);
        float bb[8];
#pragma unroll
        for (int i = 0; i < 4; ++i) { bb[2 * i] = lo16(xe[i]); bb[2 * i + 1] = hi16(xe[i]); }
        if (owner && tokg == sel) {
            const u32 rord = key >> 16;
            const float rsel = __uint_as_float(((rord & 0x8000u) ? (rord & 0x7fffu) : (~rord & 0xffffu)) << 16);
            const float sg = bfr(sigm(rsel));
            const int c0 = kh * 32 + (g & 1) * 16 + (g >> 1) * 8;
#pragma unroll
            for (int i = 0; i < 8; ++i) {
                float y = 0.f;
#pragma unroll
                for (int p = 0; p < 8; ++p) y += LDF(L_YP + p * 256 + (c0 + i) * 4);
                bb[i] = bfr(bb[i] + bfr(bfr(y) * sg));
            }
        }
        u32x4 ov;
#pragma unroll
        for (int i = 0; i < 4; ++i)
            ov[i] = pk(bfr(gt * bfr(a[2 * i])) + bfr(og * bb[2 * i]), bfr(gt * bfr(a[2 * i + 1])) + bfr(og * bb[2 * i + 1]));
        u32 oo_ = tokg * 1024 + ncol * 2;
        GST128(out, oo_, ov);
    }
    STAMP(17); STAMPC(21);
    BAR();
    if (tid == 0 && ((b >> 3) == 5 || (b >> 3) == 0)) {
#define RD(wv, i) LD32(L_CT + 64 + ((wv) * 24 + (i)) * 4)
        u32 t1 = RD(0, 23);
        printf("STMP b %d tA %u tB %u t1 %u/%u/%u prio %u/%u bar1 %u pub %u poll %u att0 %u att %u end %u\n", b, t1 % 100000, RD(0,8) - t1, RD(0,1) - t1, RD(4,1) - t1, RD(7,1) - t1, RD(0,2) - t1, RD(7,2) - t1, RD(0,3) - t1, RD(7,4) - t1, RD(4,7) - t1,
               RD(0,10) - t1, RD(0,12) - t1, RD(4,17) - t1);
    }
    }
}

extern "C" void kernel_launch(void* const* d_in, const int* in_sizes, int n_in, void* d_out, int out_size,
                              void* d_ws, size_t ws_size, hipStream_t stream) {
    (void)in_sizes; (void)n_in; (void)out_size; (void)ws_size;
    fused_kernel<<<256, 512, 0, stream>>>((const u16*)d_in[0], (const u16*)d_in[1], (const u16*)d_in[2], (const u16*)d_in[3],
                                          (const u16*)d_in[4], (const u16*)d_in[6], (const u16*)d_in[7], (const u16*)d_in[8],
                                          (const u16*)d_in[9], (const u16*)d_in[10], (u16*)d_out, (char*)d_ws);
}
```

```cpp
#include <hip/hip_runtime.h>
#include <stdint.h>
#include <stdio.h>
#define DBG_STAMP
#define V2N

typedef unsigned int u32;
typedef unsigned long long u64;
typedef unsigned short u16;
typedef u32 u32x2 __attribute__((ext_vector_type(2)));
typedef u32 u32x4 __attribute__((ext_vector_type(4)));
typedef u32 u32x8 __attribute__((ext_vector_type(8)));
typedef u32 u32x16 __attribute__((ext_vector_type(16)));
typedef float f32x2 __attribute__((ext_vector_type(2)));
typedef float f32x4 __attribute__((ext_vector_type(4)));
typedef float f32x16 __attribute__((ext_vector_type(16)));
typedef __bf16 bf16x2 __attribute__((ext_vector_type(2)));
typedef __bf16 bf16x8 __attribute__((ext_vector_type(8)));
#define LDSP __attribute__((address_space(3)))

constexpr u32 WS_E   = 0;
constexpr u32 WS_F2  = 4096;
constexpr u32 WS_G   = 24576;
constexpr u32 WS_K   = 65536;
constexpr u32 WS_V   = 131072;
constexpr u32 WS_F1  = 262144;
constexpr u32 WS_O   = 524288;
constexpr u32 WS_Y   = 2621440;
constexpr u32 WS_DBG = 4194304;

constexpr u32 L_K   = 0;
constexpr u32 L_V   = 65536;
constexpr u32 L_ONE = 131072;
constexpr u32 L_W   = 135168;
constexpr u32 L_M   = 151552;
constexpr u32 L_P1  = L_M;
constexpr u32 L_PD  = L_M + 8192;
constexpr u32 L_FP  = L_M + 8704;
constexpr u32 L_YP  = L_M + 9216;
constexpr u32 L_CT  = L_M + 11264;
constexpr u32 L_END = 163840;

constexpr u32 SPIN_MAX = 1u << 22;
constexpr u32 POISON = 0xffffffffu;

#define LD128(a) (*(const LDSP u32x4*)(lds + (a)))
#define ST128(a, v) (*(LDSP u32x4*)(lds + (a)) = (v))
#define LD32(a) (*(const LDSP u32*)(lds + (a)))
#define ST32(a, v) (*(LDSP u32*)(lds + (a)) = (v))
#define LDF(a) (*(const LDSP float*)(lds + (a)))
#define STF(a, v) (*(LDSP float*)(lds + (a)) = (v))

__device__ __forceinline__ u32x2 trread(LDSP char* lds, u32 a) {
    typedef short v4s __attribute__((ext_vector_type(4)));
    v4s r = __builtin_amdgcn_ds_read_tr16_b64_v4i16((LDSP v4s*)(lds + a));
    return __builtin_bit_cast(u32x2, r);
}
__device__ __forceinline__ bf16x8 asbf(u32x4 v) { return __builtin_bit_cast(bf16x8, v); }
__device__ __forceinline__ u32x4 cat(u32x2 a, u32x2 b) { u32x4 r = {a[0], a[1], b[0], b[1]}; return r; }
__device__ __forceinline__ u32 pk(float a, float b) {
    f32x2 f = {a, b};
    return __builtin_bit_cast(u32, __builtin_convertvector(f, bf16x2));
}
__device__ __forceinline__ float bfr(float a) {
    return __uint_as_float(pk(a, a) & 0xffff0000u);
}
__device__ __forceinline__ float lo16(u32 v) { return __uint_as_float(v << 16); }
__device__ __forceinline__ float hi16(u32 v) { return __uint_as_float(v & 0xffff0000u); }
__device__ __forceinline__ float sigm(float z) { return __builtin_amdgcn_rcpf(1.0f + __builtin_amdgcn_exp2f(-1.4426950408889634f * z)); }

__device__ __forceinline__ void dma16(const void* base, u32 voff, u32 ldsaddr) {
    asm volatile("s_mov_b32 m0, %0\n\ts_nop 0\n\tglobal_load_lds_dwordx4 %1, %2" :: "s"(ldsaddr), "v"(voff), "s"(base) : "memory");
}
__device__ __forceinline__ void dma16c4(const void* base, u32 voff, u32 ldsaddr) {
    asm volatile("s_mov_b32 m0, %0\n\ts_nop 0\n\tglobal_load_lds_dwordx4 %1, %2 sc1\n\tglobal_load_lds_dwordx4 %1, %2 offset:1024 sc1\n\t"
                 "global_load_lds_dwordx4 %1, %2 offset:2048 sc1\n\tglobal_load_lds_dwordx4 %1, %2 offset:3072 sc1"
                 :: "s"(ldsaddr), "v"(voff), "s"(base) : "memory");
}
__device__ __forceinline__ void dma16c3(const void* base, u32 voff, u32 ldsaddr) {
    asm volatile("s_mov_b32 m0, %0\n\ts_nop 0\n\tglobal_load_lds_dwordx4 %1, %2 sc1\n\tglobal_load_lds_dwordx4 %1, %2 offset:1024 sc1\n\t"
                 "global_load_lds_dwordx4 %1, %2 offset:2048 sc1"
                 :: "s"(ldsaddr), "v"(voff), "s"(base) : "memory");
}
__device__ __forceinline__ void dma16c2(const void* base, u32 voff, u32 ldsaddr) {
    asm volatile("s_mov_b32 m0, %0\n\ts_nop 0\n\tglobal_load_lds_dwordx4 %1, %2 sc1\n\tglobal_load_lds_dwordx4 %1, %2 offset:1024 sc1"
                 :: "s"(ldsaddr), "v"(voff), "s"(base) : "memory");
}
__device__ __forceinline__ void dma16c(const void* base, u32 voff, u32 ldsaddr) {
    asm volatile("s_mov_b32 m0, %0\n\ts_nop 0\n\tglobal_load_lds_dwordx4 %1, %2 sc1" :: "s"(ldsaddr), "v"(voff), "s"(base) : "memory");
}
#define GLD128(dst, base, voff, OFF)  asm volatile("global_load_dwordx4 %0, %1, %2 offset:" #OFF : "=v"(dst) : "v"(voff), "s"(base) : "memory")
#define GLD128C(dst, base, voff, OFF) asm volatile("global_load_dwordx4 %0, %1, %2 offset:" #OFF " sc1" : "=v"(dst) : "v"(voff), "s"(base) : "memory")
#define GLD64C(dst, base, voff, OFF)  asm volatile("global_load_dwordx2 %0, %1, %2 offset:" #OFF " sc1" : "=v"(dst) : "v"(voff), "s"(base) : "memory")
#define GLD32C(dst, base, voff, OFF)  asm volatile("global_load_dword %0, %1, %2 offset:" #OFF " sc1" : "=v"(dst) : "v"(voff), "s"(base) : "memory")
#define GLD32(dst, base, voff, OFF)   asm volatile("global_load_dword %0, %1, %2 offset:" #OFF : "=v"(dst) : "v"(voff), "s"(base) : "memory")
#define GLD16(dst, base, voff, OFF)   asm volatile("global_load_ushort %0, %1, %2 offset:" #OFF : "=v"(dst) : "v"(voff), "s"(base) : "memory")
#define GST128(base, voff, val)  asm volatile("global_store_dwordx4 %0, %1, %2\n\ts_nop 1" :: "v"(voff), "v"(val), "s"(base) : "memory")
#define GST128C(base, voff, val) asm volatile("global_store_dwordx4 %0, %1, %2 sc1\n\ts_nop 1" :: "v"(voff), "v"(val), "s"(base) : "memory")
#define GST64C(base, voff, val)  asm volatile("global_store_dwordx2 %0, %1, %2 sc1\n\ts_nop 0" :: "v"(voff), "v"(val), "s"(base) : "memory")
#define GST32C(base, voff, val)  asm volatile("global_store_dword %0, %1, %2 sc1" :: "v"(voff), "v"(val), "s"(base) : "memory")
#define VMWAIT(N) asm volatile("s_waitcnt vmcnt(" #N ")" ::: "memory")
#define BAR() asm volatile("s_waitcnt lgkmcnt(0)\n\ts_barrier" ::: "memory")

#define STAMP(i) do { u32 t_ = (u32)__builtin_amdgcn_s_memrealtime(); if (lane == 0) ST32(L_CT + 64 + (w * 24 + (i)) * 4, t_); } while (0)
#define STAMPC(i) do { u32 t_ = (u32)__builtin_amdgcn_s_memtime(); if (lane == 0) ST32(L_CT + 64 + (w * 24 + (i)) * 4, t_); } while (0)
#define STAMPDUMP() do { if (lane < 24) ((u32*)(ws + WS_DBG))[(blockIdx.x * 8 + w) * 32 + lane] = LD32(L_CT + 64 + (w * 24 + lane) * 4); } while (0)
#define FSTAMP(i) STAMP(i)
#define LSTAMP(i)

__device__ __forceinline__ u32 wave_max_u32(u32 v) {
    v = max(v, (u32)__builtin_amdgcn_update_dpp(0, (int)v, 0x111, 0xf, 0xf, false));
    v = max(v, (u32)__builtin_amdgcn_update_dpp(0, (int)v, 0x112, 0xf, 0xf, false));
    v = max(v, (u32)__builtin_amdgcn_update_dpp(0, (int)v, 0x114, 0xf, 0xf, false));
    v = max(v, (u32)__builtin_amdgcn_update_dpp(0, (int)v, 0x118, 0xf, 0xf, false));
    v = max(v, (u32)__builtin_amdgcn_update_dpp(0, (int)v, 0x142, 0xa, 0xf, false));
    v = max(v, (u32)__builtin_amdgcn_update_dpp(0, (int)v, 0x143, 0xc, 0xf, false));
    return (u32)__builtin_amdgcn_readlane((int)v, 63);
}
#define ATT_A \
    "s_mov_b32 %[cnt], 2\n" \
    "v_mov_b32 v232, -1\n" \
    "v_mov_b32 v233, -1\n" \
    "v_mov_b32 v234, -1\n" \
    "v_mov_b32 v235, -1\n" \
    "1:\n" \
    "v_exp_f32 v64, v64\n" \
    "v_exp_f32 v65, v65\n" \
    "v_smfmac_f32_32x32x32_bf16 v[144:159], v[188:191], v[120:127], v192\n" \
    "global_store_dwordx4 v220, v[232:235], %[wsb] sc1\n" \
    "ds_read_b128 v[184:187], v194 offset:1024\n" \
    "v_exp_f32 v66, v66\n" \
    "v_exp_f32 v67, v67\n" \
    "v_exp_f32 v68, v68\n" \
    "v_exp_f32 v69, v69\n" \
    "s_waitcnt lgkmcnt(1)\n" \
    "v_mfma_f32_32x32x16_bf16 v[80:95], v[172:175], v[160:163], 0\n" \
    "ds_read_b128 v[188:191], v194 offset:1536\n" \
    "v_exp_f32 v70, v70\n" \
    "v_exp_f32 v71, v71\n" \
    "v_exp_f32 v72, v72\n" \
    "v_exp_f32 v73, v73\n" \
    "v_smfmac_f32_32x32x32_bf16 v[128:143], v[176:179], v[64:71], v192\n" \
    "v_exp_f32 v74, v74\n" \
    "s_mov_b32 m0, %[skv]\n" \
    "v_exp_f32 v75, v75\n" \
    "v_exp_f32 v76, v76\n" \
    "global_load_lds_dwordx4 v196, %[kvb] offset:0 sc1\n" \
    "v_exp_f32 v77, v77\n" \
    "v_exp_f32 v78, v78\n" \
    "v_exp_f32 v79, v79\n" \
    "v_exp_f32 v96, v96\n" \
    "v_exp_f32 v97, v97\n" \
    "v_smfmac_f32_32x32x32_bf16 v[128:143], v[180:183], v[72:79], v192\n" \
    "v_exp_f32 v98, v98\n" \
    "v_exp_f32 v99, v99\n" \
    "v_exp_f32 v100, v100\n" \
    "v_exp_f32 v101, v101\n" \
    "v_mfma_f32_32x32x16_bf16 v[112:127], v[172:175], v[164:167], 0\n" \
    "ds_read_b128 v[168:171], v193 offset:2048\n" \
    "v_exp_f32 v102, v102\n" \
    "v_exp_f32 v103, v103\n" \
    "v_exp_f32 v104, v104\n" \
    "v_exp_f32 v105, v105\n" \
    "v_smfmac_f32_32x32x32_bf16 v[144:159], v[176:179], v[96:103], v192\n" \
    "v_exp_f32 v106, v106\n" \
    "v_exp_f32 v107, v107\n" \
    "v_exp_f32 v108, v108\n" \
    "v_exp_f32 v109, v109\n" \
    "v_exp_f32 v110, v110\n" \
    "v_exp_f32 v111, v111\n" \
    "v_exp_f32 v80, v80\n" \
    "v_exp_f32 v81, v81\n" \
    "v_smfmac_f32_32x32x32_bf16 v[144:159], v[180:183], v[104:111], v192\n" \
    "ds_read_b128 v[176:179], v194 offset:2048\n" \
    "v_exp_f32 v82, v82\n" \
    "v_exp_f32 v83, v83\n" \
    "v_exp_f32 v84, v84\n" \
    "v_exp_f32 v85, v85\n" \
    "s_waitcnt lgkmcnt(1)\n" \
    "v_mfma_f32_32x32x16_bf16 v[64:79], v[168:171], v[160:163], 0\n" \
    "ds_read_b128 v[180:183], v194 offset:2560\n" \
    "v_exp_f32 v86, v86\n" \
    "v_exp_f32 v87, v87\n" \
    "v_exp_f32 v88, v88\n" \
    "v_exp_f32 v89, v89\n" \
    "v_smfmac_f32_32x32x32_bf16 v[128:143], v[184:187], v[80:87], v192\n" \
    "v_exp_f32 v90, v90\n" \
    "s_mov_b32 m0, %[skv]\n" \
    "v_exp_f32 v91, v91\n" \
    "v_exp_f32 v92, v92\n" \
    "global_load_lds_dwordx4 v196, %[kvb] offset:1024 sc1\n" \
    "v_exp_f32 v93, v93\n" \
    "v_exp_f32 v94, v94\n" \
    "v_exp_f32 v95, v95\n" \
    "v_exp_f32 v112, v112\n" \
    "v_exp_f32 v113, v113\n" \
    "v_smfmac_f32_32x32x32_bf16 v[128:143], v[188:191], v[88:95], v192\n" \
    "v_exp_f32 v114, v114\n" \
    "v_exp_f32 v115, v115\n" \
    "v_exp_f32 v116, v116\n" \
    "v_exp_f32 v117, v117\n" \
    "v_mfma_f32_32x32x16_bf16 v[96:111], v[168:171], v[164:167], 0\n" \
    "ds_read_b128 v[172:175], v193 offset:3072\n" \
    "v_exp_f32 v118, v118\n" \
    "v_exp_f32 v119, v119\n" \
    "v_exp_f32 v120, v120\n" \
    "v_exp_f32 v121, v121\n" \
    "v_smfmac_f32_32x32x32_bf16 v[144:159], v[184:187], v[112:119], v192\n" \
    "v_exp_f32 v122, v122\n" \
    "s_mov_b32 m0, %[sg]\n" \
    "v_exp_f32 v123, v123\n" \
    "v_exp_f32 v124, v124\n" \
    "global_load_lds_dwordx4 v[198:199], off\n" \
    "v_exp_f32 v125, v125\n" \
    "v_exp_f32 v126, v126\n" \
    "v_exp_f32 v127, v127\n" \
    "s_cmp_eq_u32 %[cnt], 1\n" \
    "s_cbranch_scc0 8f\n" \
    "s_setprio 0\n" \
    "8:\n" \
    "v_exp_f32 v64, v64\n" \
    "v_exp_f32 v65, v65\n" \
    "v_smfmac_f32_32x32x32_bf16 v[144:159], v[188:191], v[120:127], v192\n" \
    "ds_read_b128 v[184:187], v194 offset:3072\n" \
    "v_exp_f32 v66, v66\n" \
    "v_exp_f32 v67, v67\n" \
    "v_exp_f32 v68, v68\n" \
    "v_exp_f32 v69, v69\n" \
    "s_waitcnt lgkmcnt(1)\n" \
    "v_mfma_f32_32x32x16_bf16 v[80:95], v[172:175], v[160:163], 0\n" \
    "ds_read_b128 v[188:191], v194 offset:3584\n" \
    "v_exp_f32 v70, v70\n" \
    "v_exp_f32 v71, v71\n" \
    "v_exp_f32 v72, v72\n" \
    "v_exp_f32 v73, v73\n" \
    "v_smfmac_f32_32x32x32_bf16 v[128:143], v[176:179], v[64:71], v192\n" \
    "v_exp_f32 v74, v74\n" \
    "s_mov_b32 m0, %[skv]\n" \
    "v_exp_f32 v75, v75\n" \
    "v_exp_f32 v76, v76\n" \
    "global_load_lds_dwordx4 v196, %[kvb] offset:2048 sc1\n" \
    "v_exp_f32 v77, v77\n" \
    "v_exp_f32 v78, v78\n" \
    "v_exp_f32 v79, v79\n" \
    "v_exp_f32 v96, v96\n" \
    "v_exp_f32 v97, v97\n" \
    "v_smfmac_f32_32x32x32_bf16 v[128:143], v[180:183], v[72:79], v192\n" \
    "v_exp_f32 v98, v98\n" \
    "v_exp_f32 v99, v99\n" \
    "v_exp_f32 v100, v100\n" \
    "v_exp_f32 v101, v101\n" \
    "v_mfma_f32_32x32x16_bf16 v[112:127], v[172:175], v[164:167], 0\n" \
    "ds_read_b128 v[168:171], v193 offset:4096\n" \
    "v_exp_f32 v102, v102\n" \
    "v_exp_f32 v103, v103\n" \
    "v_exp_f32 v104, v104\n" \
    "v_exp_f32 v105, v105\n" \
    "v_smfmac_f32_32x32x32_bf16 v[144:159], v[176:179], v[96:103], v192\n" \
    "v_exp_f32 v106, v106\n" \
    "v_exp_f32 v107, v107\n" \
    "v_exp_f32 v108, v108\n" \
    "v_exp_f32 v109, v109\n" \
    "v_exp_f32 v110, v110\n" \
    "v_exp_f32 v111, v111\n" \
    "v_exp_f32 v80, v80\n" \
    "v_exp_f32 v81, v81\n" \
    "v_smfmac_f32_32x32x32_bf16 v[144:159], v[180:183], v[104:111], v192\n" \
    "ds_read_b128 v[176:179], v194 offset:4096\n" \
    "v_exp_f32 v82, v82\n" \
    "v_exp_f32 v83, v83\n" \
    "v_exp_f32 v84, v84\n" \
    "v_exp_f32 v85, v85\n" \
    "s_waitcnt lgkmcnt(1)\n" \
    "v_mfma_f32_32x32x16_bf16 v[64:79], v[168:171], v[160:163], 0\n" \
    "ds_read_b128 v[180:183], v194 offset:4608\n" \
    "v_exp_f32 v86, v86\n" \
    "v_exp_f32 v87, v87\n" \
    "v_exp_f32 v88, v88\n" \
    "v_exp_f32 v89, v89\n" \
    "v_smfmac_f32_32x32x32_bf16 v[128:143], v[184:187], v[80:87], v192\n" \
    "v_exp_f32 v90, v90\n" \
    "s_mov_b32 m0, %[skv]\n" \
    "v_exp_f32 v91, v91\n" \
    "v_exp_f32 v92, v92\n" \
    "global_load_lds_dwordx4 v196, %[kvb] offset:3072 sc1\n" \
    "v_exp_f32 v93, v93\n" \
    "v_exp_f32 v94, v94\n" \
    "v_exp_f32 v95, v95\n" \
    "v_exp_f32 v112, v112\n" \
    "v_exp_f32 v113, v113\n" \
    "v_smfmac_f32_32x32x32_bf16 v[128:143], v[188:191], v[88:95], v192\n" \
    "v_exp_f32 v114, v114\n" \
    "v_exp_f32 v115, v115\n" \
    "v_exp_f32 v116, v116\n" \
    "v_exp_f32 v117, v117\n" \
    "v_mfma_f32_32x32x16_bf16 v[96:111], v[168:171], v[164:167], 0\n" \
    "ds_read_b128 v[172:175], v193 offset:5120\n" \
    "v_exp_f32 v118, v118\n" \
    "v_exp_f32 v119, v119\n" \
    "v_exp_f32 v120, v120\n" \
    "v_exp_f32 v121, v121\n" \
    "v_smfmac_f32_32x32x32_bf16 v[144:159], v[184:187], v[112:119], v192\n" \
    "v_exp_f32 v122, v122\n" \
    "v_exp_f32 v123, v123\n" \
    "v_exp_f32 v124, v124\n" \
    "v_exp_f32 v125, v125\n" \
    "v_exp_f32 v126, v126\n" \
    "v_exp_f32 v127, v127\n" \
    "v_add_u32 v196, 0x2000, v196\n" \
    "v_mov_b32 v198, v200\n" \
    "v_mov_b32 v199, v201\n" \
    "s_add_u32 %[skv], %[skv], 0x2000\n" \
    "s_add_u32 %[sg], %[sg], 0x400\n" \
    "v_mov_b32 v220, v221\n" \
    "v_add_u32 v193, 4096, v193\n" \
    "v_add_u32 v194, v195, v194\n" \
    "s_sub_u32 %[cnt], %[cnt], 1\n" \
    "s_cmp_lg_u32 %[cnt], 0\n" \
    "s_cbranch_scc1 1b\n" \
    "s_waitcnt vmcnt(5)\n"

#define ATT_A1 \
    "s_mov_b32 %[cnt], 2\n" \
    "v_mov_b32 v232, -1\n" \
    "v_mov_b32 v233, -1\n" \
    "v_mov_b32 v234, -1\n" \
    "v_mov_b32 v235, -1\n" \
    "v_exp_f32 v64, v64\n" \
    "v_exp_f32 v65, v65\n" \
    "v_smfmac_f32_32x32x32_bf16 v[144:159], v[188:191], v[120:127], v192\n" \
    "global_store_dwordx4 v220, v[232:235], %[wsb] sc1\n" \
    "ds_read_b128 v[184:187], v194 offset:1024\n" \
    "v_exp_f32 v66, v66\n" \
    "v_exp_f32 v67, v67\n" \
    "v_exp_f32 v68, v68\n" \
    "v_exp_f32 v69, v69\n" \
    "s_waitcnt lgkmcnt(1)\n" \
    "v_mfma_f32_32x32x16_bf16 v[80:95], v[172:175], v[160:163], 0\n" \
    "ds_read_b128 v[188:191], v194 offset:1536\n" \
    "v_exp_f32 v70, v70\n" \
    "v_exp_f32 v71, v71\n" \
    "v_exp_f32 v72, v72\n" \
    "v_exp_f32 v73, v73\n" \
    "v_smfmac_f32_32x32x32_bf16 v[128:143], v[176:179], v[64:71], v192\n" \
    "v_exp_f32 v74, v74\n" \
    "s_mov_b32 m0, %[skv]\n" \
    "v_exp_f32 v75, v75\n" \
    "v_exp_f32 v76, v76\n" \
    "global_load_lds_dwordx4 v196, %[kvb] offset:0 sc1\n" \
    "v_exp_f32 v77, v77\n" \
    "v_exp_f32 v78, v78\n" \
    "v_exp_f32 v79, v79\n" \
    "v_exp_f32 v96, v96\n" \
    "v_exp_f32 v97, v97\n" \
    "v_smfmac_f32_32x32x32_bf16 v[128:143], v[180:183], v[72:79], v192\n" \
    "v_exp_f32 v98, v98\n" \
    "v_exp_f32 v99, v99\n" \
    "v_exp_f32 v100, v100\n" \
    "v_exp_f32 v101, v101\n" \
    "v_mfma_f32_32x32x16_bf16 v[112:127], v[172:175], v[164:167], 0\n" \
    "ds_read_b128 v[168:171], v193 offset:2048\n" \
    "v_exp_f32 v102, v102\n" \
    "v_exp_f32 v103, v103\n" \
    "v_exp_f32 v104, v104\n" \
    "v_exp_f32 v105, v105\n" \
    "v_smfmac_f32_32x32x32_bf16 v[144:159], v[176:179], v[96:103], v192\n" \
    "v_exp_f32 v106, v106\n" \
    "v_exp_f32 v107, v107\n" \
    "v_exp_f32 v108, v108\n" \
    "v_exp_f32 v109, v109\n" \
    "v_exp_f32 v110, v110\n" \
    "v_exp_f32 v111, v111\n" \
    "v_exp_f32 v80, v80\n" \
    "v_exp_f32 v81, v81\n" \
    "v_smfmac_f32_32x32x32_bf16 v[144:159], v[180:183], v[104:111], v192\n" \
    "ds_read_b128 v[176:179], v194 offset:2048\n" \
    "v_exp_f32 v82, v82\n" \
    "v_exp_f32 v83, v83\n" \
    "v_exp_f32 v84, v84\n" \
    "v_exp_f32 v85, v85\n" \
    "s_waitcnt lgkmcnt(1)\n" \
    "v_mfma_f32_32x32x16_bf16 v[64:79], v[168:171], v[160:163], 0\n" \
    "ds_read_b128 v[180:183], v194 offset:2560\n" \
    "v_exp_f32 v86, v86\n" \
    "v_exp_f32 v87, v87\n" \
    "v_exp_f32 v88, v88\n" \
    "v_exp_f32 v89, v89\n" \
    "v_smfmac_f32_32x32x32_bf16 v[128:143], v[184:187], v[80:87], v192\n" \
    "v_exp_f32 v90, v90\n" \
    "s_mov_b32 m0, %[skv]\n" \
    "v_exp_f32 v91, v91\n" \
    "v_exp_f32 v92, v92\n" \
    "global_load_lds_dwordx4 v196, %[kvb] offset:1024 sc1\n" \
    "v_exp_f32 v93, v93\n" \
    "v_exp_f32 v94, v94\n" \
    "v_exp_f32 v95, v95\n" \
    "v_exp_f32 v112, v112\n" \
    "v_exp_f32 v113, v113\n" \
    "v_smfmac_f32_32x32x32_bf16 v[128:143], v[188:191], v[88:95], v192\n" \
    "v_exp_f32 v114, v114\n" \
    "v_exp_f32 v115, v115\n" \
    "v_exp_f32 v116, v116\n" \
    "v_exp_f32 v117, v117\n" \
    "v_mfma_f32_32x32x16_bf16 v[96:111], v[168:171], v[164:167], 0\n" \
    "ds_read_b128 v[172:175], v193 offset:3072\n" \
    "v_exp_f32 v118, v118\n" \
    "v_exp_f32 v119, v119\n" \
    "v_exp_f32 v120, v120\n" \
    "v_exp_f32 v121, v121\n" \
    "v_smfmac_f32_32x32x32_bf16 v[144:159], v[184:187], v[112:119], v192\n" \
    "v_exp_f32 v122, v122\n" \
    "s_mov_b32 m0, %[sg]\n" \
    "v_exp_f32 v123, v123\n" \
    "v_exp_f32 v124, v124\n" \
    "global_load_lds_dwordx4 v[198:199], off\n" \
    "v_exp_f32 v125, v125\n" \
    "v_exp_f32 v126, v126\n" \
    "v_exp_f32 v127, v127\n" \
    "s_cmp_eq_u32 %[cnt], 1\n" \
    "s_cbranch_scc0 8f\n" \
    "s_setprio 0\n" \
    "8:\n" \
    "v_exp_f32 v64, v64\n" \
    "v_exp_f32 v65, v65\n" \
    "v_smfmac_f32_32x32x32_bf16 v[144:159], v[188:191], v[120:127], v192\n" \
    "ds_read_b128 v[184:187], v194 offset:3072\n" \
    "v_exp_f32 v66, v66\n" \
    "v_exp_f32 v67, v67\n" \
    "v_exp_f32 v68, v68\n" \
    "v_exp_f32 v69, v69\n" \
    "s_waitcnt lgkmcnt(1)\n" \
    "v_mfma_f32_32x32x16_bf16 v[80:95], v[172:175], v[160:163], 0\n" \
    "ds_read_b128 v[188:191], v194 offset:3584\n" \
    "v_exp_f32 v70, v70\n" \
    "v_exp_f32 v71, v71\n" \
    "v_exp_f32 v72, v72\n" \
    "v_exp_f32 v73, v73\n" \
    "v_smfmac_f32_32x32x32_bf16 v[128:143], v[176:179], v[64:71], v192\n" \
    "v_exp_f32 v74, v74\n" \
    "s_mov_b32 m0, %[skv]\n" \
    "v_exp_f32 v75, v75\n" \
    "v_exp_f32 v76, v76\n" \
    "global_load_lds_dwordx4 v196, %[kvb] offset:2048 sc1\n" \
    "v_exp_f32 v77, v77\n" \
    "v_exp_f32 v78, v78\n" \
    "v_exp_f32 v79, v79\n" \
    "v_exp_f32 v96, v96\n" \
    "v_exp_f32 v97, v97\n" \
    "v_smfmac_f32_32x32x32_bf16 v[128:143], v[180:183], v[72:79], v192\n" \
    "v_exp_f32 v98, v98\n" \
    "v_exp_f32 v99, v99\n" \
    "v_exp_f32 v100, v100\n" \
    "v_exp_f32 v101, v101\n" \
    "v_mfma_f32_32x32x16_bf16 v[112:127], v[172:175], v[164:167], 0\n" \
    "ds_read_b128 v[168:171], v193 offset:4096\n" \
    "v_exp_f32 v102, v102\n" \
    "v_exp_f32 v103, v103\n" \
    "v_exp_f32 v104, v104\n" \
    "v_exp_f32 v105, v105\n" \
    "v_smfmac_f32_32x32x32_bf16 v[144:159], v[176:179], v[96:103], v192\n" \
    "v_exp_f32 v106, v106\n" \
    "v_exp_f32 v107, v107\n" \
    "v_exp_f32 v108, v108\n" \
    "v_exp_f32 v109, v109\n" \
    "v_exp_f32 v110, v110\n" \
    "v_exp_f32 v111, v111\n" \
    "v_exp_f32 v80, v80\n" \
    "v_exp_f32 v81, v81\n" \
    "v_smfmac_f32_32x32x32_bf16 v[144:159], v[180:183], v[104:111], v192\n" \
    "ds_read_b128 v[176:179], v194 offset:4096\n" \
    "v_exp_f32 v82, v82\n" \
    "v_exp_f32 v83, v83\n" \
    "v_exp_f32 v84, v84\n" \
    "v_exp_f32 v85, v85\n" \
    "s_waitcnt lgkmcnt(1)\n" \
    "v_mfma_f32_32x32x16_bf16 v[64:79], v[168:171], v[160:163], 0\n" \
    "ds_read_b128 v[180:183], v194 offset:4608\n" \
    "v_exp_f32 v86, v86\n" \
    "v_exp_f32 v87, v87\n" \
    "v_exp_f32 v88, v88\n" \
    "v_exp_f32 v89, v89\n" \
    "v_smfmac_f32_32x32x32_bf16 v[128:143], v[184:187], v[80:87], v192\n" \
    "v_exp_f32 v90, v90\n" \
    "s_mov_b32 m0, %[skv]\n" \
    "v_exp_f32 v91, v91\n" \
    "v_exp_f32 v92, v92\n" \
    "global_load_lds_dwordx4 v196, %[kvb] offset:3072 sc1\n" \
    "v_exp_f32 v93, v93\n" \
    "v_exp_f32 v94, v94\n" \
    "v_exp_f32 v95, v95\n" \
    "v_exp_f32 v112, v112\n" \
    "v_exp_f32 v113, v113\n" \
    "v_smfmac_f32_32x32x32_bf16 v[128:143], v[188:191], v[88:95], v192\n" \
    "v_exp_f32 v114, v114\n" \
    "v_exp_f32 v115, v115\n" \
    "v_exp_f32 v116, v116\n" \
    "v_exp_f32 v117, v117\n" \
    "v_mfma_f32_32x32x16_bf16 v[96:111], v[168:171], v[164:167], 0\n" \
    "ds_read_b128 v[172:175], v193 offset:5120\n" \
    "v_exp_f32 v118, v118\n" \
    "v_exp_f32 v119, v119\n" \
    "v_exp_f32 v120, v120\n" \
    "v_exp_f32 v121, v121\n" \
    "v_smfmac_f32_32x32x32_bf16 v[144:159], v[184:187], v[112:119], v192\n" \
    "v_exp_f32 v122, v122\n" \
    "v_exp_f32 v123, v123\n" \
    "v_exp_f32 v124, v124\n" \
    "v_exp_f32 v125, v125\n" \
    "v_exp_f32 v126, v126\n" \
    "v_exp_f32 v127, v127\n" \
    "v_add_u32 v196, 0x2000, v196\n" \
    "v_mov_b32 v198, v200\n" \
    "v_mov_b32 v199, v201\n" \
    "s_add_u32 %[skv], %[skv], 0x2000\n" \
    "s_add_u32 %[sg], %[sg], 0x400\n" \
    "v_mov_b32 v220, v221\n" \
    "v_add_u32 v193, 4096, v193\n" \
    "v_add_u32 v194, v195, v194\n"

#define ATT_A2 \
    "s_mov_b32 %[cnt], 1\n" \
    "v_exp_f32 v64, v64\n" \
    "v_exp_f32 v65, v65\n" \
    "v_smfmac_f32_32x32x32_bf16 v[144:159], v[188:191], v[120:127], v192\n" \
    "global_store_dwordx4 v220, v[232:235], %[wsb] sc1\n" \
    "ds_read_b128 v[184:187], v194 offset:1024\n" \
    "v_exp_f32 v66, v66\n" \
    "v_exp_f32 v67, v67\n" \
    "v_exp_f32 v68, v68\n" \
    "v_exp_f32 v69, v69\n" \
    "s_waitcnt lgkmcnt(1)\n" \
    "v_mfma_f32_32x32x16_bf16 v[80:95], v[172:175], v[160:163], 0\n" \
    "ds_read_b128 v[188:191], v194 offset:1536\n" \
    "v_exp_f32 v70, v70\n" \
    "v_exp_f32 v71, v71\n" \
    "v_exp_f32 v72, v72\n" \
    "v_exp_f32 v73, v73\n" \
    "v_smfmac_f32_32x32x32_bf16 v[128:143], v[176:179], v[64:71], v192\n" \
    "v_exp_f32 v74, v74\n" \
    "s_mov_b32 m0, %[skv]\n" \
    "v_exp_f32 v75, v75\n" \
    "v_exp_f32 v76, v76\n" \
    "global_load_lds_dwordx4 v196, %[kvb] offset:0 sc1\n" \
    "v_exp_f32 v77, v77\n" \
    "v_exp_f32 v78, v78\n" \
    "v_exp_f32 v79, v79\n" \
    "v_exp_f32 v96, v96\n" \
    "v_exp_f32 v97, v97\n" \
    "v_smfmac_f32_32x32x32_bf16 v[128:143], v[180:183], v[72:79], v192\n" \
    "v_exp_f32 v98, v98\n" \
    "v_exp_f32 v99, v99\n" \
    "v_exp_f32 v100, v100\n" \
    "v_exp_f32 v101, v101\n" \
    "v_mfma_f32_32x32x16_bf16 v[112:127], v[172:175], v[164:167], 0\n" \
    "ds_read_b128 v[168:171], v193 offset:2048\n" \
    "v_exp_f32 v102, v102\n" \
    "v_exp_f32 v103, v103\n" \
    "v_exp_f32 v104, v104\n" \
    "v_exp_f32 v105, v105\n" \
    "v_smfmac_f32_32x32x32_bf16 v[144:159], v[176:179], v[96:103], v192\n" \
    "v_exp_f32 v106, v106\n" \
    "v_exp_f32 v107, v107\n" \
    "v_exp_f32 v108, v108\n" \
    "v_exp_f32 v109, v109\n" \
    "v_exp_f32 v110, v110\n" \
    "v_exp_f32 v111, v111\n" \
    "v_exp_f32 v80, v80\n" \
    "v_exp_f32 v81, v81\n" \
    "v_smfmac_f32_32x32x32_bf16 v[144:159], v[180:183], v[104:111], v192\n" \
    "ds_read_b128 v[176:179], v194 offset:2048\n" \
    "v_exp_f32 v82, v82\n" \
    "v_exp_f32 v83, v83\n" \
    "v_exp_f32 v84, v84\n" \
    "v_exp_f32 v85, v85\n" \
    "s_waitcnt lgkmcnt(1)\n" \
    "v_mfma_f32_32x32x16_bf16 v[64:79], v[168:171], v[160:163], 0\n" \
    "ds_read_b128 v[180:183], v194 offset:2560\n" \
    "v_exp_f32 v86, v86\n" \
    "v_exp_f32 v87, v87\n" \
    "v_exp_f32 v88, v88\n" \
    "v_exp_f32 v89, v89\n" \
    "v_smfmac_f32_32x32x32_bf16 v[128:143], v[184:187], v[80:87], v192\n" \
    "v_exp_f32 v90, v90\n" \
    "s_mov_b32 m0, %[skv]\n" \
    "v_exp_f32 v91, v91\n" \
    "v_exp_f32 v92, v92\n" \
    "global_load_lds_dwordx4 v196, %[kvb] offset:1024 sc1\n" \
    "v_exp_f32 v93, v93\n" \
    "v_exp_f32 v94, v94\n" \
    "v_exp_f32 v95, v95\n" \
    "v_exp_f32 v112, v112\n" \
    "v_exp_f32 v113, v113\n" \
    "v_smfmac_f32_32x32x32_bf16 v[128:143], v[188:191], v[88:95], v192\n" \
    "v_exp_f32 v114, v114\n" \
    "v_exp_f32 v115, v115\n" \
    "v_exp_f32 v116, v116\n" \
    "v_exp_f32 v117, v117\n" \
    "v_mfma_f32_32x32x16_bf16 v[96:111], v[168:171], v[164:167], 0\n" \
    "ds_read_b128 v[172:175], v193 offset:3072\n" \
    "v_exp_f32 v118, v118\n" \
    "v_exp_f32 v119, v119\n" \
    "v_exp_f32 v120, v120\n" \
    "v_exp_f32 v121, v121\n" \
    "v_smfmac_f32_32x32x32_bf16 v[144:159], v[184:187], v[112:119], v192\n" \
    "v_exp_f32 v122, v122\n" \
    "s_mov_b32 m0, %[sg]\n" \
    "v_exp_f32 v123, v123\n" \
    "v_exp_f32 v124, v124\n" \
    "global_load_lds_dwordx4 v[198:199], off\n" \
    "v_exp_f32 v125, v125\n" \
    "v_exp_f32 v126, v126\n" \
    "v_exp_f32 v127, v127\n" \
    "s_cmp_eq_u32 %[cnt], 1\n" \
    "s_cbranch_scc0 8f\n" \
    "s_setprio 0\n" \
    "8:\n" \
    "v_exp_f32 v64, v64\n" \
    "v_exp_f32 v65, v65\n" \
    "v_smfmac_f32_32x32x32_bf16 v[144:159], v[188:191], v[120:127], v192\n" \
    "ds_read_b128 v[184:187], v194 offset:3072\n" \
    "v_exp_f32 v66, v66\n" \
    "v_exp_f32 v67, v67\n" \
    "v_exp_f32 v68, v68\n" \
    "v_exp_f32 v69, v69\n" \
    "s_waitcnt lgkmcnt(1)\n" \
    "v_mfma_f32_32x32x16_bf16 v[80:95], v[172:175], v[160:163], 0\n" \
    "ds_read_b128 v[188:191], v194 offset:3584\n" \
    "v_exp_f32 v70, v70\n" \
    "v_exp_f32 v71, v71\n" \
    "v_exp_f32 v72, v72\n" \
    "v_exp_f32 v73, v73\n" \
    "v_smfmac_f32_32x32x32_bf16 v[128:143], v[176:179], v[64:71], v192\n" \
    "v_exp_f32 v74, v74\n" \
    "s_mov_b32 m0, %[skv]\n" \
    "v_exp_f32 v75, v75\n" \
    "v_exp_f32 v76, v76\n" \
    "global_load_lds_dwordx4 v196, %[kvb] offset:2048 sc1\n" \
    "v_exp_f32 v77, v77\n" \
    "v_exp_f32 v78, v78\n" \
    "v_exp_f32 v79, v79\n" \
    "v_exp_f32 v96, v96\n" \
    "v_exp_f32 v97, v97\n" \
    "v_smfmac_f32_32x32x32_bf16 v[128:143], v[180:183], v[72:79], v192\n" \
    "v_exp_f32 v98, v98\n" \
    "v_exp_f32 v99, v99\n" \
    "v_exp_f32 v100, v100\n" \
    "v_exp_f32 v101, v101\n" \
    "v_mfma_f32_32x32x16_bf16 v[112:127], v[172:175], v[164:167], 0\n" \
    "ds_read_b128 v[168:171], v193 offset:4096\n" \
    "v_exp_f32 v102, v102\n" \
    "v_exp_f32 v103, v103\n" \
    "v_exp_f32 v104, v104\n" \
    "v_exp_f32 v105, v105\n" \
    "v_smfmac_f32_32x32x32_bf16 v[144:159], v[176:179], v[96:103], v192\n" \
    "v_exp_f32 v106, v106\n" \
    "v_exp_f32 v107, v107\n" \
    "v_exp_f32 v108, v108\n" \
    "v_exp_f32 v109, v109\n" \
    "v_exp_f32 v110, v110\n" \
    "v_exp_f32 v111, v111\n" \
    "v_exp_f32 v80, v80\n" \
    "v_exp_f32 v81, v81\n" \
    "v_smfmac_f32_32x32x32_bf16 v[144:159], v[180:183], v[104:111], v192\n" \
    "ds_read_b128 v[176:179], v194 offset:4096\n" \
    "v_exp_f32 v82, v82\n" \
    "v_exp_f32 v83, v83\n" \
    "v_exp_f32 v84, v84\n" \
    "v_exp_f32 v85, v85\n" \
    "s_waitcnt lgkmcnt(1)\n" \
    "v_mfma_f32_32x32x16_bf16 v[64:79], v[168:171], v[160:163], 0\n" \
    "ds_read_b128 v[180:183], v194 offset:4608\n" \
    "v_exp_f32 v86, v86\n" \
    "v_exp_f32 v87, v87\n" \
    "v_exp_f32 v88, v88\n" \
    "v_exp_f32 v89, v89\n" \
    "v_smfmac_f32_32x32x32_bf16 v[128:143], v[184:187], v[80:87], v192\n" \
    "v_exp_f32 v90, v90\n" \
    "s_mov_b32 m0, %[skv]\n" \
    "v_exp_f32 v91, v91\n" \
    "v_exp_f32 v92, v92\n" \
    "global_load_lds_dwordx4 v196, %[kvb] offset:3072 sc1\n" \
    "v_exp_f32 v93, v93\n" \
    "v_exp_f32 v94, v94\n" \
    "v_exp_f32 v95, v95\n" \
    "v_exp_f32 v112, v112\n" \
    "v_exp_f32 v113, v113\n" \
    "v_smfmac_f32_32x32x32_bf16 v[128:143], v[188:191], v[88:95], v192\n" \
    "v_exp_f32 v114, v114\n" \
    "v_exp_f32 v115, v115\n" \
    "v_exp_f32 v116, v116\n" \
    "v_exp_f32 v117, v117\n" \
    "v_mfma_f32_32x32x16_bf16 v[96:111], v[168:171], v[164:167], 0\n" \
    "ds_read_b128 v[172:175], v193 offset:5120\n" \
    "v_exp_f32 v118, v118\n" \
    "v_exp_f32 v119, v119\n" \
    "v_exp_f32 v120, v120\n" \
    "v_exp_f32 v121, v121\n" \
    "v_smfmac_f32_32x32x32_bf16 v[144:159], v[184:187], v[112:119], v192\n" \
    "v_exp_f32 v122, v122\n" \
    "v_exp_f32 v123, v123\n" \
    "v_exp_f32 v124, v124\n" \
    "v_exp_f32 v125, v125\n" \
    "v_exp_f32 v126, v126\n" \
    "v_exp_f32 v127, v127\n" \
    "v_add_u32 v196, 0x2000, v196\n" \
    "v_mov_b32 v198, v200\n" \
    "v_mov_b32 v199, v201\n" \
    "s_add_u32 %[skv], %[skv], 0x2000\n" \
    "s_add_u32 %[sg], %[sg], 0x400\n" \
    "v_mov_b32 v220, v221\n" \
    "v_add_u32 v193, 4096, v193\n" \
    "v_add_u32 v194, v195, v194\n"

#define ATT_B \
    "v_exp_f32 v64, v64\n" \
    "v_exp_f32 v65, v65\n" \
    "v_smfmac_f32_32x32x32_bf16 v[144:159], v[188:191], v[120:127], v192\n" \
    "ds_read_b128 v[184:187], v194 offset:1024\n" \
    "v_exp_f32 v66, v66\n" \
    "v_exp_f32 v67, v67\n" \
    "v_exp_f32 v68, v68\n" \
    "v_exp_f32 v69, v69\n" \
    "s_waitcnt lgkmcnt(1)\n" \
    "v_mfma_f32_32x32x16_bf16 v[80:95], v[172:175], v[160:163], 0\n" \
    "ds_read_b128 v[188:191], v194 offset:1536\n" \
    "v_exp_f32 v70, v70\n" \
    "v_exp_f32 v71, v71\n" \
    "v_exp_f32 v72, v72\n" \
    "v_exp_f32 v73, v73\n" \
    "v_smfmac_f32_32x32x32_bf16 v[128:143], v[176:179], v[64:71], v192\n" \
    "v_exp_f32 v74, v74\n" \
    "s_mov_b32 m0, %[skv3]\n" \
    "v_exp_f32 v75, v75\n" \
    "v_exp_f32 v76, v76\n" \
    "global_load_dwordx4 v[204:207], v202, %[xb]\n" \
    "v_exp_f32 v77, v77\n" \
    "v_exp_f32 v78, v78\n" \
    "v_exp_f32 v79, v79\n" \
    "v_exp_f32 v96, v96\n" \
    "v_exp_f32 v97, v97\n" \
    "v_smfmac_f32_32x32x32_bf16 v[128:143], v[180:183], v[72:79], v192\n" \
    "v_exp_f32 v98, v98\n" \
    "v_exp_f32 v99, v99\n" \
    "v_exp_f32 v100, v100\n" \
    "v_exp_f32 v101, v101\n" \
    "v_mfma_f32_32x32x16_bf16 v[112:127], v[172:175], v[164:167], 0\n" \
    "ds_read_b128 v[168:171], v193 offset:2048\n" \
    "v_exp_f32 v102, v102\n" \
    "v_exp_f32 v103, v103\n" \
    "v_exp_f32 v104, v104\n" \
    "v_exp_f32 v105, v105\n" \
    "v_smfmac_f32_32x32x32_bf16 v[144:159], v[176:179], v[96:103], v192\n" \
    "v_exp_f32 v106, v106\n" \
    "v_exp_f32 v107, v107\n" \
    "v_exp_f32 v108, v108\n" \
    "global_load_dwordx4 v[208:211], v202, %[xb] offset:16\n" \
    "v_exp_f32 v109, v109\n" \
    "v_exp_f32 v110, v110\n" \
    "v_exp_f32 v111, v111\n" \
    "v_exp_f32 v80, v80\n" \
    "v_exp_f32 v81, v81\n" \
    "v_smfmac_f32_32x32x32_bf16 v[144:159], v[180:183], v[104:111], v192\n" \
    "ds_read_b128 v[176:179], v194 offset:2048\n" \
    "v_exp_f32 v82, v82\n" \
    "v_exp_f32 v83, v83\n" \
    "v_exp_f32 v84, v84\n" \
    "v_exp_f32 v85, v85\n" \
    "s_waitcnt lgkmcnt(1)\n" \
    "v_mfma_f32_32x32x16_bf16 v[64:79], v[168:171], v[160:163], 0\n" \
    "ds_read_b128 v[180:183], v194 offset:2560\n" \
    "v_exp_f32 v86, v86\n" \
    "v_exp_f32 v87, v87\n" \
    "v_exp_f32 v88, v88\n" \
    "v_exp_f32 v89, v89\n" \
    "v_smfmac_f32_32x32x32_bf16 v[128:143], v[184:187], v[80:87], v192\n" \
    "v_exp_f32 v90, v90\n" \
    "v_exp_f32 v91, v91\n" \
    "v_exp_f32 v92, v92\n" \
    "global_load_lds_dwordx4 v197, %[kvb] offset:0 sc1\n" \
    "v_exp_f32 v93, v93\n" \
    "v_exp_f32 v94, v94\n" \
    "v_exp_f32 v95, v95\n" \
    "v_exp_f32 v112, v112\n" \
    "v_exp_f32 v113, v113\n" \
    "v_smfmac_f32_32x32x32_bf16 v[128:143], v[188:191], v[88:95], v192\n" \
    "v_exp_f32 v114, v114\n" \
    "v_exp_f32 v115, v115\n" \
    "v_exp_f32 v116, v116\n" \
    "v_exp_f32 v117, v117\n" \
    "v_mfma_f32_32x32x16_bf16 v[96:111], v[168:171], v[164:167], 0\n" \
    "ds_read_b128 v[172:175], v193 offset:3072\n" \
    "v_exp_f32 v118, v118\n" \
    "v_exp_f32 v119, v119\n" \
    "v_exp_f32 v120, v120\n" \
    "v_exp_f32 v121, v121\n" \
    "v_smfmac_f32_32x32x32_bf16 v[144:159], v[184:187], v[112:119], v192\n" \
    "v_exp_f32 v122, v122\n" \
    "v_exp_f32 v123, v123\n" \
    "v_exp_f32 v124, v124\n" \
    "global_load_lds_dwordx4 v197, %[kvb] offset:1024 sc1\n" \
    "v_exp_f32 v125, v125\n" \
    "v_exp_f32 v126, v126\n" \
    "v_exp_f32 v127, v127\n" \
    "v_exp_f32 v64, v64\n" \
    "v_exp_f32 v65, v65\n" \
    "v_smfmac_f32_32x32x32_bf16 v[144:159], v[188:191], v[120:127], v192\n" \
    "ds_read_b128 v[184:187], v194 offset:3072\n" \
    "v_exp_f32 v66, v66\n" \
    "v_exp_f32 v67, v67\n" \
    "v_exp_f32 v68, v68\n" \
    "v_exp_f32 v69, v69\n" \
    "s_waitcnt lgkmcnt(1)\n" \
    "v_mfma_f32_32x32x16_bf16 v[80:95], v[172:175], v[160:163], 0\n" \
    "ds_read_b128 v[188:191], v194 offset:3584\n" \
    "v_exp_f32 v70, v70\n" \
    "v_exp_f32 v71, v71\n" \
    "v_exp_f32 v72, v72\n" \
    "v_exp_f32 v73, v73\n" \
    "v_smfmac_f32_32x32x32_bf16 v[128:143], v[176:179], v[64:71], v192\n" \
    "v_exp_f32 v74, v74\n" \
    "v_exp_f32 v75, v75\n" \
    "v_exp_f32 v76, v76\n" \
    "global_load_lds_dwordx4 v197, %[kvb] offset:2048 sc1\n" \
    "v_exp_f32 v77, v77\n" \
    "v_exp_f32 v78, v78\n" \
    "v_exp_f32 v79, v79\n" \
    "v_exp_f32 v96, v96\n" \
    "v_exp_f32 v97, v97\n" \
    "v_smfmac_f32_32x32x32_bf16 v[128:143], v[180:183], v[72:79], v192\n" \
    "v_exp_f32 v98, v98\n" \
    "v_exp_f32 v99, v99\n" \
    "v_exp_f32 v100, v100\n" \
    "v_exp_f32 v101, v101\n" \
    "v_mfma_f32_32x32x16_bf16 v[112:127], v[172:175], v[164:167], 0\n" \
    "ds_read_b128 v[168:171], v193 offset:4096\n" \
    "v_exp_f32 v102, v102\n" \
    "v_exp_f32 v103, v103\n" \
    "v_exp_f32 v104, v104\n" \
    "v_exp_f32 v105, v105\n" \
    "v_smfmac_f32_32x32x32_bf16 v[144:159], v[176:179], v[96:103], v192\n" \
    "v_exp_f32 v106, v106\n" \
    "v_exp_f32 v107, v107\n" \
    "v_exp_f32 v108, v108\n" \
    "global_load_lds_dwordx4 v197, %[kvb] offset:3072 sc1\n" \
    "v_exp_f32 v109, v109\n" \
    "v_exp_f32 v110, v110\n" \
    "v_exp_f32 v111, v111\n" \
    "v_exp_f32 v80, v80\n" \
    "v_exp_f32 v81, v81\n" \
    "v_smfmac_f32_32x32x32_bf16 v[144:159], v[180:183], v[104:111], v192\n" \
    "ds_read_b128 v[176:179], v194 offset:4096\n" \
    "v_exp_f32 v82, v82\n" \
    "v_exp_f32 v83, v83\n" \
    "v_exp_f32 v84, v84\n" \
    "v_exp_f32 v85, v85\n" \
    "s_waitcnt lgkmcnt(1)\n" \
    "v_mfma_f32_32x32x16_bf16 v[64:79], v[168:171], v[160:163], 0\n" \
    "ds_read_b128 v[180:183], v194 offset:4608\n" \
    "v_exp_f32 v86, v86\n" \
    "v_exp_f32 v87, v87\n" \
    "v_exp_f32 v88, v88\n" \
    "v_exp_f32 v89, v89\n" \
    "v_smfmac_f32_32x32x32_bf16 v[128:143], v[184:187], v[80:87], v192\n" \
    "v_exp_f32 v90, v90\n" \
    "v_exp_f32 v91, v91\n" \
    "v_exp_f32 v92, v92\n" \
    "global_load_dword v212, v203, %[wdb] offset:0\n" \
    "global_load_dword v213, v203, %[wdb] offset:1024\n" \
    "v_exp_f32 v93, v93\n" \
    "v_exp_f32 v94, v94\n" \
    "v_exp_f32 v95, v95\n" \
    "v_exp_f32 v112, v112\n" \
    "v_exp_f32 v113, v113\n" \
    "v_smfmac_f32_32x32x32_bf16 v[128:143], v[188:191], v[88:95], v192\n" \
    "v_exp_f32 v114, v114\n" \
    "v_exp_f32 v115, v115\n" \
    "v_exp_f32 v116, v116\n" \
    "v_exp_f32 v117, v117\n" \
    "v_mfma_f32_32x32x16_bf16 v[96:111], v[168:171], v[164:167], 0\n" \
    "ds_read_b128 v[172:175], v193 offset:5120\n" \
    "v_exp_f32 v118, v118\n" \
    "v_exp_f32 v119, v119\n" \
    "v_exp_f32 v120, v120\n" \
    "v_exp_f32 v121, v121\n" \
    "v_smfmac_f32_32x32x32_bf16 v[144:159], v[184:187], v[112:119], v192\n" \
    "v_exp_f32 v122, v122\n" \
    "v_exp_f32 v123, v123\n" \
    "v_exp_f32 v124, v124\n" \
    "global_load_dword v214, v203, %[wdb] offset:2048\n" \
    "global_load_dword v215, v203, %[wdb] offset:3072\n" \
    "v_exp_f32 v125, v125\n" \
    "v_exp_f32 v126, v126\n" \
    "v_exp_f32 v127, v127\n" \
    "v_add_u32 v203, 0x1000, v203\n" \
    "v_add_u32 v193, 4096, v193\n" \
    "v_add_u32 v194, v195, v194\n" \
    "v_exp_f32 v64, v64\n" \
    "v_exp_f32 v65, v65\n" \
    "v_smfmac_f32_32x32x32_bf16 v[144:159], v[188:191], v[120:127], v192\n" \
    "ds_read_b128 v[184:187], v194 offset:1024\n" \
    "v_exp_f32 v66, v66\n" \
    "v_exp_f32 v67, v67\n" \
    "v_exp_f32 v68, v68\n" \
    "v_exp_f32 v69, v69\n" \
    "s_waitcnt lgkmcnt(1)\n" \
    "v_mfma_f32_32x32x16_bf16 v[80:95], v[172:175], v[160:163], 0\n" \
    "ds_read_b128 v[188:191], v194 offset:1536\n" \
    "v_exp_f32 v70, v70\n" \
    "v_exp_f32 v71, v71\n" \
    "v_exp_f32 v72, v72\n" \
    "v_exp_f32 v73, v73\n" \
    "v_smfmac_f32_32x32x32_bf16 v[128:143], v[176:179], v[64:71], v192\n" \
    "v_exp_f32 v74, v74\n" \
    "v_exp_f32 v75, v75\n" \
    "v_exp_f32 v76, v76\n" \
    "global_load_dword v216, v203, %[wdb] offset:0\n" \
    "global_load_dword v217, v203, %[wdb] offset:1024\n" \
    "v_exp_f32 v77, v77\n" \
    "v_exp_f32 v78, v78\n" \
    "v_exp_f32 v79, v79\n" \
    "v_exp_f32 v96, v96\n" \
    "v_exp_f32 v97, v97\n" \
    "v_smfmac_f32_32x32x32_bf16 v[128:143], v[180:183], v[72:79], v192\n" \
    "v_exp_f32 v98, v98\n" \
    "v_exp_f32 v99, v99\n" \
    "v_exp_f32 v100, v100\n" \
    "v_exp_f32 v101, v101\n" \
    "v_mfma_f32_32x32x16_bf16 v[112:127], v[172:175], v[164:167], 0\n" \
    "ds_read_b128 v[168:171], v193 offset:2048\n" \
    "v_exp_f32 v102, v102\n" \
    "v_exp_f32 v103, v103\n" \
    "v_exp_f32 v104, v104\n" \
    "v_exp_f32 v105, v105\n" \
    "v_smfmac_f32_32x32x32_bf16 v[144:159], v[176:179], v[96:103], v192\n" \
    "v_exp_f32 v106, v106\n" \
    "v_exp_f32 v107, v107\n" \
    "v_exp_f32 v108, v108\n" \
    "global_load_dword v218, v203, %[wdb] offset:2048\n" \
    "global_load_dword v219, v203, %[wdb] offset:3072\n" \
    "v_exp_f32 v109, v109\n" \
    "v_exp_f32 v110, v110\n" \
    "v_exp_f32 v111, v111\n" \
    "v_exp_f32 v80, v80\n" \
    "v_exp_f32 v81, v81\n" \
    "v_smfmac_f32_32x32x32_bf16 v[144:159], v[180:183], v[104:111], v192\n" \
    "s_waitcnt vmcnt(12)\n" \
    "ds_read_b64_tr_b16 v[220:221], v228\n" \
    "ds_read_b64_tr_b16 v[222:223], v229\n" \
    "ds_read_b64_tr_b16 v[224:225], v228 offset:256\n" \
    "ds_read_b64_tr_b16 v[226:227], v229 offset:256\n" \
    "ds_read_b128 v[176:179], v194 offset:2048\n" \
    "v_exp_f32 v82, v82\n" \
    "v_exp_f32 v83, v83\n" \
    "v_exp_f32 v84, v84\n" \
    "v_exp_f32 v85, v85\n" \
    "s_waitcnt lgkmcnt(1)\n" \
    "v_mfma_f32_32x32x16_bf16 v[64:79], v[168:171], v[160:163], 0\n" \
    "ds_read_b128 v[180:183], v194 offset:2560\n" \
    "v_exp_f32 v86, v86\n" \
    "v_exp_f32 v87, v87\n" \
    "v_exp_f32 v88, v88\n" \
    "v_exp_f32 v89, v89\n" \
    "v_smfmac_f32_32x32x32_bf16 v[128:143], v[184:187], v[80:87], v192\n" \
    "v_exp_f32 v90, v90\n" \
    "v_exp_f32 v91, v91\n" \
    "v_exp_f32 v92, v92\n" \
    "v_mfma_f32_16x16x32_bf16 v[232:235], v[220:223], v[204:207], 0\n" \
    "v_exp_f32 v93, v93\n" \
    "v_exp_f32 v94, v94\n" \
    "v_exp_f32 v95, v95\n" \
    "v_exp_f32 v112, v112\n" \
    "v_exp_f32 v113, v113\n" \
    "v_smfmac_f32_32x32x32_bf16 v[128:143], v[188:191], v[88:95], v192\n" \
    "v_exp_f32 v114, v114\n" \
    "v_exp_f32 v115, v115\n" \
    "v_exp_f32 v116, v116\n" \
    "v_exp_f32 v117, v117\n" \
    "v_mfma_f32_32x32x16_bf16 v[96:111], v[168:171], v[164:167], 0\n" \
    "ds_read_b128 v[172:175], v193 offset:3072\n" \
    "v_exp_f32 v118, v118\n" \
    "v_exp_f32 v119, v119\n" \
    "v_exp_f32 v120, v120\n" \
    "v_exp_f32 v121, v121\n" \
    "v_smfmac_f32_32x32x32_bf16 v[144:159], v[184:187], v[112:119], v192\n" \
    "v_exp_f32 v122, v122\n" \
    "v_exp_f32 v123, v123\n" \
    "v_exp_f32 v124, v124\n" \
    "v_exp_f32 v125, v125\n" \
    "v_exp_f32 v126, v126\n" \
    "v_exp_f32 v127, v127\n" \
    "s_setprio 0\n" \
    "v_exp_f32 v64, v64\n" \
    "v_exp_f32 v65, v65\n" \
    "v_smfmac_f32_32x32x32_bf16 v[144:159], v[188:191], v[120:127], v192\n" \
    "ds_read_b128 v[184:187], v194 offset:3072\n" \
    "v_exp_f32 v66, v66\n" \
    "v_exp_f32 v67, v67\n" \
    "v_exp_f32 v68, v68\n" \
    "v_exp_f32 v69, v69\n" \
    "s_waitcnt lgkmcnt(1)\n" \
    "v_mfma_f32_32x32x16_bf16 v[80:95], v[172:175], v[160:163], 0\n" \
    "ds_read_b128 v[188:191], v194 offset:3584\n" \
    "v_exp_f32 v70, v70\n" \
    "v_exp_f32 v71, v71\n" \
    "v_exp_f32 v72, v72\n" \
    "v_exp_f32 v73, v73\n" \
    "v_smfmac_f32_32x32x32_bf16 v[128:143], v[176:179], v[64:71], v192\n" \
    "v_exp_f32 v74, v74\n" \
    "v_exp_f32 v75, v75\n" \
    "v_exp_f32 v76, v76\n" \
    "v_mfma_f32_16x16x32_bf16 v[232:235], v[224:227], v[208:211], v[232:235]\n" \
    "v_exp_f32 v77, v77\n" \
    "v_exp_f32 v78, v78\n" \
    "v_exp_f32 v79, v79\n" \
    "v_exp_f32 v96, v96\n" \
    "v_exp_f32 v97, v97\n" \
    "v_smfmac_f32_32x32x32_bf16 v[128:143], v[180:183], v[72:79], v192\n" \
    "v_exp_f32 v98, v98\n" \
    "v_exp_f32 v99, v99\n" \
    "v_exp_f32 v100, v100\n" \
    "v_exp_f32 v101, v101\n" \
    "v_mfma_f32_32x32x16_bf16 v[112:127], v[172:175], v[164:167], 0\n" \
    "ds_read_b128 v[168:171], v193 offset:4096\n" \
    "v_exp_f32 v102, v102\n" \
    "v_exp_f32 v103, v103\n" \
    "v_exp_f32 v104, v104\n" \
    "v_exp_f32 v105, v105\n" \
    "v_smfmac_f32_32x32x32_bf16 v[144:159], v[176:179], v[96:103], v192\n" \
    "v_exp_f32 v106, v106\n" \
    "v_exp_f32 v107, v107\n" \
    "v_exp_f32 v108, v108\n" \
    "v_exp_f32 v109, v109\n" \
    "v_exp_f32 v110, v110\n" \
    "v_exp_f32 v111, v111\n" \
    "v_exp_f32 v80, v80\n" \
    "v_exp_f32 v81, v81\n" \
    "v_smfmac_f32_32x32x32_bf16 v[144:159], v[180:183], v[104:111], v192\n" \
    "ds_write_b128 v230, v[232:235]\n" \
    "ds_read_b128 v[176:179], v194 offset:4096\n" \
    "v_exp_f32 v82, v82\n" \
    "v_exp_f32 v83, v83\n" \
    "v_exp_f32 v84, v84\n" \
    "v_exp_f32 v85, v85\n" \
    "s_waitcnt lgkmcnt(1)\n" \
    "v_mfma_f32_32x32x16_bf16 v[64:79], v[168:171], v[160:163], 0\n" \
    "ds_read_b128 v[180:183], v194 offset:4608\n" \
    "v_exp_f32 v86, v86\n" \
    "v_exp_f32 v87, v87\n" \
    "v_exp_f32 v88, v88\n" \
    "v_exp_f32 v89, v89\n" \
    "v_smfmac_f32_32x32x32_bf16 v[128:143], v[184:187], v[80:87], v192\n" \
    "v_exp_f32 v90, v90\n" \
    "v_exp_f32 v91, v91\n" \
    "v_exp_f32 v92, v92\n" \
    "v_exp_f32 v93, v93\n" \
    "v_exp_f32 v94, v94\n" \
    "v_exp_f32 v95, v95\n" \
    "v_exp_f32 v112, v112\n" \
    "v_exp_f32 v113, v113\n" \
    "v_smfmac_f32_32x32x32_bf16 v[128:143], v[188:191], v[88:95], v192\n" \
    "v_exp_f32 v114, v114\n" \
    "v_exp_f32 v115, v115\n" \
    "v_exp_f32 v116, v116\n" \
    "v_exp_f32 v117, v117\n" \
    "v_mfma_f32_32x32x16_bf16 v[96:111], v[168:171], v[164:167], 0\n" \
    "ds_read_b128 v[172:175], v193 offset:5120\n" \
    "v_exp_f32 v118, v118\n" \
    "v_exp_f32 v119, v119\n" \
    "v_exp_f32 v120, v120\n" \
    "v_exp_f32 v121, v121\n" \
    "v_smfmac_f32_32x32x32_bf16 v[144:159], v[184:187], v[112:119], v192\n" \
    "v_exp_f32 v122, v122\n" \
    "v_exp_f32 v123, v123\n" \
    "v_exp_f32 v124, v124\n" \
    "v_exp_f32 v125, v125\n" \
    "v_exp_f32 v126, v126\n" \
    "v_exp_f32 v127, v127\n" \
    "v_add_u32 v193, 4096, v193\n" \
    "v_add_u32 v194, v195, v194\n" \
    "s_waitcnt vmcnt(0)\n"

#define ATT_C \
    "s_mov_b32 %[cnt], 2\n" \
    "1:\n" \
    "v_exp_f32 v64, v64\n" \
    "v_exp_f32 v65, v65\n" \
    "v_smfmac_f32_32x32x32_bf16 v[144:159], v[188:191], v[120:127], v192\n" \
    "ds_read_b128 v[184:187], v194 offset:1024\n" \
    "v_exp_f32 v66, v66\n" \
    "v_exp_f32 v67, v67\n" \
    "v_exp_f32 v68, v68\n" \
    "v_exp_f32 v69, v69\n" \
    "s_waitcnt lgkmcnt(1)\n" \
    "v_mfma_f32_32x32x16_bf16 v[80:95], v[172:175], v[160:163], 0\n" \
    "ds_read_b128 v[188:191], v194 offset:1536\n" \
    "v_exp_f32 v70, v70\n" \
    "v_exp_f32 v71, v71\n" \
    "v_exp_f32 v72, v72\n" \
    "v_exp_f32 v73, v73\n" \
    "v_smfmac_f32_32x32x32_bf16 v[128:143], v[176:179], v[64:71], v192\n" \
    "v_exp_f32 v74, v74\n" \
    "s_mov_b32 m0, %[swo]\n" \
    "v_exp_f32 v75, v75\n" \
    "v_exp_f32 v76, v76\n" \
    "global_load_lds_dwordx4 v236, %[wob] offset:0\n" \
    "v_exp_f32 v77, v77\n" \
    "v_exp_f32 v78, v78\n" \
    "v_exp_f32 v79, v79\n" \
    "v_exp_f32 v96, v96\n" \
    "v_exp_f32 v97, v97\n" \
    "v_smfmac_f32_32x32x32_bf16 v[128:143], v[180:183], v[72:79], v192\n" \
    "v_exp_f32 v98, v98\n" \
    "v_exp_f32 v99, v99\n" \
    "v_exp_f32 v100, v100\n" \
    "v_exp_f32 v101, v101\n" \
    "v_mfma_f32_32x32x16_bf16 v[112:127], v[172:175], v[164:167], 0\n" \
    "ds_read_b128 v[168:171], v193 offset:2048\n" \
    "v_exp_f32 v102, v102\n" \
    "v_exp_f32 v103, v103\n" \
    "v_exp_f32 v104, v104\n" \
    "v_exp_f32 v105, v105\n" \
    "v_smfmac_f32_32x32x32_bf16 v[144:159], v[176:179], v[96:103], v192\n" \
    "v_exp_f32 v106, v106\n" \
    "v_exp_f32 v107, v107\n" \
    "v_exp_f32 v108, v108\n" \
    "v_exp_f32 v109, v109\n" \
    "v_exp_f32 v110, v110\n" \
    "v_exp_f32 v111, v111\n" \
    "v_exp_f32 v80, v80\n" \
    "v_exp_f32 v81, v81\n" \
    "v_smfmac_f32_32x32x32_bf16 v[144:159], v[180:183], v[104:111], v192\n" \
    "ds_read_b128 v[176:179], v194 offset:2048\n" \
    "v_exp_f32 v82, v82\n" \
    "v_exp_f32 v83, v83\n" \
    "v_exp_f32 v84, v84\n" \
    "v_exp_f32 v85, v85\n" \
    "s_waitcnt lgkmcnt(1)\n" \
    "v_mfma_f32_32x32x16_bf16 v[64:79], v[168:171], v[160:163], 0\n" \
    "ds_read_b128 v[180:183], v194 offset:2560\n" \
    "v_exp_f32 v86, v86\n" \
    "v_exp_f32 v87, v87\n" \
    "v_exp_f32 v88, v88\n" \
    "v_exp_f32 v89, v89\n" \
    "v_smfmac_f32_32x32x32_bf16 v[128:143], v[184:187], v[80:87], v192\n" \
    "v_exp_f32 v90, v90\n" \
    "v_exp_f32 v91, v91\n" \
    "v_exp_f32 v92, v92\n" \
    "global_load_lds_dwordx4 v237, %[wob] offset:1024\n" \
    "v_exp_f32 v93, v93\n" \
    "v_exp_f32 v94, v94\n" \
    "v_exp_f32 v95, v95\n" \
    "v_exp_f32 v112, v112\n" \
    "v_exp_f32 v113, v113\n" \
    "v_smfmac_f32_32x32x32_bf16 v[128:143], v[188:191], v[88:95], v192\n" \
    "v_exp_f32 v114, v114\n" \
    "v_exp_f32 v115, v115\n" \
    "v_exp_f32 v116, v116\n" \
    "v_exp_f32 v117, v117\n" \
    "v_mfma_f32_32x32x16_bf16 v[96:111], v[168:171], v[164:167], 0\n" \
    "ds_read_b128 v[172:175], v193 offset:3072\n" \
    "v_exp_f32 v118, v118\n" \
    "v_exp_f32 v119, v119\n" \
    "v_exp_f32 v120, v120\n" \
    "v_exp_f32 v121, v121\n" \
    "v_smfmac_f32_32x32x32_bf16 v[144:159], v[184:187], v[112:119], v192\n" \
    "v_exp_f32 v122, v122\n" \
    "v_exp_f32 v123, v123\n" \
    "v_exp_f32 v124, v124\n" \
    "v_exp_f32 v125, v125\n" \
    "v_exp_f32 v126, v126\n" \
    "v_exp_f32 v127, v127\n" \
    "v_exp_f32 v64, v64\n" \
    "v_exp_f32 v65, v65\n" \
    "v_smfmac_f32_32x32x32_bf16 v[144:159], v[188:191], v[120:127], v192\n" \
    "ds_read_b128 v[184:187], v194 offset:3072\n" \
    "v_exp_f32 v66, v66\n" \
    "v_exp_f32 v67, v67\n" \
    "v_exp_f32 v68, v68\n" \
    "v_exp_f32 v69, v69\n" \
    "s_waitcnt lgkmcnt(1)\n" \
    "v_mfma_f32_32x32x16_bf16 v[80:95], v[172:175], v[160:163], 0\n" \
    "ds_read_b128 v[188:191], v194 offset:3584\n" \
    "v_exp_f32 v70, v70\n" \
    "v_exp_f32 v71, v71\n" \
    "v_exp_f32 v72, v72\n" \
    "v_exp_f32 v73, v73\n" \
    "v_smfmac_f32_32x32x32_bf16 v[128:143], v[176:179], v[64:71], v192\n" \
    "v_exp_f32 v74, v74\n" \
    "v_exp_f32 v75, v75\n" \
    "v_exp_f32 v76, v76\n" \
    "global_load_lds_dwordx4 v238, %[wob] offset:2048\n" \
    "v_exp_f32 v77, v77\n" \
    "v_exp_f32 v78, v78\n" \
    "v_exp_f32 v79, v79\n" \
    "v_exp_f32 v96, v96\n" \
    "v_exp_f32 v97, v97\n" \
    "v_smfmac_f32_32x32x32_bf16 v[128:143], v[180:183], v[72:79], v192\n" \
    "v_exp_f32 v98, v98\n" \
    "v_exp_f32 v99, v99\n" \
    "v_exp_f32 v100, v100\n" \
    "v_exp_f32 v101, v101\n" \
    "v_mfma_f32_32x32x16_bf16 v[112:127], v[172:175], v[164:167], 0\n" \
    "ds_read_b128 v[168:171], v193 offset:4096\n" \
    "v_exp_f32 v102, v102\n" \
    "v_exp_f32 v103, v103\n" \
    "v_exp_f32 v104, v104\n" \
    "v_exp_f32 v105, v105\n" \
    "v_smfmac_f32_32x32x32_bf16 v[144:159], v[176:179], v[96:103], v192\n" \
    "v_exp_f32 v106, v106\n" \
    "v_exp_f32 v107, v107\n" \
    "v_exp_f32 v108, v108\n" \
    "v_exp_f32 v109, v109\n" \
    "v_exp_f32 v110, v110\n" \
    "v_exp_f32 v111, v111\n" \
    "v_exp_f32 v80, v80\n" \
    "v_exp_f32 v81, v81\n" \
    "v_smfmac_f32_32x32x32_bf16 v[144:159], v[180:183], v[104:111], v192\n" \
    "ds_read_b128 v[176:179], v194 offset:4096\n" \
    "v_exp_f32 v82, v82\n" \
    "v_exp_f32 v83, v83\n" \
    "v_exp_f32 v84, v84\n" \
    "v_exp_f32 v85, v85\n" \
    "s_waitcnt lgkmcnt(1)\n" \
    "v_mfma_f32_32x32x16_bf16 v[64:79], v[168:171], v[160:163], 0\n" \
    "ds_read_b128 v[180:183], v194 offset:4608\n" \
    "v_exp_f32 v86, v86\n" \
    "v_exp_f32 v87, v87\n" \
    "v_exp_f32 v88, v88\n" \
    "v_exp_f32 v89, v89\n" \
    "v_smfmac_f32_32x32x32_bf16 v[128:143], v[184:187], v[80:87], v192\n" \
    "v_exp_f32 v90, v90\n" \
    "v_exp_f32 v91, v91\n" \
    "v_exp_f32 v92, v92\n" \
    "global_load_lds_dwordx4 v239, %[wob] offset:3072\n" \
    "v_exp_f32 v93, v93\n" \
    "v_exp_f32 v94, v94\n" \
    "v_exp_f32 v95, v95\n" \
    "v_exp_f32 v112, v112\n" \
    "v_exp_f32 v113, v113\n" \
    "v_smfmac_f32_32x32x32_bf16 v[128:143], v[188:191], v[88:95], v192\n" \
    "v_exp_f32 v114, v114\n" \
    "v_exp_f32 v115, v115\n" \
    "v_exp_f32 v116, v116\n" \
    "v_exp_f32 v117, v117\n" \
    "v_mfma_f32_32x32x16_bf16 v[96:111], v[168:171], v[164:167], 0\n" \
    "ds_read_b128 v[172:175], v193 offset:5120\n" \
    "v_exp_f32 v118, v118\n" \
    "v_exp_f32 v119, v119\n" \
    "v_exp_f32 v120, v120\n" \
    "v_exp_f32 v121, v121\n" \
    "v_smfmac_f32_32x32x32_bf16 v[144:159], v[184:187], v[112:119], v192\n" \
    "v_exp_f32 v122, v122\n" \
    "v_exp_f32 v123, v123\n" \
    "v_exp_f32 v124, v124\n" \
    "v_exp_f32 v125, v125\n" \
    "v_exp_f32 v126, v126\n" \
    "v_exp_f32 v127, v127\n" \
    "v_add_u32 v236, 0x8000, v236\n" \
    "v_add_u32 v237, 0x8000, v237\n" \
    "v_add_u32 v238, 0x8000, v238\n" \
    "v_add_u32 v239, 0x8000, v239\n" \
    "s_add_u32 %[swo], %[swo], 0x1000\n" \
    "v_add_u32 v193, 4096, v193\n" \
    "v_add_u32 v194, v195, v194\n" \
    "s_sub_u32 %[cnt], %[cnt], 1\n" \
    "s_cmp_lg_u32 %[cnt], 0\n" \
    "s_cbranch_scc1 1b\n" \
    "global_load_dword v220, v221, %[wsb] sc1\n" \
    "s_mov_b32 %[cnt], 2\n" \
    "2:\n" \
    "v_exp_f32 v64, v64\n" \
    "v_exp_f32 v65, v65\n" \
    "v_smfmac_f32_32x32x32_bf16 v[144:159], v[188:191], v[120:127], v192\n" \
    "ds_read_b128 v[184:187], v194 offset:1024\n" \
    "v_exp_f32 v66, v66\n" \
    "v_exp_f32 v67, v67\n" \
    "v_exp_f32 v68, v68\n" \
    "v_exp_f32 v69, v69\n" \
    "s_waitcnt lgkmcnt(1)\n" \
    "v_mfma_f32_32x32x16_bf16 v[80:95], v[172:175], v[160:163], 0\n" \
    "ds_read_b128 v[188:191], v194 offset:1536\n" \
    "v_exp_f32 v70, v70\n" \
    "v_exp_f32 v71, v71\n" \
    "v_exp_f32 v72, v72\n" \
    "v_exp_f32 v73, v73\n" \
    "v_smfmac_f32_32x32x32_bf16 v[128:143], v[176:179], v[64:71], v192\n" \
    "v_exp_f32 v74, v74\n" \
    "v_exp_f32 v75, v75\n" \
    "v_exp_f32 v76, v76\n" \
    "v_exp_f32 v77, v77\n" \
    "v_exp_f32 v78, v78\n" \
    "v_exp_f32 v79, v79\n" \
    "v_exp_f32 v96, v96\n" \
    "v_exp_f32 v97, v97\n" \
    "v_smfmac_f32_32x32x32_bf16 v[128:143], v[180:183], v[72:79], v192\n" \
    "v_exp_f32 v98, v98\n" \
    "v_exp_f32 v99, v99\n" \
    "v_exp_f32 v100, v100\n" \
    "v_exp_f32 v101, v101\n" \
    "v_mfma_f32_32x32x16_bf16 v[112:127], v[172:175], v[164:167], 0\n" \
    "ds_read_b128 v[168:171], v193 offset:2048\n" \
    "v_exp_f32 v102, v102\n" \
    "v_exp_f32 v103, v103\n" \
    "v_exp_f32 v104, v104\n" \
    "v_exp_f32 v105, v105\n" \
    "v_smfmac_f32_32x32x32_bf16 v[144:159], v[176:179], v[96:103], v192\n" \
    "v_exp_f32 v106, v106\n" \
    "v_exp_f32 v107, v107\n" \
    "v_exp_f32 v108, v108\n" \
    "v_exp_f32 v109, v109\n" \
    "v_exp_f32 v110, v110\n" \
    "v_exp_f32 v111, v111\n" \
    "v_exp_f32 v80, v80\n" \
    "v_exp_f32 v81, v81\n" \
    "v_smfmac_f32_32x32x32_bf16 v[144:159], v[180:183], v[104:111], v192\n" \
    "ds_read_b128 v[176:179], v194 offset:2048\n" \
    "v_exp_f32 v82, v82\n" \
    "v_exp_f32 v83, v83\n" \
    "v_exp_f32 v84, v84\n" \
    "v_exp_f32 v85, v85\n" \
    "s_waitcnt lgkmcnt(1)\n" \
    "v_mfma_f32_32x32x16_bf16 v[64:79], v[168:171], v[160:163], 0\n" \
    "ds_read_b128 v[180:183], v194 offset:2560\n" \
    "v_exp_f32 v86, v86\n" \
    "v_exp_f32 v87, v87\n" \
    "v_exp_f32 v88, v88\n" \
    "v_exp_f32 v89, v89\n" \
    "v_smfmac_f32_32x32x32_bf16 v[128:143], v[184:187], v[80:87], v192\n" \
    "v_exp_f32 v90, v90\n" \
    "v_exp_f32 v91, v91\n" \
    "v_exp_f32 v92, v92\n" \
    "v_exp_f32 v93, v93\n" \
    "v_exp_f32 v94, v94\n" \
    "v_exp_f32 v95, v95\n" \
    "v_exp_f32 v112, v112\n" \
    "v_exp_f32 v113, v113\n" \
    "v_smfmac_f32_32x32x32_bf16 v[128:143], v[188:191], v[88:95], v192\n" \
    "v_exp_f32 v114, v114\n" \
    "v_exp_f32 v115, v115\n" \
    "v_exp_f32 v116, v116\n" \
    "v_exp_f32 v117, v117\n" \
    "v_mfma_f32_32x32x16_bf16 v[96:111], v[168:171], v[164:167], 0\n" \
    "ds_read_b128 v[172:175], v193 offset:3072\n" \
    "v_exp_f32 v118, v118\n" \
    "v_exp_f32 v119, v119\n" \
    "v_exp_f32 v120, v120\n" \
    "v_exp_f32 v121, v121\n" \
    "v_smfmac_f32_32x32x32_bf16 v[144:159], v[184:187], v[112:119], v192\n" \
    "v_exp_f32 v122, v122\n" \
    "v_exp_f32 v123, v123\n" \
    "v_exp_f32 v124, v124\n" \
    "v_exp_f32 v125, v125\n" \
    "v_exp_f32 v126, v126\n" \
    "v_exp_f32 v127, v127\n" \
    "v_exp_f32 v64, v64\n" \
    "v_exp_f32 v65, v65\n" \
    "v_smfmac_f32_32x32x32_bf16 v[144:159], v[188:191], v[120:127], v192\n" \
    "ds_read_b128 v[184:187], v194 offset:3072\n" \
    "v_exp_f32 v66, v66\n" \
    "v_exp_f32 v67, v67\n" \
    "v_exp_f32 v68, v68\n" \
    "v_exp_f32 v69, v69\n" \
    "s_waitcnt lgkmcnt(1)\n" \
    "v_mfma_f32_32x32x16_bf16 v[80:95], v[172:175], v[160:163], 0\n" \
    "ds_read_b128 v[188:191], v194 offset:3584\n" \
    "v_exp_f32 v70, v70\n" \
    "v_exp_f32 v71, v71\n" \
    "v_exp_f32 v72, v72\n" \
    "v_exp_f32 v73, v73\n" \
    "v_smfmac_f32_32x32x32_bf16 v[128:143], v[176:179], v[64:71], v192\n" \
    "v_exp_f32 v74, v74\n" \
    "v_exp_f32 v75, v75\n" \
    "v_exp_f32 v76, v76\n" \
    "v_exp_f32 v77, v77\n" \
    "v_exp_f32 v78, v78\n" \
    "v_exp_f32 v79, v79\n" \
    "v_exp_f32 v96, v96\n" \
    "v_exp_f32 v97, v97\n" \
    "v_smfmac_f32_32x32x32_bf16 v[128:143], v[180:183], v[72:79], v192\n" \
    "v_exp_f32 v98, v98\n" \
    "v_exp_f32 v99, v99\n" \
    "v_exp_f32 v100, v100\n" \
    "v_exp_f32 v101, v101\n" \
    "v_mfma_f32_32x32x16_bf16 v[112:127], v[172:175], v[164:167], 0\n" \
    "ds_read_b128 v[168:171], v193 offset:4096\n" \
    "v_exp_f32 v102, v102\n" \
    "v_exp_f32 v103, v103\n" \
    "v_exp_f32 v104, v104\n" \
    "v_exp_f32 v105, v105\n" \
    "v_smfmac_f32_32x32x32_bf16 v[144:159], v[176:179], v[96:103], v192\n" \
    "v_exp_f32 v106, v106\n" \
    "v_exp_f32 v107, v107\n" \
    "v_exp_f32 v108, v108\n" \
    "v_exp_f32 v109, v109\n" \
    "v_exp_f32 v110, v110\n" \
    "v_exp_f32 v111, v111\n" \
    "v_exp_f32 v80, v80\n" \
    "v_exp_f32 v81, v81\n" \
    "v_smfmac_f32_32x32x32_bf16 v[144:159], v[180:183], v[104:111], v192\n" \
    "ds_read_b128 v[176:179], v194 offset:4096\n" \
    "v_exp_f32 v82, v82\n" \
    "v_exp_f32 v83, v83\n" \
    "v_exp_f32 v84, v84\n" \
    "v_exp_f32 v85, v85\n" \
    "s_waitcnt lgkmcnt(1)\n" \
    "v_mfma_f32_32x32x16_bf16 v[64:79], v[168:171], v[160:163], 0\n" \
    "ds_read_b128 v[180:183], v194 offset:4608\n" \
    "v_exp_f32 v86, v86\n" \
    "v_exp_f32 v87, v87\n" \
    "v_exp_f32 v88, v88\n" \
    "v_exp_f32 v89, v89\n" \
    "v_smfmac_f32_32x32x32_bf16 v[128:143], v[184:187], v[80:87], v192\n" \
    "v_exp_f32 v90, v90\n" \
    "v_exp_f32 v91, v91\n" \
    "v_exp_f32 v92, v92\n" \
    "v_exp_f32 v93, v93\n" \
    "v_exp_f32 v94, v94\n" \
    "v_exp_f32 v95, v95\n" \
    "v_exp_f32 v112, v112\n" \
    "v_exp_f32 v113, v113\n" \
    "v_smfmac_f32_32x32x32_bf16 v[128:143], v[188:191], v[88:95], v192\n" \
    "v_exp_f32 v114, v114\n" \
    "v_exp_f32 v115, v115\n" \
    "v_exp_f32 v116, v116\n" \
    "v_exp_f32 v117, v117\n" \
    "v_mfma_f32_32x32x16_bf16 v[96:111], v[168:171], v[164:167], 0\n" \
    "ds_read_b128 v[172:175], v193 offset:5120\n" \
    "v_exp_f32 v118, v118\n" \
    "v_exp_f32 v119, v119\n" \
    "v_exp_f32 v120, v120\n" \
    "v_exp_f32 v121, v121\n" \
    "v_smfmac_f32_32x32x32_bf16 v[144:159], v[184:187], v[112:119], v192\n" \
    "v_exp_f32 v122, v122\n" \
    "v_exp_f32 v123, v123\n" \
    "v_exp_f32 v124, v124\n" \
    "v_exp_f32 v125, v125\n" \
    "v_exp_f32 v126, v126\n" \
    "v_exp_f32 v127, v127\n" \
    "s_cmp_eq_u32 %[cnt], 2\n" \
    "s_cbranch_scc0 8f\n" \
    "s_setprio 0\n" \
    "8:\n" \
    "v_add_u32 v193, 4096, v193\n" \
    "v_add_u32 v194, v195, v194\n" \
    "s_sub_u32 %[cnt], %[cnt], 1\n" \
    "s_cmp_lg_u32 %[cnt], 0\n" \
    "s_cbranch_scc1 2b\n" \
    "s_nop 1\n" \
    "v_smfmac_f32_32x32x32_bf16 v[144:159], v[188:191], v[120:127], v192\n" \
    "s_setprio 0\n" \
    "s_waitcnt vmcnt(0)\n" \
    "s_nop 7\n" \
    "s_nop 7\n" \
    "s_nop 3\n" \
    "s_waitcnt lgkmcnt(0)\n"

__device__ __forceinline__ void slow_block(const char* ws, u32x4 q, int lane, float* o, float& lsum) {
    const u32 koff = WS_K + lane * 16;
    const u32 r32 = lane & 31;
    float m = -3.0e38f;
    for (int t = 0; t < 64; ++t) {
        u32x4 kf;
        GLD128C(kf, ws, koff + t * 1024, 0);
        asm volatile("s_waitcnt vmcnt(0)" : "+v"(kf) :: "memory");
        f32x16 s = {0};
        s = __builtin_amdgcn_mfma_f32_32x32x16_bf16(asbf(kf), asbf(q), s, 0, 0, 0);
#pragma unroll
        for (int i = 0; i < 16; ++i) m = fmaxf(m, s[i]);
    }
    {
        auto sw = __builtin_amdgcn_permlane32_swap(__float_as_uint(m), __float_as_uint(m), false, false);
        m = fmaxf(__uint_as_float(sw[0]), __uint_as_float(sw[1]));
    }
    f32x16 acc = {0};
    const u32 voff = WS_V + (lane >> 5) * 256 + (lane & 15) * 16;
    for (int t = 0; t < 64; ++t) {
        u32x4 kf, v0, v1;
        GLD128C(kf, ws, koff + t * 1024, 0);
        GLD128C(v0, ws, voff + t * 1024, 0);
        GLD128C(v1, ws, voff + t * 1024, 512);
        asm volatile("s_waitcnt vmcnt(0)" : "+v"(kf), "+v"(v0), "+v"(v1) :: "memory");
        if (r32 >= 16) { v0 = (u32x4){0x3f803f80u, 0x3f803f80u, 0x3f803f80u, 0x3f803f80u}; v1 = v0; }
        f32x16 s = {0};
        s = __builtin_amdgcn_mfma_f32_32x32x16_bf16(asbf(kf), asbf(q), s, 0, 0, 0);
        u32 p[8];
#pragma unroll
        for (int i = 0; i < 8; ++i)
            p[i] = pk(__builtin_amdgcn_exp2f(s[2 * i] - m), __builtin_amdgcn_exp2f(s[2 * i + 1] - m));
#pragma unroll
        for (int st = 0; st < 2; ++st) {
            u32x4 vv = st ? v1 : v0;
            auto a = __builtin_amdgcn_permlane32_swap(vv[0], vv[2], false, false);
            auto c = __builtin_amdgcn_permlane32_swap(vv[1], vv[3], false, false);
            u32x4 af = {a[0], c[0], a[1], c[1]};
            u32x4 bf = {p[4 * st], p[4 * st + 1], p[4 * st + 2], p[4 * st + 3]};
            acc = __builtin_amdgcn_mfma_f32_32x32x16_bf16(asbf(af), asbf(bf), acc, 0, 0, 0);
        }
    }
#pragma unroll
    for (int i = 0; i < 8; ++i) o[i] = acc[i];
    lsum = acc[8];
}

#define PRIO_MODE 1
#define PRIO_START() do { if (w >= 4) __builtin_amdgcn_s_setprio(1); } while (0)
#define PRIO_END() __builtin_amdgcn_s_setprio(0)
__global__ void __launch_bounds__(512, 2)
fused_kernel(const u16* x_, const u16* wq_, const u16* wk_, const u16* wv_, const u16* wo_, const u16* wr_, const u16* wg_,
             const u16* wu_, const u16* wd_, const u16* gw_, u16* out_, char* ws_)
{
    u32x16 ka0; u32x8 ka1;
    {
        const void* kp = (const void*)__builtin_amdgcn_kernarg_segment_ptr();
        asm volatile("s_load_dwordx16 %0, %2, 0\n\ts_load_dwordx8 %1, %2, 64\n\ts_waitcnt lgkmcnt(0)"
                     : "=&s"(ka0), "=&s"(ka1) : "s"(kp) : "memory");
    }
#define KP(v, i) ((u64)v[2 * (i)] | ((u64)v[2 * (i) + 1] << 32))
    const u16* x  = (const u16*)KP(ka0, 0); const u16* wq = (const u16*)KP(ka0, 1);
    const u16* wk = (const u16*)KP(ka0, 2); const u16* wv = (const u16*)KP(ka0, 3);
    const u16* wo = (const u16*)KP(ka0, 4); const u16* wr = (const u16*)KP(ka0, 5);
    const u16* wg = (const u16*)KP(ka0, 6); const u16* wu = (const u16*)KP(ka0, 7);
    const u16* wd = (const u16*)KP(ka1, 0); const u16* gw = (const u16*)KP(ka1, 1);
    u16* out = (u16*)KP(ka1, 2); char* ws = (char*)KP(ka1, 3);

    __shared__ __attribute__((aligned(1024))) char smem[L_END];
    LDSP char* lds = (LDSP char*)smem;
    const u32 lb = (u32)(size_t)lds;
    const int tid = threadIdx.x;
    const int lane = tid & 63;
    const int w = __builtin_amdgcn_readfirstlane(tid >> 6);
    const int b = blockIdx.x;
    const int xcd = b & 7, jj = b >> 3;
    const int tb = xcd * 4 + (jj >> 3), hg = jj & 7;
    const int g = lane >> 4, l15 = lane & 15, h = lane >> 5, l31 = lane & 31;
    const bool isK = hg < 4;
    const bool ffn = jj < 28;
    const int fj = xcd * 28 + min(jj, 27);
    STAMP(1); STAMPC(18); if (tid == 0 && (jj == 0 || jj == 31)) { u32 t_ = (u32)__builtin_amdgcn_s_memrealtime(); u32 o_ = WS_DBG + ((jj ? 8 : 0) + xcd) * 4; GST32C(ws, o_, t_); }

    const int th = w & 1, hp = (w >> 1) & 1, blk = w >> 2;
    const int T0 = tb * 64 + (hg & 3) * 16;
    u32x4 xf0, xf1, vf0, vf1;
    {
        const u16* wsrc = isK ? wk : wv;
#pragma unroll
        for (int i = 0; i < 2; ++i) {
            u32 slot = 64 * i + lane, pos = slot >> 3, B = pos ^ ((pos >> 2) & 1);
            u32 row = 4 * B + ((slot >> 1) & 3);
            dma16(wsrc, (64 * w + row) * 32 + (slot & 1) * 16, lb + L_W + w * 2048 + i * 1024);
        }
        u32 xo = (T0 + l15) * 1024 + (64 * w + 16 * g) * 2;
        GLD128(xf0, x, xo, 0);
        GLD128(xf1, x, xo, 16);
        const u16* vec = isK ? wr : gw;
        u32 vo = (64 * w + 16 * g) * 2;
        GLD128(vf0, vec, vo, 0);
        GLD128(vf1, vec, vo, 16);
    }
    u32 epo;
    { u32 z = 0; GLD32C(epo, ws, z, 0); }
    asm volatile("s_barrier" ::: "memory");
    if (w < 7) {
#pragma unroll
        for (int i = 0; i < 19; ++i) {
            const int p = i < 18 ? w + 7 * i : __builtin_amdgcn_readfirstlane(min(w + 7 * i, 127));
            if (i < 9 || (i == 9 && w == 0)) {
                dma16(x, (tb * 64 + p) * 1024 + ((lane ^ (p & 15)) << 4), lb + L_K + p * 1024);
            } else {
                const int G = p - 64;
                dma16(wq, (8 * G + ((lane >> 2) & 7)) * 1024 + hg * 128 + (lane >> 5) * 64 + (lane & 3) * 16, lb + L_V + G * 1024);
            }
        }
    }
    if (tid < 256) { u32 o1 = (tid & 15) == 0 ? 0x3f803f80u : 0u; ST128(L_ONE + tid * 16, ((u32x4){o1, o1, o1, o1})); }
    asm volatile("s_cmp_eq_u32 %[w], 7\n\ts_cbranch_scc1 1f\n\ts_waitcnt vmcnt(20)\n\ts_branch 2f\n1:\n\ts_waitcnt vmcnt(1)\n2:"
                 : "+v"(xf0), "+v"(xf1), "+v"(vf0), "+v"(vf1) : [w] "s"(w) : "memory", "scc");
    FSTAMP(2);
    {
        const u32 ta = L_W + w * 2048 + l15 * 8;
        const u32 gx = g & 1;
        u32x4 wf0 = cat(trread(lds, ta + ((4 * g + 0) ^ gx) * 128), trread(lds, ta + ((4 * g + 1) ^ gx) * 128));
        u32x4 wf1 = cat(trread(lds, ta + ((4 * g + 2) ^ gx) * 128), trread(lds, ta + ((4 * g + 3) ^ gx) * 128));
        f32x4 acc = {0, 0, 0, 0}, dacc = {0, 0, 0, 0};
        if (isK) {
            acc = __builtin_amdgcn_mfma_f32_16x16x32_bf16(asbf(wf0), asbf(xf0), acc, 0, 0, 0);
            acc = __builtin_amdgcn_mfma_f32_16x16x32_bf16(asbf(wf1), asbf(xf1), acc, 0, 0, 0);
        } else {
            acc = __builtin_amdgcn_mfma_f32_16x16x32_bf16(asbf(xf0), asbf(wf0), acc, 0, 0, 0);
            acc = __builtin_amdgcn_mfma_f32_16x16x32_bf16(asbf(xf1), asbf(wf1), acc, 0, 0, 0);
        }
        dacc = __builtin_amdgcn_mfma_f32_16x16x32_bf16(asbf(vf0), asbf(xf0), dacc, 0, 0, 0);
        dacc = __builtin_amdgcn_mfma_f32_16x16x32_bf16(asbf(vf1), asbf(xf1), dacc, 0, 0, 0);
        ST128(L_P1 + w * 1024 + lane * 16, __builtin_bit_cast(u32x4, acc));
        if (g == 0) STF(L_PD + w * 64 + l15 * 4, dacc[0]);
    }
    BAR();
    FSTAMP(3);
    asm volatile("s_cmp_eq_u32 %[w], 7\n\ts_cbranch_scc1 1f\n\ts_waitcnt vmcnt(19)\n\ts_branch 2f\n1:\n\ts_waitcnt vmcnt(0)\n2:"
                 : "+v"(epo) : [w] "s"(w) : "memory", "scc");
    const u32 epoch = (u32)__builtin_amdgcn_readfirstlane((int)epo) + 1u;
    if (w == 7) {
        f32x4 ps[8]; float pd[8];
#pragma unroll
        for (int i = 0; i < 8; ++i) {
            ps[i] = __builtin_bit_cast(f32x4, LD128(L_P1 + i * 1024 + lane * 16));
            pd[i] = LDF(L_PD + i * 64 + l15 * 4);
        }
        __builtin_amdgcn_sched_barrier(0);
        const f32x4 s = ((ps[0] + ps[1]) + (ps[2] + ps[3])) + ((ps[4] + ps[5]) + (ps[6] + ps[7]));
        const float d = ((pd[0] + pd[1]) + (pd[2] + pd[3])) + ((pd[4] + pd[5]) + (pd[6] + pd[7]));
        u32x2 pv = {pk(s[0], s[1]), pk(s[2], s[3])};
        const int tile = T0 >> 5, sub = (T0 >> 4) & 1;
        u32 key = 0;
        if (isK) {
            u32 off = WS_K + tile * 1024 + ((g & 1) * 32 + sub * 16 + l15) * 16 + (g >> 1) * 8;
            GST64C(ws, off, pv);
            u32 rb = pk(d, d) & 0xffffu;
            u32 ord = (rb & 0x8000u) ? (~rb & 0xffffu) : (rb | 0x8000u);
            key = wave_max_u32((ord << 16) | (0xffffu - (u32)(T0 + l15)));
        } else {
            u32 off = WS_V + tile * 1024 + sub * 512 + (g >> 1) * 256 + l15 * 16 + (g & 1) * 8;
            GST64C(ws, off, pv);
            if (g == 0) { u32 go = WS_G + (T0 + l15) * 4; u32 dv = __float_as_uint(d); GST32C(ws, go, dv); }
        }
        VMWAIT(0);
        if (lane < 8) { u32x2 fl = {key, epoch}; u32 fo = WS_F1 + lane * 32768 + (tile * 4 + (isK ? 0 : 2) + sub) * 128; GST64C(ws, fo, fl); }
        FSTAMP(4);
    } else {
        VMWAIT(0);
    }
    BAR();
    FSTAMP(5);

    f32x16 qacc = {0};
    if (w < 4) {
        const int qh = w >> 1;
        const int tok = th * 32 + l31;
        u32 abase = L_V + h * 1024 + qh * 512 + ((l15 >> 2) * 64) + ((lane >> 4) & 1) * 32 + (l15 & 3) * 8;
        asm volatile("" : "+v"(abase));
        u32 bb[8];
#pragma unroll
        for (int j = 0; j < 8; ++j) { bb[j] = L_K + tok * 1024 + (((2 * j + h) ^ (tok & 15)) << 4); asm volatile("" : "+v"(bb[j])); }
#pragma unroll
        for (int s = 0; s < 32; ++s) {
            u32x4 af = cat(trread(lds, abase + s * 2048), trread(lds, abase + s * 2048 + 256));
            u32x4 bf = LD128(bb[s & 7] + (s >> 3) * 256);
            qacc = __builtin_amdgcn_mfma_f32_32x32x16_bf16(asbf(af), asbf(bf), qacc, 0, 0, 0);
        }
        FSTAMP(6);
    } else {
        const int j = w - 4;
        const u32 fo = WS_F1 + xcd * 32768 + (j * 64 + lane) * 128;
        u32x2 f;
        u32 spins = 0;
        do {
            asm volatile("global_load_dwordx2 %0, %1, %2 sc1\n\ts_waitcnt vmcnt(0)" : "=&v"(f) : "v"(fo), "s"(ws) : "memory");
        } while ((f[1] != epoch) && ++spins <= SPIN_MAX);
        const u32 key = wave_max_u32(f[0]);
        if (lane == 0) ST32(L_CT + j * 4, key);
        FSTAMP(7);
    }
    BAR();
    FSTAMP(9);
    u32 kvo = (w & 1) * 65536 + ((w >> 1) & 1) * 32768;
    if (w >= 4) {
        dma16c4(ws + WS_K, kvo + lane * 16, lb + L_K + kvo);
        dma16c4(ws + WS_K, kvo + 4096 + lane * 16, lb + L_K + kvo + 4096);
        dma16c2(ws + WS_K, kvo + 8192 + lane * 16, lb + L_K + kvo + 8192);
    } else {
        const float qs = 0.25f * 1.4426950408889634f;
        u32x4 qx, qy;
#pragma unroll
        for (int i = 0; i < 4; ++i) {
            u32 a = pk(qacc[2 * i], qacc[2 * i + 1]), c = pk(qacc[8 + 2 * i], qacc[8 + 2 * i + 1]);
            qx[i] = pk(lo16(a) * qs, hi16(a) * qs);
            qy[i] = pk(lo16(c) * qs, hi16(c) * qs);
        }
        ST128(L_P1 + w * 2048 + lane * 16, qx);
        ST128(L_P1 + w * 2048 + 1024 + lane * 16, qy);
    }
    const u32 key = max(max(LD32(L_CT), LD32(L_CT + 4)), max(LD32(L_CT + 8), LD32(L_CT + 12)));
    const int sel = __builtin_amdgcn_readfirstlane(0xffff - (key & 0xffff)) & 2047;
    u32x8 EXA; u32x4 EXB, WOA;
    u32 skv = lb + L_K + kvo + (10 + 4 * (w >> 2)) * 1024, sg = lb + L_W + w * 2048;
    const u32 skv3 = lb + L_K + kvo + (26 + 2 * (w >> 2)) * 1024;
    u32 swo = lb + ((w & 4) ? L_V : L_K) + ((w >> 1) & 1) * 32768 + (w & 1) * 8192;
    {
        EXA[0] = kvo + (10 + 4 * (w >> 2)) * 1024 + lane * 16;
        EXA[1] = kvo + (26 + 2 * (w >> 2)) * 1024 + lane * 16;
        const u32 pos = lane >> 3, B = pos ^ ((pos >> 2) & 1);
        const u32 row = 64 * w + 4 * B + ((lane >> 1) & 3);
        const u64 g0 = (u64)((lane & 1) ? wu : wg) + (u64)(row * 3584 + fj * 16), g1 = g0 + 32 * 3584;
        EXA[2] = (u32)g0; EXA[3] = (u32)(g0 >> 32); EXA[4] = (u32)g1; EXA[5] = (u32)(g1 >> 32);
        EXA[6] = sel * 1024 + (64 * w + 16 * g) * 2;
        EXA[7] = fj * 8192 + (128 * (w & 3) + 2 * lane) * 2;
        const u32 ta = lb + L_W + w * 2048 + l15 * 8, gx = g & 1;
        EXB[0] = ta + (4 * g + gx) * 128;
        EXB[1] = ta + (4 * g + (gx ^ 1)) * 128;
        EXB[2] = lb + (l15 == 0 ? L_FP + w * 64 + g * 16 : L_P1 + w * 1024 + lane * 16);
        EXB[3] = 0;
#pragma unroll
        for (int i = 0; i < 4; ++i) {
            const u32 r = 64 * w + 8 * i + (lane >> 3), slot = lane & 7;
            const u32 f = ((r >> 1) & 1) | ((i & 1) << 1);
            const u32 c = (((slot >> 1) ^ f) << 1) | (slot & 1);
            WOA[i] = r * 1024 + hg * 128 + c * 16 - i * 1024;
        }
    }
    u32x2 PZA;
    {
        const int head = hg * 4 + hp * 2 + blk, tl = th * 32 + l31;
        PZA[0] = WS_O + ((tb * 4 + (tl >> 4)) * 16 + (head >> 1)) * 1024 + (((head & 1) * 2 + h) * 16 + (tl & 15)) * 16;
        PZA[1] = (ffn && w < 4) ? WS_Y + fj * 2048 + w * 512 + l31 * 16 : PZA[0];
    }
    const int hf = w >> 2;
    u32 ka = lb + L_K + hf * 32768 + lane * 16;
    const bool vlane = l31 < 16;
    u32 va = vlane ? (lb + L_V + hf * 32768 + h * 256 + l15 * 16) : (lb + L_ONE - 1024 + h * 256 + l15 * 16);
    u32 vinc = vlane ? 4096u : 0u;
    u32 va0 = vlane ? va : va + 1024;
    u32 qa = lb + L_P1 + (w & 3) * 2048 + lane * 16;
    asm volatile("" : "+{v[196:203]}"(EXA), "+{v[228:231]}"(EXB), "+{v[236:239]}"(WOA), "+{v[220:221]}"(PZA), "+{v193}"(ka), "+{v194}"(va), "+{v195}"(vinc), "+v"(va0), "+v"(qa),
                 "+s"(skv), "+s"(sg), "+s"(swo));
    if (w >= 4) VMWAIT(0);
    BAR();
    STAMP(10); STAMPC(19);
    if (b == 0 && tid == 256) { u32 z = 0; GST32C(ws, z, epoch); }

    u32x4 qx, qy;
    f32x16 SX0, SX1, SY0, SY1, OX, OY;
    u32x4 K0, K1, V00, V01, V10, V11;
    u32 idx = 0xDDDDu;
    {
        asm volatile(
            "ds_read_b128 v[160:163], %[qa]\n"
            "ds_read_b128 v[164:167], %[qa] offset:1024\n"
            "ds_read_b128 v[168:171], v193\n"
            "ds_read_b128 v[172:175], v193 offset:1024\n"
            "ds_read_b128 v[176:179], %[va0]\n"
            "ds_read_b128 v[180:183], %[va0] offset:512\n"
            "v_mov_b32 v128, 0\n v_mov_b32 v129, 0\n v_mov_b32 v130, 0\n v_mov_b32 v131, 0\n"
            "v_mov_b32 v132, 0\n v_mov_b32 v133, 0\n v_mov_b32 v134, 0\n v_mov_b32 v135, 0\n"
            "v_mov_b32 v136, 0\n v_mov_b32 v137, 0\n v_mov_b32 v138, 0\n v_mov_b32 v139, 0\n"
            "v_mov_b32 v140, 0\n v_mov_b32 v141, 0\n v_mov_b32 v142, 0\n v_mov_b32 v143, 0\n"
            "v_mov_b32 v144, 0\n v_mov_b32 v145, 0\n v_mov_b32 v146, 0\n v_mov_b32 v147, 0\n"
            "v_mov_b32 v148, 0\n v_mov_b32 v149, 0\n v_mov_b32 v150, 0\n v_mov_b32 v151, 0\n"
            "v_mov_b32 v152, 0\n v_mov_b32 v153, 0\n v_mov_b32 v154, 0\n v_mov_b32 v155, 0\n"
            "v_mov_b32 v156, 0\n v_mov_b32 v157, 0\n v_mov_b32 v158, 0\n v_mov_b32 v159, 0\n"
            "v_mov_b32 v120, 0\n v_mov_b32 v121, 0\n v_mov_b32 v122, 0\n v_mov_b32 v123, 0\n"
            "v_mov_b32 v124, 0\n v_mov_b32 v125, 0\n v_mov_b32 v126, 0\n v_mov_b32 v127, 0\n"
            "v_mov_b32 v188, 0\n v_mov_b32 v189, 0\n v_mov_b32 v190, 0\n v_mov_b32 v191, 0\n"
            "s_waitcnt lgkmcnt(0)\n"
            "v_mfma_f32_32x32x16_bf16 v[64:79], v[168:171], v[160:163], 0\n"
            "v_mfma_f32_32x32x16_bf16 v[96:111], v[168:171], v[164:167], 0\n"
            "s_nop 7\n s_nop 7\n"
            : "={v[64:79]}"(SX0), "={v[80:95]}"(SX1), "={v[96:111]}"(SY0), "={v[112:127]}"(SY1),
              "={v[128:143]}"(OX), "={v[144:159]}"(OY),
              "={v[168:171]}"(K0), "={v[172:175]}"(K1), "={v[176:179]}"(V00), "={v[180:183]}"(V01),
              "={v[184:187]}"(V10), "={v[188:191]}"(V11), "=&{v[160:163]}"(qx), "=&{v[164:167]}"(qy)
            : "{v193}"(ka), [va0] "v"(va0), [qa] "v"(qa)
            : "memory");
    }
#define ATT_OPS \
            : "+{v[64:79]}"(SX0), "+{v[80:95]}"(SX1), "+{v[96:111]}"(SY0), "+{v[112:127]}"(SY1), \
              "+{v[128:143]}"(OX), "+{v[144:159]}"(OY), \
              "+{v[168:171]}"(K0), "+{v[172:175]}"(K1), "+{v[176:179]}"(V00), "+{v[180:183]}"(V01), \
              "+{v[184:187]}"(V10), "+{v[188:191]}"(V11), "+{v193}"(ka), "+{v194}"(va), [cnt] "=&s"(cnt_)
#define ATT_INS : "{v[160:163]}"(qx), "{v[164:167]}"(qy), "{v192}"(idx), "{v195}"(vinc)
    u32 cnt_;
    const char* kvb = ws + WS_K;
    PRIO_START();
    asm volatile(ATT_A ATT_OPS, "+{v[196:203]}"(EXA), "+{v[228:231]}"(EXB), "+{v[236:239]}"(WOA), "+{v[220:221]}"(PZA), [skv] "+s"(skv), [sg] "+s"(sg)
                 ATT_INS, [kvb] "s"(kvb), [wsb] "s"(ws) : "memory", "scc", "v232", "v233", "v234", "v235");
    LSTAMP(4);
    BAR();
    LSTAMP(8);
    if (tid == 448) { u32 fo = WS_F2 + b * 4; GST32C(ws, fo, epoch); }
    PRIO_START();
    u32x16 EXD;
    asm volatile(ATT_B ATT_OPS, "+{v[196:203]}"(EXA), "=&{v[204:219]}"(EXD), "+{v[236:239]}"(WOA)
                 ATT_INS, "{v[228:231]}"(EXB), [kvb] "s"(kvb), [skv3] "s"(skv3), [xb] "s"(x), [wdb] "s"(wd)
                 : "memory", "scc", "v220", "v221", "v222", "v223", "v224", "v225", "v226", "v227", "v232", "v233", "v234", "v235");
    LSTAMP(9);
    BAR();
    LSTAMP(11);
    if (ffn && w < 4) {
        float sum = LDF(L_FP + l15 * 4);
#pragma unroll
        for (int i = 1; i < 8; ++i) sum += LDF(L_FP + i * 64 + l15 * 4);
        sum = bfr(sum);
        float up = __shfl(sum, (lane & 7) + 8);
        float gt = __shfl(sum, lane & 7);
        float hv = bfr(bfr(gt * sigm(gt)) * up);
        u32 hb = __float_as_uint(hv);
        float y0 = 0.f, y1 = 0.f;
#pragma unroll
        for (int i = 0; i < 8; ++i) {
            const float hs = __uint_as_float((u32)__builtin_amdgcn_readlane(hb, i));
            y0 += hs * lo16(EXD[8 + i]);
            y1 += hs * hi16(EXD[8 + i]);
        }
        u32x2 gr = {__float_as_uint(y0), __float_as_uint(y1)};
        gr[0] = gr[0] == POISON ? 0x7fc00000u : gr[0];
        gr[1] = gr[1] == POISON ? 0x7fc00000u : gr[1];
        u32 yo = WS_Y + fj * 2048 + (128 * w + 2 * lane) * 4;
        GST64C(ws, yo, gr);
    }
    LSTAMP(22);
    PRIO_START();
    u32 f2v; const u32 f2a = WS_F2 + (32 * w + l31) * 4;
    asm volatile(ATT_C ATT_OPS, "+{v[236:239]}"(WOA), "=&{v220}"(f2v), [swo] "+s"(swo) ATT_INS, "{v221}"(f2a), [wob] "s"(wo), [wsb] "s"(ws) : "memory", "scc");
    const int tq = w & 3, kh = w >> 2;
    const bool owner = (sel >> 6) == tb;
    int lane_b = lane;
    asm volatile("" : "+v"(lane_b));
    {
    const int lane = lane_b;
    const int g = lane >> 4, l15 = lane & 15, h = lane >> 5, l31 = lane & 31;
    const int tokg = tb * 64 + tq * 16 + l15;
    const int ncol = hg * 64 + kh * 32 + (g & 1) * 16 + (g >> 1) * 8;
    u32 oslot;
    {
        const int head = hg * 4 + hp * 2 + blk, tl = th * 32 + l31;
        oslot = WS_O + ((tb * 4 + (tl >> 4)) * 16 + (head >> 1)) * 1024 + (((head & 1) * 2 + h) * 16 + (tl & 15)) * 16;
    }
    STAMP(12); STAMPC(20);
    {
        u32 spins = 0;
        while (!__all(f2v == epoch) && ++spins <= SPIN_MAX) {
            const u32 a = WS_F2 + (32 * w + l31) * 4;
            asm volatile("global_load_dword %0, %1, %2 sc1\n\ts_waitcnt vmcnt(0)" : "=&v"(f2v) : "v"(a), "s"(ws) : "memory");
        }
    }
    u32x4 xe, Y0, Y1, Y2, Y3, Y4, Y5, Y6; u32 gl;
    const u32 yo0 = WS_Y + (28 * w + g) * 2048 + hg * 256 + l15 * 16;
    {
        u32 xo = tokg * 1024 + ncol * 2;
        GLD128(xe, x, xo, 0);
        u32 go = WS_G + tokg * 4;
        GLD32C(gl, ws, go, 0);
    }

    {
        f32x16& mine = blk ? OY : OX;
        f32x16& give = blk ? OX : OY;
        const u32 cw = L_W + w * 2304, cr = L_W + (w ^ 4) * 2304;
        ST128(cw + lane * 16, ((u32x4){__float_as_uint(give[0]), __float_as_uint(give[1]), __float_as_uint(give[2]), __float_as_uint(give[3])}));
        ST128(cw + 1024 + lane * 16, ((u32x4){__float_as_uint(give[4]), __float_as_uint(give[5]), __float_as_uint(give[6]), __float_as_uint(give[7])}));
        STF(cw + 2048 + lane * 4, give[8]);
        BAR();
        f32x4 p0 = __builtin_bit_cast(f32x4, LD128(cr + lane * 16));
        f32x4 p1 = __builtin_bit_cast(f32x4, LD128(cr + 1024 + lane * 16));
        float l = LDF(cr + 2048 + lane * 4) + mine[8];
        float o[8];
#pragma unroll
        for (int i = 0; i < 4; ++i) { o[i] = mine[i] + p0[i]; o[4 + i] = mine[4 + i] + p1[i]; }
        {
            auto sw = __builtin_amdgcn_permlane32_swap(__float_as_uint(l), __float_as_uint(l), false, false);
            l = __uint_as_float(sw[0]);
        }
        float amax = fmaxf(fmaxf(fmaxf(fabsf(o[0]), fabsf(o[1])), fmaxf(fabsf(o[2]), fabsf(o[3]))),
                           fmaxf(fmaxf(fabsf(o[4]), fabsf(o[5])), fmaxf(fabsf(o[6]), fabsf(o[7]))));
        bool safe = (l >= 8.6736174e-19f) && (l <= 1.2676506e30f) && (amax <= 3.0e38f);
        if (!__all(safe)) {
            slow_block(ws, blk ? qy : qx, lane, o, l);
            auto sw = __builtin_amdgcn_permlane32_swap(__float_as_uint(l), __float_as_uint(l), false, false);
            l = __uint_as_float(sw[0]);
        }
        const float ri = __builtin_amdgcn_rcpf(l);
        u32 A0 = pk(o[0] * ri, o[1] * ri), A1 = pk(o[2] * ri, o[3] * ri);
        u32 B0 = pk(o[4] * ri, o[5] * ri), B1 = pk(o[6] * ri, o[7] * ri);
        auto s0 = __builtin_amdgcn_permlane32_swap(A0, B0, false, false);
        auto s1 = __builtin_amdgcn_permlane32_swap(A1, B1, false, false);
        u32x4 ov = {s0[0], s1[0], s0[1], s1[1]};
#pragma unroll
        for (int i = 0; i < 4; ++i) ov[i] = ov[i] == POISON ? 0x7fc07fc0u : ov[i];
        GST128C(ws, oslot, ov);
    }
    {
        u32 yo = yo0;
        asm volatile("s_cmp_lg_u32 %[stb], %[tb]\n\ts_cbranch_scc1 3f\n"
                     "global_load_dwordx4 %0, %[yo], %[ws] sc1\n v_add_u32 %[yo], 0x2000, %[yo]\n"
                     "global_load_dwordx4 %1, %[yo], %[ws] sc1\n v_add_u32 %[yo], 0x2000, %[yo]\n"
                     "global_load_dwordx4 %2, %[yo], %[ws] sc1\n v_add_u32 %[yo], 0x2000, %[yo]\n"
                     "global_load_dwordx4 %3, %[yo], %[ws] sc1\n v_add_u32 %[yo], 0x2000, %[yo]\n"
                     "global_load_dwordx4 %4, %[yo], %[ws] sc1\n v_add_u32 %[yo], 0x2000, %[yo]\n"
                     "global_load_dwordx4 %5, %[yo], %[ws] sc1\n v_add_u32 %[yo], 0x2000, %[yo]\n"
                     "global_load_dwordx4 %6, %[yo], %[ws] sc1\n3:"
                     : "=&v"(Y0), "=&v"(Y1), "=&v"(Y2), "=&v"(Y3), "=&v"(Y4), "=&v"(Y5), "=&v"(Y6), [yo] "+v"(yo)
                     : [stb] "s"(sel >> 6), [tb] "s"(tb), [ws] "s"(ws) : "memory", "scc");
    }
    STAMP(13);

    u32x4 af[4][8];
    {
        const u32 f = ((l15 >> 3) & 1) | ((g & 1) << 1);
        const u32 rb = (kh ? L_V : L_K) + (8 * g + (l15 >> 2)) * 128 + (l15 & 3) * 8;
#pragma unroll
        for (int nb = 0; nb < 4; ++nb) {
            u32 a = rb + ((nb ^ f) * 32);
            asm volatile("" : "+v"(a));
#pragma unroll
            for (int st = 0; st < 8; ++st) {
                const u32 o_ = (st >> 2) * 32768 + (st & 3) * 4096;
                af[nb][st] = cat(trread(lds, a + o_), trread(lds, a + o_ + 512));
            }
        }
    }
    STAMP(14);
    asm volatile("s_cmp_lg_u32 %[stb], %[tb]\n\ts_cbranch_scc1 3f\n\ts_waitcnt vmcnt(0)\n3:\n\ts_waitcnt vmcnt(1)"
                 : "+v"(xe), "+v"(gl), "+v"(Y0), "+v"(Y1), "+v"(Y2), "+v"(Y3), "+v"(Y4), "+v"(Y5), "+v"(Y6)
                 : [stb] "s"(sel >> 6), [tb] "s"(tb) : "memory", "scc");
    if (owner) {
        u32 spins = 0;
#define YMX(v) max(max(v[0], v[1]), max(v[2], v[3]))
        while (!__all(max(max(max(YMX(Y0), YMX(Y1)), max(YMX(Y2), YMX(Y3))), max(max(YMX(Y4), YMX(Y5)), YMX(Y6))) != POISON) && ++spins <= SPIN_MAX) {
            u32 yo = yo0;
            asm volatile("global_load_dwordx4 %0, %[yo], %[ws] sc1\n v_add_u32 %[yo], 0x2000, %[yo]\n"
                         "global_load_dwordx4 %1, %[yo], %[ws] sc1\n v_add_u32 %[yo], 0x2000, %[yo]\n"
                         "global_load_dwordx4 %2, %[yo], %[ws] sc1\n v_add_u32 %[yo], 0x2000, %[yo]\n"
                         "global_load_dwordx4 %3, %[yo], %[ws] sc1\n v_add_u32 %[yo], 0x2000, %[yo]\n"
                         "global_load_dwordx4 %4, %[yo], %[ws] sc1\n v_add_u32 %[yo], 0x2000, %[yo]\n"
                         "global_load_dwordx4 %5, %[yo], %[ws] sc1\n v_add_u32 %[yo], 0x2000, %[yo]\n"
                         "global_load_dwordx4 %6, %[yo], %[ws] sc1\n s_waitcnt vmcnt(0)"
                         : "=&v"(Y0), "=&v"(Y1), "=&v"(Y2), "=&v"(Y3), "=&v"(Y4), "=&v"(Y5), "=&v"(Y6), [yo] "+v"(yo)
                         : [ws] "s"(ws) : "memory");
        }
        f32x4 ys;
#pragma unroll
        for (int i = 0; i < 4; ++i) {
            float v = ((__uint_as_float(Y0[i]) + __uint_as_float(Y1[i])) + (__uint_as_float(Y2[i]) + __uint_as_float(Y3[i])))
                    + ((__uint_as_float(Y4[i]) + __uint_as_float(Y5[i])) + __uint_as_float(Y6[i]));
            auto a = __builtin_amdgcn_permlane32_swap(__float_as_uint(v), __float_as_uint(v), false, false);
            v = __uint_as_float(a[0]) + __uint_as_float(a[1]);
            auto c = __builtin_amdgcn_permlane16_swap(__float_as_uint(v), __float_as_uint(v), false, false);
            ys[i] = __uint_as_float(c[0]) + __uint_as_float(c[1]);
        }
        if (g == 0) ST128(L_YP + w * 256 + l15 * 16, __builtin_bit_cast(u32x4, ys));
    }
    u32x4 of0, of1, of2, of3, of4, of5, of6, of7;
    {
        const u32 oo = WS_O + ((tb * 4 + tq) * 16 + 8 * kh) * 1024 + lane * 16, o2 = oo + 4096;
        u32 spins = 0;
        while (true) {
            asm volatile("global_load_dwordx4 %0, %8, %10 sc1\n\tglobal_load_dwordx4 %1, %8, %10 offset:1024 sc1\n\t"
                         "global_load_dwordx4 %2, %8, %10 offset:2048 sc1\n\tglobal_load_dwordx4 %3, %8, %10 offset:3072 sc1\n\t"
                         "global_load_dwordx4 %4, %9, %10 sc1\n\tglobal_load_dwordx4 %5, %9, %10 offset:1024 sc1\n\t"
                         "global_load_dwordx4 %6, %9, %10 offset:2048 sc1\n\tglobal_load_dwordx4 %7, %9, %10 offset:3072 sc1\n\t"
                         "s_waitcnt vmcnt(0)"
                         : "=&v"(of0), "=&v"(of1), "=&v"(of2), "=&v"(of3), "=&v"(of4), "=&v"(of5), "=&v"(of6), "=&v"(of7)
                         : "v"(oo), "v"(o2), "s"(ws) : "memory");
            u32 mx = 0;
#define OMX(v) mx = max(max(mx, max(v[0], v[1])), max(v[2], v[3]));
            OMX(of0) OMX(of1) OMX(of2) OMX(of3) OMX(of4) OMX(of5) OMX(of6) OMX(of7)
            if (__all(mx != POISON) || ++spins > SPIN_MAX) break;
        }
    }
    STAMP(15);
    f32x4 acc[4] = {{0, 0, 0, 0}, {0, 0, 0, 0}, {0, 0, 0, 0}, {0, 0, 0, 0}};
#define OSTEP(OF, ST) \
    _Pragma("unroll") for (int nb = 0; nb < 4; ++nb) acc[nb] = __builtin_amdgcn_mfma_f32_16x16x32_bf16(asbf(af[nb][ST]), asbf(OF), acc[nb], 0, 0, 0);
    OSTEP(of0, 0) OSTEP(of1, 1) OSTEP(of2, 2) OSTEP(of3, 3) OSTEP(of4, 4) OSTEP(of5, 5) OSTEP(of6, 6) OSTEP(of7, 7)
    STAMP(16);
    {
        f32x4 k0 = kh ? acc[2] : acc[0], k1 = kh ? acc[3] : acc[1];
        f32x4 g0 = kh ? acc[0] : acc[2], g1 = kh ? acc[1] : acc[3];
        ST128(L_K + 16384 + w * 2048 + lane * 16, __builtin_bit_cast(u32x4, g0));
        ST128(L_K + 16384 + w * 2048 + 1024 + lane * 16, __builtin_bit_cast(u32x4, g1));
        BAR();
        k0 += __builtin_bit_cast(f32x4, LD128(L_K + 16384 + (w ^ 4) * 2048 + lane * 16));
        k1 += __builtin_bit_cast(f32x4, LD128(L_K + 16384 + (w ^ 4) * 2048 + 1024 + lane * 16));
        float a[8];
#pragma unroll
        for (int i = 0; i < 4; ++i) {
            auto sw = __builtin_amdgcn_permlane16_swap(__float_as_uint(k0[i]), __float_as_uint(k1[i]), false, false);
            a[i] = __uint_as_float(sw[0]); a[4 + i] = __uint_as_float(sw[1]);
        }
        const float gt = bfr(sigm(bfr(__uint_as_float(gl))));
        const float og = bfr(1.0f - gt);
        float bb[8];
#pragma unroll
        for (int i = 0; i < 4; ++i) { bb[2 * i] = lo16(xe[i]); bb[2 * i + 1] = hi16(xe[i]); }
        if (owner && tokg == sel) {
            const u32 rord = key >> 16;
            const float rsel = __uint_as_float(((rord & 0x8000u) ? (rord & 0x7fffu) : (~rord & 0xffffu)) << 16);
            const float sg = bfr(sigm(rsel));
            const int c0 = kh * 32 + (g & 1) * 16 + (g >> 1) * 8;
#pragma unroll
            for (int i = 0; i < 8; ++i) {
                float y = 0.f;
#pragma unroll
                for (int p = 0; p < 8; ++p) y += LDF(L_YP + p * 256 + (c0 + i) * 4);
                bb[i] = bfr(bb[i] + bfr(bfr(y) * sg));
            }
        }
        u32x4 ov;
#pragma unroll
        for (int i = 0; i < 4; ++i)
            ov[i] = pk(bfr(gt * bfr(a[2 * i])) + bfr(og * bb[2 * i]), bfr(gt * bfr(a[2 * i + 1])) + bfr(og * bb[2 * i + 1]));
        u32 oo_ = tokg * 1024 + ncol * 2;
        GST128(out, oo_, ov);
    }
    STAMP(17); STAMPC(21);
    BAR();
    if (tid == 0 && b == 0) {
        u32 t[16]; u32 mn = 0xffffffffu;
        for (int i = 0; i < 16; ++i) { u32 o_ = WS_DBG + i * 4; u32 v_; asm volatile("global_load_dword %0, %1, %2 sc1\n\ts_waitcnt vmcnt(0)" : "=&v"(v_) : "v"(o_), "s"(ws) : "memory"); t[i] = v_; }
        u32 t00 = t[1];
        u32 te = (u32)__builtin_amdgcn_s_memrealtime();
        printf("SKEW first %d %d %d %d %d %d %d %d last %d %d %d %d %d %d %d %d end0 %d\n", (int)(t[0]-t00), (int)(t[1]-t00), (int)(t[2]-t00), (int)(t[3]-t00), (int)(t[4]-t00), (int)(t[5]-t00), (int)(t[6]-t00), (int)(t[7]-t00),
          (int)(t[8]-t00), (int)(t[9]-t00), (int)(t[10]-t00), (int)(t[11]-t00), (int)(t[12]-t00), (int)(t[13]-t00), (int)(t[14]-t00), (int)(t[15]-t00), (int)(te - t00));
    }
    }
}

extern "C" void kernel_launch(void* const* d_in, const int* in_sizes, int n_in, void* d_out, int out_size,
                              void* d_ws, size_t ws_size, hipStream_t stream) {
    (void)in_sizes; (void)n_in; (void)out_size; (void)ws_size;
    fused_kernel<<<256, 512, 0, stream>>>((const u16*)d_in[0], (const u16*)d_in[1], (const u16*)d_in[2], (const u16*)d_in[3],
                                          (const u16*)d_in[4], (const u16*)d_in[6], (const u16*)d_in[7], (const u16*)d_in[8],
                                          (const u16*)d_in[9], (const u16*)d_in[10], (u16*)d_out, (char*)d_ws);
}
```

```cpp
#include <hip/hip_runtime.h>
#include <stdint.h>
#define DBG_STAMP
#define V2N

typedef unsigned int u32;
typedef unsigned long long u64;
typedef unsigned short u16;
typedef u32 u32x2 __attribute__((ext_vector_type(2)));
typedef u32 u32x4 __attribute__((ext_vector_type(4)));
typedef u32 u32x8 __attribute__((ext_vector_type(8)));
typedef u32 u32x16 __attribute__((ext_vector_type(16)));
typedef float f32x2 __attribute__((ext_vector_type(2)));
typedef float f32x4 __attribute__((ext_vector_type(4)));
typedef float f32x16 __attribute__((ext_vector_type(16)));
typedef __bf16 bf16x2 __attribute__((ext_vector_type(2)));
typedef __bf16 bf16x8 __attribute__((ext_vector_type(8)));
#define LDSP __attribute__((address_space(3)))

constexpr u32 WS_E   = 0;
constexpr u32 WS_F2  = 4096;
constexpr u32 WS_G   = 24576;
constexpr u32 WS_K   = 65536;
constexpr u32 WS_V   = 131072;
constexpr u32 WS_F1  = 262144;
constexpr u32 WS_O   = 524288;
constexpr u32 WS_Y   = 2621440;
constexpr u32 WS_DBG = 4194304;

constexpr u32 L_K   = 0;
constexpr u32 L_V   = 65536;
constexpr u32 L_ONE = 131072;
constexpr u32 L_W   = 135168;
constexpr u32 L_M   = 151552;
constexpr u32 L_P1  = L_M;
constexpr u32 L_PD  = L_M + 8192;
constexpr u32 L_FP  = L_M + 8704;
constexpr u32 L_YP  = L_M + 9216;
constexpr u32 L_CT  = L_M + 11264;
constexpr u32 L_END = 163840;

constexpr u32 SPIN_MAX = 1u << 22;
constexpr u32 POISON = 0xffffffffu;

#define LD128(a) (*(const LDSP u32x4*)(lds + (a)))
#define ST128(a, v) (*(LDSP u32x4*)(lds + (a)) = (v))
#define LD32(a) (*(const LDSP u32*)(lds + (a)))
#define ST32(a, v) (*(LDSP u32*)(lds + (a)) = (v))
#define LDF(a) (*(const LDSP float*)(lds + (a)))
#define STF(a, v) (*(LDSP float*)(lds + (a)) = (v))

__device__ __forceinline__ u32x2 trread(LDSP char* lds, u32 a) {
    typedef short v4s __attribute__((ext_vector_type(4)));
    v4s r = __builtin_amdgcn_ds_read_tr16_b64_v4i16((LDSP v4s*)(lds + a));
    return __builtin_bit_cast(u32x2, r);
}
__device__ __forceinline__ bf16x8 asbf(u32x4 v) { return __builtin_bit_cast(bf16x8, v); }
__device__ __forceinline__ u32x4 cat(u32x2 a, u32x2 b) { u32x4 r = {a[0], a[1], b[0], b[1]}; return r; }
__device__ __forceinline__ u32 pk(float a, float b) {
    f32x2 f = {a, b};
    return __builtin_bit_cast(u32, __builtin_convertvector(f, bf16x2));
}
__device__ __forceinline__ float bfr(float a) {
    return __uint_as_float(pk(a, a) & 0xffff0000u);
}
__device__ __forceinline__ float lo16(u32 v) { return __uint_as_float(v << 16); }
__device__ __forceinline__ float hi16(u32 v) { return __uint_as_float(v & 0xffff0000u); }
__device__ __forceinline__ float sigm(float z) { return __builtin_amdgcn_rcpf(1.0f + __builtin_amdgcn_exp2f(-1.4426950408889634f * z)); }

__device__ __forceinline__ void dma16(const void* base, u32 voff, u32 ldsaddr) {
    asm volatile("s_mov_b32 m0, %0\n\ts_nop 0\n\tglobal_load_lds_dwordx4 %1, %2" :: "s"(ldsaddr), "v"(voff), "s"(base) : "memory");
}
__device__ __forceinline__ void dma16c4(const void* base, u32 voff, u32 ldsaddr) {
    asm volatile("s_mov_b32 m0, %0\n\ts_nop 0\n\tglobal_load_lds_dwordx4 %1, %2 sc1\n\tglobal_load_lds_dwordx4 %1, %2 offset:1024 sc1\n\t"
                 "global_load_lds_dwordx4 %1, %2 offset:2048 sc1\n\tglobal_load_lds_dwordx4 %1, %2 offset:3072 sc1"
                 :: "s"(ldsaddr), "v"(voff), "s"(base) : "memory");
}
__device__ __forceinline__ void dma16c3(const void* base, u32 voff, u32 ldsaddr) {
    asm volatile("s_mov_b32 m0, %0\n\ts_nop 0\n\tglobal_load_lds_dwordx4 %1, %2 sc1\n\tglobal_load_lds_dwordx4 %1, %2 offset:1024 sc1\n\t"
                 "global_load_lds_dwordx4 %1, %2 offset:2048 sc1"
                 :: "s"(ldsaddr), "v"(voff), "s"(base) : "memory");
}
__device__ __forceinline__ void dma16c2(const void* base, u32 voff, u32 ldsaddr) {
    asm volatile("s_mov_b32 m0, %0\n\ts_nop 0\n\tglobal_load_lds_dwordx4 %1, %2 sc1\n\tglobal_load_lds_dwordx4 %1, %2 offset:1024 sc1"
                 :: "s"(ldsaddr), "v"(voff), "s"(base) : "memory");
}
__device__ __forceinline__ void dma16c(const void* base, u32 voff, u32 ldsaddr) {
    asm volatile("s_mov_b32 m0, %0\n\ts_nop 0\n\tglobal_load_lds_dwordx4 %1, %2 sc1" :: "s"(ldsaddr), "v"(voff), "s"(base) : "memory");
}
#define GLD128(dst, base, voff, OFF)  asm volatile("global_load_dwordx4 %0, %1, %2 offset:" #OFF : "=v"(dst) : "v"(voff), "s"(base) : "memory")
#define GLD128C(dst, base, voff, OFF) asm volatile("global_load_dwordx4 %0, %1, %2 offset:" #OFF " sc1" : "=v"(dst) : "v"(voff), "s"(base) : "memory")
#define GLD64C(dst, base, voff, OFF)  asm volatile("global_load_dwordx2 %0, %1, %2 offset:" #OFF " sc1" : "=v"(dst) : "v"(voff), "s"(base) : "memory")
#define GLD32C(dst, base, voff, OFF)  asm volatile("global_load_dword %0, %1, %2 offset:" #OFF " sc1" : "=v"(dst) : "v"(voff), "s"(base) : "memory")
#define GLD32(dst, base, voff, OFF)   asm volatile("global_load_dword %0, %1, %2 offset:" #OFF : "=v"(dst) : "v"(voff), "s"(base) : "memory")
#define GLD16(dst, base, voff, OFF)   asm volatile("global_load_ushort %0, %1, %2 offset:" #OFF : "=v"(dst) : "v"(voff), "s"(base) : "memory")
#define GST128(base, voff, val)  asm volatile("global_store_dwordx4 %0, %1, %2\n\ts_nop 1" :: "v"(voff), "v"(val), "s"(base) : "memory")
#define GST128C(base, voff, val) asm volatile("global_store_dwordx4 %0, %1, %2 sc1\n\ts_nop 1" :: "v"(voff), "v"(val), "s"(base) : "memory")
#define GST64C(base, voff, val)  asm volatile("global_store_dwordx2 %0, %1, %2 sc1\n\ts_nop 0" :: "v"(voff), "v"(val), "s"(base) : "memory")
#define GST32C(base, voff, val)  asm volatile("global_store_dword %0, %1, %2 sc1" :: "v"(voff), "v"(val), "s"(base) : "memory")
#define VMWAIT(N) asm volatile("s_waitcnt vmcnt(" #N ")" ::: "memory")
#define BAR() asm volatile("s_waitcnt lgkmcnt(0)\n\ts_barrier" ::: "memory")

#define STAMP(i) do { u32 t_ = (u32)__builtin_amdgcn_s_memrealtime(); if (lane == 0) ST32(L_CT + 64 + (w * 24 + (i)) * 4, t_); } while (0)
#define STAMPC(i) do { u32 t_ = (u32)__builtin_amdgcn_s_memtime(); if (lane == 0) ST32(L_CT + 64 + (w * 24 + (i)) * 4, t_); } while (0)
#define STAMPDUMP() do { if (lane < 24) ((u32*)(ws + WS_DBG))[(blockIdx.x * 8 + w) * 32 + lane] = LD32(L_CT + 64 + (w * 24 + lane) * 4); } while (0)
#define FSTAMP(i) STAMP(i)
#define LSTAMP(i)

__device__ __forceinline__ u32 wave_max_u32(u32 v) {
    v = max(v, (u32)__builtin_amdgcn_update_dpp(0, (int)v, 0x111, 0xf, 0xf, false));
    v = max(v, (u32)__builtin_amdgcn_update_dpp(0, (int)v, 0x112, 0xf, 0xf, false));
    v = max(v, (u32)__builtin_amdgcn_update_dpp(0, (int)v, 0x114, 0xf, 0xf, false));
    v = max(v, (u32)__builtin_amdgcn_update_dpp(0, (int)v, 0x118, 0xf, 0xf, false));
    v = max(v, (u32)__builtin_amdgcn_update_dpp(0, (int)v, 0x142, 0xa, 0xf, false));
    v = max(v, (u32)__builtin_amdgcn_update_dpp(0, (int)v, 0x143, 0xc, 0xf, false));
    return (u32)__builtin_amdgcn_readlane((int)v, 63);
}
#define ATT_A \
    "s_mov_b32 %[cnt], 2\n" \
    "v_mov_b32 v232, -1\n" \
    "v_mov_b32 v233, -1\n" \
    "v_mov_b32 v234, -1\n" \
    "v_mov_b32 v235, -1\n" \
    "1:\n" \
    "v_exp_f32 v64, v64\n" \
    "v_exp_f32 v65, v65\n" \
    "v_smfmac_f32_32x32x32_bf16 v[144:159], v[188:191], v[120:127], v192\n" \
    "global_store_dwordx4 v220, v[232:235], %[wsb] sc1\n" \
    "ds_read_b128 v[184:187], v194 offset:1024\n" \
    "v_exp_f32 v66, v66\n" \
    "v_exp_f32 v67, v67\n" \
    "v_exp_f32 v68, v68\n" \
    "v_exp_f32 v69, v69\n" \
    "s_waitcnt lgkmcnt(1)\n" \
    "v_mfma_f32_32x32x16_bf16 v[80:95], v[172:175], v[160:163], 0\n" \
    "ds_read_b128 v[188:191], v194 offset:1536\n" \
    "v_exp_f32 v70, v70\n" \
    "v_exp_f32 v71, v71\n" \
    "v_exp_f32 v72, v72\n" \
    "v_exp_f32 v73, v73\n" \
    "v_smfmac_f32_32x32x32_bf16 v[128:143], v[176:179], v[64:71], v192\n" \
    "v_exp_f32 v74, v74\n" \
    "s_mov_b32 m0, %[skv]\n" \
    "v_exp_f32 v75, v75\n" \
    "v_exp_f32 v76, v76\n" \
    "global_load_lds_dwordx4 v196, %[kvb] offset:0 sc1\n" \
    "v_exp_f32 v77, v77\n" \
    "v_exp_f32 v78, v78\n" \
    "v_exp_f32 v79, v79\n" \
    "v_exp_f32 v96, v96\n" \
    "v_exp_f32 v97, v97\n" \
    "v_smfmac_f32_32x32x32_bf16 v[128:143], v[180:183], v[72:79], v192\n" \
    "v_exp_f32 v98, v98\n" \
    "v_exp_f32 v99, v99\n" \
    "v_exp_f32 v100, v100\n" \
    "v_exp_f32 v101, v101\n" \
    "v_mfma_f32_32x32x16_bf16 v[112:127], v[172:175], v[164:167], 0\n" \
    "ds_read_b128 v[168:171], v193 offset:2048\n" \
    "v_exp_f32 v102, v102\n" \
    "v_exp_f32 v103, v103\n" \
    "v_exp_f32 v104, v104\n" \
    "v_exp_f32 v105, v105\n" \
    "v_smfmac_f32_32x32x32_bf16 v[144:159], v[176:179], v[96:103], v192\n" \
    "v_exp_f32 v106, v106\n" \
    "v_exp_f32 v107, v107\n" \
    "v_exp_f32 v108, v108\n" \
    "v_exp_f32 v109, v109\n" \
    "v_exp_f32 v110, v110\n" \
    "v_exp_f32 v111, v111\n" \
    "v_exp_f32 v80, v80\n" \
    "v_exp_f32 v81, v81\n" \
    "v_smfmac_f32_32x32x32_bf16 v[144:159], v[180:183], v[104:111], v192\n" \
    "ds_read_b128 v[176:179], v194 offset:2048\n" \
    "v_exp_f32 v82, v82\n" \
    "v_exp_f32 v83, v83\n" \
    "v_exp_f32 v84, v84\n" \
    "v_exp_f32 v85, v85\n" \
    "s_waitcnt lgkmcnt(1)\n" \
    "v_mfma_f32_32x32x16_bf16 v[64:79], v[168:171], v[160:163], 0\n" \
    "ds_read_b128 v[180:183], v194 offset:2560\n" \
    "v_exp_f32 v86, v86\n" \
    "v_exp_f32 v87, v87\n" \
    "v_exp_f32 v88, v88\n" \
    "v_exp_f32 v89, v89\n" \
    "v_smfmac_f32_32x32x32_bf16 v[128:143], v[184:187], v[80:87], v192\n" \
    "v_exp_f32 v90, v90\n" \
    "s_mov_b32 m0, %[skv]\n" \
    "v_exp_f32 v91, v91\n" \
    "v_exp_f32 v92, v92\n" \
    "global_load_lds_dwordx4 v196, %[kvb] offset:1024 sc1\n" \
    "v_exp_f32 v93, v93\n" \
    "v_exp_f32 v94, v94\n" \
    "v_exp_f32 v95, v95\n" \
    "v_exp_f32 v112, v112\n" \
    "v_exp_f32 v113, v113\n" \
    "v_smfmac_f32_32x32x32_bf16 v[128:143], v[188:191], v[88:95], v192\n" \
    "v_exp_f32 v114, v114\n" \
    "v_exp_f32 v115, v115\n" \
    "v_exp_f32 v116, v116\n" \
    "v_exp_f32 v117, v117\n" \
    "v_mfma_f32_32x32x16_bf16 v[96:111], v[168:171], v[164:167], 0\n" \
    "ds_read_b128 v[172:175], v193 offset:3072\n" \
    "v_exp_f32 v118, v118\n" \
    "v_exp_f32 v119, v119\n" \
    "v_exp_f32 v120, v120\n" \
    "v_exp_f32 v121, v121\n" \
    "v_smfmac_f32_32x32x32_bf16 v[144:159], v[184:187], v[112:119], v192\n" \
    "v_exp_f32 v122, v122\n" \
    "s_mov_b32 m0, %[sg]\n" \
    "v_exp_f32 v123, v123\n" \
    "v_exp_f32 v124, v124\n" \
    "global_load_lds_dwordx4 v[198:199], off\n" \
    "v_exp_f32 v125, v125\n" \
    "v_exp_f32 v126, v126\n" \
    "v_exp_f32 v127, v127\n" \
    "s_cmp_eq_u32 %[cnt], 1\n" \
    "s_cbranch_scc0 8f\n" \
    "s_setprio 0\n" \
    "8:\n" \
    "v_exp_f32 v64, v64\n" \
    "v_exp_f32 v65, v65\n" \
    "v_smfmac_f32_32x32x32_bf16 v[144:159], v[188:191], v[120:127], v192\n" \
    "ds_read_b128 v[184:187], v194 offset:3072\n" \
    "v_exp_f32 v66, v66\n" \
    "v_exp_f32 v67, v67\n" \
    "v_exp_f32 v68, v68\n" \
    "v_exp_f32 v69, v69\n" \
    "s_waitcnt lgkmcnt(1)\n" \
    "v_mfma_f32_32x32x16_bf16 v[80:95], v[172:175], v[160:163], 0\n" \
    "ds_read_b128 v[188:191], v194 offset:3584\n" \
    "v_exp_f32 v70, v70\n" \
    "v_exp_f32 v71, v71\n" \
    "v_exp_f32 v72, v72\n" \
    "v_exp_f32 v73, v73\n" \
    "v_smfmac_f32_32x32x32_bf16 v[128:143], v[176:179], v[64:71], v192\n" \
    "v_exp_f32 v74, v74\n" \
    "s_mov_b32 m0, %[skv]\n" \
    "v_exp_f32 v75, v75\n" \
    "v_exp_f32 v76, v76\n" \
    "global_load_lds_dwordx4 v196, %[kvb] offset:2048 sc1\n" \
    "v_exp_f32 v77, v77\n" \
    "v_exp_f32 v78, v78\n" \
    "v_exp_f32 v79, v79\n" \
    "v_exp_f32 v96, v96\n" \
    "v_exp_f32 v97, v97\n" \
    "v_smfmac_f32_32x32x32_bf16 v[128:143], v[180:183], v[72:79], v192\n" \
    "v_exp_f32 v98, v98\n" \
    "v_exp_f32 v99, v99\n" \
    "v_exp_f32 v100, v100\n" \
    "v_exp_f32 v101, v101\n" \
    "v_mfma_f32_32x32x16_bf16 v[112:127], v[172:175], v[164:167], 0\n" \
    "ds_read_b128 v[168:171], v193 offset:4096\n" \
    "v_exp_f32 v102, v102\n" \
    "v_exp_f32 v103, v103\n" \
    "v_exp_f32 v104, v104\n" \
    "v_exp_f32 v105, v105\n" \
    "v_smfmac_f32_32x32x32_bf16 v[144:159], v[176:179], v[96:103], v192\n" \
    "v_exp_f32 v106, v106\n" \
    "v_exp_f32 v107, v107\n" \
    "v_exp_f32 v108, v108\n" \
    "v_exp_f32 v109, v109\n" \
    "v_exp_f32 v110, v110\n" \
    "v_exp_f32 v111, v111\n" \
    "v_exp_f32 v80, v80\n" \
    "v_exp_f32 v81, v81\n" \
    "v_smfmac_f32_32x32x32_bf16 v[144:159], v[180:183], v[104:111], v192\n" \
    "ds_read_b128 v[176:179], v194 offset:4096\n" \
    "v_exp_f32 v82, v82\n" \
    "v_exp_f32 v83, v83\n" \
    "v_exp_f32 v84, v84\n" \
    "v_exp_f32 v85, v85\n" \
    "s_waitcnt lgkmcnt(1)\n" \
    "v_mfma_f32_32x32x16_bf16 v[64:79], v[168:171], v[160:163], 0\n" \
    "ds_read_b128 v[180:183], v194 offset:4608\n" \
    "v_exp_f32 v86, v86\n" \
    "v_exp_f32 v87, v87\n" \
    "v_exp_f32 v88, v88\n" \
    "v_exp_f32 v89, v89\n" \
    "v_smfmac_f32_32x32x32_bf16 v[128:143], v[184:187], v[80:87], v192\n" \
    "v_exp_f32 v90, v90\n" \
    "s_mov_b32 m0, %[skv]\n" \
    "v_exp_f32 v91, v91\n" \
    "v_exp_f32 v92, v92\n" \
    "global_load_lds_dwordx4 v196, %[kvb] offset:3072 sc1\n" \
    "v_exp_f32 v93, v93\n" \
    "v_exp_f32 v94, v94\n" \
    "v_exp_f32 v95, v95\n" \
    "v_exp_f32 v112, v112\n" \
    "v_exp_f32 v113, v113\n" \
    "v_smfmac_f32_32x32x32_bf16 v[128:143], v[188:191], v[88:95], v192\n" \
    "v_exp_f32 v114, v114\n" \
    "v_exp_f32 v115, v115\n" \
    "v_exp_f32 v116, v116\n" \
    "v_exp_f32 v117, v117\n" \
    "v_mfma_f32_32x32x16_bf16 v[96:111], v[168:171], v[164:167], 0\n" \
    "ds_read_b128 v[172:175], v193 offset:5120\n" \
    "v_exp_f32 v118, v118\n" \
    "v_exp_f32 v119, v119\n" \
    "v_exp_f32 v120, v120\n" \
    "v_exp_f32 v121, v121\n" \
    "v_smfmac_f32_32x32x32_bf16 v[144:159], v[184:187], v[112:119], v192\n" \
    "v_exp_f32 v122, v122\n" \
    "v_exp_f32 v123, v123\n" \
    "v_exp_f32 v124, v124\n" \
    "v_exp_f32 v125, v125\n" \
    "v_exp_f32 v126, v126\n" \
    "v_exp_f32 v127, v127\n" \
    "v_add_u32 v196, 0x2000, v196\n" \
    "v_mov_b32 v198, v200\n" \
    "v_mov_b32 v199, v201\n" \
    "s_add_u32 %[skv], %[skv], 0x2000\n" \
    "s_add_u32 %[sg], %[sg], 0x400\n" \
    "v_mov_b32 v220, v221\n" \
    "v_add_u32 v193, 4096, v193\n" \
    "v_add_u32 v194, v195, v194\n" \
    "s_sub_u32 %[cnt], %[cnt], 1\n" \
    "s_cmp_lg_u32 %[cnt], 0\n" \
    "s_cbranch_scc1 1b\n" \
    "s_waitcnt vmcnt(5)\n"

#define ATT_A1 \
    "s_mov_b32 %[cnt], 2\n" \
    "v_mov_b32 v232, -1\n" \
    "v_mov_b32 v233, -1\n" \
    "v_mov_b32 v234, -1\n" \
    "v_mov_b32 v235, -1\n" \
    "v_exp_f32 v64, v64\n" \
    "v_exp_f32 v65, v65\n" \
    "v_smfmac_f32_32x32x32_bf16 v[144:159], v[188:191], v[120:127], v192\n" \
    "global_store_dwordx4 v220, v[232:235], %[wsb] sc1\n" \
    "ds_read_b128 v[184:187], v194 offset:1024\n" \
    "v_exp_f32 v66, v66\n" \
    "v_exp_f32 v67, v67\n" \
    "v_exp_f32 v68, v68\n" \
    "v_exp_f32 v69, v69\n" \
    "s_waitcnt lgkmcnt(1)\n" \
    "v_mfma_f32_32x32x16_bf16 v[80:95], v[172:175], v[160:163], 0\n" \
    "ds_read_b128 v[188:191], v194 offset:1536\n" \
    "v_exp_f32 v70, v70\n" \
    "v_exp_f32 v71, v71\n" \
    "v_exp_f32 v72, v72\n" \
    "v_exp_f32 v73, v73\n" \
    "v_smfmac_f32_32x32x32_bf16 v[128:143], v[176:179], v[64:71], v192\n" \
    "v_exp_f32 v74, v74\n" \
    "s_mov_b32 m0, %[skv]\n" \
    "v_exp_f32 v75, v75\n" \
    "v_exp_f32 v76, v76\n" \
    "global_load_lds_dwordx4 v196, %[kvb] offset:0 sc1\n" \
    "v_exp_f32 v77, v77\n" \
    "v_exp_f32 v78, v78\n" \
    "v_exp_f32 v79, v79\n" \
    "v_exp_f32 v96, v96\n" \
    "v_exp_f32 v97, v97\n" \
    "v_smfmac_f32_32x32x32_bf16 v[128:143], v[180:183], v[72:79], v192\n" \
    "v_exp_f32 v98, v98\n" \
    "v_exp_f32 v99, v99\n" \
    "v_exp_f32 v100, v100\n" \
    "v_exp_f32 v101, v101\n" \
    "v_mfma_f32_32x32x16_bf16 v[112:127], v[172:175], v[164:167], 0\n" \
    "ds_read_b128 v[168:171], v193 offset:2048\n" \
    "v_exp_f32 v102, v102\n" \
    "v_exp_f32 v103, v103\n" \
    "v_exp_f32 v104, v104\n" \
    "v_exp_f32 v105, v105\n" \
    "v_smfmac_f32_32x32x32_bf16 v[144:159], v[176:179], v[96:103], v192\n" \
    "v_exp_f32 v106, v106\n" \
    "v_exp_f32 v107, v107\n" \
    "v_exp_f32 v108, v108\n" \
    "v_exp_f32 v109, v109\n" \
    "v_exp_f32 v110, v110\n" \
    "v_exp_f32 v111, v111\n" \
    "v_exp_f32 v80, v80\n" \
    "v_exp_f32 v81, v81\n" \
    "v_smfmac_f32_32x32x32_bf16 v[144:159], v[180:183], v[104:111], v192\n" \
    "ds_read_b128 v[176:179], v194 offset:2048\n" \
    "v_exp_f32 v82, v82\n" \
    "v_exp_f32 v83, v83\n" \
    "v_exp_f32 v84, v84\n" \
    "v_exp_f32 v85, v85\n" \
    "s_waitcnt lgkmcnt(1)\n" \
    "v_mfma_f32_32x32x16_bf16 v[64:79], v[168:171], v[160:163], 0\n" \
    "ds_read_b128 v[180:183], v194 offset:2560\n" \
    "v_exp_f32 v86, v86\n" \
    "v_exp_f32 v87, v87\n" \
    "v_exp_f32 v88, v88\n" \
    "v_exp_f32 v89, v89\n" \
    "v_smfmac_f32_32x32x32_bf16 v[128:143], v[184:187], v[80:87], v192\n" \
    "v_exp_f32 v90, v90\n" \
    "s_mov_b32 m0, %[skv]\n" \
    "v_exp_f32 v91, v91\n" \
    "v_exp_f32 v92, v92\n" \
    "global_load_lds_dwordx4 v196, %[kvb] offset:1024 sc1\n" \
    "v_exp_f32 v93, v93\n" \
    "v_exp_f32 v94, v94\n" \
    "v_exp_f32 v95, v95\n" \
    "v_exp_f32 v112, v112\n" \
    "v_exp_f32 v113, v113\n" \
    "v_smfmac_f32_32x32x32_bf16 v[128:143], v[188:191], v[88:95], v192\n" \
    "v_exp_f32 v114, v114\n" \
    "v_exp_f32 v115, v115\n" \
    "v_exp_f32 v116, v116\n" \
    "v_exp_f32 v117, v117\n" \
    "v_mfma_f32_32x32x16_bf16 v[96:111], v[168:171], v[164:167], 0\n" \
    "ds_read_b128 v[172:175], v193 offset:3072\n" \
    "v_exp_f32 v118, v118\n" \
    "v_exp_f32 v119, v119\n" \
    "v_exp_f32 v120, v120\n" \
    "v_exp_f32 v121, v121\n" \
    "v_smfmac_f32_32x32x32_bf16 v[144:159], v[184:187], v[112:119], v192\n" \
    "v_exp_f32 v122, v122\n" \
    "s_mov_b32 m0, %[sg]\n" \
    "v_exp_f32 v123, v123\n" \
    "v_exp_f32 v124, v124\n" \
    "global_load_lds_dwordx4 v[198:199], off\n" \
    "v_exp_f32 v125, v125\n" \
    "v_exp_f32 v126, v126\n" \
    "v_exp_f32 v127, v127\n" \
    "s_cmp_eq_u32 %[cnt], 1\n" \
    "s_cbranch_scc0 8f\n" \
    "s_setprio 0\n" \
    "8:\n" \
    "v_exp_f32 v64, v64\n" \
    "v_exp_f32 v65, v65\n" \
    "v_smfmac_f32_32x32x32_bf16 v[144:159], v[188:191], v[120:127], v192\n" \
    "ds_read_b128 v[184:187], v194 offset:3072\n" \
    "v_exp_f32 v66, v66\n" \
    "v_exp_f32 v67, v67\n" \
    "v_exp_f32 v68, v68\n" \
    "v_exp_f32 v69, v69\n" \
    "s_waitcnt lgkmcnt(1)\n" \
    "v_mfma_f32_32x32x16_bf16 v[80:95], v[172:175], v[160:163], 0\n" \
    "ds_read_b128 v[188:191], v194 offset:3584\n" \
    "v_exp_f32 v70, v70\n" \
    "v_exp_f32 v71, v71\n" \
    "v_exp_f32 v72, v72\n" \
    "v_exp_f32 v73, v73\n" \
    "v_smfmac_f32_32x32x32_bf16 v[128:143], v[176:179], v[64:71], v192\n" \
    "v_exp_f32 v74, v74\n" \
    "s_mov_b32 m0, %[skv]\n" \
    "v_exp_f32 v75, v75\n" \
    "v_exp_f32 v76, v76\n" \
    "global_load_lds_dwordx4 v196, %[kvb] offset:2048 sc1\n" \
    "v_exp_f32 v77, v77\n" \
    "v_exp_f32 v78, v78\n" \
    "v_exp_f32 v79, v79\n" \
    "v_exp_f32 v96, v96\n" \
    "v_exp_f32 v97, v97\n" \
    "v_smfmac_f32_32x32x32_bf16 v[128:143], v[180:183], v[72:79], v192\n" \
    "v_exp_f32 v98, v98\n" \
    "v_exp_f32 v99, v99\n" \
    "v_exp_f32 v100, v100\n" \
    "v_exp_f32 v101, v101\n" \
    "v_mfma_f32_32x32x16_bf16 v[112:127], v[172:175], v[164:167], 0\n" \
    "ds_read_b128 v[168:171], v193 offset:4096\n" \
    "v_exp_f32 v102, v102\n" \
    "v_exp_f32 v103, v103\n" \
    "v_exp_f32 v104, v104\n" \
    "v_exp_f32 v105, v105\n" \
    "v_smfmac_f32_32x32x32_bf16 v[144:159], v[176:179], v[96:103], v192\n" \
    "v_exp_f32 v106, v106\n" \
    "v_exp_f32 v107, v107\n" \
    "v_exp_f32 v108, v108\n" \
    "v_exp_f32 v109, v109\n" \
    "v_exp_f32 v110, v110\n" \
    "v_exp_f32 v111, v111\n" \
    "v_exp_f32 v80, v80\n" \
    "v_exp_f32 v81, v81\n" \
    "v_smfmac_f32_32x32x32_bf16 v[144:159], v[180:183], v[104:111], v192\n" \
    "ds_read_b128 v[176:179], v194 offset:4096\n" \
    "v_exp_f32 v82, v82\n" \
    "v_exp_f32 v83, v83\n" \
    "v_exp_f32 v84, v84\n" \
    "v_exp_f32 v85, v85\n" \
    "s_waitcnt lgkmcnt(1)\n" \
    "v_mfma_f32_32x32x16_bf16 v[64:79], v[168:171], v[160:163], 0\n" \
    "ds_read_b128 v[180:183], v194 offset:4608\n" \
    "v_exp_f32 v86, v86\n" \
    "v_exp_f32 v87, v87\n" \
    "v_exp_f32 v88, v88\n" \
    "v_exp_f32 v89, v89\n" \
    "v_smfmac_f32_32x32x32_bf16 v[128:143], v[184:187], v[80:87], v192\n" \
    "v_exp_f32 v90, v90\n" \
    "s_mov_b32 m0, %[skv]\n" \
    "v_exp_f32 v91, v91\n" \
    "v_exp_f32 v92, v92\n" \
    "global_load_lds_dwordx4 v196, %[kvb] offset:3072 sc1\n" \
    "v_exp_f32 v93, v93\n" \
    "v_exp_f32 v94, v94\n" \
    "v_exp_f32 v95, v95\n" \
    "v_exp_f32 v112, v112\n" \
    "v_exp_f32 v113, v113\n" \
    "v_smfmac_f32_32x32x32_bf16 v[128:143], v[188:191], v[88:95], v192\n" \
    "v_exp_f32 v114, v114\n" \
    "v_exp_f32 v115, v115\n" \
    "v_exp_f32 v116, v116\n" \
    "v_exp_f32 v117, v117\n" \
    "v_mfma_f32_32x32x16_bf16 v[96:111], v[168:171], v[164:167], 0\n" \
    "ds_read_b128 v[172:175], v193 offset:5120\n" \
    "v_exp_f32 v118, v118\n" \
    "v_exp_f32 v119, v119\n" \
    "v_exp_f32 v120, v120\n" \
    "v_exp_f32 v121, v121\n" \
    "v_smfmac_f32_32x32x32_bf16 v[144:159], v[184:187], v[112:119], v192\n" \
    "v_exp_f32 v122, v122\n" \
    "v_exp_f32 v123, v123\n" \
    "v_exp_f32 v124, v124\n" \
    "v_exp_f32 v125, v125\n" \
    "v_exp_f32 v126, v126\n" \
    "v_exp_f32 v127, v127\n" \
    "v_add_u32 v196, 0x2000, v196\n" \
    "v_mov_b32 v198, v200\n" \
    "v_mov_b32 v199, v201\n" \
    "s_add_u32 %[skv], %[skv], 0x2000\n" \
    "s_add_u32 %[sg], %[sg], 0x400\n" \
    "v_mov_b32 v220, v221\n" \
    "v_add_u32 v193, 4096, v193\n" \
    "v_add_u32 v194, v195, v194\n"

#define ATT_A2 \
    "s_mov_b32 %[cnt], 1\n" \
    "v_exp_f32 v64, v64\n" \
    "v_exp_f32 v65, v65\n" \
    "v_smfmac_f32_32x32x32_bf16 v[144:159], v[188:191], v[120:127], v192\n" \
    "global_store_dwordx4 v220, v[232:235], %[wsb] sc1\n" \
    "ds_read_b128 v[184:187], v194 offset:1024\n" \
    "v_exp_f32 v66, v66\n" \
    "v_exp_f32 v67, v67\n" \
    "v_exp_f32 v68, v68\n" \
    "v_exp_f32 v69, v69\n" \
    "s_waitcnt lgkmcnt(1)\n" \
    "v_mfma_f32_32x32x16_bf16 v[80:95], v[172:175], v[160:163], 0\n" \
    "ds_read_b128 v[188:191], v194 offset:1536\n" \
    "v_exp_f32 v70, v70\n" \
    "v_exp_f32 v71, v71\n" \
    "v_exp_f32 v72, v72\n" \
    "v_exp_f32 v73, v73\n" \
    "v_smfmac_f32_32x32x32_bf16 v[128:143], v[176:179], v[64:71], v192\n" \
    "v_exp_f32 v74, v74\n" \
    "s_mov_b32 m0, %[skv]\n" \
    "v_exp_f32 v75, v75\n" \
    "v_exp_f32 v76, v76\n" \
    "global_load_lds_dwordx4 v196, %[kvb] offset:0 sc1\n" \
    "v_exp_f32 v77, v77\n" \
    "v_exp_f32 v78, v78\n" \
    "v_exp_f32 v79, v79\n" \
    "v_exp_f32 v96, v96\n" \
    "v_exp_f32 v97, v97\n" \
    "v_smfmac_f32_32x32x32_bf16 v[128:143], v[180:183], v[72:79], v192\n" \
    "v_exp_f32 v98, v98\n" \
    "v_exp_f32 v99, v99\n" \
    "v_exp_f32 v100, v100\n" \
    "v_exp_f32 v101, v101\n" \
    "v_mfma_f32_32x32x16_bf16 v[112:127], v[172:175], v[164:167], 0\n" \
    "ds_read_b128 v[168:171], v193 offset:2048\n" \
    "v_exp_f32 v102, v102\n" \
    "v_exp_f32 v103, v103\n" \
    "v_exp_f32 v104, v104\n" \
    "v_exp_f32 v105, v105\n" \
    "v_smfmac_f32_32x32x32_bf16 v[144:159], v[176:179], v[96:103], v192\n" \
    "v_exp_f32 v106, v106\n" \
    "v_exp_f32 v107, v107\n" \
    "v_exp_f32 v108, v108\n" \
    "v_exp_f32 v109, v109\n" \
    "v_exp_f32 v110, v110\n" \
    "v_exp_f32 v111, v111\n" \
    "v_exp_f32 v80, v80\n" \
    "v_exp_f32 v81, v81\n" \
    "v_smfmac_f32_32x32x32_bf16 v[144:159], v[180:183], v[104:111], v192\n" \
    "ds_read_b128 v[176:179], v194 offset:2048\n" \
    "v_exp_f32 v82, v82\n" \
    "v_exp_f32 v83, v83\n" \
    "v_exp_f32 v84, v84\n" \
    "v_exp_f32 v85, v85\n" \
    "s_waitcnt lgkmcnt(1)\n" \
    "v_mfma_f32_32x32x16_bf16 v[64:79], v[168:171], v[160:163], 0\n" \
    "ds_read_b128 v[180:183], v194 offset:2560\n" \
    "v_exp_f32 v86, v86\n" \
    "v_exp_f32 v87, v87\n" \
    "v_exp_f32 v88, v88\n" \
    "v_exp_f32 v89, v89\n" \
    "v_smfmac_f32_32x32x32_bf16 v[128:143], v[184:187], v[80:87], v192\n" \
    "v_exp_f32 v90, v90\n" \
    "s_mov_b32 m0, %[skv]\n" \
    "v_exp_f32 v91, v91\n" \
    "v_exp_f32 v92, v92\n" \
    "global_load_lds_dwordx4 v196, %[kvb] offset:1024 sc1\n" \
    "v_exp_f32 v93, v93\n" \
    "v_exp_f32 v94, v94\n" \
    "v_exp_f32 v95, v95\n" \
    "v_exp_f32 v112, v112\n" \
    "v_exp_f32 v113, v113\n" \
    "v_smfmac_f32_32x32x32_bf16 v[128:143], v[188:191], v[88:95], v192\n" \
    "v_exp_f32 v114, v114\n" \
    "v_exp_f32 v115, v115\n" \
    "v_exp_f32 v116, v116\n" \
    "v_exp_f32 v117, v117\n" \
    "v_mfma_f32_32x32x16_bf16 v[96:111], v[168:171], v[164:167], 0\n" \
    "ds_read_b128 v[172:175], v193 offset:3072\n" \
    "v_exp_f32 v118, v118\n" \
    "v_exp_f32 v119, v119\n" \
    "v_exp_f32 v120, v120\n" \
    "v_exp_f32 v121, v121\n" \
    "v_smfmac_f32_32x32x32_bf16 v[144:159], v[184:187], v[112:119], v192\n" \
    "v_exp_f32 v122, v122\n" \
    "s_mov_b32 m0, %[sg]\n" \
    "v_exp_f32 v123, v123\n" \
    "v_exp_f32 v124, v124\n" \
    "global_load_lds_dwordx4 v[198:199], off\n" \
    "v_exp_f32 v125, v125\n" \
    "v_exp_f32 v126, v126\n" \
    "v_exp_f32 v127, v127\n" \
    "s_cmp_eq_u32 %[cnt], 1\n" \
    "s_cbranch_scc0 8f\n" \
    "s_setprio 0\n" \
    "8:\n" \
    "v_exp_f32 v64, v64\n" \
    "v_exp_f32 v65, v65\n" \
    "v_smfmac_f32_32x32x32_bf16 v[144:159], v[188:191], v[120:127], v192\n" \
    "ds_read_b128 v[184:187], v194 offset:3072\n" \
    "v_exp_f32 v66, v66\n" \
    "v_exp_f32 v67, v67\n" \
    "v_exp_f32 v68, v68\n" \
    "v_exp_f32 v69, v69\n" \
    "s_waitcnt lgkmcnt(1)\n" \
    "v_mfma_f32_32x32x16_bf16 v[80:95], v[172:175], v[160:163], 0\n" \
    "ds_read_b128 v[188:191], v194 offset:3584\n" \
    "v_exp_f32 v70, v70\n" \
    "v_exp_f32 v71, v71\n" \
    "v_exp_f32 v72, v72\n" \
    "v_exp_f32 v73, v73\n" \
    "v_smfmac_f32_32x32x32_bf16 v[128:143], v[176:179], v[64:71], v192\n" \
    "v_exp_f32 v74, v74\n" \
    "s_mov_b32 m0, %[skv]\n" \
    "v_exp_f32 v75, v75\n" \
    "v_exp_f32 v76, v76\n" \
    "global_load_lds_dwordx4 v196, %[kvb] offset:2048 sc1\n" \
    "v_exp_f32 v77, v77\n" \
    "v_exp_f32 v78, v78\n" \
    "v_exp_f32 v79, v79\n" \
    "v_exp_f32 v96, v96\n" \
    "v_exp_f32 v97, v97\n" \
    "v_smfmac_f32_32x32x32_bf16 v[128:143], v[180:183], v[72:79], v192\n" \
    "v_exp_f32 v98, v98\n" \
    "v_exp_f32 v99, v99\n" \
    "v_exp_f32 v100, v100\n" \
    "v_exp_f32 v101, v101\n" \
    "v_mfma_f32_32x32x16_bf16 v[112:127], v[172:175], v[164:167], 0\n" \
    "ds_read_b128 v[168:171], v193 offset:4096\n" \
    "v_exp_f32 v102, v102\n" \
    "v_exp_f32 v103, v103\n" \
    "v_exp_f32 v104, v104\n" \
    "v_exp_f32 v105, v105\n" \
    "v_smfmac_f32_32x32x32_bf16 v[144:159], v[176:179], v[96:103], v192\n" \
    "v_exp_f32 v106, v106\n" \
    "v_exp_f32 v107, v107\n" \
    "v_exp_f32 v108, v108\n" \
    "v_exp_f32 v109, v109\n" \
    "v_exp_f32 v110, v110\n" \
    "v_exp_f32 v111, v111\n" \
    "v_exp_f32 v80, v80\n" \
    "v_exp_f32 v81, v81\n" \
    "v_smfmac_f32_32x32x32_bf16 v[144:159], v[180:183], v[104:111], v192\n" \
    "ds_read_b128 v[176:179], v194 offset:4096\n" \
    "v_exp_f32 v82, v82\n" \
    "v_exp_f32 v83, v83\n" \
    "v_exp_f32 v84, v84\n" \
    "v_exp_f32 v85, v85\n" \
    "s_waitcnt lgkmcnt(1)\n" \
    "v_mfma_f32_32x32x16_bf16 v[64:79], v[168:171], v[160:163], 0\n" \
    "ds_read_b128 v[180:183], v194 offset:4608\n" \
    "v_exp_f32 v86, v86\n" \
    "v_exp_f32 v87, v87\n" \
    "v_exp_f32 v88, v88\n" \
    "v_exp_f32 v89, v89\n" \
    "v_smfmac_f32_32x32x32_bf16 v[128:143], v[184:187], v[80:87], v192\n" \
    "v_exp_f32 v90, v90\n" \
    "s_mov_b32 m0, %[skv]\n" \
    "v_exp_f32 v91, v91\n" \
    "v_exp_f32 v92, v92\n" \
    "global_load_lds_dwordx4 v196, %[kvb] offset:3072 sc1\n" \
    "v_exp_f32 v93, v93\n" \
    "v_exp_f32 v94, v94\n" \
    "v_exp_f32 v95, v95\n" \
    "v_exp_f32 v112, v112\n" \
    "v_exp_f32 v113, v113\n" \
    "v_smfmac_f32_32x32x32_bf16 v[128:143], v[188:191], v[88:95], v192\n" \
    "v_exp_f32 v114, v114\n" \
    "v_exp_f32 v115, v115\n" \
    "v_exp_f32 v116, v116\n" \
    "v_exp_f32 v117, v117\n" \
    "v_mfma_f32_32x32x16_bf16 v[96:111], v[168:171], v[164:167], 0\n" \
    "ds_read_b128 v[172:175], v193 offset:5120\n" \
    "v_exp_f32 v118, v118\n" \
    "v_exp_f32 v119, v119\n" \
    "v_exp_f32 v120, v120\n" \
    "v_exp_f32 v121, v121\n" \
    "v_smfmac_f32_32x32x32_bf16 v[144:159], v[184:187], v[112:119], v192\n" \
    "v_exp_f32 v122, v122\n" \
    "v_exp_f32 v123, v123\n" \
    "v_exp_f32 v124, v124\n" \
    "v_exp_f32 v125, v125\n" \
    "v_exp_f32 v126, v126\n" \
    "v_exp_f32 v127, v127\n" \
    "v_add_u32 v196, 0x2000, v196\n" \
    "v_mov_b32 v198, v200\n" \
    "v_mov_b32 v199, v201\n" \
    "s_add_u32 %[skv], %[skv], 0x2000\n" \
    "s_add_u32 %[sg], %[sg], 0x400\n" \
    "v_mov_b32 v220, v221\n" \
    "v_add_u32 v193, 4096, v193\n" \
    "v_add_u32 v194, v195, v194\n"

#define ATT_B \
    "v_exp_f32 v64, v64\n" \
    "v_exp_f32 v65, v65\n" \
    "v_smfmac_f32_32x32x32_bf16 v[144:159], v[188:191], v[120:127], v192\n" \
    "ds_read_b128 v[184:187], v194 offset:1024\n" \
    "v_exp_f32 v66, v66\n" \
    "v_exp_f32 v67, v67\n" \
    "v_exp_f32 v68, v68\n" \
    "v_exp_f32 v69, v69\n" \
    "s_waitcnt lgkmcnt(1)\n" \
    "v_mfma_f32_32x32x16_bf16 v[80:95], v[172:175], v[160:163], 0\n" \
    "ds_read_b128 v[188:191], v194 offset:1536\n" \
    "v_exp_f32 v70, v70\n" \
    "v_exp_f32 v71, v71\n" \
    "v_exp_f32 v72, v72\n" \
    "v_exp_f32 v73, v73\n" \
    "v_smfmac_f32_32x32x32_bf16 v[128:143], v[176:179], v[64:71], v192\n" \
    "v_exp_f32 v74, v74\n" \
    "s_mov_b32 m0, %[skv3]\n" \
    "v_exp_f32 v75, v75\n" \
    "v_exp_f32 v76, v76\n" \
    "global_load_dwordx4 v[204:207], v202, %[xb]\n" \
    "v_exp_f32 v77, v77\n" \
    "v_exp_f32 v78, v78\n" \
    "v_exp_f32 v79, v79\n" \
    "v_exp_f32 v96, v96\n" \
    "v_exp_f32 v97, v97\n" \
    "v_smfmac_f32_32x32x32_bf16 v[128:143], v[180:183], v[72:79], v192\n" \
    "v_exp_f32 v98, v98\n" \
    "v_exp_f32 v99, v99\n" \
    "v_exp_f32 v100, v100\n" \
    "v_exp_f32 v101, v101\n" \
    "v_mfma_f32_32x32x16_bf16 v[112:127], v[172:175], v[164:167], 0\n" \
    "ds_read_b128 v[168:171], v193 offset:2048\n" \
    "v_exp_f32 v102, v102\n" \
    "v_exp_f32 v103, v103\n" \
    "v_exp_f32 v104, v104\n" \
    "v_exp_f32 v105, v105\n" \
    "v_smfmac_f32_32x32x32_bf16 v[144:159], v[176:179], v[96:103], v192\n" \
    "v_exp_f32 v106, v106\n" \
    "v_exp_f32 v107, v107\n" \
    "v_exp_f32 v108, v108\n" \
    "global_load_dwordx4 v[208:211], v202, %[xb] offset:16\n" \
    "v_exp_f32 v109, v109\n" \
    "v_exp_f32 v110, v110\n" \
    "v_exp_f32 v111, v111\n" \
    "v_exp_f32 v80, v80\n" \
    "v_exp_f32 v81, v81\n" \
    "v_smfmac_f32_32x32x32_bf16 v[144:159], v[180:183], v[104:111], v192\n" \
    "ds_read_b128 v[176:179], v194 offset:2048\n" \
    "v_exp_f32 v82, v82\n" \
    "v_exp_f32 v83, v83\n" \
    "v_exp_f32 v84, v84\n" \
    "v_exp_f32 v85, v85\n" \
    "s_waitcnt lgkmcnt(1)\n" \
    "v_mfma_f32_32x32x16_bf16 v[64:79], v[168:171], v[160:163], 0\n" \
    "ds_read_b128 v[180:183], v194 offset:2560\n" \
    "v_exp_f32 v86, v86\n" \
    "v_exp_f32 v87, v87\n" \
    "v_exp_f32 v88, v88\n" \
    "v_exp_f32 v89, v89\n" \
    "v_smfmac_f32_32x32x32_bf16 v[128:143], v[184:187], v[80:87], v192\n" \
    "v_exp_f32 v90, v90\n" \
    "v_exp_f32 v91, v91\n" \
    "v_exp_f32 v92, v92\n" \
    "global_load_lds_dwordx4 v197, %[kvb] offset:0 sc1\n" \
    "v_exp_f32 v93, v93\n" \
    "v_exp_f32 v94, v94\n" \
    "v_exp_f32 v95, v95\n" \
    "v_exp_f32 v112, v112\n" \
    "v_exp_f32 v113, v113\n" \
    "v_smfmac_f32_32x32x32_bf16 v[128:143], v[188:191], v[88:95], v192\n" \
    "v_exp_f32 v114, v114\n" \
    "v_exp_f32 v115, v115\n" \
    "v_exp_f32 v116, v116\n" \
    "v_exp_f32 v117, v117\n" \
    "v_mfma_f32_32x32x16_bf16 v[96:111], v[168:171], v[164:167], 0\n" \
    "ds_read_b128 v[172:175], v193 offset:3072\n" \
    "v_exp_f32 v118, v118\n" \
    "v_exp_f32 v119, v119\n" \
    "v_exp_f32 v120, v120\n" \
    "v_exp_f32 v121, v121\n" \
    "v_smfmac_f32_32x32x32_bf16 v[144:159], v[184:187], v[112:119], v192\n" \
    "v_exp_f32 v122, v122\n" \
    "v_exp_f32 v123, v123\n" \
    "v_exp_f32 v124, v124\n" \
    "global_load_lds_dwordx4 v197, %[kvb] offset:1024 sc1\n" \
    "v_exp_f32 v125, v125\n" \
    "v_exp_f32 v126, v126\n" \
    "v_exp_f32 v127, v127\n" \
    "v_exp_f32 v64, v64\n" \
    "v_exp_f32 v65, v65\n" \
    "v_smfmac_f32_32x32x32_bf16 v[144:159], v[188:191], v[120:127], v192\n" \
    "ds_read_b128 v[184:187], v194 offset:3072\n" \
    "v_exp_f32 v66, v66\n" \
    "v_exp_f32 v67, v67\n" \
    "v_exp_f32 v68, v68\n" \
    "v_exp_f32 v69, v69\n" \
    "s_waitcnt lgkmcnt(1)\n" \
    "v_mfma_f32_32x32x16_bf16 v[80:95], v[172:175], v[160:163], 0\n" \
    "ds_read_b128 v[188:191], v194 offset:3584\n" \
    "v_exp_f32 v70, v70\n" \
    "v_exp_f32 v71, v71\n" \
    "v_exp_f32 v72, v72\n" \
    "v_exp_f32 v73, v73\n" \
    "v_smfmac_f32_32x32x32_bf16 v[128:143], v[176:179], v[64:71], v192\n" \
    "v_exp_f32 v74, v74\n" \
    "v_exp_f32 v75, v75\n" \
    "v_exp_f32 v76, v76\n" \
    "global_load_lds_dwordx4 v197, %[kvb] offset:2048 sc1\n" \
    "v_exp_f32 v77, v77\n" \
    "v_exp_f32 v78, v78\n" \
    "v_exp_f32 v79, v79\n" \
    "v_exp_f32 v96, v96\n" \
    "v_exp_f32 v97, v97\n" \
    "v_smfmac_f32_32x32x32_bf16 v[128:143], v[180:183], v[72:79], v192\n" \
    "v_exp_f32 v98, v98\n" \
    "v_exp_f32 v99, v99\n" \
    "v_exp_f32 v100, v100\n" \
    "v_exp_f32 v101, v101\n" \
    "v_mfma_f32_32x32x16_bf16 v[112:127], v[172:175], v[164:167], 0\n" \
    "ds_read_b128 v[168:171], v193 offset:4096\n" \
    "v_exp_f32 v102, v102\n" \
    "v_exp_f32 v103, v103\n" \
    "v_exp_f32 v104, v104\n" \
    "v_exp_f32 v105, v105\n" \
    "v_smfmac_f32_32x32x32_bf16 v[144:159], v[176:179], v[96:103], v192\n" \
    "v_exp_f32 v106, v106\n" \
    "v_exp_f32 v107, v107\n" \
    "v_exp_f32 v108, v108\n" \
    "global_load_lds_dwordx4 v197, %[kvb] offset:3072 sc1\n" \
    "v_exp_f32 v109, v109\n" \
    "v_exp_f32 v110, v110\n" \
    "v_exp_f32 v111, v111\n" \
    "v_exp_f32 v80, v80\n" \
    "v_exp_f32 v81, v81\n" \
    "v_smfmac_f32_32x32x32_bf16 v[144:159], v[180:183], v[104:111], v192\n" \
    "ds_read_b128 v[176:179], v194 offset:4096\n" \
    "v_exp_f32 v82, v82\n" \
    "v_exp_f32 v83, v83\n" \
    "v_exp_f32 v84, v84\n" \
    "v_exp_f32 v85, v85\n" \
    "s_waitcnt lgkmcnt(1)\n" \
    "v_mfma_f32_32x32x16_bf16 v[64:79], v[168:171], v[160:163], 0\n" \
    "ds_read_b128 v[180:183], v194 offset:4608\n" \
    "v_exp_f32 v86, v86\n" \
    "v_exp_f32 v87, v87\n" \
    "v_exp_f32 v88, v88\n" \
    "v_exp_f32 v89, v89\n" \
    "v_smfmac_f32_32x32x32_bf16 v[128:143], v[184:187], v[80:87], v192\n" \
    "v_exp_f32 v90, v90\n" \
    "v_exp_f32 v91, v91\n" \
    "v_exp_f32 v92, v92\n" \
    "global_load_dword v212, v203, %[wdb] offset:0\n" \
    "global_load_dword v213, v203, %[wdb] offset:1024\n" \
    "v_exp_f32 v93, v93\n" \
    "v_exp_f32 v94, v94\n" \
    "v_exp_f32 v95, v95\n" \
    "v_exp_f32 v112, v112\n" \
    "v_exp_f32 v113, v113\n" \
    "v_smfmac_f32_32x32x32_bf16 v[128:143], v[188:191], v[88:95], v192\n" \
    "v_exp_f32 v114, v114\n" \
    "v_exp_f32 v115, v115\n" \
    "v_exp_f32 v116, v116\n" \
    "v_exp_f32 v117, v117\n" \
    "v_mfma_f32_32x32x16_bf16 v[96:111], v[168:171], v[164:167], 0\n" \
    "ds_read_b128 v[172:175], v193 offset:5120\n" \
    "v_exp_f32 v118, v118\n" \
    "v_exp_f32 v119, v119\n" \
    "v_exp_f32 v120, v120\n" \
    "v_exp_f32 v121, v121\n" \
    "v_smfmac_f32_32x32x32_bf16 v[144:159], v[184:187], v[112:119], v192\n" \
    "v_exp_f32 v122, v122\n" \
    "v_exp_f32 v123, v123\n" \
    "v_exp_f32 v124, v124\n" \
    "global_load_dword v214, v203, %[wdb] offset:2048\n" \
    "global_load_dword v215, v203, %[wdb] offset:3072\n" \
    "v_exp_f32 v125, v125\n" \
    "v_exp_f32 v126, v126\n" \
    "v_exp_f32 v127, v127\n" \
    "v_add_u32 v203, 0x1000, v203\n" \
    "v_add_u32 v193, 4096, v193\n" \
    "v_add_u32 v194, v195, v194\n" \
    "v_exp_f32 v64, v64\n" \
    "v_exp_f32 v65, v65\n" \
    "v_smfmac_f32_32x32x32_bf16 v[144:159], v[188:191], v[120:127], v192\n" \
    "ds_read_b128 v[184:187], v194 offset:1024\n" \
    "v_exp_f32 v66, v66\n" \
    "v_exp_f32 v67, v67\n" \
    "v_exp_f32 v68, v68\n" \
    "v_exp_f32 v69, v69\n" \
    "s_waitcnt lgkmcnt(1)\n" \
    "v_mfma_f32_32x32x16_bf16 v[80:95], v[172:175], v[160:163], 0\n" \
    "ds_read_b128 v[188:191], v194 offset:1536\n" \
    "v_exp_f32 v70, v70\n" \
    "v_exp_f32 v71, v71\n" \
    "v_exp_f32 v72, v72\n" \
    "v_exp_f32 v73, v73\n" \
    "v_smfmac_f32_32x32x32_bf16 v[128:143], v[176:179], v[64:71], v192\n" \
    "v_exp_f32 v74, v74\n" \
    "v_exp_f32 v75, v75\n" \
    "v_exp_f32 v76, v76\n" \
    "global_load_dword v216, v203, %[wdb] offset:0\n" \
    "global_load_dword v217, v203, %[wdb] offset:1024\n" \
    "v_exp_f32 v77, v77\n" \
    "v_exp_f32 v78, v78\n" \
    "v_exp_f32 v79, v79\n" \
    "v_exp_f32 v96, v96\n" \
    "v_exp_f32 v97, v97\n" \
    "v_smfmac_f32_32x32x32_bf16 v[128:143], v[180:183], v[72:79], v192\n" \
    "v_exp_f32 v98, v98\n" \
    "v_exp_f32 v99, v99\n" \
    "v_exp_f32 v100, v100\n" \
    "v_exp_f32 v101, v101\n" \
    "v_mfma_f32_32x32x16_bf16 v[112:127], v[172:175], v[164:167], 0\n" \
    "ds_read_b128 v[168:171], v193 offset:2048\n" \
    "v_exp_f32 v102, v102\n" \
    "v_exp_f32 v103, v103\n" \
    "v_exp_f32 v104, v104\n" \
    "v_exp_f32 v105, v105\n" \
    "v_smfmac_f32_32x32x32_bf16 v[144:159], v[176:179], v[96:103], v192\n" \
    "v_exp_f32 v106, v106\n" \
    "v_exp_f32 v107, v107\n" \
    "v_exp_f32 v108, v108\n" \
    "global_load_dword v218, v203, %[wdb] offset:2048\n" \
    "global_load_dword v219, v203, %[wdb] offset:3072\n" \
    "v_exp_f32 v109, v109\n" \
    "v_exp_f32 v110, v110\n" \
    "v_exp_f32 v111, v111\n" \
    "v_exp_f32 v80, v80\n" \
    "v_exp_f32 v81, v81\n" \
    "v_smfmac_f32_32x32x32_bf16 v[144:159], v[180:183], v[104:111], v192\n" \
    "s_waitcnt vmcnt(12)\n" \
    "ds_read_b64_tr_b16 v[220:221], v228\n" \
    "ds_read_b64_tr_b16 v[222:223], v229\n" \
    "ds_read_b64_tr_b16 v[224:225], v228 offset:256\n" \
    "ds_read_b64_tr_b16 v[226:227], v229 offset:256\n" \
    "ds_read_b128 v[176:179], v194 offset:2048\n" \
    "v_exp_f32 v82, v82\n" \
    "v_exp_f32 v83, v83\n" \
    "v_exp_f32 v84, v84\n" \
    "v_exp_f32 v85, v85\n" \
    "s_waitcnt lgkmcnt(1)\n" \
    "v_mfma_f32_32x32x16_bf16 v[64:79], v[168:171], v[160:163], 0\n" \
    "ds_read_b128 v[180:183], v194 offset:2560\n" \
    "v_exp_f32 v86, v86\n" \
    "v_exp_f32 v87, v87\n" \
    "v_exp_f32 v88, v88\n" \
    "v_exp_f32 v89, v89\n" \
    "v_smfmac_f32_32x32x32_bf16 v[128:143], v[184:187], v[80:87], v192\n" \
    "v_exp_f32 v90, v90\n" \
    "v_exp_f32 v91, v91\n" \
    "v_exp_f32 v92, v92\n" \
    "v_mfma_f32_16x16x32_bf16 v[232:235], v[220:223], v[204:207], 0\n" \
    "v_exp_f32 v93, v93\n" \
    "v_exp_f32 v94, v94\n" \
    "v_exp_f32 v95, v95\n" \
    "v_exp_f32 v112, v112\n" \
    "v_exp_f32 v113, v113\n" \
    "v_smfmac_f32_32x32x32_bf16 v[128:143], v[188:191], v[88:95], v192\n" \
    "v_exp_f32 v114, v114\n" \
    "v_exp_f32 v115, v115\n" \
    "v_exp_f32 v116, v116\n" \
    "v_exp_f32 v117, v117\n" \
    "v_mfma_f32_32x32x16_bf16 v[96:111], v[168:171], v[164:167], 0\n" \
    "ds_read_b128 v[172:175], v193 offset:3072\n" \
    "v_exp_f32 v118, v118\n" \
    "v_exp_f32 v119, v119\n" \
    "v_exp_f32 v120, v120\n" \
    "v_exp_f32 v121, v121\n" \
    "v_smfmac_f32_32x32x32_bf16 v[144:159], v[184:187], v[112:119], v192\n" \
    "v_exp_f32 v122, v122\n" \
    "v_exp_f32 v123, v123\n" \
    "v_exp_f32 v124, v124\n" \
    "v_exp_f32 v125, v125\n" \
    "v_exp_f32 v126, v126\n" \
    "v_exp_f32 v127, v127\n" \
    "s_setprio 0\n" \
    "v_exp_f32 v64, v64\n" \
    "v_exp_f32 v65, v65\n" \
    "v_smfmac_f32_32x32x32_bf16 v[144:159], v[188:191], v[120:127], v192\n" \
    "ds_read_b128 v[184:187], v194 offset:3072\n" \
    "v_exp_f32 v66, v66\n" \
    "v_exp_f32 v67, v67\n" \
    "v_exp_f32 v68, v68\n" \
    "v_exp_f32 v69, v69\n" \
    "s_waitcnt lgkmcnt(1)\n" \
    "v_mfma_f32_32x32x16_bf16 v[80:95], v[172:175], v[160:163], 0\n" \
    "ds_read_b128 v[188:191], v194 offset:3584\n" \
    "v_exp_f32 v70, v70\n" \
    "v_exp_f32 v71, v71\n" \
    "v_exp_f32 v72, v72\n" \
    "v_exp_f32 v73, v73\n" \
    "v_smfmac_f32_32x32x32_bf16 v[128:143], v[176:179], v[64:71], v192\n" \
    "v_exp_f32 v74, v74\n" \
    "v_exp_f32 v75, v75\n" \
    "v_exp_f32 v76, v76\n" \
    "v_mfma_f32_16x16x32_bf16 v[232:235], v[224:227], v[208:211], v[232:235]\n" \
    "v_exp_f32 v77, v77\n" \
    "v_exp_f32 v78, v78\n" \
    "v_exp_f32 v79, v79\n" \
    "v_exp_f32 v96, v96\n" \
    "v_exp_f32 v97, v97\n" \
    "v_smfmac_f32_32x32x32_bf16 v[128:143], v[180:183], v[72:79], v192\n" \
    "v_exp_f32 v98, v98\n" \
    "v_exp_f32 v99, v99\n" \
    "v_exp_f32 v100, v100\n" \
    "v_exp_f32 v101, v101\n" \
    "v_mfma_f32_32x32x16_bf16 v[112:127], v[172:175], v[164:167], 0\n" \
    "ds_read_b128 v[168:171], v193 offset:4096\n" \
    "v_exp_f32 v102, v102\n" \
    "v_exp_f32 v103, v103\n" \
    "v_exp_f32 v104, v104\n" \
    "v_exp_f32 v105, v105\n" \
    "v_smfmac_f32_32x32x32_bf16 v[144:159], v[176:179], v[96:103], v192\n" \
    "v_exp_f32 v106, v106\n" \
    "v_exp_f32 v107, v107\n" \
    "v_exp_f32 v108, v108\n" \
    "v_exp_f32 v109, v109\n" \
    "v_exp_f32 v110, v110\n" \
    "v_exp_f32 v111, v111\n" \
    "v_exp_f32 v80, v80\n" \
    "v_exp_f32 v81, v81\n" \
    "v_smfmac_f32_32x32x32_bf16 v[144:159], v[180:183], v[104:111], v192\n" \
    "ds_write_b128 v230, v[232:235]\n" \
    "ds_read_b128 v[176:179], v194 offset:4096\n" \
    "v_exp_f32 v82, v82\n" \
    "v_exp_f32 v83, v83\n" \
    "v_exp_f32 v84, v84\n" \
    "v_exp_f32 v85, v85\n" \
    "s_waitcnt lgkmcnt(1)\n" \
    "v_mfma_f32_32x32x16_bf16 v[64:79], v[168:171], v[160:163], 0\n" \
    "ds_read_b128 v[180:183], v194 offset:4608\n" \
    "v_exp_f32 v86, v86\n" \
    "v_exp_f32 v87, v87\n" \
    "v_exp_f32 v88, v88\n" \
    "v_exp_f32 v89, v89\n" \
    "v_smfmac_f32_32x32x32_bf16 v[128:143], v[184:187], v[80:87], v192\n" \
    "v_exp_f32 v90, v90\n" \
    "v_exp_f32 v91, v91\n" \
    "v_exp_f32 v92, v92\n" \
    "v_exp_f32 v93, v93\n" \
    "v_exp_f32 v94, v94\n" \
    "v_exp_f32 v95, v95\n" \
    "v_exp_f32 v112, v112\n" \
    "v_exp_f32 v113, v113\n" \
    "v_smfmac_f32_32x32x32_bf16 v[128:143], v[188:191], v[88:95], v192\n" \
    "v_exp_f32 v114, v114\n" \
    "v_exp_f32 v115, v115\n" \
    "v_exp_f32 v116, v116\n" \
    "v_exp_f32 v117, v117\n" \
    "v_mfma_f32_32x32x16_bf16 v[96:111], v[168:171], v[164:167], 0\n" \
    "ds_read_b128 v[172:175], v193 offset:5120\n" \
    "v_exp_f32 v118, v118\n" \
    "v_exp_f32 v119, v119\n" \
    "v_exp_f32 v120, v120\n" \
    "v_exp_f32 v121, v121\n" \
    "v_smfmac_f32_32x32x32_bf16 v[144:159], v[184:187], v[112:119], v192\n" \
    "v_exp_f32 v122, v122\n" \
    "v_exp_f32 v123, v123\n" \
    "v_exp_f32 v124, v124\n" \
    "v_exp_f32 v125, v125\n" \
    "v_exp_f32 v126, v126\n" \
    "v_exp_f32 v127, v127\n" \
    "v_add_u32 v193, 4096, v193\n" \
    "v_add_u32 v194, v195, v194\n" \
    "s_waitcnt vmcnt(0)\n"

#define ATT_C \
    "s_mov_b32 %[cnt], 2\n" \
    "1:\n" \
    "v_exp_f32 v64, v64\n" \
    "v_exp_f32 v65, v65\n" \
    "v_smfmac_f32_32x32x32_bf16 v[144:159], v[188:191], v[120:127], v192\n" \
    "ds_read_b128 v[184:187], v194 offset:1024\n" \
    "v_exp_f32 v66, v66\n" \
    "v_exp_f32 v67, v67\n" \
    "v_exp_f32 v68, v68\n" \
    "v_exp_f32 v69, v69\n" \
    "s_waitcnt lgkmcnt(1)\n" \
    "v_mfma_f32_32x32x16_bf16 v[80:95], v[172:175], v[160:163], 0\n" \
    "ds_read_b128 v[188:191], v194 offset:1536\n" \
    "v_exp_f32 v70, v70\n" \
    "v_exp_f32 v71, v71\n" \
    "v_exp_f32 v72, v72\n" \
    "v_exp_f32 v73, v73\n" \
    "v_smfmac_f32_32x32x32_bf16 v[128:143], v[176:179], v[64:71], v192\n" \
    "v_exp_f32 v74, v74\n" \
    "s_mov_b32 m0, %[swo]\n" \
    "v_exp_f32 v75, v75\n" \
    "v_exp_f32 v76, v76\n" \
    "global_load_lds_dwordx4 v236, %[wob] offset:0\n" \
    "v_exp_f32 v77, v77\n" \
    "v_exp_f32 v78, v78\n" \
    "v_exp_f32 v79, v79\n" \
    "v_exp_f32 v96, v96\n" \
    "v_exp_f32 v97, v97\n" \
    "v_smfmac_f32_32x32x32_bf16 v[128:143], v[180:183], v[72:79], v192\n" \
    "v_exp_f32 v98, v98\n" \
    "v_exp_f32 v99, v99\n" \
    "v_exp_f32 v100, v100\n" \
    "v_exp_f32 v101, v101\n" \
    "v_mfma_f32_32x32x16_bf16 v[112:127], v[172:175], v[164:167], 0\n" \
    "ds_read_b128 v[168:171], v193 offset:2048\n" \
    "v_exp_f32 v102, v102\n" \
    "v_exp_f32 v103, v103\n" \
    "v_exp_f32 v104, v104\n" \
    "v_exp_f32 v105, v105\n" \
    "v_smfmac_f32_32x32x32_bf16 v[144:159], v[176:179], v[96:103], v192\n" \
    "v_exp_f32 v106, v106\n" \
    "v_exp_f32 v107, v107\n" \
    "v_exp_f32 v108, v108\n" \
    "v_exp_f32 v109, v109\n" \
    "v_exp_f32 v110, v110\n" \
    "v_exp_f32 v111, v111\n" \
    "v_exp_f32 v80, v80\n" \
    "v_exp_f32 v81, v81\n" \
    "v_smfmac_f32_32x32x32_bf16 v[144:159], v[180:183], v[104:111], v192\n" \
    "ds_read_b128 v[176:179], v194 offset:2048\n" \
    "v_exp_f32 v82, v82\n" \
    "v_exp_f32 v83, v83\n" \
    "v_exp_f32 v84, v84\n" \
    "v_exp_f32 v85, v85\n" \
    "s_waitcnt lgkmcnt(1)\n" \
    "v_mfma_f32_32x32x16_bf16 v[64:79], v[168:171], v[160:163], 0\n" \
    "ds_read_b128 v[180:183], v194 offset:2560\n" \
    "v_exp_f32 v86, v86\n" \
    "v_exp_f32 v87, v87\n" \
    "v_exp_f32 v88, v88\n" \
    "v_exp_f32 v89, v89\n" \
    "v_smfmac_f32_32x32x32_bf16 v[128:143], v[184:187], v[80:87], v192\n" \
    "v_exp_f32 v90, v90\n" \
    "v_exp_f32 v91, v91\n" \
    "v_exp_f32 v92, v92\n" \
    "global_load_lds_dwordx4 v237, %[wob] offset:1024\n" \
    "v_exp_f32 v93, v93\n" \
    "v_exp_f32 v94, v94\n" \
    "v_exp_f32 v95, v95\n" \
    "v_exp_f32 v112, v112\n" \
    "v_exp_f32 v113, v113\n" \
    "v_smfmac_f32_32x32x32_bf16 v[128:143], v[188:191], v[88:95], v192\n" \
    "v_exp_f32 v114, v114\n" \
    "v_exp_f32 v115, v115\n" \
    "v_exp_f32 v116, v116\n" \
    "v_exp_f32 v117, v117\n" \
    "v_mfma_f32_32x32x16_bf16 v[96:111], v[168:171], v[164:167], 0\n" \
    "ds_read_b128 v[172:175], v193 offset:3072\n" \
    "v_exp_f32 v118, v118\n" \
    "v_exp_f32 v119, v119\n" \
    "v_exp_f32 v120, v120\n" \
    "v_exp_f32 v121, v121\n" \
    "v_smfmac_f32_32x32x32_bf16 v[144:159], v[184:187], v[112:119], v192\n" \
    "v_exp_f32 v122, v122\n" \
    "v_exp_f32 v123, v123\n" \
    "v_exp_f32 v124, v124\n" \
    "v_exp_f32 v125, v125\n" \
    "v_exp_f32 v126, v126\n" \
    "v_exp_f32 v127, v127\n" \
    "v_exp_f32 v64, v64\n" \
    "v_exp_f32 v65, v65\n" \
    "v_smfmac_f32_32x32x32_bf16 v[144:159], v[188:191], v[120:127], v192\n" \
    "ds_read_b128 v[184:187], v194 offset:3072\n" \
    "v_exp_f32 v66, v66\n" \
    "v_exp_f32 v67, v67\n" \
    "v_exp_f32 v68, v68\n" \
    "v_exp_f32 v69, v69\n" \
    "s_waitcnt lgkmcnt(1)\n" \
    "v_mfma_f32_32x32x16_bf16 v[80:95], v[172:175], v[160:163], 0\n" \
    "ds_read_b128 v[188:191], v194 offset:3584\n" \
    "v_exp_f32 v70, v70\n" \
    "v_exp_f32 v71, v71\n" \
    "v_exp_f32 v72, v72\n" \
    "v_exp_f32 v73, v73\n" \
    "v_smfmac_f32_32x32x32_bf16 v[128:143], v[176:179], v[64:71], v192\n" \
    "v_exp_f32 v74, v74\n" \
    "v_exp_f32 v75, v75\n" \
    "v_exp_f32 v76, v76\n" \
    "global_load_lds_dwordx4 v238, %[wob] offset:2048\n" \
    "v_exp_f32 v77, v77\n" \
    "v_exp_f32 v78, v78\n" \
    "v_exp_f32 v79, v79\n" \
    "v_exp_f32 v96, v96\n" \
    "v_exp_f32 v97, v97\n" \
    "v_smfmac_f32_32x32x32_bf16 v[128:143], v[180:183], v[72:79], v192\n" \
    "v_exp_f32 v98, v98\n" \
    "v_exp_f32 v99, v99\n" \
    "v_exp_f32 v100, v100\n" \
    "v_exp_f32 v101, v101\n" \
    "v_mfma_f32_32x32x16_bf16 v[112:127], v[172:175], v[164:167], 0\n" \
    "ds_read_b128 v[168:171], v193 offset:4096\n" \
    "v_exp_f32 v102, v102\n" \
    "v_exp_f32 v103, v103\n" \
    "v_exp_f32 v104, v104\n" \
    "v_exp_f32 v105, v105\n" \
    "v_smfmac_f32_32x32x32_bf16 v[144:159], v[176:179], v[96:103], v192\n" \
    "v_exp_f32 v106, v106\n" \
    "v_exp_f32 v107, v107\n" \
    "v_exp_f32 v108, v108\n" \
    "v_exp_f32 v109, v109\n" \
    "v_exp_f32 v110, v110\n" \
    "v_exp_f32 v111, v111\n" \
    "v_exp_f32 v80, v80\n" \
    "v_exp_f32 v81, v81\n" \
    "v_smfmac_f32_32x32x32_bf16 v[144:159], v[180:183], v[104:111], v192\n" \
    "ds_read_b128 v[176:179], v194 offset:4096\n" \
    "v_exp_f32 v82, v82\n" \
    "v_exp_f32 v83, v83\n" \
    "v_exp_f32 v84, v84\n" \
    "v_exp_f32 v85, v85\n" \
    "s_waitcnt lgkmcnt(1)\n" \
    "v_mfma_f32_32x32x16_bf16 v[64:79], v[168:171], v[160:163], 0\n" \
    "ds_read_b128 v[180:183], v194 offset:4608\n" \
    "v_exp_f32 v86, v86\n" \
    "v_exp_f32 v87, v87\n" \
    "v_exp_f32 v88, v88\n" \
    "v_exp_f32 v89, v89\n" \
    "v_smfmac_f32_32x32x32_bf16 v[128:143], v[184:187], v[80:87], v192\n" \
    "v_exp_f32 v90, v90\n" \
    "v_exp_f32 v91, v91\n" \
    "v_exp_f32 v92, v92\n" \
    "global_load_lds_dwordx4 v239, %[wob] offset:3072\n" \
    "v_exp_f32 v93, v93\n" \
    "v_exp_f32 v94, v94\n" \
    "v_exp_f32 v95, v95\n" \
    "v_exp_f32 v112, v112\n" \
    "v_exp_f32 v113, v113\n" \
    "v_smfmac_f32_32x32x32_bf16 v[128:143], v[188:191], v[88:95], v192\n" \
    "v_exp_f32 v114, v114\n" \
    "v_exp_f32 v115, v115\n" \
    "v_exp_f32 v116, v116\n" \
    "v_exp_f32 v117, v117\n" \
    "v_mfma_f32_32x32x16_bf16 v[96:111], v[168:171], v[164:167], 0\n" \
    "ds_read_b128 v[172:175], v193 offset:5120\n" \
    "v_exp_f32 v118, v118\n" \
    "v_exp_f32 v119, v119\n" \
    "v_exp_f32 v120, v120\n" \
    "v_exp_f32 v121, v121\n" \
    "v_smfmac_f32_32x32x32_bf16 v[144:159], v[184:187], v[112:119], v192\n" \
    "v_exp_f32 v122, v122\n" \
    "v_exp_f32 v123, v123\n" \
    "v_exp_f32 v124, v124\n" \
    "v_exp_f32 v125, v125\n" \
    "v_exp_f32 v126, v126\n" \
    "v_exp_f32 v127, v127\n" \
    "v_add_u32 v236, 0x8000, v236\n" \
    "v_add_u32 v237, 0x8000, v237\n" \
    "v_add_u32 v238, 0x8000, v238\n" \
    "v_add_u32 v239, 0x8000, v239\n" \
    "s_add_u32 %[swo], %[swo], 0x1000\n" \
    "v_add_u32 v193, 4096, v193\n" \
    "v_add_u32 v194, v195, v194\n" \
    "s_sub_u32 %[cnt], %[cnt], 1\n" \
    "s_cmp_lg_u32 %[cnt], 0\n" \
    "s_cbranch_scc1 1b\n" \
    "global_load_dword v220, v221, %[wsb] sc1\n" \
    "s_mov_b32 %[cnt], 2\n" \
    "2:\n" \
    "v_exp_f32 v64, v64\n" \
    "v_exp_f32 v65, v65\n" \
    "v_smfmac_f32_32x32x32_bf16 v[144:159], v[188:191], v[120:127], v192\n" \
    "ds_read_b128 v[184:187], v194 offset:1024\n" \
    "v_exp_f32 v66, v66\n" \
    "v_exp_f32 v67, v67\n" \
    "v_exp_f32 v68, v68\n" \
    "v_exp_f32 v69, v69\n" \
    "s_waitcnt lgkmcnt(1)\n" \
    "v_mfma_f32_32x32x16_bf16 v[80:95], v[172:175], v[160:163], 0\n" \
    "ds_read_b128 v[188:191], v194 offset:1536\n" \
    "v_exp_f32 v70, v70\n" \
    "v_exp_f32 v71, v71\n" \
    "v_exp_f32 v72, v72\n" \
    "v_exp_f32 v73, v73\n" \
    "v_smfmac_f32_32x32x32_bf16 v[128:143], v[176:179], v[64:71], v192\n" \
    "v_exp_f32 v74, v74\n" \
    "v_exp_f32 v75, v75\n" \
    "v_exp_f32 v76, v76\n" \
    "v_exp_f32 v77, v77\n" \
    "v_exp_f32 v78, v78\n" \
    "v_exp_f32 v79, v79\n" \
    "v_exp_f32 v96, v96\n" \
    "v_exp_f32 v97, v97\n" \
    "v_smfmac_f32_32x32x32_bf16 v[128:143], v[180:183], v[72:79], v192\n" \
    "v_exp_f32 v98, v98\n" \
    "v_exp_f32 v99, v99\n" \
    "v_exp_f32 v100, v100\n" \
    "v_exp_f32 v101, v101\n" \
    "v_mfma_f32_32x32x16_bf16 v[112:127], v[172:175], v[164:167], 0\n" \
    "ds_read_b128 v[168:171], v193 offset:2048\n" \
    "v_exp_f32 v102, v102\n" \
    "v_exp_f32 v103, v103\n" \
    "v_exp_f32 v104, v104\n" \
    "v_exp_f32 v105, v105\n" \
    "v_smfmac_f32_32x32x32_bf16 v[144:159], v[176:179], v[96:103], v192\n" \
    "v_exp_f32 v106, v106\n" \
    "v_exp_f32 v107, v107\n" \
    "v_exp_f32 v108, v108\n" \
    "v_exp_f32 v109, v109\n" \
    "v_exp_f32 v110, v110\n" \
    "v_exp_f32 v111, v111\n" \
    "v_exp_f32 v80, v80\n" \
    "v_exp_f32 v81, v81\n" \
    "v_smfmac_f32_32x32x32_bf16 v[144:159], v[180:183], v[104:111], v192\n" \
    "ds_read_b128 v[176:179], v194 offset:2048\n" \
    "v_exp_f32 v82, v82\n" \
    "v_exp_f32 v83, v83\n" \
    "v_exp_f32 v84, v84\n" \
    "v_exp_f32 v85, v85\n" \
    "s_waitcnt lgkmcnt(1)\n" \
    "v_mfma_f32_32x32x16_bf16 v[64:79], v[168:171], v[160:163], 0\n" \
    "ds_read_b128 v[180:183], v194 offset:2560\n" \
    "v_exp_f32 v86, v86\n" \
    "v_exp_f32 v87, v87\n" \
    "v_exp_f32 v88, v88\n" \
    "v_exp_f32 v89, v89\n" \
    "v_smfmac_f32_32x32x32_bf16 v[128:143], v[184:187], v[80:87], v192\n" \
    "v_exp_f32 v90, v90\n" \
    "v_exp_f32 v91, v91\n" \
    "v_exp_f32 v92, v92\n" \
    "v_exp_f32 v93, v93\n" \
    "v_exp_f32 v94, v94\n" \
    "v_exp_f32 v95, v95\n" \
    "v_exp_f32 v112, v112\n" \
    "v_exp_f32 v113, v113\n" \
    "v_smfmac_f32_32x32x32_bf16 v[128:143], v[188:191], v[88:95], v192\n" \
    "v_exp_f32 v114, v114\n" \
    "v_exp_f32 v115, v115\n" \
    "v_exp_f32 v116, v116\n" \
    "v_exp_f32 v117, v117\n" \
    "v_mfma_f32_32x32x16_bf16 v[96:111], v[168:171], v[164:167], 0\n" \
    "ds_read_b128 v[172:175], v193 offset:3072\n" \
    "v_exp_f32 v118, v118\n" \
    "v_exp_f32 v119, v119\n" \
    "v_exp_f32 v120, v120\n" \
    "v_exp_f32 v121, v121\n" \
    "v_smfmac_f32_32x32x32_bf16 v[144:159], v[184:187], v[112:119], v192\n" \
    "v_exp_f32 v122, v122\n" \
    "v_exp_f32 v123, v123\n" \
    "v_exp_f32 v124, v124\n" \
    "v_exp_f32 v125, v125\n" \
    "v_exp_f32 v126, v126\n" \
    "v_exp_f32 v127, v127\n" \
    "v_exp_f32 v64, v64\n" \
    "v_exp_f32 v65, v65\n" \
    "v_smfmac_f32_32x32x32_bf16 v[144:159], v[188:191], v[120:127], v192\n" \
    "ds_read_b128 v[184:187], v194 offset:3072\n" \
    "v_exp_f32 v66, v66\n" \
    "v_exp_f32 v67, v67\n" \
    "v_exp_f32 v68, v68\n" \
    "v_exp_f32 v69, v69\n" \
    "s_waitcnt lgkmcnt(1)\n" \
    "v_mfma_f32_32x32x16_bf16 v[80:95], v[172:175], v[160:163], 0\n" \
    "ds_read_b128 v[188:191], v194 offset:3584\n" \
    "v_exp_f32 v70, v70\n" \
    "v_exp_f32 v71, v71\n" \
    "v_exp_f32 v72, v72\n" \
    "v_exp_f32 v73, v73\n" \
    "v_smfmac_f32_32x32x32_bf16 v[128:143], v[176:179], v[64:71], v192\n" \
    "v_exp_f32 v74, v74\n" \
    "v_exp_f32 v75, v75\n" \
    "v_exp_f32 v76, v76\n" \
    "v_exp_f32 v77, v77\n" \
    "v_exp_f32 v78, v78\n" \
    "v_exp_f32 v79, v79\n" \
    "v_exp_f32 v96, v96\n" \
    "v_exp_f32 v97, v97\n" \
    "v_smfmac_f32_32x32x32_bf16 v[128:143], v[180:183], v[72:79], v192\n" \
    "v_exp_f32 v98, v98\n" \
    "v_exp_f32 v99, v99\n" \
    "v_exp_f32 v100, v100\n" \
    "v_exp_f32 v101, v101\n" \
    "v_mfma_f32_32x32x16_bf16 v[112:127], v[172:175], v[164:167], 0\n" \
    "ds_read_b128 v[168:171], v193 offset:4096\n" \
    "v_exp_f32 v102, v102\n" \
    "v_exp_f32 v103, v103\n" \
    "v_exp_f32 v104, v104\n" \
    "v_exp_f32 v105, v105\n" \
    "v_smfmac_f32_32x32x32_bf16 v[144:159], v[176:179], v[96:103], v192\n" \
    "v_exp_f32 v106, v106\n" \
    "v_exp_f32 v107, v107\n" \
    "v_exp_f32 v108, v108\n" \
    "v_exp_f32 v109, v109\n" \
    "v_exp_f32 v110, v110\n" \
    "v_exp_f32 v111, v111\n" \
    "v_exp_f32 v80, v80\n" \
    "v_exp_f32 v81, v81\n" \
    "v_smfmac_f32_32x32x32_bf16 v[144:159], v[180:183], v[104:111], v192\n" \
    "ds_read_b128 v[176:179], v194 offset:4096\n" \
    "v_exp_f32 v82, v82\n" \
    "v_exp_f32 v83, v83\n" \
    "v_exp_f32 v84, v84\n" \
    "v_exp_f32 v85, v85\n" \
    "s_waitcnt lgkmcnt(1)\n" \
    "v_mfma_f32_32x32x16_bf16 v[64:79], v[168:171], v[160:163], 0\n" \
    "ds_read_b128 v[180:183], v194 offset:4608\n" \
    "v_exp_f32 v86, v86\n" \
    "v_exp_f32 v87, v87\n" \
    "v_exp_f32 v88, v88\n" \
    "v_exp_f32 v89, v89\n" \
    "v_smfmac_f32_32x32x32_bf16 v[128:143], v[184:187], v[80:87], v192\n" \
    "v_exp_f32 v90, v90\n" \
    "v_exp_f32 v91, v91\n" \
    "v_exp_f32 v92, v92\n" \
    "v_exp_f32 v93, v93\n" \
    "v_exp_f32 v94, v94\n" \
    "v_exp_f32 v95, v95\n" \
    "v_exp_f32 v112, v112\n" \
    "v_exp_f32 v113, v113\n" \
    "v_smfmac_f32_32x32x32_bf16 v[128:143], v[188:191], v[88:95], v192\n" \
    "v_exp_f32 v114, v114\n" \
    "v_exp_f32 v115, v115\n" \
    "v_exp_f32 v116, v116\n" \
    "v_exp_f32 v117, v117\n" \
    "v_mfma_f32_32x32x16_bf16 v[96:111], v[168:171], v[164:167], 0\n" \
    "ds_read_b128 v[172:175], v193 offset:5120\n" \
    "v_exp_f32 v118, v118\n" \
    "v_exp_f32 v119, v119\n" \
    "v_exp_f32 v120, v120\n" \
    "v_exp_f32 v121, v121\n" \
    "v_smfmac_f32_32x32x32_bf16 v[144:159], v[184:187], v[112:119], v192\n" \
    "v_exp_f32 v122, v122\n" \
    "v_exp_f32 v123, v123\n" \
    "v_exp_f32 v124, v124\n" \
    "v_exp_f32 v125, v125\n" \
    "v_exp_f32 v126, v126\n" \
    "v_exp_f32 v127, v127\n" \
    "s_cmp_eq_u32 %[cnt], 2\n" \
    "s_cbranch_scc0 8f\n" \
    "s_setprio 0\n" \
    "8:\n" \
    "v_add_u32 v193, 4096, v193\n" \
    "v_add_u32 v194, v195, v194\n" \
    "s_sub_u32 %[cnt], %[cnt], 1\n" \
    "s_cmp_lg_u32 %[cnt], 0\n" \
    "s_cbranch_scc1 2b\n" \
    "s_nop 1\n" \
    "v_smfmac_f32_32x32x32_bf16 v[144:159], v[188:191], v[120:127], v192\n" \
    "s_setprio 0\n" \
    "s_waitcnt vmcnt(0)\n" \
    "s_nop 7\n" \
    "s_nop 7\n" \
    "s_nop 3\n" \
    "s_waitcnt lgkmcnt(0)\n"

__device__ __forceinline__ void slow_block(const char* ws, u32x4 q, int lane, float* o, float& lsum) {
    const u32 koff = WS_K + lane * 16;
    const u32 r32 = lane & 31;
    float m = -3.0e38f;
    for (int t = 0; t < 64; ++t) {
        u32x4 kf;
        GLD128C(kf, ws, koff + t * 1024, 0);
        asm volatile("s_waitcnt vmcnt(0)" : "+v"(kf) :: "memory");
        f32x16 s = {0};
        s = __builtin_amdgcn_mfma_f32_32x32x16_bf16(asbf(kf), asbf(q), s, 0, 0, 0);
#pragma unroll
        for (int i = 0; i < 16; ++i) m = fmaxf(m, s[i]);
    }
    {
        auto sw = __builtin_amdgcn_permlane32_swap(__float_as_uint(m), __float_as_uint(m), false, false);
        m = fmaxf(__uint_as_float(sw[0]), __uint_as_float(sw[1]));
    }
    f32x16 acc = {0};
    const u32 voff = WS_V + (lane >> 5) * 256 + (lane & 15) * 16;
    for (int t = 0; t < 64; ++t) {
        u32x4 kf, v0, v1;
        GLD128C(kf, ws, koff + t * 1024, 0);
        GLD128C(v0, ws, voff + t * 1024, 0);
        GLD128C(v1, ws, voff + t * 1024, 512);
        asm volatile("s_waitcnt vmcnt(0)" : "+v"(kf), "+v"(v0), "+v"(v1) :: "memory");
        if (r32 >= 16) { v0 = (u32x4){0x3f803f80u, 0x3f803f80u, 0x3f803f80u, 0x3f803f80u}; v1 = v0; }
        f32x16 s = {0};
        s = __builtin_amdgcn_mfma_f32_32x32x16_bf16(asbf(kf), asbf(q), s, 0, 0, 0);
        u32 p[8];
#pragma unroll
        for (int i = 0; i < 8; ++i)
            p[i] = pk(__builtin_amdgcn_exp2f(s[2 * i] - m), __builtin_amdgcn_exp2f(s[2 * i + 1] - m));
#pragma unroll
        for (int st = 0; st < 2; ++st) {
            u32x4 vv = st ? v1 : v0;
            auto a = __builtin_amdgcn_permlane32_swap(vv[0], vv[2], false, false);
            auto c = __builtin_amdgcn_permlane32_swap(vv[1], vv[3], false, false);
            u32x4 af = {a[0], c[0], a[1], c[1]};
            u32x4 bf = {p[4 * st], p[4 * st + 1], p[4 * st + 2], p[4 * st + 3]};
            acc = __builtin_amdgcn_mfma_f32_32x32x16_bf16(asbf(af), asbf(bf), acc, 0, 0, 0);
        }
    }
#pragma unroll
    for (int i = 0; i < 8; ++i) o[i] = acc[i];
    lsum = acc[8];
}

#define PRIO_MODE 1
#define PRIO_START() do { if (w >= 4) __builtin_amdgcn_s_setprio(1); } while (0)
#define PRIO_END() __builtin_amdgcn_s_setprio(0)
__global__ void __launch_bounds__(512, 2)
fused_kernel(const u16* x_, const u16* wq_, const u16* wk_, const u16* wv_, const u16* wo_, const u16* wr_, const u16* wg_,
             const u16* wu_, const u16* wd_, const u16* gw_, u16* out_, char* ws_)
{
    u32x16 ka0; u32x8 ka1;
    {
        const void* kp = (const void*)__builtin_amdgcn_kernarg_segment_ptr();
        asm volatile("s_load_dwordx16 %0, %2, 0\n\ts_load_dwordx8 %1, %2, 64\n\ts_waitcnt lgkmcnt(0)"
                     : "=&s"(ka0), "=&s"(ka1) : "s"(kp) : "memory");
    }
#define KP(v, i) ((u64)v[2 * (i)] | ((u64)v[2 * (i) + 1] << 32))
    const u16* x  = (const u16*)KP(ka0, 0); const u16* wq = (const u16*)KP(ka0, 1);
    const u16* wk = (const u16*)KP(ka0, 2); const u16* wv = (const u16*)KP(ka0, 3);
    const u16* wo = (const u16*)KP(ka0, 4); const u16* wr = (const u16*)KP(ka0, 5);
    const u16* wg = (const u16*)KP(ka0, 6); const u16* wu = (const u16*)KP(ka0, 7);
    const u16* wd = (const u16*)KP(ka1, 0); const u16* gw = (const u16*)KP(ka1, 1);
    u16* out = (u16*)KP(ka1, 2); char* ws = (char*)KP(ka1, 3);

    __shared__ __attribute__((aligned(1024))) char smem[L_END];
    LDSP char* lds = (LDSP char*)smem;
    const u32 lb = (u32)(size_t)lds;
    const int tid = threadIdx.x;
    const int lane = tid & 63;
    const int w = __builtin_amdgcn_readfirstlane(tid >> 6);
    const int b = blockIdx.x;
    const int xcd = b & 7, jj = b >> 3;
    const int tb = xcd * 4 + (jj >> 3), hg = jj & 7;
    const int g = lane >> 4, l15 = lane & 15, h = lane >> 5, l31 = lane & 31;
    const bool isK = hg < 4;
    const bool ffn = jj < 28;
    const int fj = xcd * 28 + min(jj, 27);
    STAMP(1); STAMPC(18);

    const int th = w & 1, hp = (w >> 1) & 1, blk = w >> 2;
    const int T0 = tb * 64 + (hg & 3) * 16;
    u32x4 xf0, xf1, vf0, vf1;
    {
        const u16* wsrc = isK ? wk : wv;
#pragma unroll
        for (int i = 0; i < 2; ++i) {
            u32 slot = 64 * i + lane, pos = slot >> 3, B = pos ^ ((pos >> 2) & 1);
            u32 row = 4 * B + ((slot >> 1) & 3);
            dma16(wsrc, (64 * w + row) * 32 + (slot & 1) * 16, lb + L_W + w * 2048 + i * 1024);
        }
        u32 xo = (T0 + l15) * 1024 + (64 * w + 16 * g) * 2;
        GLD128(xf0, x, xo, 0);
        GLD128(xf1, x, xo, 16);
        const u16* vec = isK ? wr : gw;
        u32 vo = (64 * w + 16 * g) * 2;
        GLD128(vf0, vec, vo, 0);
        GLD128(vf1, vec, vo, 16);
    }
    u32 epo;
    { u32 z = 0; GLD32C(epo, ws, z, 0); }
    asm volatile("s_barrier" ::: "memory");
    if (w < 7) {
#pragma unroll
        for (int i = 0; i < 19; ++i) {
            const int p = i < 18 ? w + 7 * i : __builtin_amdgcn_readfirstlane(min(w + 7 * i, 127));
            if (i < 9 || (i == 9 && w == 0)) {
                dma16(x, (tb * 64 + p) * 1024 + ((lane ^ (p & 15)) << 4), lb + L_K + p * 1024);
            } else {
                const int G = p - 64;
                dma16(wq, (8 * G + ((lane >> 2) & 7)) * 1024 + hg * 128 + (lane >> 5) * 64 + (lane & 3) * 16, lb + L_V + G * 1024);
            }
        }
    }
    if (tid < 256) { u32 o1 = (tid & 15) == 0 ? 0x3f803f80u : 0u; ST128(L_ONE + tid * 16, ((u32x4){o1, o1, o1, o1})); }
    asm volatile("s_cmp_eq_u32 %[w], 7\n\ts_cbranch_scc1 1f\n\ts_waitcnt vmcnt(20)\n\ts_branch 2f\n1:\n\ts_waitcnt vmcnt(1)\n2:"
                 : "+v"(xf0), "+v"(xf1), "+v"(vf0), "+v"(vf1) : [w] "s"(w) : "memory", "scc");
    FSTAMP(2);
    {
        const u32 ta = L_W + w * 2048 + l15 * 8;
        const u32 gx = g & 1;
        u32x4 wf0 = cat(trread(lds, ta + ((4 * g + 0) ^ gx) * 128), trread(lds, ta + ((4 * g + 1) ^ gx) * 128));
        u32x4 wf1 = cat(trread(lds, ta + ((4 * g + 2) ^ gx) * 128), trread(lds, ta + ((4 * g + 3) ^ gx) * 128));
        f32x4 acc = {0, 0, 0, 0}, dacc = {0, 0, 0, 0};
        if (isK) {
            acc = __builtin_amdgcn_mfma_f32_16x16x32_bf16(asbf(wf0), asbf(xf0), acc, 0, 0, 0);
            acc = __builtin_amdgcn_mfma_f32_16x16x32_bf16(asbf(wf1), asbf(xf1), acc, 0, 0, 0);
        } else {
            acc = __builtin_amdgcn_mfma_f32_16x16x32_bf16(asbf(xf0), asbf(wf0), acc, 0, 0, 0);
            acc = __builtin_amdgcn_mfma_f32_16x16x32_bf16(asbf(xf1), asbf(wf1), acc, 0, 0, 0);
        }
        dacc = __builtin_amdgcn_mfma_f32_16x16x32_bf16(asbf(vf0), asbf(xf0), dacc, 0, 0, 0);
        dacc = __builtin_amdgcn_mfma_f32_16x16x32_bf16(asbf(vf1), asbf(xf1), dacc, 0, 0, 0);
        ST128(L_P1 + w * 1024 + lane * 16, __builtin_bit_cast(u32x4, acc));
        if (g == 0) STF(L_PD + w * 64 + l15 * 4, dacc[0]);
    }
    BAR();
    FSTAMP(3);
    asm volatile("s_cmp_eq_u32 %[w], 7\n\ts_cbranch_scc1 1f\n\ts_waitcnt vmcnt(19)\n\ts_branch 2f\n1:\n\ts_waitcnt vmcnt(0)\n2:"
                 : "+v"(epo) : [w] "s"(w) : "memory", "scc");
    const u32 epoch = (u32)__builtin_amdgcn_readfirstlane((int)epo) + 1u;
    if (w == 7) {
        f32x4 ps[8]; float pd[8];
#pragma unroll
        for (int i = 0; i < 8; ++i) {
            ps[i] = __builtin_bit_cast(f32x4, LD128(L_P1 + i * 1024 + lane * 16));
            pd[i] = LDF(L_PD + i * 64 + l15 * 4);
        }
        __builtin_amdgcn_sched_barrier(0);
        const f32x4 s = ((ps[0] + ps[1]) + (ps[2] + ps[3])) + ((ps[4] + ps[5]) + (ps[6] + ps[7]));
        const float d = ((pd[0] + pd[1]) + (pd[2] + pd[3])) + ((pd[4] + pd[5]) + (pd[6] + pd[7]));
        u32x2 pv = {pk(s[0], s[1]), pk(s[2], s[3])};
        const int tile = T0 >> 5, sub = (T0 >> 4) & 1;
        u32 key = 0;
        if (isK) {
            u32 off = WS_K + tile * 1024 + ((g & 1) * 32 + sub * 16 + l15) * 16 + (g >> 1) * 8;
            GST64C(ws, off, pv);
            u32 rb = pk(d, d) & 0xffffu;
            u32 ord = (rb & 0x8000u) ? (~rb & 0xffffu) : (rb | 0x8000u);
            key = wave_max_u32((ord << 16) | (0xffffu - (u32)(T0 + l15)));
        } else {
            u32 off = WS_V + tile * 1024 + sub * 512 + (g >> 1) * 256 + l15 * 16 + (g & 1) * 8;
            GST64C(ws, off, pv);
            if (g == 0) { u32 go = WS_G + (T0 + l15) * 4; u32 dv = __float_as_uint(d); GST32C(ws, go, dv); }
        }
        VMWAIT(0);
        if (lane < 8) { u32x2 fl = {key, epoch}; u32 fo = WS_F1 + lane * 32768 + (tile * 4 + (isK ? 0 : 2) + sub) * 128; GST64C(ws, fo, fl); }
        FSTAMP(4);
    } else {
        VMWAIT(0);
    }
    BAR();
    FSTAMP(5);

    f32x16 qacc = {0};
    if (w < 4) {
        const int qh = w >> 1;
        const int tok = th * 32 + l31;
        u32 abase = L_V + h * 1024 + qh * 512 + ((l15 >> 2) * 64) + ((lane >> 4) & 1) * 32 + (l15 & 3) * 8;
        asm volatile("" : "+v"(abase));
        u32 bb[8];
#pragma unroll
        for (int j = 0; j < 8; ++j) { bb[j] = L_K + tok * 1024 + (((2 * j + h) ^ (tok & 15)) << 4); asm volatile("" : "+v"(bb[j])); }
#pragma unroll
        for (int s = 0; s < 32; ++s) {
            u32x4 af = cat(trread(lds, abase + s * 2048), trread(lds, abase + s * 2048 + 256));
            u32x4 bf = LD128(bb[s & 7] + (s >> 3) * 256);
            qacc = __builtin_amdgcn_mfma_f32_32x32x16_bf16(asbf(af), asbf(bf), qacc, 0, 0, 0);
        }
        FSTAMP(6);
    } else {
        const int j = w - 4;
        const u32 fo = WS_F1 + xcd * 32768 + (j * 64 + lane) * 128;
        u32x2 f;
        u32 spins = 0;
        do {
            asm volatile("global_load_dwordx2 %0, %1, %2 sc1\n\ts_waitcnt vmcnt(0)" : "=&v"(f) : "v"(fo), "s"(ws) : "memory");
        } while ((f[1] != epoch) && ++spins <= SPIN_MAX);
        const u32 key = wave_max_u32(f[0]);
        if (lane == 0) ST32(L_CT + j * 4, key);
        FSTAMP(7);
    }
    BAR();
    FSTAMP(9);
    u32 kvo = (w & 1) * 65536 + ((w >> 1) & 1) * 32768;
    if (w >= 4) {
        dma16c4(ws + WS_K, kvo + lane * 16, lb + L_K + kvo);
        dma16c4(ws + WS_K, kvo + 4096 + lane * 16, lb + L_K + kvo + 4096);
        dma16c2(ws + WS_K, kvo + 8192 + lane * 16, lb + L_K + kvo + 8192);
    } else {
        const float qs = 0.25f * 1.4426950408889634f;
        u32x4 qx, qy;
#pragma unroll
        for (int i = 0; i < 4; ++i) {
            u32 a = pk(qacc[2 * i], qacc[2 * i + 1]), c = pk(qacc[8 + 2 * i], qacc[8 + 2 * i + 1]);
            qx[i] = pk(lo16(a) * qs, hi16(a) * qs);
            qy[i] = pk(lo16(c) * qs, hi16(c) * qs);
        }
        ST128(L_P1 + w * 2048 + lane * 16, qx);
        ST128(L_P1 + w * 2048 + 1024 + lane * 16, qy);
    }
    const u32 key = max(max(LD32(L_CT), LD32(L_CT + 4)), max(LD32(L_CT + 8), LD32(L_CT + 12)));
    const int sel = __builtin_amdgcn_readfirstlane(0xffff - (key & 0xffff)) & 2047;
    u32x8 EXA; u32x4 EXB, WOA;
    u32 skv = lb + L_K + kvo + (10 + 4 * (w >> 2)) * 1024, sg = lb + L_W + w * 2048;
    const u32 skv3 = lb + L_K + kvo + (26 + 2 * (w >> 2)) * 1024;
    u32 swo = lb + ((w & 4) ? L_V : L_K) + ((w >> 1) & 1) * 32768 + (w & 1) * 8192;
    {
        EXA[0] = kvo + (10 + 4 * (w >> 2)) * 1024 + lane * 16;
        EXA[1] = kvo + (26 + 2 * (w >> 2)) * 1024 + lane * 16;
        const u32 pos = lane >> 3, B = pos ^ ((pos >> 2) & 1);
        const u32 row = 64 * w + 4 * B + ((lane >> 1) & 3);
        const u64 g0 = (u64)((lane & 1) ? wu : wg) + (u64)(row * 3584 + fj * 16), g1 = g0 + 32 * 3584;
        EXA[2] = (u32)g0; EXA[3] = (u32)(g0 >> 32); EXA[4] = (u32)g1; EXA[5] = (u32)(g1 >> 32);
        EXA[6] = sel * 1024 + (64 * w + 16 * g) * 2;
        EXA[7] = fj * 8192 + (128 * (w & 3) + 2 * lane) * 2;
        const u32 ta = lb + L_W + w * 2048 + l15 * 8, gx = g & 1;
        EXB[0] = ta + (4 * g + gx) * 128;
        EXB[1] = ta + (4 * g + (gx ^ 1)) * 128;
        EXB[2] = lb + (l15 == 0 ? L_FP + w * 64 + g * 16 : L_P1 + w * 1024 + lane * 16);
        EXB[3] = 0;
#pragma unroll
        for (int i = 0; i < 4; ++i) {
            const u32 r = 64 * w + 8 * i + (lane >> 3), slot = lane & 7;
            const u32 f = ((r >> 1) & 1) | ((i & 1) << 1);
            const u32 c = (((slot >> 1) ^ f) << 1) | (slot & 1);
            WOA[i] = r * 1024 + hg * 128 + c * 16 - i * 1024;
        }
    }
    u32x2 PZA;
    {
        const int head = hg * 4 + hp * 2 + blk, tl = th * 32 + l31;
        PZA[0] = WS_O + ((tb * 4 + (tl >> 4)) * 16 + (head >> 1)) * 1024 + (((head & 1) * 2 + h) * 16 + (tl & 15)) * 16;
        PZA[1] = (ffn && w < 4) ? WS_Y + fj * 2048 + w * 512 + l31 * 16 : PZA[0];
    }
    const int hf = w >> 2;
    u32 ka = lb + L_K + hf * 32768 + lane * 16;
    const bool vlane = l31 < 16;
    u32 va = vlane ? (lb + L_V + hf * 32768 + h * 256 + l15 * 16) : (lb + L_ONE - 1024 + h * 256 + l15 * 16);
    u32 vinc = vlane ? 4096u : 0u;
    u32 va0 = vlane ? va : va + 1024;
    u32 qa = lb + L_P1 + (w & 3) * 2048 + lane * 16;
    asm volatile("" : "+{v[196:203]}"(EXA), "+{v[228:231]}"(EXB), "+{v[236:239]}"(WOA), "+{v[220:221]}"(PZA), "+{v193}"(ka), "+{v194}"(va), "+{v195}"(vinc), "+v"(va0), "+v"(qa),
                 "+s"(skv), "+s"(sg), "+s"(swo));
    if (w >= 4) VMWAIT(0);
    BAR();
    STAMP(10); STAMPC(19);
    if (b == 0 && tid == 256) { u32 z = 0; GST32C(ws, z, epoch); }

    u32x4 qx, qy;
    f32x16 SX0, SX1, SY0, SY1, OX, OY;
    u32x4 K0, K1, V00, V01, V10, V11;
    u32 idx = 0xDDDDu;
    {
        asm volatile(
            "ds_read_b128 v[160:163], %[qa]\n"
            "ds_read_b128 v[164:167], %[qa] offset:1024\n"
            "ds_read_b128 v[168:171], v193\n"
            "ds_read_b128 v[172:175], v193 offset:1024\n"
            "ds_read_b128 v[176:179], %[va0]\n"
            "ds_read_b128 v[180:183], %[va0] offset:512\n"
            "v_mov_b32 v128, 0\n v_mov_b32 v129, 0\n v_mov_b32 v130, 0\n v_mov_b32 v131, 0\n"
            "v_mov_b32 v132, 0\n v_mov_b32 v133, 0\n v_mov_b32 v134, 0\n v_mov_b32 v135, 0\n"
            "v_mov_b32 v136, 0\n v_mov_b32 v137, 0\n v_mov_b32 v138, 0\n v_mov_b32 v139, 0\n"
            "v_mov_b32 v140, 0\n v_mov_b32 v141, 0\n v_mov_b32 v142, 0\n v_mov_b32 v143, 0\n"
            "v_mov_b32 v144, 0\n v_mov_b32 v145, 0\n v_mov_b32 v146, 0\n v_mov_b32 v147, 0\n"
            "v_mov_b32 v148, 0\n v_mov_b32 v149, 0\n v_mov_b32 v150, 0\n v_mov_b32 v151, 0\n"
            "v_mov_b32 v152, 0\n v_mov_b32 v153, 0\n v_mov_b32 v154, 0\n v_mov_b32 v155, 0\n"
            "v_mov_b32 v156, 0\n v_mov_b32 v157, 0\n v_mov_b32 v158, 0\n v_mov_b32 v159, 0\n"
            "v_mov_b32 v120, 0\n v_mov_b32 v121, 0\n v_mov_b32 v122, 0\n v_mov_b32 v123, 0\n"
            "v_mov_b32 v124, 0\n v_mov_b32 v125, 0\n v_mov_b32 v126, 0\n v_mov_b32 v127, 0\n"
            "v_mov_b32 v188, 0\n v_mov_b32 v189, 0\n v_mov_b32 v190, 0\n v_mov_b32 v191, 0\n"
            "s_waitcnt lgkmcnt(0)\n"
            "v_mfma_f32_32x32x16_bf16 v[64:79], v[168:171], v[160:163], 0\n"
            "v_mfma_f32_32x32x16_bf16 v[96:111], v[168:171], v[164:167], 0\n"
            "s_nop 7\n s_nop 7\n"
            : "={v[64:79]}"(SX0), "={v[80:95]}"(SX1), "={v[96:111]}"(SY0), "={v[112:127]}"(SY1),
              "={v[128:143]}"(OX), "={v[144:159]}"(OY),
              "={v[168:171]}"(K0), "={v[172:175]}"(K1), "={v[176:179]}"(V00), "={v[180:183]}"(V01),
              "={v[184:187]}"(V10), "={v[188:191]}"(V11), "=&{v[160:163]}"(qx), "=&{v[164:167]}"(qy)
            : "{v193}"(ka), [va0] "v"(va0), [qa] "v"(qa)
            : "memory");
    }
#define ATT_OPS \
            : "+{v[64:79]}"(SX0), "+{v[80:95]}"(SX1), "+{v[96:111]}"(SY0), "+{v[112:127]}"(SY1), \
              "+{v[128:143]}"(OX), "+{v[144:159]}"(OY), \
              "+{v[168:171]}"(K0), "+{v[172:175]}"(K1), "+{v[176:179]}"(V00), "+{v[180:183]}"(V01), \
              "+{v[184:187]}"(V10), "+{v[188:191]}"(V11), "+{v193}"(ka), "+{v194}"(va), [cnt] "=&s"(cnt_)
#define ATT_INS : "{v[160:163]}"(qx), "{v[164:167]}"(qy), "{v192}"(idx), "{v195}"(vinc)
    u32 cnt_;
    const char* kvb = ws + WS_K;
    PRIO_START();
    asm volatile(ATT_A ATT_OPS, "+{v[196:203]}"(EXA), "+{v[228:231]}"(EXB), "+{v[236:239]}"(WOA), "+{v[220:221]}"(PZA), [skv] "+s"(skv), [sg] "+s"(sg)
                 ATT_INS, [kvb] "s"(kvb), [wsb] "s"(ws) : "memory", "scc", "v232", "v233", "v234", "v235");
    LSTAMP(4);
    BAR();
    LSTAMP(8);
    if (tid == 448) { u32 fo = WS_F2 + b * 4; GST32C(ws, fo, epoch); }
    PRIO_START();
    u32x16 EXD;
    asm volatile(ATT_B ATT_OPS, "+{v[196:203]}"(EXA), "=&{v[204:219]}"(EXD), "+{v[236:239]}"(WOA)
                 ATT_INS, "{v[228:231]}"(EXB), [kvb] "s"(kvb), [skv3] "s"(skv3), [xb] "s"(x), [wdb] "s"(wd)
                 : "memory", "scc", "v220", "v221", "v222", "v223", "v224", "v225", "v226", "v227", "v232", "v233", "v234", "v235");
    LSTAMP(9);
    BAR();
    LSTAMP(11);
    if (ffn && w < 4) {
        float sum = LDF(L_FP + l15 * 4);
#pragma unroll
        for (int i = 1; i < 8; ++i) sum += LDF(L_FP + i * 64 + l15 * 4);
        sum = bfr(sum);
        float up = __shfl(sum, (lane & 7) + 8);
        float gt = __shfl(sum, lane & 7);
        float hv = bfr(bfr(gt * sigm(gt)) * up);
        u32 hb = __float_as_uint(hv);
        float y0 = 0.f, y1 = 0.f;
#pragma unroll
        for (int i = 0; i < 8; ++i) {
            const float hs = __uint_as_float((u32)__builtin_amdgcn_readlane(hb, i));
            y0 += hs * lo16(EXD[8 + i]);
            y1 += hs * hi16(EXD[8 + i]);
        }
        u32x2 gr = {__float_as_uint(y0), __float_as_uint(y1)};
        gr[0] = gr[0] == POISON ? 0x7fc00000u : gr[0];
        gr[1] = gr[1] == POISON ? 0x7fc00000u : gr[1];
        u32 yo = WS_Y + fj * 2048 + (128 * w + 2 * lane) * 4;
        GST64C(ws, yo, gr);
    }
    LSTAMP(22);
    PRIO_START();
    u32 f2v; const u32 f2a = WS_F2 + (32 * w + l31) * 4;
    asm volatile(ATT_C ATT_OPS, "+{v[236:239]}"(WOA), "=&{v220}"(f2v), [swo] "+s"(swo) ATT_INS, "{v221}"(f2a), [wob] "s"(wo), [wsb] "s"(ws) : "memory", "scc");
    const int tq = w & 3, kh = w >> 2;
    const bool owner = (sel >> 6) == tb;
    int lane_b = lane;
    asm volatile("" : "+v"(lane_b));
    {
    const int lane = lane_b;
    const int g = lane >> 4, l15 = lane & 15, h = lane >> 5, l31 = lane & 31;
    const int tokg = tb * 64 + tq * 16 + l15;
    const int ncol = hg * 64 + kh * 32 + (g & 1) * 16 + (g >> 1) * 8;
    u32 oslot;
    {
        const int head = hg * 4 + hp * 2 + blk, tl = th * 32 + l31;
        oslot = WS_O + ((tb * 4 + (tl >> 4)) * 16 + (head >> 1)) * 1024 + (((head & 1) * 2 + h) * 16 + (tl & 15)) * 16;
    }
    STAMP(12); STAMPC(20);
    {
        u32 spins = 0;
        while (!__all(f2v == epoch) && ++spins <= SPIN_MAX) {
            const u32 a = WS_F2 + (32 * w + l31) * 4;
            asm volatile("global_load_dword %0, %1, %2 sc1\n\ts_waitcnt vmcnt(0)" : "=&v"(f2v) : "v"(a), "s"(ws) : "memory");
        }
    }
    u32x4 xe, Y0, Y1, Y2, Y3, Y4, Y5, Y6; u32 gl;
    const u32 yo0 = WS_Y + (28 * w + g) * 2048 + hg * 256 + l15 * 16;
    {
        u32 xo = tokg * 1024 + ncol * 2;
        GLD128(xe, x, xo, 0);
        u32 go = WS_G + tokg * 4;
        GLD32C(gl, ws, go, 0);
    }

    {
        f32x16& mine = blk ? OY : OX;
        f32x16& give = blk ? OX : OY;
        const u32 cw = L_W + w * 2304, cr = L_W + (w ^ 4) * 2304;
        ST128(cw + lane * 16, ((u32x4){__float_as_uint(give[0]), __float_as_uint(give[1]), __float_as_uint(give[2]), __float_as_uint(give[3])}));
        ST128(cw + 1024 + lane * 16, ((u32x4){__float_as_uint(give[4]), __float_as_uint(give[5]), __float_as_uint(give[6]), __float_as_uint(give[7])}));
        STF(cw + 2048 + lane * 4, give[8]);
        BAR();
        f32x4 p0 = __builtin_bit_cast(f32x4, LD128(cr + lane * 16));
        f32x4 p1 = __builtin_bit_cast(f32x4, LD128(cr + 1024 + lane * 16));
        float l = LDF(cr + 2048 + lane * 4) + mine[8];
        float o[8];
#pragma unroll
        for (int i = 0; i < 4; ++i) { o[i] = mine[i] + p0[i]; o[4 + i] = mine[4 + i] + p1[i]; }
        {
            auto sw = __builtin_amdgcn_permlane32_swap(__float_as_uint(l), __float_as_uint(l), false, false);
            l = __uint_as_float(sw[0]);
        }
        float amax = fmaxf(fmaxf(fmaxf(fabsf(o[0]), fabsf(o[1])), fmaxf(fabsf(o[2]), fabsf(o[3]))),
                           fmaxf(fmaxf(fabsf(o[4]), fabsf(o[5])), fmaxf(fabsf(o[6]), fabsf(o[7]))));
        bool safe = (l >= 8.6736174e-19f) && (l <= 1.2676506e30f) && (amax <= 3.0e38f);
        if (!__all(safe)) {
            slow_block(ws, blk ? qy : qx, lane, o, l);
            auto sw = __builtin_amdgcn_permlane32_swap(__float_as_uint(l), __float_as_uint(l), false, false);
            l = __uint_as_float(sw[0]);
        }
        const float ri = __builtin_amdgcn_rcpf(l);
        u32 A0 = pk(o[0] * ri, o[1] * ri), A1 = pk(o[2] * ri, o[3] * ri);
        u32 B0 = pk(o[4] * ri, o[5] * ri), B1 = pk(o[6] * ri, o[7] * ri);
        auto s0 = __builtin_amdgcn_permlane32_swap(A0, B0, false, false);
        auto s1 = __builtin_amdgcn_permlane32_swap(A1, B1, false, false);
        u32x4 ov = {s0[0], s1[0], s0[1], s1[1]};
#pragma unroll
        for (int i = 0; i < 4; ++i) ov[i] = ov[i] == POISON ? 0x7fc07fc0u : ov[i];
        GST128C(ws, oslot, ov);
    }
    {
        u32 yo = yo0;
        asm volatile("s_cmp_lg_u32 %[stb], %[tb]\n\ts_cbranch_scc1 3f\n"
                     "global_load_dwordx4 %0, %[yo], %[ws] sc1\n v_add_u32 %[yo], 0x2000, %[yo]\n"
                     "global_load_dwordx4 %1, %[yo], %[ws] sc1\n v_add_u32 %[yo], 0x2000, %[yo]\n"
                     "global_load_dwordx4 %2, %[yo], %[ws] sc1\n v_add_u32 %[yo], 0x2000, %[yo]\n"
                     "global_load_dwordx4 %3, %[yo], %[ws] sc1\n v_add_u32 %[yo], 0x2000, %[yo]\n"
                     "global_load_dwordx4 %4, %[yo], %[ws] sc1\n v_add_u32 %[yo], 0x2000, %[yo]\n"
                     "global_load_dwordx4 %5, %[yo], %[ws] sc1\n v_add_u32 %[yo], 0x2000, %[yo]\n"
                     "global_load_dwordx4 %6, %[yo], %[ws] sc1\n3:"
                     : "=&v"(Y0), "=&v"(Y1), "=&v"(Y2), "=&v"(Y3), "=&v"(Y4), "=&v"(Y5), "=&v"(Y6), [yo] "+v"(yo)
                     : [stb] "s"(sel >> 6), [tb] "s"(tb), [ws] "s"(ws) : "memory", "scc");
    }
    STAMP(13);

    u32x4 af[4][8];
    {
        const u32 f = ((l15 >> 3) & 1) | ((g & 1) << 1);
        const u32 rb = (kh ? L_V : L_K) + (8 * g + (l15 >> 2)) * 128 + (l15 & 3) * 8;
#pragma unroll
        for (int nb = 0; nb < 4; ++nb) {
            u32 a = rb + ((nb ^ f) * 32);
            asm volatile("" : "+v"(a));
#pragma unroll
            for (int st = 0; st < 8; ++st) {
                const u32 o_ = (st >> 2) * 32768 + (st & 3) * 4096;
                af[nb][st] = cat(trread(lds, a + o_), trread(lds, a + o_ + 512));
            }
        }
    }
    STAMP(14);
    asm volatile("s_cmp_lg_u32 %[stb], %[tb]\n\ts_cbranch_scc1 3f\n\ts_waitcnt vmcnt(0)\n3:\n\ts_waitcnt vmcnt(1)"
                 : "+v"(xe), "+v"(gl), "+v"(Y0), "+v"(Y1), "+v"(Y2), "+v"(Y3), "+v"(Y4), "+v"(Y5), "+v"(Y6)
                 : [stb] "s"(sel >> 6), [tb] "s"(tb) : "memory", "scc");
    if (owner) {
        u32 spins = 0;
#define YMX(v) max(max(v[0], v[1]), max(v[2], v[3]))
        while (!__all(max(max(max(YMX(Y0), YMX(Y1)), max(YMX(Y2), YMX(Y3))), max(max(YMX(Y4), YMX(Y5)), YMX(Y6))) != POISON) && ++spins <= SPIN_MAX) {
            u32 yo = yo0;
            asm volatile("global_load_dwordx4 %0, %[yo], %[ws] sc1\n v_add_u32 %[yo], 0x2000, %[yo]\n"
                         "global_load_dwordx4 %1, %[yo], %[ws] sc1\n v_add_u32 %[yo], 0x2000, %[yo]\n"
                         "global_load_dwordx4 %2, %[yo], %[ws] sc1\n v_add_u32 %[yo], 0x2000, %[yo]\n"
                         "global_load_dwordx4 %3, %[yo], %[ws] sc1\n v_add_u32 %[yo], 0x2000, %[yo]\n"
                         "global_load_dwordx4 %4, %[yo], %[ws] sc1\n v_add_u32 %[yo], 0x2000, %[yo]\n"
                         "global_load_dwordx4 %5, %[yo], %[ws] sc1\n v_add_u32 %[yo], 0x2000, %[yo]\n"
                         "global_load_dwordx4 %6, %[yo], %[ws] sc1\n s_waitcnt vmcnt(0)"
                         : "=&v"(Y0), "=&v"(Y1), "=&v"(Y2), "=&v"(Y3), "=&v"(Y4), "=&v"(Y5), "=&v"(Y6), [yo] "+v"(yo)
                         : [ws] "s"(ws) : "memory");
        }
        f32x4 ys;
#pragma unroll
        for (int i = 0; i < 4; ++i) {
            float v = ((__uint_as_float(Y0[i]) + __uint_as_float(Y1[i])) + (__uint_as_float(Y2[i]) + __uint_as_float(Y3[i])))
                    + ((__uint_as_float(Y4[i]) + __uint_as_float(Y5[i])) + __uint_as_float(Y6[i]));
            auto a = __builtin_amdgcn_permlane32_swap(__float_as_uint(v), __float_as_uint(v), false, false);
            v = __uint_as_float(a[0]) + __uint_as_float(a[1]);
            auto c = __builtin_amdgcn_permlane16_swap(__float_as_uint(v), __float_as_uint(v), false, false);
            ys[i] = __uint_as_float(c[0]) + __uint_as_float(c[1]);
        }
        if (g == 0) ST128(L_YP + w * 256 + l15 * 16, __builtin_bit_cast(u32x4, ys));
    }
    u32x4 of0, of1, of2, of3, of4, of5, of6, of7;
    {
        const u32 oo = WS_O + ((tb * 4 + tq) * 16 + 8 * kh) * 1024 + lane * 16, o2 = oo + 4096;
        u32 spins = 0;
        while (true) {
            asm volatile("global_load_dwordx4 %0, %8, %10 sc1\n\tglobal_load_dwordx4 %1, %8, %10 offset:1024 sc1\n\t"
                         "global_load_dwordx4 %2, %8, %10 offset:2048 sc1\n\tglobal_load_dwordx4 %3, %8, %10 offset:3072 sc1\n\t"
                         "global_load_dwordx4 %4, %9, %10 sc1\n\tglobal_load_dwordx4 %5, %9, %10 offset:1024 sc1\n\t"
                         "global_load_dwordx4 %6, %9, %10 offset:2048 sc1\n\tglobal_load_dwordx4 %7, %9, %10 offset:3072 sc1\n\t"
                         "s_waitcnt vmcnt(0)"
                         : "=&v"(of0), "=&v"(of1), "=&v"(of2), "=&v"(of3), "=&v"(of4), "=&v"(of5), "=&v"(of6), "=&v"(of7)
                         : "v"(oo), "v"(o2), "s"(ws) : "memory");
            u32 mx = 0;
#define OMX(v) mx = max(max(mx, max(v[0], v[1])), max(v[2], v[3]));
            OMX(of0) OMX(of1) OMX(of2) OMX(of3) OMX(of4) OMX(of5) OMX(of6) OMX(of7)
            if (__all(mx != POISON) || ++spins > SPIN_MAX) break;
        }
    }
    STAMP(15);
    f32x4 acc[4] = {{0, 0, 0, 0}, {0, 0, 0, 0}, {0, 0, 0, 0}, {0, 0, 0, 0}};
#define OSTEP(OF, ST) \
    _Pragma("unroll") for (int nb = 0; nb < 4; ++nb) acc[nb] = __builtin_amdgcn_mfma_f32_16x16x32_bf16(asbf(af[nb][ST]), asbf(OF), acc[nb], 0, 0, 0);
    OSTEP(of0, 0) OSTEP(of1, 1) OSTEP(of2, 2) OSTEP(of3, 3) OSTEP(of4, 4) OSTEP(of5, 5) OSTEP(of6, 6) OSTEP(of7, 7)
    STAMP(16);
    {
        f32x4 k0 = kh ? acc[2] : acc[0], k1 = kh ? acc[3] : acc[1];
        f32x4 g0 = kh ? acc[0] : acc[2], g1 = kh ? acc[1] : acc[3];
        ST128(L_K + 16384 + w * 2048 + lane * 16, __builtin_bit_cast(u32x4, g0));
        ST128(L_K + 16384 + w * 2048 + 1024 + lane * 16, __builtin_bit_cast(u32x4, g1));
        BAR();
        k0 += __builtin_bit_cast(f32x4, LD128(L_K + 16384 + (w ^ 4) * 2048 + lane * 16));
        k1 += __builtin_bit_cast(f32x4, LD128(L_K + 16384 + (w ^ 4) * 2048 + 1024 + lane * 16));
        float a[8];
#pragma unroll
        for (int i = 0; i < 4; ++i) {
            auto sw = __builtin_amdgcn_permlane16_swap(__float_as_uint(k0[i]), __float_as_uint(k1[i]), false, false);
            a[i] = __uint_as_float(sw[0]); a[4 + i] = __uint_as_float(sw[1]);
        }
        const float gt = bfr(sigm(bfr(__uint_as_float(gl))));
        const float og = bfr(1.0f - gt);
        float bb[8];
#pragma unroll
        for (int i = 0; i < 4; ++i) { bb[2 * i] = lo16(xe[i]); bb[2 * i + 1] = hi16(xe[i]); }
        if (owner && tokg == sel) {
            const u32 rord = key >> 16;
            const float rsel = __uint_as_float(((rord & 0x8000u) ? (rord & 0x7fffu) : (~rord & 0xffffu)) << 16);
            const float sg = bfr(sigm(rsel));
            const int c0 = kh * 32 + (g & 1) * 16 + (g >> 1) * 8;
#pragma unroll
            for (int i = 0; i < 8; ++i) {
                float y = 0.f;
#pragma unroll
                for (int p = 0; p < 8; ++p) y += LDF(L_YP + p * 256 + (c0 + i) * 4);
                bb[i] = bfr(bb[i] + bfr(bfr(y) * sg));
            }
        }
        u32x4 ov;
#pragma unroll
        for (int i = 0; i < 4; ++i)
            ov[i] = pk(bfr(gt * bfr(a[2 * i])) + bfr(og * bb[2 * i]), bfr(gt * bfr(a[2 * i + 1])) + bfr(og * bb[2 * i + 1]));
        u32 oo_ = tokg * 1024 + ncol * 2;
        GST128(out, oo_, ov);
    }
    STAMP(17); STAMPC(21);
    BAR();
    if (lane < 24) { u32 v_ = LD32(L_CT + 64 + (w * 24 + lane) * 4); u32 o_ = WS_DBG + ((b * 8 + w) * 32 + lane) * 4; GST32C(ws, o_, v_); }
    VMWAIT(0);
    BAR();
    if (tid == 0) { u32 o_ = WS_DBG + 524288 + b * 4; GST32C(ws, o_, epoch); }
    if (b == 0 && w == 0) {
        auto LDG = [&](u32 o_) { u32 v_; asm volatile("global_load_dword %0, %1, %2 sc1\n\ts_waitcnt vmcnt(0)" : "=&v"(v_) : "v"(o_), "s"(ws) : "memory"); return v_; };
        for (int q = 0; q < 4; ++q) { u32 sp = 0; while (!__all(LDG(WS_DBG + 524288 + (q * 64 + lane) * 4) == epoch) && ++sp < 100000u) {} }
        auto S = [&](int bb, int wv, int i) { return LDG(WS_DBG + ((bb * 8 + wv) * 32 + i) * 4); };
        auto RMIN = [&](int v) { for (int o = 32; o; o >>= 1) v = min(v, __shfl_xor(v, o)); return v; };
        auto RMAX = [&](int v) { for (int o = 32; o; o >>= 1) v = max(v, __shfl_xor(v, o)); return v; };
        auto RSUM = [&](int v) { for (int o = 32; o; o >>= 1) v = v + __shfl_xor(v, o); return v; };
        const u32 ref = S(0, 0, 1);
#define ALLMIN(F) RMIN(min(min([&](int bb){ return (int)(F); }(lane), [&](int bb){ return (int)(F); }(lane + 64)), min([&](int bb){ return (int)(F); }(lane + 128), [&](int bb){ return (int)(F); }(lane + 192))))
#define ALLMAX(F) RMAX(max(max([&](int bb){ return (int)(F); }(lane), [&](int bb){ return (int)(F); }(lane + 64)), max([&](int bb){ return (int)(F); }(lane + 128), [&](int bb){ return (int)(F); }(lane + 192))))
#define ALLAVG(F) (RSUM([&](int bb){ return (int)(F); }(lane) + [&](int bb){ return (int)(F); }(lane + 64) + [&](int bb){ return (int)(F); }(lane + 128) + [&](int bb){ return (int)(F); }(lane + 192)) / 256)
#define XMIN(x, F) RMIN([&](int bb){ return (int)(F); }((lane & 31) * 8 + (x)))
#define XMAX(x, F) RMAX([&](int bb){ return (int)(F); }((lane & 31) * 8 + (x)))
#define XAVG(x, F) (RSUM([&](int bb){ return (int)(F); }((lane & 31) * 8 + (x))) / 64)
        const int t0min = ALLMIN(S(bb, 0, 1) - ref);
        int xl = 0, xe = 0; { int bl = -1000000, be = 1000000; for (int x = 0; x < 8; ++x) { int v = XMIN(x, S(bb, 0, 1) - ref); if (v > bl) { bl = v; xl = x; } if (v < be) { be = v; xe = x; } } }
        int qa = (0), qb = (0);
        qa = min(max(qa, 0), 127); qb = min(max(qb, 0), 127);
        const u32 dly = 40u * (u32)qa + 5120u * (u32)qb;
        const u32 ts = (u32)__builtin_amdgcn_s_memrealtime(); u32 n_ = 0;
        while ((u32)__builtin_amdgcn_s_memrealtime() - ts < dly && ++n_ < 4000000u) __builtin_amdgcn_s_sleep(2);
    }
    }
}

extern "C" void kernel_launch(void* const* d_in, const int* in_sizes, int n_in, void* d_out, int out_size,
                              void* d_ws, size_t ws_size, hipStream_t stream) {
    (void)in_sizes; (void)n_in; (void)out_size; (void)ws_size;
    fused_kernel<<<256, 512, 0, stream>>>((const u16*)d_in[0], (const u16*)d_in[1], (const u16*)d_in[2], (const u16*)d_in[3],
                                          (const u16*)d_in[4], (const u16*)d_in[6], (const u16*)d_in[7], (const u16*)d_in[8],
                                          (const u16*)d_in[9], (const u16*)d_in[10], (u16*)d_out, (char*)d_ws);
}
```

```cpp
#include <hip/hip_runtime.h>
#include <stdint.h>

typedef unsigned int u32;
typedef unsigned long long u64;
typedef unsigned short u16;
typedef u32 u32x2 __attribute__((ext_vector_type(2)));
typedef u32 u32x4 __attribute__((ext_vector_type(4)));
typedef u32 u32x8 __attribute__((ext_vector_type(8)));
typedef u32 u32x16 __attribute__((ext_vector_type(16)));
typedef u32 u32x32 __attribute__((ext_vector_type(32)));
typedef float f32x8 __attribute__((ext_vector_type(8)));
typedef float f32x2 __attribute__((ext_vector_type(2)));
typedef float f32x4 __attribute__((ext_vector_type(4)));
typedef float f32x16 __attribute__((ext_vector_type(16)));
typedef __bf16 bf16x2 __attribute__((ext_vector_type(2)));
typedef __bf16 bf16x8 __attribute__((ext_vector_type(8)));
#define LDSP __attribute__((address_space(3)))

constexpr u32 WS_E   = 0;
constexpr u32 WS_F2  = 4096;
constexpr u32 WS_G   = 24576;
constexpr u32 WS_K   = 65536;
constexpr u32 WS_V   = 131072;
constexpr u32 WS_F1  = 262144;
constexpr u32 WS_O   = 524288;
constexpr u32 WS_Y   = 2621440;
constexpr u32 WS_DBG = 4194304;

constexpr u32 L_K   = 0;
constexpr u32 L_V   = 65536;
constexpr u32 L_ONE = 131072;
constexpr u32 L_W   = 135168;
constexpr u32 L_M   = 151552;
constexpr u32 L_P1  = L_M;
constexpr u32 L_FP  = L_M + 8704;
constexpr u32 L_YP  = L_M + 9216;
constexpr u32 L_CT  = L_M + 11264;
constexpr u32 C_PROD = L_CT + 16;
constexpr u32 C_PUB  = L_CT + 20;
constexpr u32 C_H0   = L_CT + 24;
constexpr u32 C_H1   = L_CT + 28;
constexpr u32 C_Q1   = L_CT + 32;
constexpr u32 C_Y    = L_CT + 36;
constexpr u32 L_EP   = L_CT + 48;
constexpr u32 C_WO   = L_CT + 40;
constexpr u32 L_END = 163840;

constexpr u32 SPIN_MAX = 1u << 22;
constexpr u32 POISON = 0xffffffffu;

#define LD128(a) (*(const LDSP u32x4*)(lds + (a)))
#define ST128(a, v) (*(LDSP u32x4*)(lds + (a)) = (v))
#define LD32(a) (*(const LDSP u32*)(lds + (a)))
#define ST32(a, v) (*(LDSP u32*)(lds + (a)) = (v))
#define LDF(a) (*(const LDSP float*)(lds + (a)))
#define STF(a, v) (*(LDSP float*)(lds + (a)) = (v))

__device__ __forceinline__ u32x2 trread(LDSP char* lds, u32 a) {
    typedef short v4s __attribute__((ext_vector_type(4)));
    v4s r = __builtin_amdgcn_ds_read_tr16_b64_v4i16((LDSP v4s*)(lds + a));
    return __builtin_bit_cast(u32x2, r);
}
__device__ __forceinline__ bf16x8 asbf(u32x4 v) { return __builtin_bit_cast(bf16x8, v); }
__device__ __forceinline__ u32x4 cat(u32x2 a, u32x2 b) { u32x4 r = {a[0], a[1], b[0], b[1]}; return r; }
__device__ __forceinline__ u32 pk(float a, float b) {
    f32x2 f = {a, b};
    return __builtin_bit_cast(u32, __builtin_convertvector(f, bf16x2));
}
__device__ __forceinline__ float bfr(float a) {
    return __uint_as_float(pk(a, a) & 0xffff0000u);
}
__device__ __forceinline__ float lo16(u32 v) { return __uint_as_float(v << 16); }
__device__ __forceinline__ float hi16(u32 v) { return __uint_as_float(v & 0xffff0000u); }
__device__ __forceinline__ float sigm(float z) { return __builtin_amdgcn_rcpf(1.0f + __builtin_amdgcn_exp2f(-1.4426950408889634f * z)); }

__device__ __forceinline__ void dma16(const void* base, u32 voff, u32 ldsaddr) {
    asm volatile("s_mov_b32 m0, %0\n\ts_nop 0\n\tglobal_load_lds_dwordx4 %1, %2" :: "s"(ldsaddr), "v"(voff), "s"(base) : "memory");
}
__device__ __forceinline__ void dma16c4(const void* base, u32 voff, u32 ldsaddr) {
    asm volatile("s_mov_b32 m0, %0\n\ts_nop 0\n\tglobal_load_lds_dwordx4 %1, %2 sc1\n\tglobal_load_lds_dwordx4 %1, %2 offset:1024 sc1\n\t"
                 "global_load_lds_dwordx4 %1, %2 offset:2048 sc1\n\tglobal_load_lds_dwordx4 %1, %2 offset:3072 sc1"
                 :: "s"(ldsaddr), "v"(voff), "s"(base) : "memory");
}
__device__ __forceinline__ void dma16c3(const void* base, u32 voff, u32 ldsaddr) {
    asm volatile("s_mov_b32 m0, %0\n\ts_nop 0\n\tglobal_load_lds_dwordx4 %1, %2 sc1\n\tglobal_load_lds_dwordx4 %1, %2 offset:1024 sc1\n\t"
                 "global_load_lds_dwordx4 %1, %2 offset:2048 sc1"
                 :: "s"(ldsaddr), "v"(voff), "s"(base) : "memory");
}
__device__ __forceinline__ void dma16c2(const void* base, u32 voff, u32 ldsaddr) {
    asm volatile("s_mov_b32 m0, %0\n\ts_nop 0\n\tglobal_load_lds_dwordx4 %1, %2 sc1\n\tglobal_load_lds_dwordx4 %1, %2 offset:1024 sc1"
                 :: "s"(ldsaddr), "v"(voff), "s"(base) : "memory");
}
__device__ __forceinline__ void dma16c(const void* base, u32 voff, u32 ldsaddr) {
    asm volatile("s_mov_b32 m0, %0\n\ts_nop 0\n\tglobal_load_lds_dwordx4 %1, %2 sc1" :: "s"(ldsaddr), "v"(voff), "s"(base) : "memory");
}
__device__ __forceinline__ void lds_signal(u32 addr) {
    u32 one = 1;
    asm volatile("s_mov_b64 exec, 1\n\tds_add_u32 %0, %1\n\ts_mov_b64 exec, -1" :: "v"(addr), "v"(one) : "memory");
}
__device__ __forceinline__ void lds_wait(u32 addr, u32 n) {
    u32 spins = 0, v;
    do {
        asm volatile("ds_read_b32 %0, %1\n\ts_waitcnt lgkmcnt(0)" : "=v"(v) : "v"(addr) : "memory");
    } while ((u32)__builtin_amdgcn_readfirstlane((int)v) < n && ++spins <= SPIN_MAX);
}
#define GLD128(dst, base, voff, OFF)  asm volatile("global_load_dwordx4 %0, %1, %2 offset:" #OFF : "=v"(dst) : "v"(voff), "s"(base) : "memory")
#define GLD128C(dst, base, voff, OFF) asm volatile("global_load_dwordx4 %0, %1, %2 offset:" #OFF " sc1" : "=v"(dst) : "v"(voff), "s"(base) : "memory")
#define GLD64C(dst, base, voff, OFF)  asm volatile("global_load_dwordx2 %0, %1, %2 offset:" #OFF " sc1" : "=v"(dst) : "v"(voff), "s"(base) : "memory")
#define GLD32C(dst, base, voff, OFF)  asm volatile("global_load_dword %0, %1, %2 offset:" #OFF " sc1" : "=v"(dst) : "v"(voff), "s"(base) : "memory")
#define GLD32(dst, base, voff, OFF)   asm volatile("global_load_dword %0, %1, %2 offset:" #OFF : "=v"(dst) : "v"(voff), "s"(base) : "memory")
#define GLD16(dst, base, voff, OFF)   asm volatile("global_load_ushort %0, %1, %2 offset:" #OFF : "=v"(dst) : "v"(voff), "s"(base) : "memory")
#define GST128(base, voff, val)  asm volatile("global_store_dwordx4 %0, %1, %2\n\ts_nop 1" :: "v"(voff), "v"(val), "s"(base) : "memory")
#define GST128C(base, voff, val) asm volatile("global_store_dwordx4 %0, %1, %2 sc1\n\ts_nop 1" :: "v"(voff), "v"(val), "s"(base) : "memory")
#define GST64C(base, voff, val)  asm volatile("global_store_dwordx2 %0, %1, %2 sc1\n\ts_nop 0" :: "v"(voff), "v"(val), "s"(base) : "memory")
#define GST32C(base, voff, val)  asm volatile("global_store_dword %0, %1, %2 sc1" :: "v"(voff), "v"(val), "s"(base) : "memory")
#define VMWAIT(N) asm volatile("s_waitcnt vmcnt(" #N ")" ::: "memory")
#define BAR() asm volatile("s_waitcnt lgkmcnt(0)\n\ts_barrier" ::: "memory")

#define STAMP(i)
#define STAMPC(i)
#define STAMPDUMP()
#define FSTAMP(i) STAMP(i)
#define LSTAMP(i)

__device__ __forceinline__ u32 wave_max_u32(u32 v) {
    v = max(v, (u32)__builtin_amdgcn_update_dpp(0, (int)v, 0x111, 0xf, 0xf, false));
    v = max(v, (u32)__builtin_amdgcn_update_dpp(0, (int)v, 0x112, 0xf, 0xf, false));
    v = max(v, (u32)__builtin_amdgcn_update_dpp(0, (int)v, 0x114, 0xf, 0xf, false));
    v = max(v, (u32)__builtin_amdgcn_update_dpp(0, (int)v, 0x118, 0xf, 0xf, false));
    v = max(v, (u32)__builtin_amdgcn_update_dpp(0, (int)v, 0x142, 0xa, 0xf, false));
    v = max(v, (u32)__builtin_amdgcn_update_dpp(0, (int)v, 0x143, 0xc, 0xf, false));
    return (u32)__builtin_amdgcn_readlane((int)v, 63);
}
#define LATE 0
#define FLF 0
#define GFL 0
#define ATT_A \
    "s_mov_b32 %[cnt], 2\n" \
    "1:\n" \
    "v_exp_f32 v64, v64\n" \
    "v_exp_f32 v65, v65\n" \
    "v_smfmac_f32_32x32x32_bf16 v[144:159], v[188:191], v[120:127], v192\n" \
    "global_store_dwordx4 v44, v[232:235], %[wsb] sc1\n" \
    "ds_read_b128 v[184:187], v194 offset:1024\n" \
    "v_exp_f32 v66, v66\n" \
    "v_exp_f32 v67, v67\n" \
    "v_exp_f32 v68, v68\n" \
    "v_exp_f32 v69, v69\n" \
    "s_waitcnt lgkmcnt(1)\n" \
    "v_mfma_f32_32x32x16_bf16 v[80:95], v[172:175], v[160:163], 0\n" \
    "ds_read_b128 v[188:191], v194 offset:1536\n" \
    "v_exp_f32 v70, v70\n" \
    "v_exp_f32 v71, v71\n" \
    "v_exp_f32 v72, v72\n" \
    "v_exp_f32 v73, v73\n" \
    "v_smfmac_f32_32x32x32_bf16 v[128:143], v[176:179], v[64:71], v192\n" \
    "v_exp_f32 v74, v74\n" \
    "s_mov_b32 m0, %[skv]\n" \
    "v_exp_f32 v75, v75\n" \
    "v_exp_f32 v76, v76\n" \
    "global_load_lds_dwordx4 v32, %[kvb] offset:0 sc1\n" \
    "v_exp_f32 v77, v77\n" \
    "v_exp_f32 v78, v78\n" \
    "v_exp_f32 v79, v79\n" \
    "v_exp_f32 v96, v96\n" \
    "v_exp_f32 v97, v97\n" \
    "v_smfmac_f32_32x32x32_bf16 v[128:143], v[180:183], v[72:79], v192\n" \
    "v_exp_f32 v98, v98\n" \
    "v_exp_f32 v99, v99\n" \
    "v_exp_f32 v100, v100\n" \
    "v_exp_f32 v101, v101\n" \
    "v_mfma_f32_32x32x16_bf16 v[112:127], v[172:175], v[164:167], 0\n" \
    "ds_read_b128 v[168:171], v193 offset:2048\n" \
    "v_exp_f32 v102, v102\n" \
    "v_exp_f32 v103, v103\n" \
    "v_exp_f32 v104, v104\n" \
    "v_exp_f32 v105, v105\n" \
    "v_smfmac_f32_32x32x32_bf16 v[144:159], v[176:179], v[96:103], v192\n" \
    "v_exp_f32 v106, v106\n" \
    "v_exp_f32 v107, v107\n" \
    "v_exp_f32 v108, v108\n" \
    "v_exp_f32 v109, v109\n" \
    "v_exp_f32 v110, v110\n" \
    "v_exp_f32 v111, v111\n" \
    "v_exp_f32 v80, v80\n" \
    "v_exp_f32 v81, v81\n" \
    "v_smfmac_f32_32x32x32_bf16 v[144:159], v[180:183], v[104:111], v192\n" \
    "ds_read_b128 v[176:179], v194 offset:2048\n" \
    "v_exp_f32 v82, v82\n" \
    "v_exp_f32 v83, v83\n" \
    "v_exp_f32 v84, v84\n" \
    "v_exp_f32 v85, v85\n" \
    "s_waitcnt lgkmcnt(1)\n" \
    "v_mfma_f32_32x32x16_bf16 v[64:79], v[168:171], v[160:163], 0\n" \
    "ds_read_b128 v[180:183], v194 offset:2560\n" \
    "v_exp_f32 v86, v86\n" \
    "v_exp_f32 v87, v87\n" \
    "v_exp_f32 v88, v88\n" \
    "v_exp_f32 v89, v89\n" \
    "v_smfmac_f32_32x32x32_bf16 v[128:143], v[184:187], v[80:87], v192\n" \
    "v_exp_f32 v90, v90\n" \
    "s_mov_b32 m0, %[skv]\n" \
    "v_exp_f32 v91, v91\n" \
    "v_exp_f32 v92, v92\n" \
    "global_load_lds_dwordx4 v32, %[kvb] offset:1024 sc1\n" \
    "v_exp_f32 v93, v93\n" \
    "v_exp_f32 v94, v94\n" \
    "v_exp_f32 v95, v95\n" \
    "v_exp_f32 v112, v112\n" \
    "v_exp_f32 v113, v113\n" \
    "v_smfmac_f32_32x32x32_bf16 v[128:143], v[188:191], v[88:95], v192\n" \
    "v_exp_f32 v114, v114\n" \
    "v_exp_f32 v115, v115\n" \
    "v_exp_f32 v116, v116\n" \
    "v_exp_f32 v117, v117\n" \
    "v_mfma_f32_32x32x16_bf16 v[96:111], v[168:171], v[164:167], 0\n" \
    "ds_read_b128 v[172:175], v193 offset:3072\n" \
    "v_exp_f32 v118, v118\n" \
    "v_exp_f32 v119, v119\n" \
    "v_exp_f32 v120, v120\n" \
    "v_exp_f32 v121, v121\n" \
    "v_smfmac_f32_32x32x32_bf16 v[144:159], v[184:187], v[112:119], v192\n" \
    "v_exp_f32 v122, v122\n" \
    "s_mov_b32 m0, %[sg]\n" \
    "v_exp_f32 v123, v123\n" \
    "v_exp_f32 v124, v124\n" \
    "global_load_lds_dwordx4 v[34:35], off\n" \
    "v_exp_f32 v125, v125\n" \
    "v_exp_f32 v126, v126\n" \
    "v_exp_f32 v127, v127\n" \
    "s_cmp_eq_u32 %[cnt], 1\n" \
    "s_cbranch_scc0 8f\n" \
    "s_setprio 0\n" \
    "8:\n" \
    "v_exp_f32 v64, v64\n" \
    "v_exp_f32 v65, v65\n" \
    "v_smfmac_f32_32x32x32_bf16 v[144:159], v[188:191], v[120:127], v192\n" \
    "ds_read_b128 v[184:187], v194 offset:3072\n" \
    "v_exp_f32 v66, v66\n" \
    "v_exp_f32 v67, v67\n" \
    "v_exp_f32 v68, v68\n" \
    "v_exp_f32 v69, v69\n" \
    "s_waitcnt lgkmcnt(1)\n" \
    "v_mfma_f32_32x32x16_bf16 v[80:95], v[172:175], v[160:163], 0\n" \
    "ds_read_b128 v[188:191], v194 offset:3584\n" \
    "v_exp_f32 v70, v70\n" \
    "v_exp_f32 v71, v71\n" \
    "v_exp_f32 v72, v72\n" \
    "v_exp_f32 v73, v73\n" \
    "v_smfmac_f32_32x32x32_bf16 v[128:143], v[176:179], v[64:71], v192\n" \
    "v_exp_f32 v74, v74\n" \
    "s_mov_b32 m0, %[skv]\n" \
    "v_exp_f32 v75, v75\n" \
    "v_exp_f32 v76, v76\n" \
    "global_load_lds_dwordx4 v32, %[kvb] offset:2048 sc1\n" \
    "v_exp_f32 v77, v77\n" \
    "v_exp_f32 v78, v78\n" \
    "v_exp_f32 v79, v79\n" \
    "v_exp_f32 v96, v96\n" \
    "v_exp_f32 v97, v97\n" \
    "v_smfmac_f32_32x32x32_bf16 v[128:143], v[180:183], v[72:79], v192\n" \
    "v_exp_f32 v98, v98\n" \
    "v_exp_f32 v99, v99\n" \
    "v_exp_f32 v100, v100\n" \
    "v_exp_f32 v101, v101\n" \
    "v_mfma_f32_32x32x16_bf16 v[112:127], v[172:175], v[164:167], 0\n" \
    "ds_read_b128 v[168:171], v193 offset:4096\n" \
    "v_exp_f32 v102, v102\n" \
    "v_exp_f32 v103, v103\n" \
    "v_exp_f32 v104, v104\n" \
    "v_exp_f32 v105, v105\n" \
    "v_smfmac_f32_32x32x32_bf16 v[144:159], v[176:179], v[96:103], v192\n" \
    "v_exp_f32 v106, v106\n" \
    "v_exp_f32 v107, v107\n" \
    "v_exp_f32 v108, v108\n" \
    "v_exp_f32 v109, v109\n" \
    "v_exp_f32 v110, v110\n" \
    "v_exp_f32 v111, v111\n" \
    "v_exp_f32 v80, v80\n" \
    "v_exp_f32 v81, v81\n" \
    "v_smfmac_f32_32x32x32_bf16 v[144:159], v[180:183], v[104:111], v192\n" \
    "ds_read_b128 v[176:179], v194 offset:4096\n" \
    "v_exp_f32 v82, v82\n" \
    "v_exp_f32 v83, v83\n" \
    "v_exp_f32 v84, v84\n" \
    "v_exp_f32 v85, v85\n" \
    "s_waitcnt lgkmcnt(1)\n" \
    "v_mfma_f32_32x32x16_bf16 v[64:79], v[168:171], v[160:163], 0\n" \
    "ds_read_b128 v[180:183], v194 offset:4608\n" \
    "v_exp_f32 v86, v86\n" \
    "v_exp_f32 v87, v87\n" \
    "v_exp_f32 v88, v88\n" \
    "v_exp_f32 v89, v89\n" \
    "v_smfmac_f32_32x32x32_bf16 v[128:143], v[184:187], v[80:87], v192\n" \
    "v_exp_f32 v90, v90\n" \
    "s_mov_b32 m0, %[skv]\n" \
    "v_exp_f32 v91, v91\n" \
    "v_exp_f32 v92, v92\n" \
    "global_load_lds_dwordx4 v32, %[kvb] offset:3072 sc1\n" \
    "v_exp_f32 v93, v93\n" \
    "v_exp_f32 v94, v94\n" \
    "v_exp_f32 v95, v95\n" \
    "v_exp_f32 v112, v112\n" \
    "v_exp_f32 v113, v113\n" \
    "v_smfmac_f32_32x32x32_bf16 v[128:143], v[188:191], v[88:95], v192\n" \
    "v_exp_f32 v114, v114\n" \
    "v_exp_f32 v115, v115\n" \
    "v_exp_f32 v116, v116\n" \
    "v_exp_f32 v117, v117\n" \
    "v_mfma_f32_32x32x16_bf16 v[96:111], v[168:171], v[164:167], 0\n" \
    "ds_read_b128 v[172:175], v193 offset:5120\n" \
    "v_exp_f32 v118, v118\n" \
    "v_exp_f32 v119, v119\n" \
    "v_exp_f32 v120, v120\n" \
    "v_exp_f32 v121, v121\n" \
    "v_smfmac_f32_32x32x32_bf16 v[144:159], v[184:187], v[112:119], v192\n" \
    "v_exp_f32 v122, v122\n" \
    "v_exp_f32 v123, v123\n" \
    "v_exp_f32 v124, v124\n" \
    "v_exp_f32 v125, v125\n" \
    "v_exp_f32 v126, v126\n" \
    "v_exp_f32 v127, v127\n" \
    "v_add_u32 v32, 0x2000, v32\n" \
    "v_mov_b64 v[34:35], v[36:37]\n" \
    "s_add_u32 %[skv], %[skv], 0x2000\n" \
    "s_add_u32 %[sg], %[sg], 0x400\n" \
    "v_mov_b32 v44, v45\n" \
    "v_add_u32 v193, 4096, v193\n" \
    "v_add_u32 v194, v195, v194\n" \
    "s_sub_u32 %[cnt], %[cnt], 1\n" \
    "s_cmp_lg_u32 %[cnt], 0\n" \
    "s_cbranch_scc1 1b\n" \
    "s_waitcnt vmcnt(5)\n"

#define ATT_B \
    "v_exp_f32 v64, v64\n" \
    "v_exp_f32 v65, v65\n" \
    "v_smfmac_f32_32x32x32_bf16 v[144:159], v[188:191], v[120:127], v192\n" \
    "ds_read_b64 v[222:223], %[cta]\n" \
    "ds_read_b128 v[184:187], v194 offset:1024\n" \
    "v_exp_f32 v66, v66\n" \
    "v_exp_f32 v67, v67\n" \
    "v_exp_f32 v68, v68\n" \
    "v_exp_f32 v69, v69\n" \
    "s_waitcnt lgkmcnt(1)\n" \
    "v_mfma_f32_32x32x16_bf16 v[80:95], v[172:175], v[160:163], 0\n" \
    "ds_read_b128 v[188:191], v194 offset:1536\n" \
    "v_exp_f32 v70, v70\n" \
    "v_exp_f32 v71, v71\n" \
    "v_exp_f32 v72, v72\n" \
    "v_exp_f32 v73, v73\n" \
    "v_smfmac_f32_32x32x32_bf16 v[128:143], v[176:179], v[64:71], v192\n" \
    "v_exp_f32 v74, v74\n" \
    "s_mov_b32 m0, %[skv3]\n" \
    "v_readfirstlane_b32 %[cnt], v222\n" \
    "v_readfirstlane_b32 %[key], v223\n" \
    "s_max_u32 %[key], %[key], %[cnt]\n" \
    "s_and_b32 %[cnt], %[key], 0xffff\n" \
    "s_sub_u32 %[cnt], 0xffff, %[cnt]\n" \
    "s_and_b32 %[cnt], %[cnt], 2047\n" \
    "s_lshl_b32 %[cnt], %[cnt], 10\n" \
    "v_add_u32 v38, %[cnt], v38\n" \
    "v_exp_f32 v75, v75\n" \
    "v_exp_f32 v76, v76\n" \
    "global_load_dwordx4 v[204:207], v38, %[xb]\n" \
    "v_exp_f32 v77, v77\n" \
    "v_exp_f32 v78, v78\n" \
    "v_exp_f32 v79, v79\n" \
    "v_exp_f32 v96, v96\n" \
    "v_exp_f32 v97, v97\n" \
    "v_smfmac_f32_32x32x32_bf16 v[128:143], v[180:183], v[72:79], v192\n" \
    "v_exp_f32 v98, v98\n" \
    "v_exp_f32 v99, v99\n" \
    "v_exp_f32 v100, v100\n" \
    "v_exp_f32 v101, v101\n" \
    "v_mfma_f32_32x32x16_bf16 v[112:127], v[172:175], v[164:167], 0\n" \
    "ds_read_b128 v[168:171], v193 offset:2048\n" \
    "v_exp_f32 v102, v102\n" \
    "v_exp_f32 v103, v103\n" \
    "v_exp_f32 v104, v104\n" \
    "v_exp_f32 v105, v105\n" \
    "v_smfmac_f32_32x32x32_bf16 v[144:159], v[176:179], v[96:103], v192\n" \
    "v_exp_f32 v106, v106\n" \
    "v_exp_f32 v107, v107\n" \
    "v_exp_f32 v108, v108\n" \
    "global_load_dwordx4 v[208:211], v38, %[xb] offset:16\n" \
    "v_exp_f32 v109, v109\n" \
    "v_exp_f32 v110, v110\n" \
    "v_exp_f32 v111, v111\n" \
    "v_exp_f32 v80, v80\n" \
    "v_exp_f32 v81, v81\n" \
    "v_smfmac_f32_32x32x32_bf16 v[144:159], v[180:183], v[104:111], v192\n" \
    "ds_read_b128 v[176:179], v194 offset:2048\n" \
    "v_exp_f32 v82, v82\n" \
    "v_exp_f32 v83, v83\n" \
    "v_exp_f32 v84, v84\n" \
    "v_exp_f32 v85, v85\n" \
    "s_waitcnt lgkmcnt(1)\n" \
    "v_mfma_f32_32x32x16_bf16 v[64:79], v[168:171], v[160:163], 0\n" \
    "ds_read_b128 v[180:183], v194 offset:2560\n" \
    "v_exp_f32 v86, v86\n" \
    "v_exp_f32 v87, v87\n" \
    "v_exp_f32 v88, v88\n" \
    "v_exp_f32 v89, v89\n" \
    "v_smfmac_f32_32x32x32_bf16 v[128:143], v[184:187], v[80:87], v192\n" \
    "v_exp_f32 v90, v90\n" \
    "v_exp_f32 v91, v91\n" \
    "v_exp_f32 v92, v92\n" \
    "global_load_lds_dwordx4 v33, %[kvb] offset:0 sc1\n" \
    "v_exp_f32 v93, v93\n" \
    "v_exp_f32 v94, v94\n" \
    "v_exp_f32 v95, v95\n" \
    "v_exp_f32 v112, v112\n" \
    "v_exp_f32 v113, v113\n" \
    "v_smfmac_f32_32x32x32_bf16 v[128:143], v[188:191], v[88:95], v192\n" \
    "v_exp_f32 v114, v114\n" \
    "v_exp_f32 v115, v115\n" \
    "v_exp_f32 v116, v116\n" \
    "v_exp_f32 v117, v117\n" \
    "v_mfma_f32_32x32x16_bf16 v[96:111], v[168:171], v[164:167], 0\n" \
    "ds_read_b128 v[172:175], v193 offset:3072\n" \
    "v_exp_f32 v118, v118\n" \
    "v_exp_f32 v119, v119\n" \
    "v_exp_f32 v120, v120\n" \
    "v_exp_f32 v121, v121\n" \
    "v_smfmac_f32_32x32x32_bf16 v[144:159], v[184:187], v[112:119], v192\n" \
    "v_exp_f32 v122, v122\n" \
    "v_exp_f32 v123, v123\n" \
    "v_exp_f32 v124, v124\n" \
    "global_load_lds_dwordx4 v33, %[kvb] offset:1024 sc1\n" \
    "v_exp_f32 v125, v125\n" \
    "v_exp_f32 v126, v126\n" \
    "v_exp_f32 v127, v127\n" \
    "v_exp_f32 v64, v64\n" \
    "v_exp_f32 v65, v65\n" \
    "v_smfmac_f32_32x32x32_bf16 v[144:159], v[188:191], v[120:127], v192\n" \
    "ds_read_b128 v[184:187], v194 offset:3072\n" \
    "v_exp_f32 v66, v66\n" \
    "v_exp_f32 v67, v67\n" \
    "v_exp_f32 v68, v68\n" \
    "v_exp_f32 v69, v69\n" \
    "s_waitcnt lgkmcnt(1)\n" \
    "v_mfma_f32_32x32x16_bf16 v[80:95], v[172:175], v[160:163], 0\n" \
    "ds_read_b128 v[188:191], v194 offset:3584\n" \
    "v_exp_f32 v70, v70\n" \
    "v_exp_f32 v71, v71\n" \
    "v_exp_f32 v72, v72\n" \
    "v_exp_f32 v73, v73\n" \
    "v_smfmac_f32_32x32x32_bf16 v[128:143], v[176:179], v[64:71], v192\n" \
    "v_exp_f32 v74, v74\n" \
    "v_exp_f32 v75, v75\n" \
    "v_exp_f32 v76, v76\n" \
    "global_load_lds_dwordx4 v33, %[kvb] offset:2048 sc1\n" \
    "v_exp_f32 v77, v77\n" \
    "v_exp_f32 v78, v78\n" \
    "v_exp_f32 v79, v79\n" \
    "v_exp_f32 v96, v96\n" \
    "v_exp_f32 v97, v97\n" \
    "v_smfmac_f32_32x32x32_bf16 v[128:143], v[180:183], v[72:79], v192\n" \
    "v_exp_f32 v98, v98\n" \
    "v_exp_f32 v99, v99\n" \
    "v_exp_f32 v100, v100\n" \
    "v_exp_f32 v101, v101\n" \
    "v_mfma_f32_32x32x16_bf16 v[112:127], v[172:175], v[164:167], 0\n" \
    "ds_read_b128 v[168:171], v193 offset:4096\n" \
    "v_exp_f32 v102, v102\n" \
    "v_exp_f32 v103, v103\n" \
    "v_exp_f32 v104, v104\n" \
    "v_exp_f32 v105, v105\n" \
    "v_smfmac_f32_32x32x32_bf16 v[144:159], v[176:179], v[96:103], v192\n" \
    "v_exp_f32 v106, v106\n" \
    "v_exp_f32 v107, v107\n" \
    "v_exp_f32 v108, v108\n" \
    "global_load_lds_dwordx4 v33, %[kvb] offset:3072 sc1\n" \
    "v_exp_f32 v109, v109\n" \
    "v_exp_f32 v110, v110\n" \
    "v_exp_f32 v111, v111\n" \
    "v_exp_f32 v80, v80\n" \
    "v_exp_f32 v81, v81\n" \
    "v_smfmac_f32_32x32x32_bf16 v[144:159], v[180:183], v[104:111], v192\n" \
    "ds_read_b128 v[176:179], v194 offset:4096\n" \
    "v_exp_f32 v82, v82\n" \
    "v_exp_f32 v83, v83\n" \
    "v_exp_f32 v84, v84\n" \
    "v_exp_f32 v85, v85\n" \
    "s_waitcnt lgkmcnt(1)\n" \
    "v_mfma_f32_32x32x16_bf16 v[64:79], v[168:171], v[160:163], 0\n" \
    "ds_read_b128 v[180:183], v194 offset:4608\n" \
    "v_exp_f32 v86, v86\n" \
    "v_exp_f32 v87, v87\n" \
    "v_exp_f32 v88, v88\n" \
    "v_exp_f32 v89, v89\n" \
    "v_smfmac_f32_32x32x32_bf16 v[128:143], v[184:187], v[80:87], v192\n" \
    "v_exp_f32 v90, v90\n" \
    "v_exp_f32 v91, v91\n" \
    "v_exp_f32 v92, v92\n" \
    "global_load_dword v212, v39, %[wdb] offset:0\n" \
    "global_load_dword v213, v39, %[wdb] offset:1024\n" \
    "v_exp_f32 v93, v93\n" \
    "v_exp_f32 v94, v94\n" \
    "v_exp_f32 v95, v95\n" \
    "v_exp_f32 v112, v112\n" \
    "v_exp_f32 v113, v113\n" \
    "v_smfmac_f32_32x32x32_bf16 v[128:143], v[188:191], v[88:95], v192\n" \
    "v_exp_f32 v114, v114\n" \
    "v_exp_f32 v115, v115\n" \
    "v_exp_f32 v116, v116\n" \
    "v_exp_f32 v117, v117\n" \
    "v_mfma_f32_32x32x16_bf16 v[96:111], v[168:171], v[164:167], 0\n" \
    "ds_read_b128 v[172:175], v193 offset:5120\n" \
    "v_exp_f32 v118, v118\n" \
    "v_exp_f32 v119, v119\n" \
    "v_exp_f32 v120, v120\n" \
    "v_exp_f32 v121, v121\n" \
    "v_smfmac_f32_32x32x32_bf16 v[144:159], v[184:187], v[112:119], v192\n" \
    "v_exp_f32 v122, v122\n" \
    "v_exp_f32 v123, v123\n" \
    "v_exp_f32 v124, v124\n" \
    "global_load_dword v214, v39, %[wdb] offset:2048\n" \
    "global_load_dword v215, v39, %[wdb] offset:3072\n" \
    "v_exp_f32 v125, v125\n" \
    "v_exp_f32 v126, v126\n" \
    "v_exp_f32 v127, v127\n" \
    "v_add_u32 v39, 0x1000, v39\n" \
    "v_add_u32 v193, 4096, v193\n" \
    "v_add_u32 v194, v195, v194\n" \
    "v_exp_f32 v64, v64\n" \
    "v_exp_f32 v65, v65\n" \
    "v_smfmac_f32_32x32x32_bf16 v[144:159], v[188:191], v[120:127], v192\n" \
    "ds_read_b128 v[184:187], v194 offset:1024\n" \
    "v_exp_f32 v66, v66\n" \
    "v_exp_f32 v67, v67\n" \
    "v_exp_f32 v68, v68\n" \
    "v_exp_f32 v69, v69\n" \
    "s_waitcnt lgkmcnt(1)\n" \
    "v_mfma_f32_32x32x16_bf16 v[80:95], v[172:175], v[160:163], 0\n" \
    "ds_read_b128 v[188:191], v194 offset:1536\n" \
    "v_exp_f32 v70, v70\n" \
    "v_exp_f32 v71, v71\n" \
    "v_exp_f32 v72, v72\n" \
    "v_exp_f32 v73, v73\n" \
    "v_smfmac_f32_32x32x32_bf16 v[128:143], v[176:179], v[64:71], v192\n" \
    "v_exp_f32 v74, v74\n" \
    "v_exp_f32 v75, v75\n" \
    "v_exp_f32 v76, v76\n" \
    "global_load_dword v216, v39, %[wdb] offset:0\n" \
    "global_load_dword v217, v39, %[wdb] offset:1024\n" \
    "v_exp_f32 v77, v77\n" \
    "v_exp_f32 v78, v78\n" \
    "v_exp_f32 v79, v79\n" \
    "v_exp_f32 v96, v96\n" \
    "v_exp_f32 v97, v97\n" \
    "v_smfmac_f32_32x32x32_bf16 v[128:143], v[180:183], v[72:79], v192\n" \
    "v_exp_f32 v98, v98\n" \
    "v_exp_f32 v99, v99\n" \
    "v_exp_f32 v100, v100\n" \
    "v_exp_f32 v101, v101\n" \
    "v_mfma_f32_32x32x16_bf16 v[112:127], v[172:175], v[164:167], 0\n" \
    "ds_read_b128 v[168:171], v193 offset:2048\n" \
    "v_exp_f32 v102, v102\n" \
    "v_exp_f32 v103, v103\n" \
    "v_exp_f32 v104, v104\n" \
    "v_exp_f32 v105, v105\n" \
    "v_smfmac_f32_32x32x32_bf16 v[144:159], v[176:179], v[96:103], v192\n" \
    "v_exp_f32 v106, v106\n" \
    "v_exp_f32 v107, v107\n" \
    "v_exp_f32 v108, v108\n" \
    "global_load_dword v218, v39, %[wdb] offset:2048\n" \
    "global_load_dword v219, v39, %[wdb] offset:3072\n" \
    "v_exp_f32 v109, v109\n" \
    "v_exp_f32 v110, v110\n" \
    "v_exp_f32 v111, v111\n" \
    "v_exp_f32 v80, v80\n" \
    "v_exp_f32 v81, v81\n" \
    "v_smfmac_f32_32x32x32_bf16 v[144:159], v[180:183], v[104:111], v192\n" \
    "s_waitcnt vmcnt(12)\n" \
    "ds_read_b64_tr_b16 v[220:221], v40\n" \
    "ds_read_b64_tr_b16 v[222:223], v41\n" \
    "ds_read_b64_tr_b16 v[224:225], v40 offset:256\n" \
    "ds_read_b64_tr_b16 v[226:227], v41 offset:256\n" \
    "ds_read_b128 v[176:179], v194 offset:2048\n" \
    "v_exp_f32 v82, v82\n" \
    "v_exp_f32 v83, v83\n" \
    "v_exp_f32 v84, v84\n" \
    "v_exp_f32 v85, v85\n" \
    "s_waitcnt lgkmcnt(1)\n" \
    "v_mfma_f32_32x32x16_bf16 v[64:79], v[168:171], v[160:163], 0\n" \
    "ds_read_b128 v[180:183], v194 offset:2560\n" \
    "v_exp_f32 v86, v86\n" \
    "v_exp_f32 v87, v87\n" \
    "v_exp_f32 v88, v88\n" \
    "v_exp_f32 v89, v89\n" \
    "v_smfmac_f32_32x32x32_bf16 v[128:143], v[184:187], v[80:87], v192\n" \
    "v_exp_f32 v90, v90\n" \
    "v_exp_f32 v91, v91\n" \
    "v_exp_f32 v92, v92\n" \
    "v_mfma_f32_16x16x32_bf16 v[232:235], v[220:223], v[204:207], 0\n" \
    "v_exp_f32 v93, v93\n" \
    "v_exp_f32 v94, v94\n" \
    "v_exp_f32 v95, v95\n" \
    "v_exp_f32 v112, v112\n" \
    "v_exp_f32 v113, v113\n" \
    "v_smfmac_f32_32x32x32_bf16 v[128:143], v[188:191], v[88:95], v192\n" \
    "v_exp_f32 v114, v114\n" \
    "v_exp_f32 v115, v115\n" \
    "v_exp_f32 v116, v116\n" \
    "v_exp_f32 v117, v117\n" \
    "v_mfma_f32_32x32x16_bf16 v[96:111], v[168:171], v[164:167], 0\n" \
    "ds_read_b128 v[172:175], v193 offset:3072\n" \
    "v_exp_f32 v118, v118\n" \
    "v_exp_f32 v119, v119\n" \
    "v_exp_f32 v120, v120\n" \
    "v_exp_f32 v121, v121\n" \
    "v_smfmac_f32_32x32x32_bf16 v[144:159], v[184:187], v[112:119], v192\n" \
    "v_exp_f32 v122, v122\n" \
    "v_exp_f32 v123, v123\n" \
    "v_exp_f32 v124, v124\n" \
    "v_exp_f32 v125, v125\n" \
    "v_exp_f32 v126, v126\n" \
    "v_exp_f32 v127, v127\n" \
    "s_setprio 0\n" \
    "v_exp_f32 v64, v64\n" \
    "v_exp_f32 v65, v65\n" \
    "v_smfmac_f32_32x32x32_bf16 v[144:159], v[188:191], v[120:127], v192\n" \
    "ds_read_b128 v[184:187], v194 offset:3072\n" \
    "v_exp_f32 v66, v66\n" \
    "v_exp_f32 v67, v67\n" \
    "v_exp_f32 v68, v68\n" \
    "v_exp_f32 v69, v69\n" \
    "s_waitcnt lgkmcnt(1)\n" \
    "v_mfma_f32_32x32x16_bf16 v[80:95], v[172:175], v[160:163], 0\n" \
    "ds_read_b128 v[188:191], v194 offset:3584\n" \
    "v_exp_f32 v70, v70\n" \
    "v_exp_f32 v71, v71\n" \
    "v_exp_f32 v72, v72\n" \
    "v_exp_f32 v73, v73\n" \
    "v_smfmac_f32_32x32x32_bf16 v[128:143], v[176:179], v[64:71], v192\n" \
    "v_exp_f32 v74, v74\n" \
    "v_exp_f32 v75, v75\n" \
    "v_exp_f32 v76, v76\n" \
    "v_mfma_f32_16x16x32_bf16 v[232:235], v[224:227], v[208:211], v[232:235]\n" \
    "v_exp_f32 v77, v77\n" \
    "v_exp_f32 v78, v78\n" \
    "v_exp_f32 v79, v79\n" \
    "v_exp_f32 v96, v96\n" \
    "v_exp_f32 v97, v97\n" \
    "v_smfmac_f32_32x32x32_bf16 v[128:143], v[180:183], v[72:79], v192\n" \
    "v_exp_f32 v98, v98\n" \
    "v_exp_f32 v99, v99\n" \
    "v_exp_f32 v100, v100\n" \
    "v_exp_f32 v101, v101\n" \
    "v_mfma_f32_32x32x16_bf16 v[112:127], v[172:175], v[164:167], 0\n" \
    "ds_read_b128 v[168:171], v193 offset:4096\n" \
    "v_exp_f32 v102, v102\n" \
    "v_exp_f32 v103, v103\n" \
    "v_exp_f32 v104, v104\n" \
    "v_exp_f32 v105, v105\n" \
    "v_smfmac_f32_32x32x32_bf16 v[144:159], v[176:179], v[96:103], v192\n" \
    "v_exp_f32 v106, v106\n" \
    "v_exp_f32 v107, v107\n" \
    "v_exp_f32 v108, v108\n" \
    "v_exp_f32 v109, v109\n" \
    "v_exp_f32 v110, v110\n" \
    "v_exp_f32 v111, v111\n" \
    "v_exp_f32 v80, v80\n" \
    "v_exp_f32 v81, v81\n" \
    "v_smfmac_f32_32x32x32_bf16 v[144:159], v[180:183], v[104:111], v192\n" \
    "ds_write_b128 v42, v[232:235]\n" \
    "ds_read_b128 v[176:179], v194 offset:4096\n" \
    "v_exp_f32 v82, v82\n" \
    "v_exp_f32 v83, v83\n" \
    "v_exp_f32 v84, v84\n" \
    "v_exp_f32 v85, v85\n" \
    "s_waitcnt lgkmcnt(1)\n" \
    "v_mfma_f32_32x32x16_bf16 v[64:79], v[168:171], v[160:163], 0\n" \
    "ds_read_b128 v[180:183], v194 offset:4608\n" \
    "v_exp_f32 v86, v86\n" \
    "v_exp_f32 v87, v87\n" \
    "v_exp_f32 v88, v88\n" \
    "v_exp_f32 v89, v89\n" \
    "v_smfmac_f32_32x32x32_bf16 v[128:143], v[184:187], v[80:87], v192\n" \
    "v_exp_f32 v90, v90\n" \
    "v_exp_f32 v91, v91\n" \
    "v_exp_f32 v92, v92\n" \
    "v_exp_f32 v93, v93\n" \
    "v_exp_f32 v94, v94\n" \
    "v_exp_f32 v95, v95\n" \
    "v_exp_f32 v112, v112\n" \
    "v_exp_f32 v113, v113\n" \
    "v_smfmac_f32_32x32x32_bf16 v[128:143], v[188:191], v[88:95], v192\n" \
    "v_exp_f32 v114, v114\n" \
    "v_exp_f32 v115, v115\n" \
    "v_exp_f32 v116, v116\n" \
    "v_exp_f32 v117, v117\n" \
    "v_mfma_f32_32x32x16_bf16 v[96:111], v[168:171], v[164:167], 0\n" \
    "ds_read_b128 v[172:175], v193 offset:5120\n" \
    "v_exp_f32 v118, v118\n" \
    "v_exp_f32 v119, v119\n" \
    "v_exp_f32 v120, v120\n" \
    "v_exp_f32 v121, v121\n" \
    "v_smfmac_f32_32x32x32_bf16 v[144:159], v[184:187], v[112:119], v192\n" \
    "v_exp_f32 v122, v122\n" \
    "v_exp_f32 v123, v123\n" \
    "v_exp_f32 v124, v124\n" \
    "v_exp_f32 v125, v125\n" \
    "v_exp_f32 v126, v126\n" \
    "v_exp_f32 v127, v127\n" \
    "v_add_u32 v193, 4096, v193\n" \
    "v_add_u32 v194, v195, v194\n" \
    "s_waitcnt vmcnt(0)\n"

#define MEAS 0
#define ATT_C \
    "s_mov_b32 s80, 2\n" \
    "1:\n" \
    "v_exp_f32 v64, v64\n" \
    "v_exp_f32 v65, v65\n" \
    "v_smfmac_f32_32x32x32_bf16 v[144:159], v[188:191], v[120:127], v192\n" \
    "ds_read_b128 v[184:187], v194 offset:1024\n" \
    "v_exp_f32 v66, v66\n" \
    "v_exp_f32 v67, v67\n" \
    "v_exp_f32 v68, v68\n" \
    "v_exp_f32 v69, v69\n" \
    "s_waitcnt lgkmcnt(1)\n" \
    "v_mfma_f32_32x32x16_bf16 v[80:95], v[172:175], v[160:163], 0\n" \
    "ds_read_b128 v[188:191], v194 offset:1536\n" \
    "v_exp_f32 v70, v70\n" \
    "v_exp_f32 v71, v71\n" \
    "v_exp_f32 v72, v72\n" \
    "v_exp_f32 v73, v73\n" \
    "v_smfmac_f32_32x32x32_bf16 v[128:143], v[176:179], v[64:71], v192\n" \
    "v_exp_f32 v74, v74\n" \
    "s_mov_b32 m0, %[swo]\n" \
    "v_exp_f32 v75, v75\n" \
    "v_exp_f32 v76, v76\n" \
    "global_load_lds_dwordx4 v236, %[wob] offset:0\n" \
    "v_exp_f32 v77, v77\n" \
    "v_exp_f32 v78, v78\n" \
    "v_exp_f32 v79, v79\n" \
    "v_exp_f32 v96, v96\n" \
    "v_exp_f32 v97, v97\n" \
    "v_smfmac_f32_32x32x32_bf16 v[128:143], v[180:183], v[72:79], v192\n" \
    "v_exp_f32 v98, v98\n" \
    "v_exp_f32 v99, v99\n" \
    "v_exp_f32 v100, v100\n" \
    "v_exp_f32 v101, v101\n" \
    "v_mfma_f32_32x32x16_bf16 v[112:127], v[172:175], v[164:167], 0\n" \
    "ds_read_b128 v[168:171], v193 offset:2048\n" \
    "v_exp_f32 v102, v102\n" \
    "v_exp_f32 v103, v103\n" \
    "v_exp_f32 v104, v104\n" \
    "v_exp_f32 v105, v105\n" \
    "v_smfmac_f32_32x32x32_bf16 v[144:159], v[176:179], v[96:103], v192\n" \
    "v_exp_f32 v106, v106\n" \
    "v_exp_f32 v107, v107\n" \
    "v_exp_f32 v108, v108\n" \
    "v_exp_f32 v109, v109\n" \
    "v_exp_f32 v110, v110\n" \
    "v_exp_f32 v111, v111\n" \
    "v_exp_f32 v80, v80\n" \
    "v_exp_f32 v81, v81\n" \
    "v_smfmac_f32_32x32x32_bf16 v[144:159], v[180:183], v[104:111], v192\n" \
    "ds_read_b128 v[176:179], v194 offset:2048\n" \
    "v_exp_f32 v82, v82\n" \
    "v_exp_f32 v83, v83\n" \
    "v_exp_f32 v84, v84\n" \
    "v_exp_f32 v85, v85\n" \
    "s_waitcnt lgkmcnt(1)\n" \
    "v_mfma_f32_32x32x16_bf16 v[64:79], v[168:171], v[160:163], 0\n" \
    "ds_read_b128 v[180:183], v194 offset:2560\n" \
    "v_exp_f32 v86, v86\n" \
    "v_exp_f32 v87, v87\n" \
    "v_exp_f32 v88, v88\n" \
    "v_exp_f32 v89, v89\n" \
    "v_smfmac_f32_32x32x32_bf16 v[128:143], v[184:187], v[80:87], v192\n" \
    "v_exp_f32 v90, v90\n" \
    "v_exp_f32 v91, v91\n" \
    "v_exp_f32 v92, v92\n" \
    "global_load_lds_dwordx4 v237, %[wob] offset:1024\n" \
    "v_exp_f32 v93, v93\n" \
    "v_exp_f32 v94, v94\n" \
    "v_exp_f32 v95, v95\n" \
    "v_exp_f32 v112, v112\n" \
    "v_exp_f32 v113, v113\n" \
    "v_smfmac_f32_32x32x32_bf16 v[128:143], v[188:191], v[88:95], v192\n" \
    "v_exp_f32 v114, v114\n" \
    "v_exp_f32 v115, v115\n" \
    "v_exp_f32 v116, v116\n" \
    "v_exp_f32 v117, v117\n" \
    "v_mfma_f32_32x32x16_bf16 v[96:111], v[168:171], v[164:167], 0\n" \
    "ds_read_b128 v[172:175], v193 offset:3072\n" \
    "v_exp_f32 v118, v118\n" \
    "v_exp_f32 v119, v119\n" \
    "v_exp_f32 v120, v120\n" \
    "v_exp_f32 v121, v121\n" \
    "v_smfmac_f32_32x32x32_bf16 v[144:159], v[184:187], v[112:119], v192\n" \
    "v_exp_f32 v122, v122\n" \
    "v_exp_f32 v123, v123\n" \
    "v_exp_f32 v124, v124\n" \
    "v_exp_f32 v125, v125\n" \
    "v_exp_f32 v126, v126\n" \
    "v_exp_f32 v127, v127\n" \
    "v_exp_f32 v64, v64\n" \
    "v_exp_f32 v65, v65\n" \
    "v_smfmac_f32_32x32x32_bf16 v[144:159], v[188:191], v[120:127], v192\n" \
    "ds_read_b128 v[184:187], v194 offset:3072\n" \
    "v_exp_f32 v66, v66\n" \
    "v_exp_f32 v67, v67\n" \
    "v_exp_f32 v68, v68\n" \
    "v_exp_f32 v69, v69\n" \
    "s_waitcnt lgkmcnt(1)\n" \
    "v_mfma_f32_32x32x16_bf16 v[80:95], v[172:175], v[160:163], 0\n" \
    "ds_read_b128 v[188:191], v194 offset:3584\n" \
    "v_exp_f32 v70, v70\n" \
    "v_exp_f32 v71, v71\n" \
    "v_exp_f32 v72, v72\n" \
    "v_exp_f32 v73, v73\n" \
    "v_smfmac_f32_32x32x32_bf16 v[128:143], v[176:179], v[64:71], v192\n" \
    "v_exp_f32 v74, v74\n" \
    "v_exp_f32 v75, v75\n" \
    "v_exp_f32 v76, v76\n" \
    "global_load_lds_dwordx4 v238, %[wob] offset:2048\n" \
    "v_exp_f32 v77, v77\n" \
    "v_exp_f32 v78, v78\n" \
    "v_exp_f32 v79, v79\n" \
    "v_exp_f32 v96, v96\n" \
    "v_exp_f32 v97, v97\n" \
    "v_smfmac_f32_32x32x32_bf16 v[128:143], v[180:183], v[72:79], v192\n" \
    "v_exp_f32 v98, v98\n" \
    "v_exp_f32 v99, v99\n" \
    "v_exp_f32 v100, v100\n" \
    "v_exp_f32 v101, v101\n" \
    "v_mfma_f32_32x32x16_bf16 v[112:127], v[172:175], v[164:167], 0\n" \
    "ds_read_b128 v[168:171], v193 offset:4096\n" \
    "v_exp_f32 v102, v102\n" \
    "v_exp_f32 v103, v103\n" \
    "v_exp_f32 v104, v104\n" \
    "v_exp_f32 v105, v105\n" \
    "v_smfmac_f32_32x32x32_bf16 v[144:159], v[176:179], v[96:103], v192\n" \
    "v_exp_f32 v106, v106\n" \
    "v_exp_f32 v107, v107\n" \
    "v_exp_f32 v108, v108\n" \
    "v_exp_f32 v109, v109\n" \
    "v_exp_f32 v110, v110\n" \
    "v_exp_f32 v111, v111\n" \
    "v_exp_f32 v80, v80\n" \
    "v_exp_f32 v81, v81\n" \
    "v_smfmac_f32_32x32x32_bf16 v[144:159], v[180:183], v[104:111], v192\n" \
    "ds_read_b128 v[176:179], v194 offset:4096\n" \
    "v_exp_f32 v82, v82\n" \
    "v_exp_f32 v83, v83\n" \
    "v_exp_f32 v84, v84\n" \
    "v_exp_f32 v85, v85\n" \
    "s_waitcnt lgkmcnt(1)\n" \
    "v_mfma_f32_32x32x16_bf16 v[64:79], v[168:171], v[160:163], 0\n" \
    "ds_read_b128 v[180:183], v194 offset:4608\n" \
    "v_exp_f32 v86, v86\n" \
    "v_exp_f32 v87, v87\n" \
    "v_exp_f32 v88, v88\n" \
    "v_exp_f32 v89, v89\n" \
    "v_smfmac_f32_32x32x32_bf16 v[128:143], v[184:187], v[80:87], v192\n" \
    "v_exp_f32 v90, v90\n" \
    "v_exp_f32 v91, v91\n" \
    "v_exp_f32 v92, v92\n" \
    "global_load_lds_dwordx4 v239, %[wob] offset:3072\n" \
    "v_exp_f32 v93, v93\n" \
    "v_exp_f32 v94, v94\n" \
    "v_exp_f32 v95, v95\n" \
    "v_exp_f32 v112, v112\n" \
    "v_exp_f32 v113, v113\n" \
    "v_smfmac_f32_32x32x32_bf16 v[128:143], v[188:191], v[88:95], v192\n" \
    "v_exp_f32 v114, v114\n" \
    "v_exp_f32 v115, v115\n" \
    "v_exp_f32 v116, v116\n" \
    "v_exp_f32 v117, v117\n" \
    "v_mfma_f32_32x32x16_bf16 v[96:111], v[168:171], v[164:167], 0\n" \
    "ds_read_b128 v[172:175], v193 offset:5120\n" \
    "v_exp_f32 v118, v118\n" \
    "v_exp_f32 v119, v119\n" \
    "v_exp_f32 v120, v120\n" \
    "v_exp_f32 v121, v121\n" \
    "v_smfmac_f32_32x32x32_bf16 v[144:159], v[184:187], v[112:119], v192\n" \
    "v_exp_f32 v122, v122\n" \
    "v_exp_f32 v123, v123\n" \
    "v_exp_f32 v124, v124\n" \
    "v_exp_f32 v125, v125\n" \
    "v_exp_f32 v126, v126\n" \
    "v_exp_f32 v127, v127\n" \
    "v_add_u32 v236, 0x8000, v236\n" \
    "v_add_u32 v237, 0x8000, v237\n" \
    "v_add_u32 v238, 0x8000, v238\n" \
    "v_add_u32 v239, 0x8000, v239\n" \
    "s_add_u32 %[swo], %[swo], 0x1000\n" \
    "v_add_u32 v193, 4096, v193\n" \
    "v_add_u32 v194, v195, v194\n" \
    "s_sub_u32 s80, s80, 1\n" \
    "s_cmp_lg_u32 s80, 0\n" \
    "s_cbranch_scc1 1b\n" \
    "global_load_dword v220, v240, %[wsb] sc1\n" \
    "s_mov_b32 s81, 0x7f800000\n" \
    "v_exp_f32 v64, v64\n" \
    "v_exp_f32 v65, v65\n" \
    "v_smfmac_f32_32x32x32_bf16 v[144:159], v[188:191], v[120:127], v192\n" \
    "ds_read_b128 v[184:187], v194 offset:1024\n" \
    "v_exp_f32 v66, v66\n" \
    "v_exp_f32 v67, v67\n" \
    "v_exp_f32 v68, v68\n" \
    "v_exp_f32 v69, v69\n" \
    "s_waitcnt lgkmcnt(1)\n" \
    "v_mfma_f32_32x32x16_bf16 v[80:95], v[172:175], v[160:163], 0\n" \
    "ds_read_b128 v[188:191], v194 offset:1536\n" \
    "v_exp_f32 v70, v70\n" \
    "v_exp_f32 v71, v71\n" \
    "v_exp_f32 v72, v72\n" \
    "v_exp_f32 v73, v73\n" \
    "v_smfmac_f32_32x32x32_bf16 v[128:143], v[176:179], v[64:71], v192\n" \
    "v_exp_f32 v74, v74\n" \
    "v_exp_f32 v75, v75\n" \
    "v_exp_f32 v76, v76\n" \
    "v_exp_f32 v77, v77\n" \
    "v_exp_f32 v78, v78\n" \
    "v_exp_f32 v79, v79\n" \
    "v_exp_f32 v96, v96\n" \
    "v_exp_f32 v97, v97\n" \
    "v_smfmac_f32_32x32x32_bf16 v[128:143], v[180:183], v[72:79], v192\n" \
    "v_exp_f32 v98, v98\n" \
    "v_exp_f32 v99, v99\n" \
    "v_exp_f32 v100, v100\n" \
    "v_exp_f32 v101, v101\n" \
    "v_mfma_f32_32x32x16_bf16 v[112:127], v[172:175], v[164:167], 0\n" \
    "ds_read_b128 v[168:171], v193 offset:2048\n" \
    "v_exp_f32 v102, v102\n" \
    "v_exp_f32 v103, v103\n" \
    "v_exp_f32 v104, v104\n" \
    "v_exp_f32 v105, v105\n" \
    "v_smfmac_f32_32x32x32_bf16 v[144:159], v[176:179], v[96:103], v192\n" \
    "v_exp_f32 v106, v106\n" \
    "v_exp_f32 v107, v107\n" \
    "v_exp_f32 v108, v108\n" \
    "v_exp_f32 v109, v109\n" \
    "v_exp_f32 v110, v110\n" \
    "v_exp_f32 v111, v111\n" \
    "v_exp_f32 v80, v80\n" \
    "v_exp_f32 v81, v81\n" \
    "v_smfmac_f32_32x32x32_bf16 v[144:159], v[180:183], v[104:111], v192\n" \
    "ds_read_b128 v[176:179], v194 offset:2048\n" \
    "v_exp_f32 v82, v82\n" \
    "v_exp_f32 v83, v83\n" \
    "v_exp_f32 v84, v84\n" \
    "v_exp_f32 v85, v85\n" \
    "s_waitcnt lgkmcnt(1)\n" \
    "v_mfma_f32_32x32x16_bf16 v[64:79], v[168:171], v[160:163], 0\n" \
    "ds_read_b128 v[180:183], v194 offset:2560\n" \
    "v_exp_f32 v86, v86\n" \
    "v_exp_f32 v87, v87\n" \
    "v_exp_f32 v88, v88\n" \
    "v_exp_f32 v89, v89\n" \
    "v_smfmac_f32_32x32x32_bf16 v[128:143], v[184:187], v[80:87], v192\n" \
    "v_exp_f32 v90, v90\n" \
    "v_exp_f32 v91, v91\n" \
    "v_exp_f32 v92, v92\n" \
    "v_exp_f32 v93, v93\n" \
    "v_exp_f32 v94, v94\n" \
    "v_exp_f32 v95, v95\n" \
    "v_exp_f32 v112, v112\n" \
    "v_exp_f32 v113, v113\n" \
    "v_smfmac_f32_32x32x32_bf16 v[128:143], v[188:191], v[88:95], v192\n" \
    "v_exp_f32 v114, v114\n" \
    "v_exp_f32 v115, v115\n" \
    "v_exp_f32 v116, v116\n" \
    "v_exp_f32 v117, v117\n" \
    "v_mfma_f32_32x32x16_bf16 v[96:111], v[168:171], v[164:167], 0\n" \
    "ds_read_b128 v[172:175], v193 offset:3072\n" \
    "v_exp_f32 v118, v118\n" \
    "v_exp_f32 v119, v119\n" \
    "v_exp_f32 v120, v120\n" \
    "v_exp_f32 v121, v121\n" \
    "v_smfmac_f32_32x32x32_bf16 v[144:159], v[184:187], v[112:119], v192\n" \
    "v_exp_f32 v122, v122\n" \
    "v_exp_f32 v123, v123\n" \
    "v_exp_f32 v124, v124\n" \
    "v_exp_f32 v125, v125\n" \
    "v_exp_f32 v126, v126\n" \
    "v_exp_f32 v127, v127\n" \
    "v_exp_f32 v64, v64\n" \
    "v_exp_f32 v65, v65\n" \
    "v_smfmac_f32_32x32x32_bf16 v[144:159], v[188:191], v[120:127], v192\n" \
    "ds_read_b128 v[184:187], v194 offset:3072\n" \
    "v_exp_f32 v66, v66\n" \
    "v_exp_f32 v67, v67\n" \
    "v_exp_f32 v68, v68\n" \
    "v_exp_f32 v69, v69\n" \
    "s_waitcnt lgkmcnt(1)\n" \
    "v_mfma_f32_32x32x16_bf16 v[80:95], v[172:175], v[160:163], 0\n" \
    "ds_read_b128 v[188:191], v194 offset:3584\n" \
    "v_exp_f32 v70, v70\n" \
    "v_exp_f32 v71, v71\n" \
    "v_exp_f32 v72, v72\n" \
    "v_exp_f32 v73, v73\n" \
    "v_smfmac_f32_32x32x32_bf16 v[128:143], v[176:179], v[64:71], v192\n" \
    "v_exp_f32 v74, v74\n" \
    "v_exp_f32 v75, v75\n" \
    "v_exp_f32 v76, v76\n" \
    "v_exp_f32 v77, v77\n" \
    "v_exp_f32 v78, v78\n" \
    "v_exp_f32 v79, v79\n" \
    "v_exp_f32 v96, v96\n" \
    "v_exp_f32 v97, v97\n" \
    "v_smfmac_f32_32x32x32_bf16 v[128:143], v[180:183], v[72:79], v192\n" \
    "v_exp_f32 v98, v98\n" \
    "v_exp_f32 v99, v99\n" \
    "v_exp_f32 v100, v100\n" \
    "v_exp_f32 v101, v101\n" \
    "v_mfma_f32_32x32x16_bf16 v[112:127], v[172:175], v[164:167], 0\n" \
    "ds_read_b128 v[168:171], v193 offset:4096\n" \
    "v_exp_f32 v102, v102\n" \
    "v_exp_f32 v103, v103\n" \
    "v_exp_f32 v104, v104\n" \
    "v_exp_f32 v105, v105\n" \
    "v_smfmac_f32_32x32x32_bf16 v[144:159], v[176:179], v[96:103], v192\n" \
    "v_exp_f32 v106, v106\n" \
    "v_exp_f32 v107, v107\n" \
    "v_exp_f32 v108, v108\n" \
    "v_exp_f32 v109, v109\n" \
    "v_exp_f32 v110, v110\n" \
    "v_exp_f32 v111, v111\n" \
    "global_load_dwordx4 v[228:231], v242, %[xb]\n" \
    "global_load_dword v232, v243, %[wsb] sc1\n" \
    "v_mov_b32 v233, %[cwo]\n" \
    "v_mov_b32 v234, 1\n" \
    "s_waitcnt vmcnt(2)\n" \
    "s_mov_b64 exec, 1\n" \
    "ds_add_u32 v233, v234\n" \
    "s_mov_b64 exec, -1\n" \
    "v_cmp_eq_u32 vcc, %[ep], v220\n" \
    "s_cmp_eq_u64 vcc, -1\n" \
    "s_cselect_b32 s86, 1, 0\n" \
    "s_and_b32 s84, s86, %[fl]\n" \
    "s_cbranch_scc1 71f\n" \
    "72:\n" \
    "v_exp_f32 v80, v80\n" \
    "v_exp_f32 v81, v81\n" \
    "v_smfmac_f32_32x32x32_bf16 v[144:159], v[180:183], v[104:111], v192\n" \
    "ds_read_b128 v[176:179], v194 offset:4096\n" \
    "v_exp_f32 v82, v82\n" \
    "v_exp_f32 v83, v83\n" \
    "v_exp_f32 v84, v84\n" \
    "v_exp_f32 v85, v85\n" \
    "s_waitcnt lgkmcnt(1)\n" \
    "v_mfma_f32_32x32x16_bf16 v[64:79], v[168:171], v[160:163], 0\n" \
    "ds_read_b128 v[180:183], v194 offset:4608\n" \
    "v_exp_f32 v86, v86\n" \
    "v_exp_f32 v87, v87\n" \
    "v_exp_f32 v88, v88\n" \
    "v_exp_f32 v89, v89\n" \
    "v_smfmac_f32_32x32x32_bf16 v[128:143], v[184:187], v[80:87], v192\n" \
    "v_exp_f32 v90, v90\n" \
    "v_exp_f32 v91, v91\n" \
    "v_exp_f32 v92, v92\n" \
    "ds_read_b32 v221, v233\n" \
    "v_exp_f32 v93, v93\n" \
    "v_exp_f32 v94, v94\n" \
    "v_exp_f32 v95, v95\n" \
    "v_exp_f32 v112, v112\n" \
    "v_exp_f32 v113, v113\n" \
    "v_smfmac_f32_32x32x32_bf16 v[128:143], v[188:191], v[88:95], v192\n" \
    "v_exp_f32 v114, v114\n" \
    "v_exp_f32 v115, v115\n" \
    "v_exp_f32 v116, v116\n" \
    "v_exp_f32 v117, v117\n" \
    "v_mfma_f32_32x32x16_bf16 v[96:111], v[168:171], v[164:167], 0\n" \
    "ds_read_b128 v[172:175], v193 offset:5120\n" \
    "v_exp_f32 v118, v118\n" \
    "v_exp_f32 v119, v119\n" \
    "v_exp_f32 v120, v120\n" \
    "v_exp_f32 v121, v121\n" \
    "v_smfmac_f32_32x32x32_bf16 v[144:159], v[184:187], v[112:119], v192\n" \
    "v_exp_f32 v122, v122\n" \
    "v_exp_f32 v123, v123\n" \
    "v_exp_f32 v124, v124\n" \
    "v_exp_f32 v125, v125\n" \
    "v_exp_f32 v126, v126\n" \
    "v_exp_f32 v127, v127\n" \
    "s_setprio 0\n" \
    "v_add_u32 v193, 4096, v193\n" \
    "v_add_u32 v194, v195, v194\n" \
    "v_exp_f32 v64, v64\n" \
    "v_exp_f32 v65, v65\n" \
    "v_smfmac_f32_32x32x32_bf16 v[144:159], v[188:191], v[120:127], v192\n" \
    "ds_read_b128 v[184:187], v194 offset:1024\n" \
    "v_exp_f32 v66, v66\n" \
    "v_exp_f32 v67, v67\n" \
    "v_exp_f32 v68, v68\n" \
    "v_exp_f32 v69, v69\n" \
    "s_waitcnt lgkmcnt(1)\n" \
    "v_mfma_f32_32x32x16_bf16 v[80:95], v[172:175], v[160:163], 0\n" \
    "ds_read_b128 v[188:191], v194 offset:1536\n" \
    "v_exp_f32 v70, v70\n" \
    "v_exp_f32 v71, v71\n" \
    "v_exp_f32 v72, v72\n" \
    "v_exp_f32 v73, v73\n" \
    "v_smfmac_f32_32x32x32_bf16 v[128:143], v[176:179], v[64:71], v192\n" \
    "v_exp_f32 v74, v74\n" \
    "v_readfirstlane_b32 s84, v221\n" \
    "s_cmp_ge_u32 s84, 4\n" \
    "s_cselect_b32 s85, 1, 0\n" \
    "v_exp_f32 v75, v75\n" \
    "v_exp_f32 v76, v76\n" \
    "s_cbranch_scc0 110f\n" \
    "ds_read_b64_tr_b16 v[0:1], v249 offset:0\n" \
    "ds_read_b64_tr_b16 v[2:3], v249 offset:512\n" \
    "ds_read_b64_tr_b16 v[4:5], v250 offset:0\n" \
    "ds_read_b64_tr_b16 v[6:7], v250 offset:512\n" \
    "ds_read_b64_tr_b16 v[8:9], v251 offset:0\n" \
    "ds_read_b64_tr_b16 v[10:11], v251 offset:512\n" \
    "ds_read_b64_tr_b16 v[12:13], v252 offset:0\n" \
    "ds_read_b64_tr_b16 v[14:15], v252 offset:512\n" \
    "110:\n" \
    "v_exp_f32 v77, v77\n" \
    "v_exp_f32 v78, v78\n" \
    "v_exp_f32 v79, v79\n" \
    "v_exp_f32 v96, v96\n" \
    "v_exp_f32 v97, v97\n" \
    "v_smfmac_f32_32x32x32_bf16 v[128:143], v[180:183], v[72:79], v192\n" \
    "v_exp_f32 v98, v98\n" \
    "v_exp_f32 v99, v99\n" \
    "v_exp_f32 v100, v100\n" \
    "v_exp_f32 v101, v101\n" \
    "v_mfma_f32_32x32x16_bf16 v[112:127], v[172:175], v[164:167], 0\n" \
    "ds_read_b128 v[168:171], v193 offset:2048\n" \
    "v_exp_f32 v102, v102\n" \
    "v_exp_f32 v103, v103\n" \
    "v_exp_f32 v104, v104\n" \
    "v_exp_f32 v105, v105\n" \
    "v_smfmac_f32_32x32x32_bf16 v[144:159], v[176:179], v[96:103], v192\n" \
    "v_exp_f32 v106, v106\n" \
    "v_exp_f32 v107, v107\n" \
    "v_exp_f32 v108, v108\n" \
    "v_exp_f32 v109, v109\n" \
    "v_exp_f32 v110, v110\n" \
    "v_exp_f32 v111, v111\n" \
    "v_exp_f32 v80, v80\n" \
    "v_exp_f32 v81, v81\n" \
    "v_smfmac_f32_32x32x32_bf16 v[144:159], v[180:183], v[104:111], v192\n" \
    "ds_read_b128 v[176:179], v194 offset:2048\n" \
    "v_exp_f32 v82, v82\n" \
    "v_exp_f32 v83, v83\n" \
    "v_exp_f32 v84, v84\n" \
    "v_exp_f32 v85, v85\n" \
    "s_waitcnt lgkmcnt(1)\n" \
    "v_mfma_f32_32x32x16_bf16 v[64:79], v[168:171], v[160:163], 0\n" \
    "ds_read_b128 v[180:183], v194 offset:2560\n" \
    "v_exp_f32 v86, v86\n" \
    "v_exp_f32 v87, v87\n" \
    "v_exp_f32 v88, v88\n" \
    "v_exp_f32 v89, v89\n" \
    "v_smfmac_f32_32x32x32_bf16 v[128:143], v[184:187], v[80:87], v192\n" \
    "v_exp_f32 v90, v90\n" \
    "v_exp_f32 v91, v91\n" \
    "v_exp_f32 v92, v92\n" \
    "s_cbranch_scc0 111f\n" \
    "ds_read_b64_tr_b16 v[16:17], v249 offset:4096\n" \
    "ds_read_b64_tr_b16 v[18:19], v249 offset:4608\n" \
    "ds_read_b64_tr_b16 v[20:21], v250 offset:4096\n" \
    "ds_read_b64_tr_b16 v[22:23], v250 offset:4608\n" \
    "ds_read_b64_tr_b16 v[24:25], v251 offset:4096\n" \
    "ds_read_b64_tr_b16 v[26:27], v251 offset:4608\n" \
    "ds_read_b64_tr_b16 v[28:29], v252 offset:4096\n" \
    "ds_read_b64_tr_b16 v[30:31], v252 offset:4608\n" \
    "111:\n" \
    "v_exp_f32 v93, v93\n" \
    "v_exp_f32 v94, v94\n" \
    "v_exp_f32 v95, v95\n" \
    "v_exp_f32 v112, v112\n" \
    "v_exp_f32 v113, v113\n" \
    "v_smfmac_f32_32x32x32_bf16 v[128:143], v[188:191], v[88:95], v192\n" \
    "v_exp_f32 v114, v114\n" \
    "v_exp_f32 v115, v115\n" \
    "v_exp_f32 v116, v116\n" \
    "v_exp_f32 v117, v117\n" \
    "v_mfma_f32_32x32x16_bf16 v[96:111], v[168:171], v[164:167], 0\n" \
    "ds_read_b128 v[172:175], v193 offset:3072\n" \
    "v_exp_f32 v118, v118\n" \
    "v_exp_f32 v119, v119\n" \
    "v_exp_f32 v120, v120\n" \
    "v_exp_f32 v121, v121\n" \
    "v_smfmac_f32_32x32x32_bf16 v[144:159], v[184:187], v[112:119], v192\n" \
    "v_exp_f32 v122, v122\n" \
    "v_exp_f32 v123, v123\n" \
    "v_exp_f32 v124, v124\n" \
    "v_exp_f32 v125, v125\n" \
    "v_exp_f32 v126, v126\n" \
    "v_exp_f32 v127, v127\n" \
    "v_exp_f32 v64, v64\n" \
    "v_exp_f32 v65, v65\n" \
    "v_smfmac_f32_32x32x32_bf16 v[144:159], v[188:191], v[120:127], v192\n" \
    "ds_read_b128 v[184:187], v194 offset:3072\n" \
    "v_exp_f32 v66, v66\n" \
    "v_exp_f32 v67, v67\n" \
    "v_exp_f32 v68, v68\n" \
    "v_exp_f32 v69, v69\n" \
    "s_waitcnt lgkmcnt(1)\n" \
    "v_mfma_f32_32x32x16_bf16 v[80:95], v[172:175], v[160:163], 0\n" \
    "ds_read_b128 v[188:191], v194 offset:3584\n" \
    "v_exp_f32 v70, v70\n" \
    "v_exp_f32 v71, v71\n" \
    "v_exp_f32 v72, v72\n" \
    "v_exp_f32 v73, v73\n" \
    "v_smfmac_f32_32x32x32_bf16 v[128:143], v[176:179], v[64:71], v192\n" \
    "v_exp_f32 v74, v74\n" \
    "v_exp_f32 v75, v75\n" \
    "v_exp_f32 v76, v76\n" \
    "s_cbranch_scc0 112f\n" \
    "ds_read_b64_tr_b16 v[32:33], v249 offset:8192\n" \
    "ds_read_b64_tr_b16 v[34:35], v249 offset:8704\n" \
    "ds_read_b64_tr_b16 v[36:37], v250 offset:8192\n" \
    "ds_read_b64_tr_b16 v[38:39], v250 offset:8704\n" \
    "ds_read_b64_tr_b16 v[40:41], v251 offset:8192\n" \
    "ds_read_b64_tr_b16 v[42:43], v251 offset:8704\n" \
    "ds_read_b64_tr_b16 v[44:45], v252 offset:8192\n" \
    "ds_read_b64_tr_b16 v[46:47], v252 offset:8704\n" \
    "112:\n" \
    "v_exp_f32 v77, v77\n" \
    "v_exp_f32 v78, v78\n" \
    "v_exp_f32 v79, v79\n" \
    "v_exp_f32 v96, v96\n" \
    "v_exp_f32 v97, v97\n" \
    "v_smfmac_f32_32x32x32_bf16 v[128:143], v[180:183], v[72:79], v192\n" \
    "v_exp_f32 v98, v98\n" \
    "v_exp_f32 v99, v99\n" \
    "v_exp_f32 v100, v100\n" \
    "v_exp_f32 v101, v101\n" \
    "v_mfma_f32_32x32x16_bf16 v[112:127], v[172:175], v[164:167], 0\n" \
    "v_exp_f32 v102, v102\n" \
    "v_exp_f32 v103, v103\n" \
    "v_exp_f32 v104, v104\n" \
    "v_exp_f32 v105, v105\n" \
    "v_smfmac_f32_32x32x32_bf16 v[144:159], v[176:179], v[96:103], v192\n" \
    "v_exp_f32 v106, v106\n" \
    "v_exp_f32 v107, v107\n" \
    "v_exp_f32 v108, v108\n" \
    "v_exp_f32 v109, v109\n" \
    "v_exp_f32 v110, v110\n" \
    "v_exp_f32 v111, v111\n" \
    "v_exp_f32 v80, v80\n" \
    "v_exp_f32 v81, v81\n" \
    "v_smfmac_f32_32x32x32_bf16 v[144:159], v[180:183], v[104:111], v192\n" \
    "v_exp_f32 v82, v82\n" \
    "v_exp_f32 v83, v83\n" \
    "v_exp_f32 v84, v84\n" \
    "v_exp_f32 v85, v85\n" \
    "s_waitcnt lgkmcnt(0)\n" \
    "v_exp_f32 v86, v86\n" \
    "v_exp_f32 v87, v87\n" \
    "v_exp_f32 v88, v88\n" \
    "v_exp_f32 v89, v89\n" \
    "v_smfmac_f32_32x32x32_bf16 v[128:143], v[184:187], v[80:87], v192\n" \
    "v_exp_f32 v90, v90\n" \
    "v_exp_f32 v91, v91\n" \
    "v_exp_f32 v92, v92\n" \
    "s_cbranch_scc0 113f\n" \
    "ds_read_b64_tr_b16 v[48:49], v249 offset:12288\n" \
    "ds_read_b64_tr_b16 v[50:51], v249 offset:12800\n" \
    "ds_read_b64_tr_b16 v[52:53], v250 offset:12288\n" \
    "ds_read_b64_tr_b16 v[54:55], v250 offset:12800\n" \
    "ds_read_b64_tr_b16 v[56:57], v251 offset:12288\n" \
    "ds_read_b64_tr_b16 v[58:59], v251 offset:12800\n" \
    "ds_read_b64_tr_b16 v[60:61], v252 offset:12288\n" \
    "ds_read_b64_tr_b16 v[62:63], v252 offset:12800\n" \
    "113:\n" \
    "v_exp_f32 v93, v93\n" \
    "v_exp_f32 v94, v94\n" \
    "v_exp_f32 v95, v95\n" \
    "v_exp_f32 v112, v112\n" \
    "v_exp_f32 v113, v113\n" \
    "v_smfmac_f32_32x32x32_bf16 v[128:143], v[188:191], v[88:95], v192\n" \
    "v_exp_f32 v114, v114\n" \
    "v_exp_f32 v115, v115\n" \
    "v_exp_f32 v116, v116\n" \
    "v_exp_f32 v117, v117\n" \
    "v_exp_f32 v118, v118\n" \
    "v_exp_f32 v119, v119\n" \
    "v_exp_f32 v120, v120\n" \
    "v_exp_f32 v121, v121\n" \
    "v_smfmac_f32_32x32x32_bf16 v[144:159], v[184:187], v[112:119], v192\n" \
    "v_exp_f32 v122, v122\n" \
    "s_cbranch_scc0 114f\n" \
    "ds_read_b64_tr_b16 v[64:65], v249 offset:32768\n" \
    "ds_read_b64_tr_b16 v[66:67], v249 offset:33280\n" \
    "ds_read_b64_tr_b16 v[68:69], v250 offset:32768\n" \
    "ds_read_b64_tr_b16 v[70:71], v250 offset:33280\n" \
    "ds_read_b64_tr_b16 v[72:73], v251 offset:32768\n" \
    "ds_read_b64_tr_b16 v[74:75], v251 offset:33280\n" \
    "ds_read_b64_tr_b16 v[76:77], v252 offset:32768\n" \
    "ds_read_b64_tr_b16 v[78:79], v252 offset:33280\n" \
    "114:\n" \
    "v_exp_f32 v123, v123\n" \
    "v_exp_f32 v124, v124\n" \
    "s_cbranch_scc0 115f\n" \
    "ds_read_b64_tr_b16 v[96:97], v249 offset:40960\n" \
    "ds_read_b64_tr_b16 v[98:99], v249 offset:41472\n" \
    "ds_read_b64_tr_b16 v[100:101], v250 offset:40960\n" \
    "ds_read_b64_tr_b16 v[102:103], v250 offset:41472\n" \
    "ds_read_b64_tr_b16 v[104:105], v251 offset:40960\n" \
    "ds_read_b64_tr_b16 v[106:107], v251 offset:41472\n" \
    "ds_read_b64_tr_b16 v[108:109], v252 offset:40960\n" \
    "ds_read_b64_tr_b16 v[110:111], v252 offset:41472\n" \
    "115:\n" \
    "v_exp_f32 v125, v125\n" \
    "v_exp_f32 v126, v126\n" \
    "v_exp_f32 v127, v127\n" \
    "ds_write_b128 v244, v[128:131]\n" \
    "ds_write_b128 v244, v[132:135] offset:1024\n" \
    "ds_write_b32 v246, v136\n" \
    "s_nop 1\n" \
    "v_smfmac_f32_32x32x32_bf16 v[144:159], v[188:191], v[120:127], v192\n" \
    "s_setprio 0\n" \
    "s_bitcmp1_b32 s86, 0\n" \
    "s_cbranch_scc0 76f\n" \
    "77:\n" \
    "s_waitcnt lgkmcnt(0)\n" \
    "s_barrier\n" \
    "ds_read_b32 v136, v247\n" \
    "ds_read_b128 v[128:131], v245\n" \
    "ds_read_b128 v[132:135], v245 offset:1024\n" \
    "s_nop 5\n" \
    "v_mov_b32 v137, v152\n" \
    "v_mov_b32 v138, v152\n" \
    "s_nop 1\n" \
    "v_permlane32_swap_b32 v137, v138\n" \
    "s_waitcnt lgkmcnt(2)\n" \
    "v_add_f32 v137, v137, v136\n" \
    "s_waitcnt lgkmcnt(1)\n" \
    "v_pk_add_f32 v[144:145], v[144:145], v[128:129]\n" \
    "v_rcp_f32 v138, v137\n" \
    "v_pk_add_f32 v[146:147], v[146:147], v[130:131]\n" \
    "v_subrev_u32 v139, 0x21800000, v137\n" \
    "s_waitcnt lgkmcnt(0)\n" \
    "v_pk_add_f32 v[148:149], v[148:149], v[132:133]\n" \
    "v_pk_add_f32 v[150:151], v[150:151], v[134:135]\n" \
    "v_cmp_ge_u32 vcc, 0x50000000, v139\n" \
    "v_pk_add_f32 v[140:141], v[144:145], v[146:147]\n" \
    "v_pk_mul_f32 v[128:129], v[144:145], v[138:139] op_sel_hi:[1,0]\n" \
    "v_pk_add_f32 v[142:143], v[148:149], v[150:151]\n" \
    "v_pk_mul_f32 v[130:131], v[146:147], v[138:139] op_sel_hi:[1,0]\n" \
    "v_pk_mul_f32 v[132:133], v[148:149], v[138:139] op_sel_hi:[1,0]\n" \
    "v_pk_add_f32 v[140:141], v[140:141], v[142:143]\n" \
    "v_pk_mul_f32 v[134:135], v[150:151], v[138:139] op_sel_hi:[1,0]\n" \
    "v_cvt_pk_bf16_f32 v168, v128, v129\n" \
    "v_add_f32 v140, v140, v141\n" \
    "v_cvt_pk_bf16_f32 v170, v132, v133\n" \
    "v_cvt_pk_bf16_f32 v169, v130, v131\n" \
    "v_cmp_lt_f32 s[82:83], |v140|, s81\n" \
    "v_cvt_pk_bf16_f32 v171, v134, v135\n" \
    "s_and_b64 vcc, vcc, s[82:83]\n" \
    "s_cmp_eq_u64 vcc, -1\n" \
    "s_cbranch_scc0 80f\n" \
    "81:\n" \
    "v_permlane32_swap_b32 v168, v170\n" \
    "v_permlane32_swap_b32 v169, v171\n" \
    "s_nop 0\n" \
    "global_store_dwordx4 v248, v[168:171], %[wsb] sc1\n" \
    "s_cmp_eq_u32 s85, 0\n" \
    "s_cbranch_scc1 73f\n" \
    "74:\n" \
    "ds_read_b64_tr_b16 v[80:81], v249 offset:36864\n" \
    "ds_read_b64_tr_b16 v[82:83], v249 offset:37376\n" \
    "ds_read_b64_tr_b16 v[84:85], v250 offset:36864\n" \
    "ds_read_b64_tr_b16 v[86:87], v250 offset:37376\n" \
    "ds_read_b64_tr_b16 v[88:89], v251 offset:36864\n" \
    "ds_read_b64_tr_b16 v[90:91], v251 offset:37376\n" \
    "ds_read_b64_tr_b16 v[92:93], v252 offset:36864\n" \
    "ds_read_b64_tr_b16 v[94:95], v252 offset:37376\n" \
    "ds_read_b64_tr_b16 v[112:113], v249 offset:45056\n" \
    "ds_read_b64_tr_b16 v[114:115], v249 offset:45568\n" \
    "ds_read_b64_tr_b16 v[116:117], v250 offset:45056\n" \
    "ds_read_b64_tr_b16 v[118:119], v250 offset:45568\n" \
    "ds_read_b64_tr_b16 v[120:121], v251 offset:45056\n" \
    "ds_read_b64_tr_b16 v[122:123], v251 offset:45568\n" \
    "ds_read_b64_tr_b16 v[124:125], v252 offset:45056\n" \
    "ds_read_b64_tr_b16 v[126:127], v252 offset:45568\n" \
    "s_bitcmp1_b32 %[fl], 0\n" \
    "s_cbranch_scc1 47f\n" \
    "s_sleep 14\n" \
    "48:\n" \
    "global_load_dwordx4 v[128:131], v253, %[wsb] offset:-4096 sc1\n" \
    "global_load_dwordx4 v[132:135], v253, %[wsb] offset:-3072 sc1\n" \
    "global_load_dwordx4 v[136:139], v253, %[wsb] offset:-2048 sc1\n" \
    "global_load_dwordx4 v[140:143], v253, %[wsb] offset:-1024 sc1\n" \
    "global_load_dwordx4 v[144:147], v253, %[wsb] offset:0 sc1\n" \
    "global_load_dwordx4 v[148:151], v253, %[wsb] offset:1024 sc1\n" \
    "global_load_dwordx4 v[152:155], v253, %[wsb] offset:2048 sc1\n" \
    "global_load_dwordx4 v[156:159], v253, %[wsb] offset:3072 sc1\n" \
    "s_waitcnt vmcnt(9)\n" \
    "v_cvt_pk_bf16_f32 v187, v232, v232\n" \
    "v_and_b32 v187, 0xffff0000, v187\n" \
    "v_mul_f32 v187, 0xbfb8aa3b, v187\n" \
    "v_exp_f32 v187, v187\n" \
    "v_lshlrev_b32 v176, 16, v228\n" \
    "v_and_b32 v177, 0xffff0000, v228\n" \
    "v_lshlrev_b32 v178, 16, v229\n" \
    "v_and_b32 v179, 0xffff0000, v229\n" \
    "v_add_f32 v187, 1.0, v187\n" \
    "v_rcp_f32 v187, v187\n" \
    "v_lshlrev_b32 v180, 16, v230\n" \
    "v_and_b32 v181, 0xffff0000, v230\n" \
    "v_lshlrev_b32 v182, 16, v231\n" \
    "v_and_b32 v183, 0xffff0000, v231\n" \
    "v_cvt_pk_bf16_f32 v187, v187, v187\n" \
    "v_and_b32 v184, 0xffff0000, v187\n" \
    "v_sub_f32 v187, 1.0, v184\n" \
    "v_cvt_pk_bf16_f32 v187, v187, v187\n" \
    "v_and_b32 v186, 0xffff0000, v187\n" \
    "s_bitcmp1_b32 %[fl], 1\n" \
    "s_cbranch_scc1 40f\n" \
    "41:\n" \
    "v_pk_mul_f32 v[176:177], v[176:177], v[186:187] op_sel_hi:[1,0]\n" \
    "v_pk_mul_f32 v[178:179], v[178:179], v[186:187] op_sel_hi:[1,0]\n" \
    "v_pk_mul_f32 v[180:181], v[180:181], v[186:187] op_sel_hi:[1,0]\n" \
    "v_pk_mul_f32 v[182:183], v[182:183], v[186:187] op_sel_hi:[1,0]\n" \
    "v_cvt_pk_bf16_f32 v188, v176, v177\n" \
    "v_cvt_pk_bf16_f32 v189, v178, v179\n" \
    "v_cvt_pk_bf16_f32 v190, v180, v181\n" \
    "v_cvt_pk_bf16_f32 v191, v182, v183\n" \
    "v_lshlrev_b32 v176, 16, v188\n" \
    "v_and_b32 v177, 0xffff0000, v188\n" \
    "v_lshlrev_b32 v178, 16, v189\n" \
    "v_and_b32 v179, 0xffff0000, v189\n" \
    "v_lshlrev_b32 v180, 16, v190\n" \
    "v_and_b32 v181, 0xffff0000, v190\n" \
    "v_lshlrev_b32 v182, 16, v191\n" \
    "v_and_b32 v183, 0xffff0000, v191\n" \
    "s_nop 1\n" \
    "v_permlane16_swap_b32 v176, v180\n" \
    "v_permlane16_swap_b32 v177, v181\n" \
    "v_permlane16_swap_b32 v178, v182\n" \
    "v_permlane16_swap_b32 v179, v183\n" \
    "s_mov_b32 s87, 0\n" \
    "s_waitcnt vmcnt(7)\n" \
    "10:\n" \
    "v_max3_u32 v187, v128, v129, v130\n" \
    "v_max_u32 v187, v187, v131\n" \
    "v_cmp_eq_u32 vcc, -1, v187\n" \
    "s_cbranch_vccnz 30f\n" \
    "20:\n" \
    "s_waitcnt lgkmcnt(0)\n" \
    "v_mfma_f32_16x16x32_bf16 v[168:171], v[8:11], v[128:131], 0\n" \
    "v_mfma_f32_16x16x32_bf16 v[172:175], v[12:15], v[128:131], 0\n" \
    "s_waitcnt vmcnt(6)\n" \
    "11:\n" \
    "v_max3_u32 v187, v132, v133, v134\n" \
    "v_max_u32 v187, v187, v135\n" \
    "v_cmp_eq_u32 vcc, -1, v187\n" \
    "s_cbranch_vccnz 31f\n" \
    "21:\n" \
    "v_mfma_f32_16x16x32_bf16 v[160:163], v[0:3], v[128:131], 0\n" \
    "v_mfma_f32_16x16x32_bf16 v[164:167], v[4:7], v[128:131], 0\n" \
    "v_mfma_f32_16x16x32_bf16 v[168:171], v[24:27], v[132:135], v[168:171]\n" \
    "v_mfma_f32_16x16x32_bf16 v[172:175], v[28:31], v[132:135], v[172:175]\n" \
    "s_waitcnt vmcnt(5)\n" \
    "12:\n" \
    "v_max3_u32 v187, v136, v137, v138\n" \
    "v_max_u32 v187, v187, v139\n" \
    "v_cmp_eq_u32 vcc, -1, v187\n" \
    "s_cbranch_vccnz 32f\n" \
    "22:\n" \
    "v_mfma_f32_16x16x32_bf16 v[160:163], v[16:19], v[132:135], v[160:163]\n" \
    "v_mfma_f32_16x16x32_bf16 v[164:167], v[20:23], v[132:135], v[164:167]\n" \
    "v_mfma_f32_16x16x32_bf16 v[168:171], v[40:43], v[136:139], v[168:171]\n" \
    "v_mfma_f32_16x16x32_bf16 v[172:175], v[44:47], v[136:139], v[172:175]\n" \
    "s_waitcnt vmcnt(4)\n" \
    "13:\n" \
    "v_max3_u32 v187, v140, v141, v142\n" \
    "v_max_u32 v187, v187, v143\n" \
    "v_cmp_eq_u32 vcc, -1, v187\n" \
    "s_cbranch_vccnz 33f\n" \
    "23:\n" \
    "v_mfma_f32_16x16x32_bf16 v[160:163], v[32:35], v[136:139], v[160:163]\n" \
    "v_mfma_f32_16x16x32_bf16 v[164:167], v[36:39], v[136:139], v[164:167]\n" \
    "v_mfma_f32_16x16x32_bf16 v[168:171], v[56:59], v[140:143], v[168:171]\n" \
    "v_mfma_f32_16x16x32_bf16 v[172:175], v[60:63], v[140:143], v[172:175]\n" \
    "s_waitcnt vmcnt(3)\n" \
    "14:\n" \
    "v_max3_u32 v187, v144, v145, v146\n" \
    "v_max_u32 v187, v187, v147\n" \
    "v_cmp_eq_u32 vcc, -1, v187\n" \
    "s_cbranch_vccnz 34f\n" \
    "24:\n" \
    "v_mfma_f32_16x16x32_bf16 v[160:163], v[48:51], v[140:143], v[160:163]\n" \
    "v_mfma_f32_16x16x32_bf16 v[164:167], v[52:55], v[140:143], v[164:167]\n" \
    "v_mfma_f32_16x16x32_bf16 v[168:171], v[72:75], v[144:147], v[168:171]\n" \
    "v_mfma_f32_16x16x32_bf16 v[172:175], v[76:79], v[144:147], v[172:175]\n" \
    "s_waitcnt vmcnt(2)\n" \
    "15:\n" \
    "v_max3_u32 v187, v148, v149, v150\n" \
    "v_max_u32 v187, v187, v151\n" \
    "v_cmp_eq_u32 vcc, -1, v187\n" \
    "s_cbranch_vccnz 35f\n" \
    "25:\n" \
    "v_mfma_f32_16x16x32_bf16 v[160:163], v[64:67], v[144:147], v[160:163]\n" \
    "v_mfma_f32_16x16x32_bf16 v[164:167], v[68:71], v[144:147], v[164:167]\n" \
    "s_waitcnt lgkmcnt(0)\n" \
    "v_mfma_f32_16x16x32_bf16 v[168:171], v[88:91], v[148:151], v[168:171]\n" \
    "v_mfma_f32_16x16x32_bf16 v[172:175], v[92:95], v[148:151], v[172:175]\n" \
    "s_waitcnt vmcnt(1)\n" \
    "16:\n" \
    "v_max3_u32 v187, v152, v153, v154\n" \
    "v_max_u32 v187, v187, v155\n" \
    "v_cmp_eq_u32 vcc, -1, v187\n" \
    "s_cbranch_vccnz 36f\n" \
    "26:\n" \
    "v_mfma_f32_16x16x32_bf16 v[160:163], v[80:83], v[148:151], v[160:163]\n" \
    "v_mfma_f32_16x16x32_bf16 v[164:167], v[84:87], v[148:151], v[164:167]\n" \
    "v_mfma_f32_16x16x32_bf16 v[168:171], v[104:107], v[152:155], v[168:171]\n" \
    "v_mfma_f32_16x16x32_bf16 v[172:175], v[108:111], v[152:155], v[172:175]\n" \
    "s_waitcnt vmcnt(0)\n" \
    "17:\n" \
    "v_max3_u32 v187, v156, v157, v158\n" \
    "v_max_u32 v187, v187, v159\n" \
    "v_cmp_eq_u32 vcc, -1, v187\n" \
    "s_cbranch_vccnz 37f\n" \
    "27:\n" \
    "v_mfma_f32_16x16x32_bf16 v[160:163], v[96:99], v[152:155], v[160:163]\n" \
    "v_mfma_f32_16x16x32_bf16 v[164:167], v[100:103], v[152:155], v[164:167]\n" \
    "v_mfma_f32_16x16x32_bf16 v[168:171], v[120:123], v[156:159], v[168:171]\n" \
    "v_mfma_f32_16x16x32_bf16 v[172:175], v[124:127], v[156:159], v[172:175]\n" \
    "v_mfma_f32_16x16x32_bf16 v[160:163], v[112:115], v[156:159], v[160:163]\n" \
    "v_mfma_f32_16x16x32_bf16 v[164:167], v[116:119], v[156:159], v[164:167]\n" \
    "s_nop 4\n" \
    "ds_write_b128 v254, v[168:171]\n" \
    "ds_write_b128 v254, v[172:175] offset:1024\n" \
    "s_waitcnt lgkmcnt(0)\n" \
    "s_barrier\n" \
    "ds_read_b128 v[168:171], v255\n" \
    "ds_read_b128 v[172:175], v255 offset:1024\n" \
    "v_pk_fma_f32 v[160:161], v[160:161], v[184:185], v[176:177] op_sel_hi:[1,0,1]\n" \
    "v_pk_fma_f32 v[162:163], v[162:163], v[184:185], v[178:179] op_sel_hi:[1,0,1]\n" \
    "v_pk_fma_f32 v[164:165], v[164:165], v[184:185], v[180:181] op_sel_hi:[1,0,1]\n" \
    "v_pk_fma_f32 v[166:167], v[166:167], v[184:185], v[182:183] op_sel_hi:[1,0,1]\n" \
    "s_waitcnt lgkmcnt(1)\n" \
    "v_pk_fma_f32 v[160:161], v[168:169], v[184:185], v[160:161] op_sel_hi:[1,0,1]\n" \
    "v_pk_fma_f32 v[162:163], v[170:171], v[184:185], v[162:163] op_sel_hi:[1,0,1]\n" \
    "s_waitcnt lgkmcnt(0)\n" \
    "v_pk_fma_f32 v[164:165], v[172:173], v[184:185], v[164:165] op_sel_hi:[1,0,1]\n" \
    "v_pk_fma_f32 v[166:167], v[174:175], v[184:185], v[166:167] op_sel_hi:[1,0,1]\n" \
    "v_cvt_pk_bf16_f32 v172, v160, v161\n" \
    "v_cvt_pk_bf16_f32 v173, v162, v163\n" \
    "v_cvt_pk_bf16_f32 v174, v164, v165\n" \
    "v_cvt_pk_bf16_f32 v175, v166, v167\n" \
    "s_nop 1\n" \
    "v_permlane16_swap_b32 v172, v174\n" \
    "v_permlane16_swap_b32 v173, v175\n" \
    "s_nop 0\n" \
    "global_store_dwordx4 v242, v[172:175], %[ob] sc1\n" \
    "s_endpgm\n" \
    "70:\n" \
    "v_mov_b32 v222, v241\n" \
    "global_load_dwordx4 v[196:199], v222, %[wsb] sc1\n" \
    "v_add_u32 v222, 0x2000, v222\n" \
    "global_load_dwordx4 v[200:203], v222, %[wsb] sc1\n" \
    "v_add_u32 v222, 0x2000, v222\n" \
    "global_load_dwordx4 v[204:207], v222, %[wsb] sc1\n" \
    "v_add_u32 v222, 0x2000, v222\n" \
    "global_load_dwordx4 v[208:211], v222, %[wsb] sc1\n" \
    "v_add_u32 v222, 0x2000, v222\n" \
    "global_load_dwordx4 v[212:215], v222, %[wsb] sc1\n" \
    "v_add_u32 v222, 0x2000, v222\n" \
    "global_load_dwordx4 v[216:219], v222, %[wsb] sc1\n" \
    "v_add_u32 v222, 0x2000, v222\n" \
    "global_load_dwordx4 v[224:227], v222, %[wsb] sc1\n" \
    "s_setpc_b64 s[90:91]\n" \
    "71:\n" \
    "s_call_b64 s[90:91], 70b\n" \
    "s_mov_b32 s86, 3\n" \
    "s_branch 72b\n" \
    "73:\n" \
    "ds_read_b64_tr_b16 v[0:1], v249 offset:0\n" \
    "ds_read_b64_tr_b16 v[2:3], v249 offset:512\n" \
    "ds_read_b64_tr_b16 v[4:5], v250 offset:0\n" \
    "ds_read_b64_tr_b16 v[6:7], v250 offset:512\n" \
    "ds_read_b64_tr_b16 v[8:9], v251 offset:0\n" \
    "ds_read_b64_tr_b16 v[10:11], v251 offset:512\n" \
    "ds_read_b64_tr_b16 v[12:13], v252 offset:0\n" \
    "ds_read_b64_tr_b16 v[14:15], v252 offset:512\n" \
    "ds_read_b64_tr_b16 v[16:17], v249 offset:4096\n" \
    "ds_read_b64_tr_b16 v[18:19], v249 offset:4608\n" \
    "ds_read_b64_tr_b16 v[20:21], v250 offset:4096\n" \
    "ds_read_b64_tr_b16 v[22:23], v250 offset:4608\n" \
    "ds_read_b64_tr_b16 v[24:25], v251 offset:4096\n" \
    "ds_read_b64_tr_b16 v[26:27], v251 offset:4608\n" \
    "ds_read_b64_tr_b16 v[28:29], v252 offset:4096\n" \
    "ds_read_b64_tr_b16 v[30:31], v252 offset:4608\n" \
    "ds_read_b64_tr_b16 v[32:33], v249 offset:8192\n" \
    "ds_read_b64_tr_b16 v[34:35], v249 offset:8704\n" \
    "ds_read_b64_tr_b16 v[36:37], v250 offset:8192\n" \
    "ds_read_b64_tr_b16 v[38:39], v250 offset:8704\n" \
    "ds_read_b64_tr_b16 v[40:41], v251 offset:8192\n" \
    "ds_read_b64_tr_b16 v[42:43], v251 offset:8704\n" \
    "ds_read_b64_tr_b16 v[44:45], v252 offset:8192\n" \
    "ds_read_b64_tr_b16 v[46:47], v252 offset:8704\n" \
    "ds_read_b64_tr_b16 v[48:49], v249 offset:12288\n" \
    "ds_read_b64_tr_b16 v[50:51], v249 offset:12800\n" \
    "ds_read_b64_tr_b16 v[52:53], v250 offset:12288\n" \
    "ds_read_b64_tr_b16 v[54:55], v250 offset:12800\n" \
    "ds_read_b64_tr_b16 v[56:57], v251 offset:12288\n" \
    "ds_read_b64_tr_b16 v[58:59], v251 offset:12800\n" \
    "ds_read_b64_tr_b16 v[60:61], v252 offset:12288\n" \
    "ds_read_b64_tr_b16 v[62:63], v252 offset:12800\n" \
    "ds_read_b64_tr_b16 v[64:65], v249 offset:32768\n" \
    "ds_read_b64_tr_b16 v[66:67], v249 offset:33280\n" \
    "ds_read_b64_tr_b16 v[68:69], v250 offset:32768\n" \
    "ds_read_b64_tr_b16 v[70:71], v250 offset:33280\n" \
    "ds_read_b64_tr_b16 v[72:73], v251 offset:32768\n" \
    "ds_read_b64_tr_b16 v[74:75], v251 offset:33280\n" \
    "ds_read_b64_tr_b16 v[76:77], v252 offset:32768\n" \
    "ds_read_b64_tr_b16 v[78:79], v252 offset:33280\n" \
    "ds_read_b64_tr_b16 v[96:97], v249 offset:40960\n" \
    "ds_read_b64_tr_b16 v[98:99], v249 offset:41472\n" \
    "ds_read_b64_tr_b16 v[100:101], v250 offset:40960\n" \
    "ds_read_b64_tr_b16 v[102:103], v250 offset:41472\n" \
    "ds_read_b64_tr_b16 v[104:105], v251 offset:40960\n" \
    "ds_read_b64_tr_b16 v[106:107], v251 offset:41472\n" \
    "ds_read_b64_tr_b16 v[108:109], v252 offset:40960\n" \
    "ds_read_b64_tr_b16 v[110:111], v252 offset:41472\n" \
    "s_branch 74b\n" \
    "76:\n" \
    "s_mov_b32 s87, 0\n" \
    "78:\n" \
    "global_load_dword v220, v240, %[wsb] sc1\n" \
    "s_waitcnt vmcnt(0)\n" \
    "v_cmp_eq_u32 vcc, %[ep], v220\n" \
    "s_cmp_eq_u64 vcc, -1\n" \
    "s_cbranch_scc1 77b\n" \
    "s_add_u32 s87, s87, 1\n" \
    "s_cmp_gt_u32 s87, 0x400000\n" \
    "s_cbranch_scc0 78b\n" \
    "s_branch 77b\n" \
    "49:\n" \
    "s_waitcnt vmcnt(0)\n" \
    "v_max3_u32 v187, v128, v129, v130\n" \
    "v_max_u32 v187, v187, v131\n" \
    "v_cmp_eq_u32 vcc, -1, v187\n" \
    "s_cbranch_vccz 60f\n" \
    "global_load_dwordx4 v[128:131], v253, %[wsb] offset:-4096 sc1\n" \
    "60:\n" \
    "v_max3_u32 v187, v132, v133, v134\n" \
    "v_max_u32 v187, v187, v135\n" \
    "v_cmp_eq_u32 vcc, -1, v187\n" \
    "s_cbranch_vccz 61f\n" \
    "global_load_dwordx4 v[132:135], v253, %[wsb] offset:-3072 sc1\n" \
    "61:\n" \
    "v_max3_u32 v187, v136, v137, v138\n" \
    "v_max_u32 v187, v187, v139\n" \
    "v_cmp_eq_u32 vcc, -1, v187\n" \
    "s_cbranch_vccz 62f\n" \
    "global_load_dwordx4 v[136:139], v253, %[wsb] offset:-2048 sc1\n" \
    "62:\n" \
    "v_max3_u32 v187, v140, v141, v142\n" \
    "v_max_u32 v187, v187, v143\n" \
    "v_cmp_eq_u32 vcc, -1, v187\n" \
    "s_cbranch_vccz 63f\n" \
    "global_load_dwordx4 v[140:143], v253, %[wsb] offset:-1024 sc1\n" \
    "63:\n" \
    "v_max3_u32 v187, v144, v145, v146\n" \
    "v_max_u32 v187, v187, v147\n" \
    "v_cmp_eq_u32 vcc, -1, v187\n" \
    "s_cbranch_vccz 64f\n" \
    "global_load_dwordx4 v[144:147], v253, %[wsb] offset:0 sc1\n" \
    "64:\n" \
    "v_max3_u32 v187, v148, v149, v150\n" \
    "v_max_u32 v187, v187, v151\n" \
    "v_cmp_eq_u32 vcc, -1, v187\n" \
    "s_cbranch_vccz 65f\n" \
    "global_load_dwordx4 v[148:151], v253, %[wsb] offset:1024 sc1\n" \
    "65:\n" \
    "v_max3_u32 v187, v152, v153, v154\n" \
    "v_max_u32 v187, v187, v155\n" \
    "v_cmp_eq_u32 vcc, -1, v187\n" \
    "s_cbranch_vccz 66f\n" \
    "global_load_dwordx4 v[152:155], v253, %[wsb] offset:2048 sc1\n" \
    "66:\n" \
    "v_max3_u32 v187, v156, v157, v158\n" \
    "v_max_u32 v187, v187, v159\n" \
    "v_cmp_eq_u32 vcc, -1, v187\n" \
    "s_cbranch_vccz 67f\n" \
    "global_load_dwordx4 v[156:159], v253, %[wsb] offset:3072 sc1\n" \
    "67:\n" \
    "s_waitcnt vmcnt(0)\n" \
    "s_add_u32 s87, s87, 1\n" \
    "s_cmp_gt_u32 s87, 0x400000\n" \
    "s_setpc_b64 s[90:91]\n" \
    "30:\n" \
    "s_call_b64 s[90:91], 49b\n" \
    "s_cbranch_scc1 20b\n" \
    "s_branch 10b\n" \
    "31:\n" \
    "s_call_b64 s[90:91], 49b\n" \
    "s_cbranch_scc1 21b\n" \
    "s_branch 11b\n" \
    "32:\n" \
    "s_call_b64 s[90:91], 49b\n" \
    "s_cbranch_scc1 22b\n" \
    "s_branch 12b\n" \
    "33:\n" \
    "s_call_b64 s[90:91], 49b\n" \
    "s_cbranch_scc1 23b\n" \
    "s_branch 13b\n" \
    "34:\n" \
    "s_call_b64 s[90:91], 49b\n" \
    "s_cbranch_scc1 24b\n" \
    "s_branch 14b\n" \
    "35:\n" \
    "s_call_b64 s[90:91], 49b\n" \
    "s_cbranch_scc1 25b\n" \
    "s_branch 15b\n" \
    "36:\n" \
    "s_call_b64 s[90:91], 49b\n" \
    "s_cbranch_scc1 26b\n" \
    "s_branch 16b\n" \
    "37:\n" \
    "s_call_b64 s[90:91], 49b\n" \
    "s_cbranch_scc1 27b\n" \
    "s_branch 17b\n" \
    "47:\n" \
    "s_waitcnt vmcnt(1)\n" \
    "s_mov_b32 s87, 0\n" \
    "s_bitcmp1_b32 s86, 1\n" \
    "s_cbranch_scc1 43f\n" \
    "42:\n" \
    "s_call_b64 s[90:91], 70b\n" \
    "s_waitcnt vmcnt(0)\n" \
    "43:\n" \
    "v_max3_u32 v220, v196, v197, v198\n" \
    "v_max3_u32 v221, v199, v200, v201\n" \
    "v_max3_u32 v220, v220, v202, v203\n" \
    "v_max3_u32 v221, v221, v204, v205\n" \
    "v_max3_u32 v220, v220, v206, v207\n" \
    "v_max3_u32 v221, v221, v208, v209\n" \
    "v_max3_u32 v220, v220, v210, v211\n" \
    "v_max3_u32 v221, v221, v212, v213\n" \
    "v_max3_u32 v220, v220, v214, v215\n" \
    "v_max3_u32 v221, v221, v216, v217\n" \
    "v_max3_u32 v220, v220, v218, v219\n" \
    "v_max3_u32 v221, v221, v224, v225\n" \
    "v_max3_u32 v220, v220, v226, v227\n" \
    "v_max_u32 v220, v220, v221\n" \
    "v_cmp_eq_u32 vcc, -1, v220\n" \
    "s_cbranch_vccz 44f\n" \
    "s_add_u32 s87, s87, 1\n" \
    "s_cmp_gt_u32 s87, 0x400000\n" \
    "s_cbranch_scc0 42b\n" \
    "44:\n" \
    "v_pk_add_f32 v[196:197], v[196:197], v[200:201]\n" \
    "v_pk_add_f32 v[198:199], v[198:199], v[202:203]\n" \
    "v_pk_add_f32 v[204:205], v[204:205], v[208:209]\n" \
    "v_pk_add_f32 v[206:207], v[206:207], v[210:211]\n" \
    "v_pk_add_f32 v[212:213], v[212:213], v[216:217]\n" \
    "v_pk_add_f32 v[214:215], v[214:215], v[218:219]\n" \
    "v_pk_add_f32 v[196:197], v[196:197], v[204:205]\n" \
    "v_pk_add_f32 v[198:199], v[198:199], v[206:207]\n" \
    "v_pk_add_f32 v[212:213], v[212:213], v[224:225]\n" \
    "v_pk_add_f32 v[214:215], v[214:215], v[226:227]\n" \
    "v_pk_add_f32 v[196:197], v[196:197], v[212:213]\n" \
    "v_pk_add_f32 v[198:199], v[198:199], v[214:215]\n" \
    "v_mov_b32 v200, v196\n" \
    "v_mov_b32 v201, v197\n" \
    "v_mov_b32 v202, v198\n" \
    "v_mov_b32 v203, v199\n" \
    "s_nop 1\n" \
    "v_permlane32_swap_b32 v196, v200\n" \
    "v_permlane32_swap_b32 v197, v201\n" \
    "v_permlane32_swap_b32 v198, v202\n" \
    "v_permlane32_swap_b32 v199, v203\n" \
    "s_nop 1\n" \
    "v_pk_add_f32 v[196:197], v[196:197], v[200:201]\n" \
    "v_pk_add_f32 v[198:199], v[198:199], v[202:203]\n" \
    "v_mov_b32 v200, v196\n" \
    "v_mov_b32 v201, v197\n" \
    "v_mov_b32 v202, v198\n" \
    "v_mov_b32 v203, v199\n" \
    "s_nop 1\n" \
    "v_permlane16_swap_b32 v196, v200\n" \
    "v_permlane16_swap_b32 v197, v201\n" \
    "v_permlane16_swap_b32 v198, v202\n" \
    "v_permlane16_swap_b32 v199, v203\n" \
    "s_nop 1\n" \
    "v_pk_add_f32 v[196:197], v[196:197], v[200:201]\n" \
    "v_pk_add_f32 v[198:199], v[198:199], v[202:203]\n" \
    "v_mbcnt_lo_u32_b32 v204, -1, 0\n" \
    "v_mbcnt_hi_u32_b32 v204, -1, v204\n" \
    "v_mad_u32_u24 v204, v204, 12, v246\n" \
    "s_and_b32 s84, %[cwo], -8\n" \
    "s_sub_u32 s84, s84, 4\n" \
    "v_mov_b32 v205, s84\n" \
    "v_mov_b32 v206, 1\n" \
    "s_mov_b64 exec, 0xffff\n" \
    "ds_write_b128 v204, v[196:199] offset:9216\n" \
    "s_mov_b64 exec, 1\n" \
    "ds_add_u32 v205, v206\n" \
    "s_mov_b64 exec, -1\n" \
    "s_sleep 8\n" \
    "s_branch 48b\n" \
    "40:\n" \
    "s_mov_b32 s87, 0\n" \
    "45:\n" \
    "ds_read_b32 v206, v205\n" \
    "s_waitcnt lgkmcnt(0)\n" \
    "v_readfirstlane_b32 s84, v206\n" \
    "s_cmp_ge_u32 s84, 8\n" \
    "s_cbranch_scc1 46f\n" \
    "s_add_u32 s87, s87, 1\n" \
    "s_cmp_gt_u32 s87, 0x400000\n" \
    "s_cbranch_scc0 45b\n" \
    "46:\n" \
    "v_and_b32 v204, 0x7f, v242\n" \
    "s_and_b32 s84, %[cwo], -8\n" \
    "s_sub_u32 s84, s84, 2088\n" \
    "v_lshl_add_u32 v236, v204, 1, s84\n" \
    "ds_read_b128 v[196:199], v236 offset:0\n" \
    "ds_read_b128 v[200:203], v236 offset:16\n" \
    "ds_read_b128 v[204:207], v236 offset:256\n" \
    "ds_read_b128 v[208:211], v236 offset:272\n" \
    "ds_read_b128 v[212:215], v236 offset:512\n" \
    "ds_read_b128 v[216:219], v236 offset:528\n" \
    "ds_read_b128 v[220:223], v236 offset:768\n" \
    "ds_read_b128 v[224:227], v236 offset:784\n" \
    "s_lshr_b32 s88, %[key], 16\n" \
    "s_and_b32 s89, s88, 0x7fff\n" \
    "s_andn2_b32 s92, 0xffff, s88\n" \
    "s_bitcmp1_b32 s88, 15\n" \
    "s_cselect_b32 s88, s89, s92\n" \
    "s_lshl_b32 s88, s88, 16\n" \
    "v_mov_b32 v237, s88\n" \
    "v_mul_f32 v237, 0xbfb8aa3b, v237\n" \
    "v_exp_f32 v237, v237\n" \
    "s_nop 0\n" \
    "v_add_f32 v237, 1.0, v237\n" \
    "v_rcp_f32 v237, v237\n" \
    "s_nop 0\n" \
    "v_cvt_pk_bf16_f32 v237, v237, v237\n" \
    "v_and_b32 v238, 0xffff0000, v237\n" \
    "s_and_b32 s89, %[key], 0xffff\n" \
    "s_sub_u32 s89, 0xffff, s89\n" \
    "s_and_b32 s89, s89, 2047\n" \
    "v_lshrrev_b32 v237, 10, v242\n" \
    "s_waitcnt lgkmcnt(0)\n" \
    "v_pk_add_f32 v[196:197], v[196:197], v[204:205]\n" \
    "v_pk_add_f32 v[198:199], v[198:199], v[206:207]\n" \
    "v_pk_add_f32 v[200:201], v[200:201], v[208:209]\n" \
    "v_pk_add_f32 v[202:203], v[202:203], v[210:211]\n" \
    "v_pk_add_f32 v[196:197], v[196:197], v[212:213]\n" \
    "v_pk_add_f32 v[198:199], v[198:199], v[214:215]\n" \
    "v_pk_add_f32 v[200:201], v[200:201], v[216:217]\n" \
    "v_pk_add_f32 v[202:203], v[202:203], v[218:219]\n" \
    "v_pk_add_f32 v[196:197], v[196:197], v[220:221]\n" \
    "v_pk_add_f32 v[198:199], v[198:199], v[222:223]\n" \
    "v_pk_add_f32 v[200:201], v[200:201], v[224:225]\n" \
    "v_pk_add_f32 v[202:203], v[202:203], v[226:227]\n" \
    "ds_read_b128 v[204:207], v236 offset:1024\n" \
    "ds_read_b128 v[208:211], v236 offset:1040\n" \
    "ds_read_b128 v[212:215], v236 offset:1280\n" \
    "ds_read_b128 v[216:219], v236 offset:1296\n" \
    "ds_read_b128 v[220:223], v236 offset:1536\n" \
    "ds_read_b128 v[224:227], v236 offset:1552\n" \
    "ds_read_b128 v[228:231], v236 offset:1792\n" \
    "ds_read_b128 v[232:235], v236 offset:1808\n" \
    "v_cmp_eq_u32 vcc, s89, v237\n" \
    "s_waitcnt lgkmcnt(0)\n" \
    "v_pk_add_f32 v[196:197], v[196:197], v[204:205]\n" \
    "v_pk_add_f32 v[198:199], v[198:199], v[206:207]\n" \
    "v_pk_add_f32 v[200:201], v[200:201], v[208:209]\n" \
    "v_pk_add_f32 v[202:203], v[202:203], v[210:211]\n" \
    "v_pk_add_f32 v[196:197], v[196:197], v[212:213]\n" \
    "v_pk_add_f32 v[198:199], v[198:199], v[214:215]\n" \
    "v_pk_add_f32 v[200:201], v[200:201], v[216:217]\n" \
    "v_pk_add_f32 v[202:203], v[202:203], v[218:219]\n" \
    "v_pk_add_f32 v[196:197], v[196:197], v[220:221]\n" \
    "v_pk_add_f32 v[198:199], v[198:199], v[222:223]\n" \
    "v_pk_add_f32 v[200:201], v[200:201], v[224:225]\n" \
    "v_pk_add_f32 v[202:203], v[202:203], v[226:227]\n" \
    "v_pk_add_f32 v[196:197], v[196:197], v[228:229]\n" \
    "v_pk_add_f32 v[198:199], v[198:199], v[230:231]\n" \
    "v_pk_add_f32 v[200:201], v[200:201], v[232:233]\n" \
    "v_pk_add_f32 v[202:203], v[202:203], v[234:235]\n" \
    "v_cvt_pk_bf16_f32 v204, v196, v197\n" \
    "v_cvt_pk_bf16_f32 v205, v198, v199\n" \
    "v_cvt_pk_bf16_f32 v206, v200, v201\n" \
    "v_cvt_pk_bf16_f32 v207, v202, v203\n" \
    "v_lshlrev_b32 v196, 16, v204\n" \
    "v_and_b32 v197, 0xffff0000, v204\n" \
    "v_lshlrev_b32 v198, 16, v205\n" \
    "v_and_b32 v199, 0xffff0000, v205\n" \
    "v_lshlrev_b32 v200, 16, v206\n" \
    "v_and_b32 v201, 0xffff0000, v206\n" \
    "v_lshlrev_b32 v202, 16, v207\n" \
    "v_and_b32 v203, 0xffff0000, v207\n" \
    "v_pk_mul_f32 v[196:197], v[196:197], v[238:239] op_sel_hi:[1,0]\n" \
    "v_pk_mul_f32 v[198:199], v[198:199], v[238:239] op_sel_hi:[1,0]\n" \
    "v_pk_mul_f32 v[200:201], v[200:201], v[238:239] op_sel_hi:[1,0]\n" \
    "v_pk_mul_f32 v[202:203], v[202:203], v[238:239] op_sel_hi:[1,0]\n" \
    "v_cvt_pk_bf16_f32 v204, v196, v197\n" \
    "v_cvt_pk_bf16_f32 v205, v198, v199\n" \
    "v_cvt_pk_bf16_f32 v206, v200, v201\n" \
    "v_cvt_pk_bf16_f32 v207, v202, v203\n" \
    "v_lshlrev_b32 v196, 16, v204\n" \
    "v_and_b32 v197, 0xffff0000, v204\n" \
    "v_lshlrev_b32 v198, 16, v205\n" \
    "v_and_b32 v199, 0xffff0000, v205\n" \
    "v_lshlrev_b32 v200, 16, v206\n" \
    "v_and_b32 v201, 0xffff0000, v206\n" \
    "v_lshlrev_b32 v202, 16, v207\n" \
    "v_and_b32 v203, 0xffff0000, v207\n" \
    "v_pk_add_f32 v[196:197], v[196:197], v[176:177]\n" \
    "v_pk_add_f32 v[198:199], v[198:199], v[178:179]\n" \
    "v_pk_add_f32 v[200:201], v[200:201], v[180:181]\n" \
    "v_pk_add_f32 v[202:203], v[202:203], v[182:183]\n" \
    "v_cvt_pk_bf16_f32 v204, v196, v197\n" \
    "v_cvt_pk_bf16_f32 v205, v198, v199\n" \
    "v_cvt_pk_bf16_f32 v206, v200, v201\n" \
    "v_cvt_pk_bf16_f32 v207, v202, v203\n" \
    "v_lshlrev_b32 v196, 16, v204\n" \
    "v_and_b32 v197, 0xffff0000, v204\n" \
    "v_lshlrev_b32 v198, 16, v205\n" \
    "v_and_b32 v199, 0xffff0000, v205\n" \
    "v_lshlrev_b32 v200, 16, v206\n" \
    "v_and_b32 v201, 0xffff0000, v206\n" \
    "v_lshlrev_b32 v202, 16, v207\n" \
    "v_and_b32 v203, 0xffff0000, v207\n" \
    "v_cndmask_b32 v176, v176, v196, vcc\n" \
    "v_cndmask_b32 v177, v177, v197, vcc\n" \
    "v_cndmask_b32 v178, v178, v198, vcc\n" \
    "v_cndmask_b32 v179, v179, v199, vcc\n" \
    "v_cndmask_b32 v180, v180, v200, vcc\n" \
    "v_cndmask_b32 v181, v181, v201, vcc\n" \
    "v_cndmask_b32 v182, v182, v202, vcc\n" \
    "v_cndmask_b32 v183, v183, v203, vcc\n" \
    "s_branch 41b\n" \
    "80:\n" \
    "v_mbcnt_lo_u32_b32 v124, -1, 0\n" \
    "v_mbcnt_hi_u32_b32 v124, -1, v124\n" \
    "v_and_b32 v125, 15, v124\n" \
    "v_lshlrev_b32 v125, 4, v125\n" \
    "v_lshrrev_b32 v126, 5, v124\n" \
    "v_lshl_or_b32 v125, v126, 8, v125\n" \
    "v_add_u32 v125, 0x20000, v125\n" \
    "v_and_b32 v126, 31, v124\n" \
    "v_cmp_le_u32 s[82:83], 16, v126\n" \
    "v_lshlrev_b32 v124, 4, v124\n" \
    "v_add_u32 v124, 0x10000, v124\n" \
    "v_mov_b32 v126, 0xff61b1e6\n" \
    "v_mov_b32 v127, 0x3f803f80\n" \
    "v_mov_b32 v172, v124\n" \
    "s_movk_i32 s88, 64\n" \
    "82:\n" \
    "global_load_dwordx4 v[112:115], v172, %[wsb] sc1\n" \
    "v_add_u32 v172, 0x400, v172\n" \
    "s_waitcnt vmcnt(0)\n" \
    "v_mfma_f32_32x32x16_bf16 v[80:95], v[112:115], v[164:167], 0\n" \
    "s_nop 7\n" \
    "s_nop 7\n" \
    "v_max3_f32 v126, v126, v80, v81\n" \
    "v_max3_f32 v126, v126, v82, v83\n" \
    "v_max3_f32 v126, v126, v84, v85\n" \
    "v_max3_f32 v126, v126, v86, v87\n" \
    "v_max3_f32 v126, v126, v88, v89\n" \
    "v_max3_f32 v126, v126, v90, v91\n" \
    "v_max3_f32 v126, v126, v92, v93\n" \
    "v_max3_f32 v126, v126, v94, v95\n" \
    "s_sub_u32 s88, s88, 1\n" \
    "s_cmp_lg_u32 s88, 0\n" \
    "s_cbranch_scc1 82b\n" \
    "v_mov_b32 v173, v126\n" \
    "s_nop 1\n" \
    "v_permlane32_swap_b32 v126, v173\n" \
    "s_nop 1\n" \
    "v_max_f32 v126, v126, v173\n" \
    "v_mov_b32 v144, 0\n" \
    "v_mov_b32 v145, 0\n" \
    "v_mov_b32 v146, 0\n" \
    "v_mov_b32 v147, 0\n" \
    "v_mov_b32 v148, 0\n" \
    "v_mov_b32 v149, 0\n" \
    "v_mov_b32 v150, 0\n" \
    "v_mov_b32 v151, 0\n" \
    "v_mov_b32 v152, 0\n" \
    "v_mov_b32 v153, 0\n" \
    "v_mov_b32 v154, 0\n" \
    "v_mov_b32 v155, 0\n" \
    "v_mov_b32 v156, 0\n" \
    "v_mov_b32 v157, 0\n" \
    "v_mov_b32 v158, 0\n" \
    "v_mov_b32 v159, 0\n" \
    "v_mov_b32 v172, v124\n" \
    "s_movk_i32 s88, 64\n" \
    "83:\n" \
    "global_load_dwordx4 v[112:115], v172, %[wsb] sc1\n" \
    "global_load_dwordx4 v[116:119], v125, %[wsb] sc1\n" \
    "global_load_dwordx4 v[120:123], v125, %[wsb] offset:512 sc1\n" \
    "v_add_u32 v172, 0x400, v172\n" \
    "v_add_u32 v125, 0x400, v125\n" \
    "s_waitcnt vmcnt(0)\n" \
    "v_mfma_f32_32x32x16_bf16 v[80:95], v[112:115], v[164:167], 0\n" \
    "v_cndmask_b32 v116, v116, v127, s[82:83]\n" \
    "v_cndmask_b32 v117, v117, v127, s[82:83]\n" \
    "v_cndmask_b32 v118, v118, v127, s[82:83]\n" \
    "v_cndmask_b32 v119, v119, v127, s[82:83]\n" \
    "v_cndmask_b32 v120, v120, v127, s[82:83]\n" \
    "v_cndmask_b32 v121, v121, v127, s[82:83]\n" \
    "v_cndmask_b32 v122, v122, v127, s[82:83]\n" \
    "v_cndmask_b32 v123, v123, v127, s[82:83]\n" \
    "s_nop 7\n" \
    "s_nop 3\n" \
    "v_sub_f32 v80, v80, v126\n" \
    "v_sub_f32 v81, v81, v126\n" \
    "v_sub_f32 v82, v82, v126\n" \
    "v_sub_f32 v83, v83, v126\n" \
    "v_sub_f32 v84, v84, v126\n" \
    "v_sub_f32 v85, v85, v126\n" \
    "v_sub_f32 v86, v86, v126\n" \
    "v_sub_f32 v87, v87, v126\n" \
    "v_sub_f32 v88, v88, v126\n" \
    "v_sub_f32 v89, v89, v126\n" \
    "v_sub_f32 v90, v90, v126\n" \
    "v_sub_f32 v91, v91, v126\n" \
    "v_sub_f32 v92, v92, v126\n" \
    "v_sub_f32 v93, v93, v126\n" \
    "v_sub_f32 v94, v94, v126\n" \
    "v_sub_f32 v95, v95, v126\n" \
    "v_exp_f32 v80, v80\n" \
    "v_exp_f32 v81, v81\n" \
    "v_exp_f32 v82, v82\n" \
    "v_exp_f32 v83, v83\n" \
    "v_exp_f32 v84, v84\n" \
    "v_exp_f32 v85, v85\n" \
    "v_exp_f32 v86, v86\n" \
    "v_exp_f32 v87, v87\n" \
    "v_exp_f32 v88, v88\n" \
    "v_exp_f32 v89, v89\n" \
    "v_exp_f32 v90, v90\n" \
    "v_exp_f32 v91, v91\n" \
    "v_exp_f32 v92, v92\n" \
    "v_exp_f32 v93, v93\n" \
    "v_exp_f32 v94, v94\n" \
    "v_exp_f32 v95, v95\n" \
    "s_nop 1\n" \
    "v_cvt_pk_bf16_f32 v176, v80, v81\n" \
    "v_cvt_pk_bf16_f32 v177, v82, v83\n" \
    "v_cvt_pk_bf16_f32 v178, v84, v85\n" \
    "v_cvt_pk_bf16_f32 v179, v86, v87\n" \
    "v_cvt_pk_bf16_f32 v180, v88, v89\n" \
    "v_cvt_pk_bf16_f32 v181, v90, v91\n" \
    "v_cvt_pk_bf16_f32 v182, v92, v93\n" \
    "v_cvt_pk_bf16_f32 v183, v94, v95\n" \
    "s_nop 1\n" \
    "v_permlane32_swap_b32 v116, v118\n" \
    "v_permlane32_swap_b32 v117, v119\n" \
    "v_permlane32_swap_b32 v120, v122\n" \
    "v_permlane32_swap_b32 v121, v123\n" \
    "s_nop 3\n" \
    "v_mfma_f32_32x32x16_bf16 v[144:159], v[116:119], v[176:179], v[144:159]\n" \
    "s_nop 7\n" \
    "s_nop 7\n" \
    "v_mfma_f32_32x32x16_bf16 v[144:159], v[120:123], v[180:183], v[144:159]\n" \
    "s_nop 7\n" \
    "s_nop 7\n" \
    "s_sub_u32 s88, s88, 1\n" \
    "s_cmp_lg_u32 s88, 0\n" \
    "s_cbranch_scc1 83b\n" \
    "v_mov_b32 v137, v152\n" \
    "v_mov_b32 v138, v152\n" \
    "s_nop 1\n" \
    "v_permlane32_swap_b32 v137, v138\n" \
    "s_nop 1\n" \
    "v_rcp_f32 v138, v137\n" \
    "s_nop 1\n" \
    "v_pk_mul_f32 v[128:129], v[144:145], v[138:139] op_sel_hi:[1,0]\n" \
    "v_pk_mul_f32 v[130:131], v[146:147], v[138:139] op_sel_hi:[1,0]\n" \
    "v_pk_mul_f32 v[132:133], v[148:149], v[138:139] op_sel_hi:[1,0]\n" \
    "v_pk_mul_f32 v[134:135], v[150:151], v[138:139] op_sel_hi:[1,0]\n" \
    "v_cvt_pk_bf16_f32 v168, v128, v129\n" \
    "v_cvt_pk_bf16_f32 v169, v130, v131\n" \
    "v_cvt_pk_bf16_f32 v170, v132, v133\n" \
    "v_cvt_pk_bf16_f32 v171, v134, v135\n" \
    "v_mov_b32 v172, 0x7fc07fc0\n" \
    "v_cmp_eq_u32 vcc, -1, v168\n" \
    "v_cndmask_b32 v168, v168, v172, vcc\n" \
    "v_cmp_eq_u32 vcc, -1, v169\n" \
    "v_cndmask_b32 v169, v169, v172, vcc\n" \
    "v_cmp_eq_u32 vcc, -1, v170\n" \
    "v_cndmask_b32 v170, v170, v172, vcc\n" \
    "v_cmp_eq_u32 vcc, -1, v171\n" \
    "v_cndmask_b32 v171, v171, v172, vcc\n" \
    "s_nop 1\n" \
    "s_branch 81b\n" \
    "99:\n" \
    ".Lpf_end:\n"

#define FRONT \
    "s_memrealtime s[62:63]\n" \
    "s_load_dwordx16 s[4:19], %[kp], 0\n" \
    "s_load_dwordx8 s[20:27], %[kp], 64\n" \
    "v_lshrrev_b32 v63, 6, %[tid]\n" \
    "v_and_b32 v64, 63, %[tid]\n" \
    "v_readfirstlane_b32 %[w], v63\n" \
    "v_lshlrev_b32 v65, 4, v64\n" \
    "s_bfe_u32 s64, %[b], 0x30003\n" \
    "s_cmp_lg_u32 %[w], 0\n" \
    "s_cbranch_scc1 101f\n" \
    "s_getpc_b64 s[66:67]\n" \
    ".Lpf_start:\n" \
    "s_lshr_b32 s68, %[b], 3\n" \
    "s_lshl_b32 s68, s68, 10\n" \
    "v_add_u32 v62, s68, v65\n" \
    "s_mov_b32 s68, .Lpf_end-.Lpf_start-16\n" \
    "s_and_b32 s68, s68, -16\n" \
    "v_min_u32 v62, s68, v62\n" \
    "s_and_b32 s66, s66, 0xffffff00\n" \
    "s_mov_b32 m0, 151552\n" \
    "s_cmp_lt_u32 %[w], 4\n" \
    "global_load_lds_dwordx4 v62, s[66:67]\n" \
    "s_branch 200f\n" \
    "101:\n" \
    "s_cmp_lt_u32 %[w], 4\n" \
    "s_cbranch_scc1 200f\n" \
    "s_setprio 3\n" \
    "s_sub_u32 s65, %[w], 4\n" \
    "v_and_b32 v70, 32, v64\n" \
    "s_lshl_b32 s70, s65, 12\n" \
    "v_lshlrev_b32 v70, 2, v70\n" \
    "s_add_u32 m0, s70, 135168\n" \
    "v_xad_u32 v72, v65, v70, s70\n" \
    "s_and_b32 s72, %[b], 7\n" \
    "s_lshr_b32 s73, %[b], 6\n" \
    "s_lshl2_add_u32 s74, s72, s73\n" \
    "v_lshrrev_b32 v66, 4, v64\n" \
    "s_cmp_lt_u32 s64, 4\n" \
    "s_waitcnt lgkmcnt(0)\n" \
    "s_cselect_b64 s[66:67], s[8:9], s[10:11]\n" \
    "s_cselect_b64 s[68:69], s[14:15], s[22:23]\n" \
    "global_load_lds_dwordx4 v72, s[66:67]\n" \
    "s_and_b32 s72, s64, 3\n" \
    "s_lshl2_add_u32 s74, s74, s72\n" \
    "v_and_b32 v67, 15, v64\n" \
    "global_load_lds_dwordx4 v72, s[66:67] offset:1024\n" \
    "s_lshl_b32 s72, s74, 14\n" \
    "s_lshl_b32 s73, s65, 8\n" \
    "s_add_u32 s72, s72, s73\n" \
    "v_xor_b32 v68, v66, v67\n" \
    "global_load_lds_dwordx4 v72, s[66:67] offset:2048\n" \
    "v_lshl_add_u32 v69, v66, 10, s72\n" \
    "v_lshlrev_b32 v68, 4, v68\n" \
    "s_bfe_u32 s71, s64, 0x10001\n" \
    "s_lshl_b32 s71, s71, 2\n" \
    "s_add_u32 s71, s71, s65\n" \
    "global_load_lds_dwordx4 v72, s[66:67] offset:3072\n" \
    "s_and_b32 s72, s64, 1\n" \
    "s_lshl1_add_u32 s71, s71, s72\n" \
    "s_lshl_b32 s71, s71, 12\n" \
    "v_add_u32 v73, v68, v69\n" \
    "s_mov_b32 m0, s71\n" \
    "v_add_u32 v70, 0xc00, v69\n" \
    "global_load_lds_dwordx4 v73, s[4:5]\n" \
    "v_xad_u32 v74, v68, 64, v70\n" \
    "v_xor_b32 v71, 0x80, v68\n" \
    "v_add_u32 v70, 0x1800, v69\n" \
    "global_load_lds_dwordx4 v74, s[4:5] offset:1024\n" \
    "v_add_u32 v75, v71, v70\n" \
    "v_add_u32 v70, 0x2400, v69\n" \
    "global_load_lds_dwordx4 v75, s[4:5] offset:2048\n" \
    "v_xad_u32 v76, v71, 64, v70\n" \
    "v_add_u32 v77, s73, v65\n" \
    "s_add_u32 s72, s73, 160768\n" \
    "global_load_lds_dwordx4 v76, s[4:5] offset:3072\n" \
    "s_mov_b32 m0, s72\n" \
    "v_mov_b32 v78, 0\n" \
    "v_lshrrev_b32 v70, 2, v65\n" \
    "s_mov_b64 exec, 0xffff\n" \
    "global_load_lds_dwordx4 v77, s[68:69]\n" \
    "v_add_u32 v70, 162816, v70\n" \
    "ds_write_b32 v70, v78\n" \
    "s_mov_b64 exec, -1\n" \
    "global_load_dword v79, v78, s[26:27] sc1\n" \
    "v_lshlrev_b32 v70, 1, v66\n" \
    "v_xor_b32 v70, v70, v67\n" \
    "v_lshlrev_b32 v70, 4, v70\n" \
    "v_lshl_add_u32 v71, v67, 8, s71\n" \
    "v_add_u32 v80, v70, v71\n" \
    "v_xor_b32 v81, 16, v80\n" \
    "v_xor_b32 v82, 0x80, v80\n" \
    "v_xor_b32 v83, 0x90, v80\n" \
    "s_add_u32 s72, s73, 160768\n" \
    "v_lshl_add_u32 v84, v66, 5, s72\n" \
    "s_add_u32 s72, s70, 135168\n" \
    "v_lshl_add_u32 v85, v66, 9, s72\n" \
    "v_and_b32 v70, 1, v66\n" \
    "v_lshl_add_u32 v85, v70, 7, v85\n" \
    "v_lshl_add_u32 v85, v67, 3, v85\n" \
    "v_xor_b32 v86, 0x80, v85\n" \
    "v_add_u32 v87, s72, v65\n" \
    "s_add_u32 s72, s72, 0x400\n" \
    "v_lshl_add_u32 v88, v67, 2, s72\n" \
    "v_mov_b32 v95, 162816\n" \
    "v_mov_b32 v96, 1\n" \
    "v_cmp_eq_u32 vcc, 0, v67\n" \
    "v_mov_b32 v71, 0x3f803f80\n" \
    "s_lshr_b32 s72, s70, 2\n" \
    "s_add_u32 s72, s72, 131072\n" \
    "v_add_u32 v70, s72, v65\n" \
    "v_cndmask_b32 v100, 0, v71, vcc\n" \
    "v_mov_b32 v101, v100\n" \
    "v_mov_b32 v102, v100\n" \
    "v_mov_b32 v103, v100\n" \
    "ds_write_b128 v70, v[100:103]\n" \
    "s_cmp_eq_u32 %[w], 7\n" \
    "s_cbranch_scc0 100f\n" \
    "v_add_u32 v89, 135168, v65\n" \
    "v_lshlrev_b32 v90, 2, v67\n" \
    "v_add_u32 v90, 136192, v90\n" \
    "s_lshl_b32 s72, s74, 4\n" \
    "v_add_u32 v71, s72, v67\n" \
    "v_sub_u32 v94, 0xffff, v71\n" \
    "v_lshlrev_b32 v92, 2, v71\n" \
    "v_add_u32 v92, 24576, v92\n" \
    "s_bfe_u32 s74, %[b], 0x10002\n" \
    "s_lshr_b32 s72, %[b], 6\n" \
    "s_lshl2_add_u32 s74, s74, s72\n" \
    "s_and_b32 s72, %[b], 3\n" \
    "s_lshl2_add_u32 s74, s74, s72\n" \
    "s_bfe_u32 s72, s64, 0x10001\n" \
    "s_lshl1_add_u32 s74, s74, s72\n" \
    "s_bfe_u32 s73, s64, 0x10002\n" \
    "s_lshl1_add_u32 s72, s74, s73\n" \
    "s_and_b32 s73, s64, 1\n" \
    "s_lshl1_add_u32 s72, s72, s73\n" \
    "s_lshl_b32 s72, s72, 7\n" \
    "s_add_u32 s72, s72, 262144\n" \
    "v_lshl_add_u32 v93, v64, 15, s72\n" \
    "s_lshl_b32 s72, s74, 10\n" \
    "v_and_b32 v70, 1, v66\n" \
    "v_lshrrev_b32 v71, 1, v66\n" \
    "s_cmp_lt_u32 s64, 4\n" \
    "s_cbranch_scc0 102f\n" \
    "s_lshl_b32 s73, s73, 8\n" \
    "s_add_u32 s72, s72, s73\n" \
    "s_add_u32 s72, s72, 65536\n" \
    "v_lshl_add_u32 v91, v67, 4, s72\n" \
    "v_lshl_add_u32 v91, v70, 9, v91\n" \
    "v_lshl_add_u32 v91, v71, 3, v91\n" \
    "s_branch 103f\n" \
    "102:\n" \
    "s_lshl_b32 s73, s73, 9\n" \
    "s_add_u32 s72, s72, s73\n" \
    "s_add_u32 s72, s72, 131072\n" \
    "v_lshl_add_u32 v91, v67, 4, s72\n" \
    "v_lshl_add_u32 v91, v71, 8, v91\n" \
    "v_lshl_add_u32 v91, v70, 3, v91\n" \
    "103:\n" \
    "100:\n" \
    "s_waitcnt lgkmcnt(0)\n" \
    "s_barrier\n" \
    "s_waitcnt vmcnt(1)\n" \
    "ds_read_b128 v[100:103], v80\n" \
    "ds_read_b64_tr_b16 v[132:133], v85 offset:0\n" \
    "ds_read_b64_tr_b16 v[134:135], v86 offset:0\n" \
    "ds_read_b128 v[116:119], v84 offset:0\n" \
    "ds_read_b128 v[104:107], v81\n" \
    "ds_read_b128 v[108:111], v82\n" \
    "ds_read_b128 v[112:115], v83\n" \
    "s_mov_b64 exec, 1\n" \
    "ds_add_u32 v95, v96 offset:24\n" \
    "s_mov_b64 exec, -1\n" \
    "ds_read_b64_tr_b16 v[136:137], v85 offset:256\n" \
    "ds_read_b64_tr_b16 v[138:139], v86 offset:256\n" \
    "ds_read_b128 v[120:123], v84 offset:16\n" \
    "ds_read_b64_tr_b16 v[140:141], v85 offset:2048\n" \
    "ds_read_b64_tr_b16 v[142:143], v86 offset:2048\n" \
    "ds_read_b128 v[124:127], v84 offset:128\n" \
    "ds_read_b64_tr_b16 v[144:145], v85 offset:2304\n" \
    "ds_read_b64_tr_b16 v[146:147], v86 offset:2304\n" \
    "ds_read_b128 v[128:131], v84 offset:144\n" \
    "s_cmp_lt_u32 s64, 4\n" \
    "s_cbranch_scc0 104f\n" \
    "s_waitcnt lgkmcnt(14)\n" \
    "v_mfma_f32_16x16x32_bf16 v[148:151], v[132:135], v[100:103], 0\n" \
    "s_waitcnt lgkmcnt(13)\n" \
    "v_mfma_f32_16x16x32_bf16 v[152:155], v[116:119], v[100:103], 0\n" \
    "s_waitcnt lgkmcnt(7)\n" \
    "v_mfma_f32_16x16x32_bf16 v[148:151], v[136:139], v[104:107], v[148:151]\n" \
    "s_waitcnt lgkmcnt(6)\n" \
    "v_mfma_f32_16x16x32_bf16 v[152:155], v[120:123], v[104:107], v[152:155]\n" \
    "s_waitcnt lgkmcnt(4)\n" \
    "v_mfma_f32_16x16x32_bf16 v[148:151], v[140:143], v[108:111], v[148:151]\n" \
    "s_waitcnt lgkmcnt(3)\n" \
    "v_mfma_f32_16x16x32_bf16 v[152:155], v[124:127], v[108:111], v[152:155]\n" \
    "s_waitcnt lgkmcnt(1)\n" \
    "v_mfma_f32_16x16x32_bf16 v[148:151], v[144:147], v[112:115], v[148:151]\n" \
    "s_waitcnt lgkmcnt(0)\n" \
    "v_mfma_f32_16x16x32_bf16 v[152:155], v[128:131], v[112:115], v[152:155]\n" \
    "s_branch 105f\n" \
    "104:\n" \
    "s_waitcnt lgkmcnt(14)\n" \
    "v_mfma_f32_16x16x32_bf16 v[148:151], v[100:103], v[132:135], 0\n" \
    "s_waitcnt lgkmcnt(13)\n" \
    "v_mfma_f32_16x16x32_bf16 v[152:155], v[116:119], v[100:103], 0\n" \
    "s_waitcnt lgkmcnt(7)\n" \
    "v_mfma_f32_16x16x32_bf16 v[148:151], v[104:107], v[136:139], v[148:151]\n" \
    "s_waitcnt lgkmcnt(6)\n" \
    "v_mfma_f32_16x16x32_bf16 v[152:155], v[120:123], v[104:107], v[152:155]\n" \
    "s_waitcnt lgkmcnt(4)\n" \
    "v_mfma_f32_16x16x32_bf16 v[148:151], v[108:111], v[140:143], v[148:151]\n" \
    "s_waitcnt lgkmcnt(3)\n" \
    "v_mfma_f32_16x16x32_bf16 v[152:155], v[124:127], v[108:111], v[152:155]\n" \
    "s_waitcnt lgkmcnt(1)\n" \
    "v_mfma_f32_16x16x32_bf16 v[148:151], v[112:115], v[144:147], v[148:151]\n" \
    "s_waitcnt lgkmcnt(0)\n" \
    "v_mfma_f32_16x16x32_bf16 v[152:155], v[128:131], v[112:115], v[152:155]\n" \
    "105:\n" \
    "s_cmp_eq_u32 %[w], 7\n" \
    "s_cbranch_scc1 120f\n" \
    "s_waitcnt vmcnt(0)\n" \
    "v_readfirstlane_b32 s67, v79\n" \
    "s_nop 3\n" \
    "s_add_u32 s67, s67, 1\n" \
    "ds_write_b128 v87, v[148:151]\n" \
    "ds_write_b32 v88, v152\n" \
    "s_mov_b64 exec, 1\n" \
    "ds_add_u32 v95, v96 offset:16\n" \
    "s_mov_b64 exec, -1\n" \
    "s_branch 121f\n" \
    "120:\n" \
    "s_waitcnt vmcnt(0)\n" \
    "v_readfirstlane_b32 s67, v79\n" \
    "s_add_u32 s67, s67, 1\n" \
    "s_mov_b32 s73, 0\n" \
    "106:\n" \
    "ds_read_b32 v112, v95 offset:16\n" \
    "s_waitcnt lgkmcnt(0)\n" \
    "v_readfirstlane_b32 s72, v112\n" \
    "s_cmp_ge_u32 s72, 3\n" \
    "s_cbranch_scc1 107f\n" \
    "s_add_u32 s73, s73, 1\n" \
    "s_cmp_le_u32 s73, 4194304\n" \
    "s_cbranch_scc1 106b\n" \
    "107:\n" \
    "ds_read_b128 v[100:103], v89 offset:0\n" \
    "ds_read_b32 v112, v90 offset:0\n" \
    "ds_read_b128 v[104:107], v89 offset:4096\n" \
    "ds_read_b32 v113, v90 offset:4096\n" \
    "ds_read_b128 v[108:111], v89 offset:8192\n" \
    "ds_read_b32 v114, v90 offset:8192\n" \
    "s_waitcnt lgkmcnt(2)\n" \
    "v_pk_add_f32 v[100:101], v[100:101], v[104:105]\n" \
    "v_pk_add_f32 v[102:103], v[102:103], v[106:107]\n" \
    "v_add_f32 v112, v112, v113\n" \
    "s_waitcnt lgkmcnt(0)\n" \
    "v_pk_add_f32 v[108:109], v[108:109], v[148:149]\n" \
    "v_pk_add_f32 v[110:111], v[110:111], v[150:151]\n" \
    "v_add_f32 v114, v114, v152\n" \
    "v_pk_add_f32 v[100:101], v[100:101], v[108:109]\n" \
    "v_pk_add_f32 v[102:103], v[102:103], v[110:111]\n" \
    "v_add_f32 v112, v112, v114\n" \
    "v_cvt_pk_bf16_f32 v104, v100, v101\n" \
    "v_cvt_pk_bf16_f32 v105, v102, v103\n" \
    "s_cmp_lt_u32 s64, 4\n" \
    "s_cbranch_scc0 108f\n" \
    "s_mov_b32 s72, 0x3eb8aa3b\n" \
    "v_lshlrev_b32 v100, 16, v104\n" \
    "v_and_b32 v101, 0xffff0000, v104\n" \
    "v_lshlrev_b32 v102, 16, v105\n" \
    "v_and_b32 v103, 0xffff0000, v105\n" \
    "v_mul_f32 v100, s72, v100\n" \
    "v_mul_f32 v101, s72, v101\n" \
    "v_mul_f32 v102, s72, v102\n" \
    "v_mul_f32 v103, s72, v103\n" \
    "v_cvt_pk_bf16_f32 v104, v100, v101\n" \
    "v_cvt_pk_bf16_f32 v105, v102, v103\n" \
    "global_store_dwordx2 v91, v[104:105], s[26:27] sc1\n" \
    "v_cvt_pk_bf16_f32 v112, v112, v112\n" \
    "s_mov_b32 s69, 0xffff0000\n" \
    "v_ashrrev_i32 v113, 31, v112\n" \
    "v_or_b32 v113, 0x80000000, v113\n" \
    "v_xor_b32 v112, v112, v113\n" \
    "v_and_or_b32 v112, v112, s69, v94\n" \
    "s_nop 1\n" \
    "v_max_u32_dpp v112, v112, v112 row_shr:1 row_mask:0xf bank_mask:0xf\n" \
    "s_nop 1\n" \
    "v_max_u32_dpp v112, v112, v112 row_shr:2 row_mask:0xf bank_mask:0xf\n" \
    "s_nop 1\n" \
    "v_max_u32_dpp v112, v112, v112 row_shr:4 row_mask:0xf bank_mask:0xf\n" \
    "s_nop 1\n" \
    "v_max_u32_dpp v112, v112, v112 row_shr:8 row_mask:0xf bank_mask:0xf\n" \
    "s_nop 0\n" \
    "v_readlane_b32 s66, v112, 15\n" \
    "s_branch 109f\n" \
    "108:\n" \
    "global_store_dwordx2 v91, v[104:105], s[26:27] sc1\n" \
    "s_mov_b64 exec, 0xffff\n" \
    "global_store_dword v92, v112, s[26:27] sc1\n" \
    "s_mov_b64 exec, -1\n" \
    "s_mov_b32 s66, 0\n" \
    "109:\n" \
    "v_mov_b32 v99, s67\n" \
    "v_mov_b32 v100, s67\n" \
    "v_mov_b32 v101, 1\n" \
    "v_mov_b32 v102, 162816\n" \
    "v_mov_b32 v98, s66\n" \
    "s_mov_b64 exec, 1\n" \
    "ds_write_b64 v102, v[100:101] offset:48\n" \
    "s_mov_b64 exec, -1\n" \
    "121:\n" \
    "s_and_b32 s72, %[b], 7\n" \
    "s_lshr_b32 s78, %[b], 6\n" \
    "s_lshl2_add_u32 s73, s72, s78\n" \
    "s_bfe_u32 s74, %[b], 0x30003\n" \
    "s_lshr_b32 s78, %[b], 3\n" \
    "s_min_u32 s79, s78, 27\n" \
    "s_mul_i32 s75, s72, 28\n" \
    "s_add_u32 s75, s75, s79\n" \
    "s_cmp_lt_u32 s78, 28\n" \
    "s_cselect_b32 s72, 1, 0\n" \
    "s_cmp_lt_u32 %[w], 4\n" \
    "s_cselect_b32 s72, s72, 0\n" \
    "s_lshr_b32 s76, %[w], 2\n" \
    "s_and_b32 s77, %[w], 3\n" \
    "v_and_b32 v66, 15, v64\n" \
    "v_and_b32 v67, 0xf0, v65\n" \
    "v_lshrrev_b32 v68, 4, v64\n" \
    "v_and_b32 v69, 1, v68\n" \
    "v_lshrrev_b32 v70, 5, v64\n" \
    "v_lshrrev_b32 v71, 3, v64\n" \
    "v_and_b32 v72, 31, v64\n" \
    "v_bfe_u32 v73, v64, 1, 2\n" \
    "v_lshl_add_u32 v74, v70, 8, v67\n" \
    "s_and_b32 s78, %[w], 1\n" \
    "s_lshl_b32 s78, s78, 16\n" \
    "s_bfe_u32 s79, %[w], 0x10001\n" \
    "s_lshl_b32 s79, s79, 15\n" \
    "s_or_b32 s78, s78, s79\n" \
    "s_lshl_b32 s79, s76, 11\n" \
    "s_add_u32 %[skv3], s78, s79\n" \
    "s_add_u32 %[skv], %[skv3], s79\n" \
    "s_add_u32 %[skv3], %[skv3], 26624\n" \
    "s_add_u32 %[skv], %[skv], 10240\n" \
    "v_add_u32 v32, %[skv], v65\n" \
    "v_add_u32 v33, %[skv3], v65\n" \
    "s_lshr_b32 s79, s78, 16\n" \
    "s_lshl_b32 s79, s79, 13\n" \
    "s_and_b32 s78, s78, 0x8000\n" \
    "s_or_b32 s78, s78, s79\n" \
    "s_lshl_b32 s79, s76, 16\n" \
    "s_or_b32 %[swo], s78, s79\n" \
    "v_lshl_add_u32 v249, v68, 10, s79\n" \
    "v_lshrrev_b32 v75, 2, v66\n" \
    "v_lshl_add_u32 v249, v75, 7, v249\n" \
    "v_and_b32 v75, 3, v66\n" \
    "v_lshl_add_u32 v249, v75, 3, v249\n" \
    "v_bfe_u32 v75, v64, 3, 1\n" \
    "v_lshl_add_u32 v249, v75, 5, v249\n" \
    "v_xor_b32 v75, s76, v69\n" \
    "v_lshl_add_u32 v249, v75, 6, v249\n" \
    "v_xor_b32 v250, 32, v249\n" \
    "v_xor_b32 v251, 64, v249\n" \
    "v_xor_b32 v252, 0x60, v249\n" \
    "s_lshl_b32 s78, %[w], 11\n" \
    "s_add_u32 %[sg], s78, 135168\n" \
    "v_add_u32 v244, %[sg], v65\n" \
    "s_add_u32 s79, s78, 16384\n" \
    "v_add_u32 v254, s79, v65\n" \
    "s_xor_b32 s78, s78, 0x2000\n" \
    "s_add_u32 s79, s78, 16384\n" \
    "v_add_u32 v255, s79, v65\n" \
    "s_add_u32 s79, s78, 135168\n" \
    "v_add_u32 v245, s79, v65\n" \
    "v_lshl_add_u32 v40, v68, 9, %[sg]\n" \
    "v_lshl_add_u32 v40, v69, 7, v40\n" \
    "v_lshl_add_u32 v40, v66, 3, v40\n" \
    "v_xor_b32 v41, 0x80, v40\n" \
    "s_lshl_b32 s78, %[w], 10\n" \
    "s_add_u32 s78, s78, 151552\n" \
    "v_add_u32 v42, s78, v65\n" \
    "s_lshl_b32 s78, %[w], 6\n" \
    "s_add_u32 s78, s78, 160256\n" \
    "s_mov_b32 exec_lo, 0x10001\n" \
    "s_mov_b32 exec_hi, 0x10001\n" \
    "v_lshl_add_u32 v42, v68, 4, s78\n" \
    "s_mov_b64 exec, -1\n" \
    "s_lshl_b32 s78, %[w], 8\n" \
    "s_add_u32 s79, s78, 151552\n" \
    "v_lshl_add_u32 v246, v64, 2, s79\n" \
    "s_xor_b32 s79, s79, 0x400\n" \
    "v_lshl_add_u32 v247, v72, 2, s79\n" \
    "s_lshl_b32 s78, %[w], 7\n" \
    "v_lshl_add_u32 v38, v68, 5, s78\n" \
    "s_lshl_b32 s78, s75, 13\n" \
    "s_lshl_b32 s79, s77, 8\n" \
    "s_add_u32 s78, s78, s79\n" \
    "v_lshl_add_u32 v39, v64, 2, s78\n" \
    "v_xor_b32 v75, v71, v70\n" \
    "v_lshl_add_u32 v75, v75, 2, v73\n" \
    "s_mul_i32 s78, %[w], 229376\n" \
    "s_lshl4_add_u32 s78, s75, s78\n" \
    "s_movk_i32 s79, 3584\n" \
    "v_mul_u32_u24 v75, s79, v75\n" \
    "v_add_u32 v75, s78, v75\n" \
    "v_mov_b64 v[34:35], s[16:17]\n" \
    "s_mov_b32 exec_lo, 0xaaaaaaaa\n" \
    "s_mov_b32 exec_hi, 0xaaaaaaaa\n" \
    "v_mov_b64 v[34:35], s[18:19]\n" \
    "s_mov_b64 exec, -1\n" \
    "v_add_co_u32 v34, vcc, v34, v75\n" \
    "s_nop 1\n" \
    "v_addc_co_u32 v35, vcc, 0, v35, vcc\n" \
    "v_add_co_u32 v36, vcc, 0x1c000, v34\n" \
    "s_nop 1\n" \
    "v_addc_co_u32 v37, vcc, 0, v35, vcc\n" \
    "s_lshl_b32 s78, %[w], 16\n" \
    "s_lshl_b32 s79, s74, 7\n" \
    "s_add_u32 s78, s78, s79\n" \
    "v_lshl_add_u32 v75, v71, 10, s78\n" \
    "v_and_b32 v76, 16, v65\n" \
    "v_xor_b32 v77, v73, v69\n" \
    "v_lshl_add_u32 v76, v77, 5, v76\n" \
    "v_add_u32 v236, v75, v76\n" \
    "v_xor_b32 v237, 64, v236\n" \
    "v_add_u32 v237, 0x1c00, v237\n" \
    "v_add_u32 v238, 0x3800, v236\n" \
    "v_add_u32 v239, 0x3800, v237\n" \
    "s_lshl_b32 s78, s73, 16\n" \
    "s_lshl_b32 s79, s77, 14\n" \
    "s_add_u32 s78, s78, s79\n" \
    "s_lshr_b32 s79, s78, 8\n" \
    "s_add_u32 s79, s79, 24576\n" \
    "v_lshl_add_u32 v243, v66, 2, s79\n" \
    "s_lshl_b32 s79, s76, 13\n" \
    "s_add_u32 s79, s79, s78\n" \
    "s_add_u32 s79, s79, 528384\n" \
    "v_add_u32 v253, s79, v65\n" \
    "s_lshl_b32 s79, s74, 7\n" \
    "s_add_u32 s78, s78, s79\n" \
    "s_lshl_b32 s79, s76, 6\n" \
    "s_add_u32 s78, s78, s79\n" \
    "v_lshl_add_u32 v242, v66, 10, s78\n" \
    "v_lshl_add_u32 v242, v69, 5, v242\n" \
    "v_lshl_add_u32 v242, v70, 4, v242\n" \
    "s_lshl_b32 s78, s73, 16\n" \
    "s_and_b32 s79, %[w], 1\n" \
    "s_lshl_b32 s79, s79, 15\n" \
    "s_add_u32 s78, s78, s79\n" \
    "s_lshl_b32 s79, s74, 11\n" \
    "s_add_u32 s78, s78, s79\n" \
    "s_bfe_u32 s79, %[w], 0x10001\n" \
    "s_lshl_b32 s79, s79, 10\n" \
    "s_add_u32 s78, s78, s79\n" \
    "s_lshl_b32 s79, s76, 9\n" \
    "s_add_u32 s78, s78, s79\n" \
    "s_add_u32 s78, s78, 524288\n" \
    "v_lshl_add_u32 v75, v69, 14, v74\n" \
    "v_add_u32 v248, s78, v75\n" \
    "v_mov_b32 v44, v248\n" \
    "v_mov_b32 v45, v248\n" \
    "s_lshl_b32 s78, s75, 11\n" \
    "s_lshl_b32 s79, %[w], 9\n" \
    "s_add_u32 s78, s78, s79\n" \
    "s_add_u32 s78, s78, 2621440\n" \
    "s_cmp_lg_u32 s72, 0\n" \
    "s_cselect_b64 exec, -1, 0\n" \
    "v_lshl_add_u32 v45, v72, 4, s78\n" \
    "s_mov_b64 exec, -1\n" \
    "s_lshl2_add_u32 s78, %[w], 4096\n" \
    "v_lshl_add_u32 v240, v72, 5, s78\n" \
    "s_mul_i32 s78, %[w], 57344\n" \
    "s_lshl_b32 s79, s74, 8\n" \
    "s_add_u32 s78, s78, s79\n" \
    "s_add_u32 s78, s78, 2621440\n" \
    "v_lshl_add_u32 v75, v68, 11, v67\n" \
    "v_add_u32 v241, s78, v75\n" \
    "s_lshl_b32 s78, s76, 15\n" \
    "v_add_u32 v193, s78, v65\n" \
    "s_add_u32 s78, s78, 65536\n" \
    "v_add_u32 v194, 130048, v74\n" \
    "v_mov_b32 v195, 0\n" \
    "v_add_u32 v43, 0x400, v194\n" \
    "s_mov_b32 exec_lo, 0xffff\n" \
    "s_mov_b32 exec_hi, 0xffff\n" \
    "v_add_u32 v194, s78, v74\n" \
    "v_mov_b32 v195, 0x1000\n" \
    "v_add_u32 v43, s78, v74\n" \
    "s_mov_b64 exec, -1\n" \
    "s_lshl_b32 s78, s77, 11\n" \
    "s_lshl_b32 s79, s76, 10\n" \
    "s_add_u32 s78, s78, s79\n" \
    "s_add_u32 s78, s78, 151552\n" \
    "v_add_u32 v46, s78, v65\n" \
    "v_xor_b32 v47, 0x400, v46\n" \
    "v_mov_b32 v192, 0xdddd\n" \
    "v_mov_b64 v[188:189], 0\n" \
    "v_mov_b64 v[190:191], 0\n" \
    "v_mov_b64 v[232:233], -1\n" \
    "v_mov_b64 v[234:235], -1\n" \
    "s_nop 1\n" \
    "v_mfma_f32_32x32x16_bf16 v[112:127], v[188:191], v[188:191], 0\n" \
    "v_mfma_f32_32x32x16_bf16 v[128:143], v[188:191], v[188:191], 0\n" \
    "v_mfma_f32_32x32x16_bf16 v[144:159], v[188:191], v[188:191], 0\n" \
    "s_cmp_eq_u32 %[w], 7\n" \
    "s_cbranch_scc1 130f\n" \
    "s_setprio 0\n" \
    "s_mul_i32 s70, %[w], 3\n" \
    "s_sub_u32 s70, s70, 11\n" \
    "s_cmp_eq_u32 %[w], 4\n" \
    "s_cselect_b32 s70, 0, s70\n" \
    "s_cselect_b32 s71, 32, 24\n" \
    "s_and_b32 s72, %[b], 7\n" \
    "s_lshl_b32 s72, s72, 15\n" \
    "s_lshl_b32 s73, s70, 9\n" \
    "s_add_u32 s72, s72, s73\n" \
    "s_add_u32 s72, s72, 262144\n" \
    "v_bfe_u32 v202, v64, 2, 1\n" \
    "v_lshl_add_u32 v203, v202, 14, s72\n" \
    "v_lshrrev_b32 v202, 3, v64\n" \
    "v_lshl_add_u32 v203, v202, 9, v203\n" \
    "v_and_b32 v202, 3, v64\n" \
    "v_lshl_add_u32 v203, v202, 7, v203\n" \
    "s_lshl_b32 s74, s70, 10\n" \
    "s_add_u32 s72, s74, 65536\n" \
    "v_lshl_add_u32 v202, v64, 4, s72\n" \
    "s_movk_i32 s72, 330\n" \
    "s_cmp_eq_u32 %[w], 5\n" \
    "s_cselect_b32 s72, 330, s72\n" \
    "s_cmp_eq_u32 %[w], 4\n" \
    "s_cselect_b32 s72, 330, s72\n" \
    "s_lshr_b32 s73, %[b], 3\n" \
    "s_mul_i32 s73, s73, 0\n" \
    "s_lshr_b32 s73, s73, 4\n" \
    "s_sub_u32 s72, s72, s73\n" \
    "s_add_u32 s72, s72, s62\n" \
    "s_mov_b32 s73, 0\n" \
    "122:\n" \
    "s_memrealtime s[76:77]\n" \
    "s_waitcnt lgkmcnt(0)\n" \
    "s_sub_u32 s76, s76, s72\n" \
    "s_cmp_ge_i32 s76, 0\n" \
    "s_cbranch_scc1 123f\n" \
    "s_add_u32 s73, s73, 1\n" \
    "s_cmp_le_u32 s73, 0x1000\n" \
    "s_cbranch_scc1 122b\n" \
    "123:\n" \
    "s_mov_b32 s73, 0\n" \
    "s_bfm_b64 exec, s71, 0\n" \
    "124:\n" \
    "global_load_dwordx2 v[204:205], v203, s[26:27] sc1\n" \
    "s_waitcnt vmcnt(0)\n" \
    "v_cmp_ne_u32 vcc, s67, v205\n" \
    "s_and_b64 exec, exec, vcc\n" \
    "s_cbranch_scc0 125f\n" \
    "s_add_u32 s73, s73, 1\n" \
    "s_cmp_le_u32 s73, 0x400000\n" \
    "s_cbranch_scc1 124b\n" \
    "125:\n" \
    "s_mov_b64 exec, -1\n" \
    "s_mov_b32 s73, 0\n" \
    "126:\n" \
    "ds_read_b32 v204, v95 offset:32\n" \
    "s_waitcnt lgkmcnt(0)\n" \
    "v_readfirstlane_b32 s72, v204\n" \
    "s_cmp_ge_u32 s72, 4\n" \
    "s_cbranch_scc1 127f\n" \
    "s_add_u32 s73, s73, 1\n" \
    "s_cmp_le_u32 s73, 4194304\n" \
    "s_cbranch_scc1 126b\n" \
    "127:\n" \
    "s_cmp_eq_u32 s71, 32\n" \
    "s_cselect_b64 vcc, -1, 0\n" \
    "s_mov_b32 m0, s74\n" \
    "s_nop 0\n" \
    "global_load_lds_dwordx4 v202, s[26:27] offset:0 sc1\n" \
    "global_load_lds_dwordx4 v202, s[26:27] offset:1024 sc1\n" \
    "global_load_lds_dwordx4 v202, s[26:27] offset:2048 sc1\n" \
    "s_cbranch_vccz 140f\n" \
    "global_load_lds_dwordx4 v202, s[26:27] offset:3072 sc1\n" \
    "140:\n" \
    "s_add_u32 m0, m0, 0x8000\n" \
    "v_add_u32 v202, 0x8000, v202\n" \
    "global_load_lds_dwordx4 v202, s[26:27] offset:0 sc1\n" \
    "global_load_lds_dwordx4 v202, s[26:27] offset:1024 sc1\n" \
    "global_load_lds_dwordx4 v202, s[26:27] offset:2048 sc1\n" \
    "s_cbranch_vccz 141f\n" \
    "global_load_lds_dwordx4 v202, s[26:27] offset:3072 sc1\n" \
    "141:\n" \
    "s_add_u32 m0, m0, 0x8000\n" \
    "v_add_u32 v202, 0x8000, v202\n" \
    "global_load_lds_dwordx4 v202, s[26:27] offset:0 sc1\n" \
    "global_load_lds_dwordx4 v202, s[26:27] offset:1024 sc1\n" \
    "global_load_lds_dwordx4 v202, s[26:27] offset:2048 sc1\n" \
    "s_cbranch_vccz 142f\n" \
    "global_load_lds_dwordx4 v202, s[26:27] offset:3072 sc1\n" \
    "142:\n" \
    "s_add_u32 m0, m0, 0x8000\n" \
    "v_add_u32 v202, 0x8000, v202\n" \
    "global_load_lds_dwordx4 v202, s[26:27] offset:0 sc1\n" \
    "global_load_lds_dwordx4 v202, s[26:27] offset:1024 sc1\n" \
    "global_load_lds_dwordx4 v202, s[26:27] offset:2048 sc1\n" \
    "s_cbranch_vccz 143f\n" \
    "global_load_lds_dwordx4 v202, s[26:27] offset:3072 sc1\n" \
    "143:\n" \
    "s_waitcnt vmcnt(0)\n" \
    "s_branch 300f\n" \
    "130:\n" \
    "s_waitcnt vmcnt(0)\n" \
    "s_mov_b64 exec, 0xff\n" \
    "global_store_dwordx2 v93, v[98:99], s[26:27] sc1\n" \
    "s_mov_b64 exec, -1\n" \
    "s_setprio 0\n" \
    "s_and_b32 s72, %[b], 7\n" \
    "s_lshl_b32 s72, s72, 15\n" \
    "s_add_u32 s72, s72, 262144\n" \
    "v_lshrrev_b32 v202, 1, v64\n" \
    "v_lshl_add_u32 v202, v202, 9, s72\n" \
    "v_and_b32 v203, 1, v64\n" \
    "v_lshl_add_u32 v202, v203, 7, v202\n" \
    "v_add_u32 v203, 0x4000, v202\n" \
    "s_mov_b32 s72, 340\n" \
    "s_lshr_b32 s73, %[b], 3\n" \
    "s_mul_i32 s73, s73, 0\n" \
    "s_lshr_b32 s73, s73, 4\n" \
    "s_sub_u32 s72, s72, s73\n" \
    "s_add_u32 s72, s72, s62\n" \
    "s_mov_b32 s73, 0\n" \
    "132:\n" \
    "s_memrealtime s[76:77]\n" \
    "s_waitcnt lgkmcnt(0)\n" \
    "s_sub_u32 s76, s76, s72\n" \
    "s_cmp_ge_i32 s76, 0\n" \
    "s_cbranch_scc1 133f\n" \
    "s_add_u32 s73, s73, 1\n" \
    "s_cmp_le_u32 s73, 0x1000\n" \
    "s_cbranch_scc1 132b\n" \
    "133:\n" \
    "s_mov_b32 s73, 0\n" \
    "global_load_dwordx2 v[204:205], v202, s[26:27] offset:0 sc1\n" \
    "global_load_dwordx2 v[206:207], v203, s[26:27] offset:0 sc1\n" \
    "global_load_dwordx2 v[208:209], v202, s[26:27] offset:256 sc1\n" \
    "global_load_dwordx2 v[210:211], v203, s[26:27] offset:256 sc1\n" \
    "134:\n" \
    "s_waitcnt vmcnt(0)\n" \
    "v_cmp_eq_u32 vcc, s67, v205\n" \
    "s_cmp_eq_u64 vcc, -1\n" \
    "s_cbranch_scc0 136f\n" \
    "v_cmp_eq_u32 vcc, s67, v207\n" \
    "s_cmp_eq_u64 vcc, -1\n" \
    "s_cbranch_scc0 136f\n" \
    "v_cmp_eq_u32 vcc, s67, v209\n" \
    "s_cmp_eq_u64 vcc, -1\n" \
    "s_cbranch_scc0 136f\n" \
    "v_cmp_eq_u32 vcc, s67, v211\n" \
    "s_cmp_eq_u64 vcc, -1\n" \
    "s_cbranch_scc0 136f\n" \
    "135:\n" \
    "v_max_u32 v204, v204, v206\n" \
    "s_nop 1\n" \
    "v_max_u32_dpp v204, v204, v204 row_shr:1 row_mask:0xf bank_mask:0xf\n" \
    "s_nop 1\n" \
    "v_max_u32_dpp v204, v204, v204 row_shr:2 row_mask:0xf bank_mask:0xf\n" \
    "s_nop 1\n" \
    "v_max_u32_dpp v204, v204, v204 row_shr:4 row_mask:0xf bank_mask:0xf\n" \
    "s_nop 1\n" \
    "v_max_u32_dpp v204, v204, v204 row_shr:8 row_mask:0xf bank_mask:0xf\n" \
    "s_nop 1\n" \
    "v_max_u32_dpp v204, v204, v204 row_bcast:15 row_mask:0xa bank_mask:0xf\n" \
    "s_nop 1\n" \
    "v_max_u32_dpp v204, v204, v204 row_bcast:31 row_mask:0xc bank_mask:0xf\n" \
    "s_mov_b32 exec_lo, 0\n" \
    "s_mov_b32 exec_hi, 0x80000000\n" \
    "ds_write_b32 v95, v204\n" \
    "s_mov_b64 exec, -1\n" \
    "s_branch 300f\n" \
    "136:\n" \
    "v_cmp_eq_u32 vcc, s67, v205\n" \
    "s_cmp_eq_u64 vcc, -1\n" \
    "s_cbranch_scc1 144f\n" \
    "global_load_dwordx2 v[204:205], v202, s[26:27] offset:0 sc1\n" \
    "144:\n" \
    "v_cmp_eq_u32 vcc, s67, v207\n" \
    "s_cmp_eq_u64 vcc, -1\n" \
    "s_cbranch_scc1 145f\n" \
    "global_load_dwordx2 v[206:207], v203, s[26:27] offset:0 sc1\n" \
    "145:\n" \
    "v_cmp_eq_u32 vcc, s67, v209\n" \
    "s_cmp_eq_u64 vcc, -1\n" \
    "s_cbranch_scc1 146f\n" \
    "global_load_dwordx2 v[208:209], v202, s[26:27] offset:256 sc1\n" \
    "146:\n" \
    "v_cmp_eq_u32 vcc, s67, v211\n" \
    "s_cmp_eq_u64 vcc, -1\n" \
    "s_cbranch_scc1 147f\n" \
    "global_load_dwordx2 v[210:211], v203, s[26:27] offset:256 sc1\n" \
    "147:\n" \
    "s_add_u32 s73, s73, 1\n" \
    "s_cmp_le_u32 s73, 0x400000\n" \
    "s_cbranch_scc1 134b\n" \
    "s_waitcnt vmcnt(0)\n" \
    "s_branch 135b\n" \
    "200:\n" \
    "v_lshrrev_b32 v48, 4, v64\n" \
    "v_and_b32 v49, 0xf0, v65\n" \
    "s_and_b32 s68, %[b], 7\n" \
    "s_lshr_b32 s69, %[b], 6\n" \
    "s_lshl2_add_u32 s68, s68, s69\n" \
    "s_lshl_b32 s68, s68, 16\n" \
    "v_lshl_add_u32 v50, v48, 10, v49\n" \
    "s_and_b32 s69, s64, 3\n" \
    "s_lshl_b32 s69, s69, 2\n" \
    "s_mul_i32 s65, %[w], 3\n" \
    "s_add_u32 s66, s65, 1\n" \
    "s_add_u32 s67, s65, 2\n" \
    "v_add_u32 v50, s68, v50\n" \
    "s_cmp_ge_u32 s65, s69\n" \
    "s_cselect_b32 s70, 4, 0\n" \
    "s_add_u32 s65, s65, s70\n" \
    "s_lshl_b32 s70, s65, 12\n" \
    "v_add_u32 v25, s70, v50\n" \
    "s_cmp_ge_u32 s66, s69\n" \
    "s_cselect_b32 s70, 4, 0\n" \
    "s_add_u32 s66, s66, s70\n" \
    "s_lshl_b32 s70, s66, 12\n" \
    "v_add_u32 v14, s70, v50\n" \
    "s_cmp_ge_u32 s67, s69\n" \
    "s_cselect_b32 s70, 4, 0\n" \
    "s_add_u32 s67, s67, s70\n" \
    "s_lshl_b32 s70, s67, 12\n" \
    "v_add_u32 v15, s70, v50\n" \
    "v_mov_b32 v62, 1\n" \
    "s_waitcnt lgkmcnt(0)\n" \
    "global_load_dwordx4 v[120:123], v25, s[4:5] offset:0\n" \
    "s_lshl_b32 s68, %[w], 13\n" \
    "s_lshl_b32 s69, s64, 7\n" \
    "global_load_dwordx4 v[124:127], v14, s[4:5] offset:0\n" \
    "s_add_u32 s68, s68, s69\n" \
    "s_barrier\n" \
    "global_load_dwordx4 v[128:131], v15, s[4:5] offset:0\n" \
    "v_bfe_u32 v51, v64, 2, 3\n" \
    "v_lshl_add_u32 v26, v51, 10, s68\n" \
    "global_load_dwordx4 v[132:135], v25, s[4:5] offset:256\n" \
    "v_lshrrev_b32 v52, 5, v64\n" \
    "global_load_dwordx4 v[136:139], v14, s[4:5] offset:256\n" \
    "v_lshl_add_u32 v26, v52, 6, v26\n" \
    "v_and_b32 v51, 48, v65\n" \
    "global_load_dwordx4 v[140:143], v15, s[4:5] offset:256\n" \
    "v_add_u32 v26, v51, v26\n" \
    "global_load_dwordx4 v[144:147], v26, s[6:7]\n" \
    "v_add_u32 v26, 0x8000, v26\n" \
    "v_and_b32 v51, 15, v64\n" \
    "v_xor_b32 v50, v51, v48\n" \
    "v_lshlrev_b32 v50, 4, v50\n" \
    "v_lshl_add_u32 v50, v48, 8, v50\n" \
    "s_and_b32 s68, s65, 3\n" \
    "s_lshl_b32 s68, s68, 6\n" \
    "v_xor_b32 v27, s68, v50\n" \
    "s_and_b32 s68, s65, 7\n" \
    "s_lshl_b32 s68, s68, 10\n" \
    "s_lshr_b32 s69, s65, 3\n" \
    "s_lshl_b32 s69, s69, 15\n" \
    "global_load_dwordx4 v[148:151], v26, s[6:7]\n" \
    "v_add_u32 v26, 0x8000, v26\n" \
    "s_add_u32 s68, s68, s69\n" \
    "v_add_u32 v27, s68, v27\n" \
    "s_and_b32 s68, s66, 3\n" \
    "s_lshl_b32 s68, s68, 6\n" \
    "v_xor_b32 v28, s68, v50\n" \
    "s_and_b32 s68, s66, 7\n" \
    "s_lshl_b32 s68, s68, 10\n" \
    "s_lshr_b32 s69, s66, 3\n" \
    "s_lshl_b32 s69, s69, 15\n" \
    "s_add_u32 s68, s68, s69\n" \
    "v_add_u32 v28, s68, v28\n" \
    "global_load_dwordx4 v[152:155], v26, s[6:7]\n" \
    "v_add_u32 v26, 0x8000, v26\n" \
    "s_and_b32 s68, s67, 3\n" \
    "s_lshl_b32 s68, s68, 6\n" \
    "v_xor_b32 v29, s68, v50\n" \
    "s_and_b32 s68, s67, 7\n" \
    "s_lshl_b32 s68, s68, 10\n" \
    "s_lshr_b32 s69, s67, 3\n" \
    "s_lshl_b32 s69, s69, 15\n" \
    "s_add_u32 s68, s68, s69\n" \
    "v_add_u32 v29, s68, v29\n" \
    "s_lshl_b32 s68, %[w], 10\n" \
    "s_add_u32 s68, s68, 65536\n" \
    "global_load_dwordx4 v[156:159], v26, s[6:7]\n" \
    "v_add_u32 v26, 0x8000, v26\n" \
    "v_add_u32 v30, s68, v65\n" \
    "v_mov_b32 v31, 162816\n" \
    "s_lshr_b32 s68, %[w], 1\n" \
    "s_lshl_b32 s68, s68, 9\n" \
    "s_add_u32 s68, s68, 65536\n" \
    "v_lshl_add_u32 v24, v52, 10, s68\n" \
    "v_and_b32 v50, 0xc0, v65\n" \
    "v_add_u32 v24, v50, v24\n" \
    "v_and_b32 v50, 1, v48\n" \
    "v_lshl_add_u32 v24, v50, 5, v24\n" \
    "v_and_b32 v50, 3, v64\n" \
    "global_load_dwordx4 v[160:163], v26, s[6:7]\n" \
    "v_add_u32 v26, 0x8000, v26\n" \
    "v_lshl_add_u32 v24, v50, 3, v24\n" \
    "s_and_b32 s68, %[w], 1\n" \
    "s_lshl_b32 s68, s68, 15\n" \
    "v_xor_b32 v50, v52, v51\n" \
    "v_lshlrev_b32 v50, 4, v50\n" \
    "v_and_b32 v51, 31, v64\n" \
    "v_lshl_add_u32 v50, v51, 8, v50\n" \
    "v_add_u32 v16, s68, v50\n" \
    "v_xor_b32 v17, 32, v16\n" \
    "v_xor_b32 v18, 64, v16\n" \
    "v_xor_b32 v19, 0x60, v16\n" \
    "global_load_dwordx4 v[164:167], v26, s[6:7]\n" \
    "v_add_u32 v26, 0x8000, v26\n" \
    "v_xor_b32 v20, 0x80, v16\n" \
    "v_xor_b32 v21, 0xa0, v16\n" \
    "v_xor_b32 v22, 0xc0, v16\n" \
    "v_xor_b32 v23, 0xe0, v16\n" \
    "s_and_b32 s72, %[b], 7\n" \
    "s_lshr_b32 s78, %[b], 6\n" \
    "s_lshl2_add_u32 s73, s72, s78\n" \
    "s_bfe_u32 s74, %[b], 0x30003\n" \
    "s_lshr_b32 s78, %[b], 3\n" \
    "s_min_u32 s79, s78, 27\n" \
    "s_mul_i32 s75, s72, 28\n" \
    "s_add_u32 s75, s75, s79\n" \
    "global_load_dwordx4 v[168:171], v26, s[6:7]\n" \
    "v_add_u32 v26, 0x8000, v26\n" \
    "s_cmp_lt_u32 s78, 28\n" \
    "s_cselect_b32 s72, 1, 0\n" \
    "s_cmp_lt_u32 %[w], 4\n" \
    "s_cselect_b32 s72, s72, 0\n" \
    "s_lshr_b32 s76, %[w], 2\n" \
    "s_and_b32 s77, %[w], 3\n" \
    "v_and_b32 v66, 15, v64\n" \
    "v_and_b32 v67, 0xf0, v65\n" \
    "v_lshrrev_b32 v68, 4, v64\n" \
    "v_and_b32 v69, 1, v68\n" \
    "v_lshrrev_b32 v70, 5, v64\n" \
    "global_load_dwordx4 v[172:175], v26, s[6:7]\n" \
    "v_add_u32 v26, 0x8000, v26\n" \
    "v_lshrrev_b32 v71, 3, v64\n" \
    "v_and_b32 v72, 31, v64\n" \
    "v_bfe_u32 v73, v64, 1, 2\n" \
    "v_lshl_add_u32 v74, v70, 8, v67\n" \
    "s_and_b32 s78, %[w], 1\n" \
    "s_lshl_b32 s78, s78, 16\n" \
    "s_bfe_u32 s79, %[w], 0x10001\n" \
    "s_lshl_b32 s79, s79, 15\n" \
    "s_or_b32 s78, s78, s79\n" \
    "s_lshl_b32 s79, s76, 11\n" \
    "s_add_u32 %[skv3], s78, s79\n" \
    "global_load_dwordx4 v[176:179], v25, s[4:5] offset:512\n" \
    "s_add_u32 %[skv], %[skv3], s79\n" \
    "s_add_u32 %[skv3], %[skv3], 26624\n" \
    "s_add_u32 %[skv], %[skv], 10240\n" \
    "v_add_u32 v32, %[skv], v65\n" \
    "v_add_u32 v33, %[skv3], v65\n" \
    "s_lshr_b32 s79, s78, 16\n" \
    "s_lshl_b32 s79, s79, 13\n" \
    "s_and_b32 s78, s78, 0x8000\n" \
    "s_or_b32 s78, s78, s79\n" \
    "s_lshl_b32 s79, s76, 16\n" \
    "s_or_b32 %[swo], s78, s79\n" \
    "global_load_dwordx4 v[180:183], v14, s[4:5] offset:512\n" \
    "v_lshl_add_u32 v249, v68, 10, s79\n" \
    "v_lshrrev_b32 v75, 2, v66\n" \
    "v_lshl_add_u32 v249, v75, 7, v249\n" \
    "v_and_b32 v75, 3, v66\n" \
    "v_lshl_add_u32 v249, v75, 3, v249\n" \
    "v_bfe_u32 v75, v64, 3, 1\n" \
    "v_lshl_add_u32 v249, v75, 5, v249\n" \
    "v_xor_b32 v75, s76, v69\n" \
    "v_lshl_add_u32 v249, v75, 6, v249\n" \
    "v_xor_b32 v250, 32, v249\n" \
    "v_xor_b32 v251, 64, v249\n" \
    "global_load_dwordx4 v[184:187], v15, s[4:5] offset:512\n" \
    "v_xor_b32 v252, 0x60, v249\n" \
    "s_lshl_b32 s78, %[w], 11\n" \
    "s_add_u32 %[sg], s78, 135168\n" \
    "v_add_u32 v244, %[sg], v65\n" \
    "s_add_u32 s79, s78, 16384\n" \
    "v_add_u32 v254, s79, v65\n" \
    "s_xor_b32 s78, s78, 0x2000\n" \
    "s_add_u32 s79, s78, 16384\n" \
    "v_add_u32 v255, s79, v65\n" \
    "s_add_u32 s79, s78, 135168\n" \
    "v_add_u32 v245, s79, v65\n" \
    "global_load_dwordx4 v[188:191], v25, s[4:5] offset:768\n" \
    "v_lshl_add_u32 v40, v68, 9, %[sg]\n" \
    "v_lshl_add_u32 v40, v69, 7, v40\n" \
    "v_lshl_add_u32 v40, v66, 3, v40\n" \
    "v_xor_b32 v41, 0x80, v40\n" \
    "s_lshl_b32 s78, %[w], 10\n" \
    "s_add_u32 s78, s78, 151552\n" \
    "v_add_u32 v42, s78, v65\n" \
    "s_lshl_b32 s78, %[w], 6\n" \
    "s_add_u32 s78, s78, 160256\n" \
    "s_mov_b32 exec_lo, 0x10001\n" \
    "s_mov_b32 exec_hi, 0x10001\n" \
    "v_lshl_add_u32 v42, v68, 4, s78\n" \
    "s_mov_b64 exec, -1\n" \
    "global_load_dwordx4 v[196:199], v14, s[4:5] offset:768\n" \
    "s_lshl_b32 s78, %[w], 8\n" \
    "s_add_u32 s79, s78, 151552\n" \
    "v_lshl_add_u32 v246, v64, 2, s79\n" \
    "s_xor_b32 s79, s79, 0x400\n" \
    "v_lshl_add_u32 v247, v72, 2, s79\n" \
    "s_lshl_b32 s78, %[w], 7\n" \
    "v_lshl_add_u32 v38, v68, 5, s78\n" \
    "s_lshl_b32 s78, s75, 13\n" \
    "s_lshl_b32 s79, s77, 8\n" \
    "global_load_dwordx4 v[200:203], v15, s[4:5] offset:768\n" \
    "s_add_u32 s78, s78, s79\n" \
    "v_lshl_add_u32 v39, v64, 2, s78\n" \
    "v_xor_b32 v75, v71, v70\n" \
    "v_lshl_add_u32 v75, v75, 2, v73\n" \
    "s_mul_i32 s78, %[w], 229376\n" \
    "s_lshl4_add_u32 s78, s75, s78\n" \
    "s_movk_i32 s79, 3584\n" \
    "v_mul_u32_u24 v75, s79, v75\n" \
    "v_add_u32 v75, s78, v75\n" \
    "v_mov_b64 v[34:35], s[16:17]\n" \
    "global_load_dwordx4 v[204:207], v26, s[6:7]\n" \
    "v_add_u32 v26, 0x8000, v26\n" \
    "s_mov_b32 exec_lo, 0xaaaaaaaa\n" \
    "s_mov_b32 exec_hi, 0xaaaaaaaa\n" \
    "v_mov_b64 v[34:35], s[18:19]\n" \
    "s_mov_b64 exec, -1\n" \
    "v_add_co_u32 v34, vcc, v34, v75\n" \
    "s_nop 1\n" \
    "v_addc_co_u32 v35, vcc, 0, v35, vcc\n" \
    "v_add_co_u32 v36, vcc, 0x1c000, v34\n" \
    "s_nop 1\n" \
    "v_addc_co_u32 v37, vcc, 0, v35, vcc\n" \
    "s_lshl_b32 s78, %[w], 16\n" \
    "s_lshl_b32 s79, s74, 7\n" \
    "global_load_dwordx4 v[208:211], v26, s[6:7]\n" \
    "v_add_u32 v26, 0x8000, v26\n" \
    "s_add_u32 s78, s78, s79\n" \
    "v_lshl_add_u32 v75, v71, 10, s78\n" \
    "v_and_b32 v76, 16, v65\n" \
    "v_xor_b32 v77, v73, v69\n" \
    "v_lshl_add_u32 v76, v77, 5, v76\n" \
    "v_add_u32 v236, v75, v76\n" \
    "v_xor_b32 v237, 64, v236\n" \
    "v_add_u32 v237, 0x1c00, v237\n" \
    "v_add_u32 v238, 0x3800, v236\n" \
    "v_add_u32 v239, 0x3800, v237\n" \
    "s_lshl_b32 s78, s73, 16\n" \
    "global_load_dwordx4 v[212:215], v26, s[6:7]\n" \
    "v_add_u32 v26, 0x8000, v26\n" \
    "s_lshl_b32 s79, s77, 14\n" \
    "s_add_u32 s78, s78, s79\n" \
    "s_lshr_b32 s79, s78, 8\n" \
    "s_add_u32 s79, s79, 24576\n" \
    "v_lshl_add_u32 v243, v66, 2, s79\n" \
    "s_lshl_b32 s79, s76, 13\n" \
    "s_add_u32 s79, s79, s78\n" \
    "s_add_u32 s79, s79, 528384\n" \
    "v_add_u32 v253, s79, v65\n" \
    "s_lshl_b32 s79, s74, 7\n" \
    "s_add_u32 s78, s78, s79\n" \
    "s_lshl_b32 s79, s76, 6\n" \
    "global_load_dwordx4 v[216:219], v26, s[6:7]\n" \
    "v_add_u32 v26, 0x8000, v26\n" \
    "s_add_u32 s78, s78, s79\n" \
    "v_lshl_add_u32 v242, v66, 10, s78\n" \
    "v_lshl_add_u32 v242, v69, 5, v242\n" \
    "v_lshl_add_u32 v242, v70, 4, v242\n" \
    "s_lshl_b32 s78, s73, 16\n" \
    "s_and_b32 s79, %[w], 1\n" \
    "s_lshl_b32 s79, s79, 15\n" \
    "s_add_u32 s78, s78, s79\n" \
    "s_lshl_b32 s79, s74, 11\n" \
    "s_add_u32 s78, s78, s79\n" \
    "s_bfe_u32 s79, %[w], 0x10001\n" \
    "global_load_dwordx4 v[220:223], v26, s[6:7]\n" \
    "v_add_u32 v26, 0x8000, v26\n" \
    "s_lshl_b32 s79, s79, 10\n" \
    "s_add_u32 s78, s78, s79\n" \
    "s_lshl_b32 s79, s76, 9\n" \
    "s_add_u32 s78, s78, s79\n" \
    "s_add_u32 s78, s78, 524288\n" \
    "v_lshl_add_u32 v75, v69, 14, v74\n" \
    "v_add_u32 v248, s78, v75\n" \
    "v_mov_b32 v44, v248\n" \
    "v_mov_b32 v45, v248\n" \
    "s_lshl_b32 s78, s75, 11\n" \
    "s_lshl_b32 s79, %[w], 9\n" \
    "global_load_dwordx4 v[224:227], v26, s[6:7]\n" \
    "v_add_u32 v26, 0x8000, v26\n" \
    "s_add_u32 s78, s78, s79\n" \
    "s_add_u32 s78, s78, 2621440\n" \
    "s_cmp_lg_u32 s72, 0\n" \
    "s_cselect_b64 exec, -1, 0\n" \
    "v_lshl_add_u32 v45, v72, 4, s78\n" \
    "s_mov_b64 exec, -1\n" \
    "s_lshl2_add_u32 s78, %[w], 4096\n" \
    "v_lshl_add_u32 v240, v72, 5, s78\n" \
    "s_mul_i32 s78, %[w], 57344\n" \
    "s_lshl_b32 s79, s74, 8\n" \
    "s_add_u32 s78, s78, s79\n" \
    "global_load_dwordx4 v[228:231], v26, s[6:7]\n" \
    "v_add_u32 v26, 0x8000, v26\n" \
    "s_add_u32 s78, s78, 2621440\n" \
    "v_lshl_add_u32 v75, v68, 11, v67\n" \
    "v_add_u32 v241, s78, v75\n" \
    "s_lshl_b32 s78, s76, 15\n" \
    "v_add_u32 v193, s78, v65\n" \
    "s_add_u32 s78, s78, 65536\n" \
    "v_add_u32 v194, 130048, v74\n" \
    "v_mov_b32 v195, 0\n" \
    "v_add_u32 v43, 0x400, v194\n" \
    "global_load_dwordx4 v[232:235], v26, s[6:7]\n" \
    "v_add_u32 v26, 0x8000, v26\n" \
    "s_mov_b32 exec_lo, 0xffff\n" \
    "s_mov_b32 exec_hi, 0xffff\n" \
    "v_add_u32 v194, s78, v74\n" \
    "v_mov_b32 v195, 0x1000\n" \
    "v_add_u32 v43, s78, v74\n" \
    "s_mov_b64 exec, -1\n" \
    "s_lshl_b32 s78, s77, 11\n" \
    "s_lshl_b32 s79, s76, 10\n" \
    "s_add_u32 s78, s78, s79\n" \
    "s_add_u32 s78, s78, 151552\n" \
    "v_add_u32 v46, s78, v65\n" \
    "v_xor_b32 v47, 0x400, v46\n" \
    "v_mov_b32 v192, 0xdddd\n" \
    "s_waitcnt vmcnt(27)\n" \
    "ds_write_b128 v27, v[120:123] offset:0\n" \
    "s_waitcnt vmcnt(26)\n" \
    "ds_write_b128 v28, v[124:127] offset:0\n" \
    "s_waitcnt vmcnt(25)\n" \
    "ds_write_b128 v29, v[128:131] offset:0\n" \
    "s_waitcnt vmcnt(24)\n" \
    "ds_write_b128 v27, v[132:135] offset:8192\n" \
    "s_waitcnt vmcnt(23)\n" \
    "ds_write_b128 v28, v[136:139] offset:8192\n" \
    "s_waitcnt vmcnt(22)\n" \
    "ds_write_b128 v29, v[140:143] offset:8192\n" \
    "s_waitcnt vmcnt(21)\n" \
    "ds_write_b128 v30, v[144:147] offset:0\n" \
    "s_waitcnt vmcnt(20)\n" \
    "ds_write_b128 v30, v[148:151] offset:4096\n" \
    "s_waitcnt vmcnt(19)\n" \
    "ds_write_b128 v30, v[152:155] offset:8192\n" \
    "s_waitcnt vmcnt(18)\n" \
    "ds_write_b128 v30, v[156:159] offset:12288\n" \
    "s_waitcnt vmcnt(17)\n" \
    "ds_write_b128 v30, v[160:163] offset:32768\n" \
    "s_waitcnt vmcnt(16)\n" \
    "ds_write_b128 v30, v[164:167] offset:36864\n" \
    "s_waitcnt vmcnt(15)\n" \
    "ds_write_b128 v30, v[168:171] offset:40960\n" \
    "s_waitcnt vmcnt(14)\n" \
    "ds_write_b128 v30, v[172:175] offset:45056\n" \
    "s_mov_b64 exec, 1\n" \
    "ds_add_u32 v31, v62 offset:24\n" \
    "s_mov_b64 exec, -1\n" \
    "s_mov_b32 s71, 0\n" \
    "210:\n" \
    "ds_read_b32 v63, v31 offset:24\n" \
    "s_waitcnt lgkmcnt(0)\n" \
    "v_readfirstlane_b32 s70, v63\n" \
    "s_cmp_ge_u32 s70, 8\n" \
    "s_cbranch_scc1 211f\n" \
    "s_add_u32 s71, s71, 1\n" \
    "s_cmp_le_u32 s71, 4194304\n" \
    "s_cbranch_scc1 210b\n" \
    "211:\n" \
    "ds_read_b64_tr_b16 v[80:81], v24 offset:0\n" \
    "ds_read_b64_tr_b16 v[82:83], v24 offset:256\n" \
    "ds_read_b128 v[84:87], v16 offset:0\n" \
    "ds_read_b64_tr_b16 v[88:89], v24 offset:2048\n" \
    "ds_read_b64_tr_b16 v[90:91], v24 offset:2304\n" \
    "ds_read_b128 v[92:95], v17 offset:0\n" \
    "ds_read_b64_tr_b16 v[96:97], v24 offset:4096\n" \
    "ds_read_b64_tr_b16 v[98:99], v24 offset:4352\n" \
    "ds_read_b128 v[100:103], v18 offset:0\n" \
    "ds_read_b64_tr_b16 v[104:105], v24 offset:6144\n" \
    "ds_read_b64_tr_b16 v[106:107], v24 offset:6400\n" \
    "ds_read_b128 v[108:111], v19 offset:0\n" \
    "s_waitcnt lgkmcnt(9)\n" \
    "v_mfma_f32_32x32x16_bf16 v[64:79], v[80:83], v[84:87], 0\n" \
    "ds_read_b64_tr_b16 v[112:113], v24 offset:8192\n" \
    "ds_read_b64_tr_b16 v[114:115], v24 offset:8448\n" \
    "ds_read_b128 v[116:119], v20 offset:0\n" \
    "s_waitcnt vmcnt(13)\n" \
    "ds_write_b128 v27, v[176:179] offset:16384\n" \
    "s_waitcnt lgkmcnt(10)\n" \
    "v_mfma_f32_32x32x16_bf16 v[64:79], v[88:91], v[92:95], v[64:79]\n" \
    "ds_read_b64_tr_b16 v[80:81], v24 offset:10240\n" \
    "ds_read_b64_tr_b16 v[82:83], v24 offset:10496\n" \
    "ds_read_b128 v[84:87], v21 offset:0\n" \
    "s_waitcnt vmcnt(12)\n" \
    "ds_write_b128 v28, v[180:183] offset:16384\n" \
    "s_waitcnt lgkmcnt(11)\n" \
    "v_mfma_f32_32x32x16_bf16 v[64:79], v[96:99], v[100:103], v[64:79]\n" \
    "ds_read_b64_tr_b16 v[88:89], v24 offset:12288\n" \
    "ds_read_b64_tr_b16 v[90:91], v24 offset:12544\n" \
    "ds_read_b128 v[92:95], v22 offset:0\n" \
    "s_waitcnt vmcnt(11)\n" \
    "ds_write_b128 v29, v[184:187] offset:16384\n" \
    "s_waitcnt lgkmcnt(12)\n" \
    "v_mfma_f32_32x32x16_bf16 v[64:79], v[104:107], v[108:111], v[64:79]\n" \
    "ds_read_b64_tr_b16 v[96:97], v24 offset:14336\n" \
    "ds_read_b64_tr_b16 v[98:99], v24 offset:14592\n" \
    "ds_read_b128 v[100:103], v23 offset:0\n" \
    "s_waitcnt vmcnt(10)\n" \
    "ds_write_b128 v27, v[188:191] offset:24576\n" \
    "s_waitcnt lgkmcnt(13)\n" \
    "v_mfma_f32_32x32x16_bf16 v[64:79], v[112:115], v[116:119], v[64:79]\n" \
    "ds_read_b64_tr_b16 v[104:105], v24 offset:32768\n" \
    "ds_read_b64_tr_b16 v[106:107], v24 offset:33024\n" \
    "ds_read_b128 v[108:111], v16 offset:8192\n" \
    "s_waitcnt vmcnt(9)\n" \
    "ds_write_b128 v28, v[196:199] offset:24576\n" \
    "s_waitcnt lgkmcnt(13)\n" \
    "v_mfma_f32_32x32x16_bf16 v[64:79], v[80:83], v[84:87], v[64:79]\n" \
    "ds_read_b64_tr_b16 v[112:113], v24 offset:34816\n" \
    "ds_read_b64_tr_b16 v[114:115], v24 offset:35072\n" \
    "ds_read_b128 v[116:119], v17 offset:8192\n" \
    "s_waitcnt vmcnt(8)\n" \
    "ds_write_b128 v29, v[200:203] offset:24576\n" \
    "s_waitcnt lgkmcnt(13)\n" \
    "v_mfma_f32_32x32x16_bf16 v[64:79], v[88:91], v[92:95], v[64:79]\n" \
    "ds_read_b64_tr_b16 v[80:81], v24 offset:36864\n" \
    "ds_read_b64_tr_b16 v[82:83], v24 offset:37120\n" \
    "ds_read_b128 v[84:87], v18 offset:8192\n" \
    "s_waitcnt vmcnt(7)\n" \
    "ds_write_b128 v30, v[204:207] offset:16384\n" \
    "s_waitcnt lgkmcnt(13)\n" \
    "v_mfma_f32_32x32x16_bf16 v[64:79], v[96:99], v[100:103], v[64:79]\n" \
    "ds_read_b64_tr_b16 v[88:89], v24 offset:38912\n" \
    "ds_read_b64_tr_b16 v[90:91], v24 offset:39168\n" \
    "ds_read_b128 v[92:95], v19 offset:8192\n" \
    "s_waitcnt vmcnt(6)\n" \
    "ds_write_b128 v30, v[208:211] offset:20480\n" \
    "s_waitcnt lgkmcnt(13)\n" \
    "v_mfma_f32_32x32x16_bf16 v[64:79], v[104:107], v[108:111], v[64:79]\n" \
    "ds_read_b64_tr_b16 v[96:97], v24 offset:40960\n" \
    "ds_read_b64_tr_b16 v[98:99], v24 offset:41216\n" \
    "ds_read_b128 v[100:103], v20 offset:8192\n" \
    "s_waitcnt vmcnt(5)\n" \
    "ds_write_b128 v30, v[212:215] offset:24576\n" \
    "s_waitcnt lgkmcnt(13)\n" \
    "v_mfma_f32_32x32x16_bf16 v[64:79], v[112:115], v[116:119], v[64:79]\n" \
    "ds_read_b64_tr_b16 v[104:105], v24 offset:43008\n" \
    "ds_read_b64_tr_b16 v[106:107], v24 offset:43264\n" \
    "ds_read_b128 v[108:111], v21 offset:8192\n" \
    "s_waitcnt vmcnt(4)\n" \
    "ds_write_b128 v30, v[216:219] offset:28672\n" \
    "s_waitcnt lgkmcnt(13)\n" \
    "v_mfma_f32_32x32x16_bf16 v[64:79], v[80:83], v[84:87], v[64:79]\n" \
    "ds_read_b64_tr_b16 v[112:113], v24 offset:45056\n" \
    "ds_read_b64_tr_b16 v[114:115], v24 offset:45312\n" \
    "ds_read_b128 v[116:119], v22 offset:8192\n" \
    "s_waitcnt vmcnt(3)\n" \
    "ds_write_b128 v30, v[220:223] offset:49152\n" \
    "s_waitcnt lgkmcnt(13)\n" \
    "v_mfma_f32_32x32x16_bf16 v[64:79], v[88:91], v[92:95], v[64:79]\n" \
    "ds_read_b64_tr_b16 v[80:81], v24 offset:47104\n" \
    "ds_read_b64_tr_b16 v[82:83], v24 offset:47360\n" \
    "ds_read_b128 v[84:87], v23 offset:8192\n" \
    "s_waitcnt vmcnt(2)\n" \
    "ds_write_b128 v30, v[224:227] offset:53248\n" \
    "s_waitcnt lgkmcnt(13)\n" \
    "v_mfma_f32_32x32x16_bf16 v[64:79], v[96:99], v[100:103], v[64:79]\n" \
    "s_waitcnt vmcnt(1)\n" \
    "ds_write_b128 v30, v[228:231] offset:57344\n" \
    "s_waitcnt lgkmcnt(10)\n" \
    "v_mfma_f32_32x32x16_bf16 v[64:79], v[104:107], v[108:111], v[64:79]\n" \
    "s_waitcnt vmcnt(0)\n" \
    "ds_write_b128 v30, v[232:235] offset:61440\n" \
    "s_waitcnt lgkmcnt(7)\n" \
    "v_mfma_f32_32x32x16_bf16 v[64:79], v[112:115], v[116:119], v[64:79]\n" \
    "s_waitcnt lgkmcnt(3)\n" \
    "v_mfma_f32_32x32x16_bf16 v[64:79], v[80:83], v[84:87], v[64:79]\n" \
    "s_mov_b64 exec, 1\n" \
    "ds_add_u32 v31, v62 offset:32\n" \
    "s_mov_b64 exec, -1\n" \
    "s_mov_b64 exec, 1\n" \
    "ds_add_u32 v31, v62 offset:28\n" \
    "s_mov_b64 exec, -1\n" \
    "s_mov_b32 s71, 0\n" \
    "212:\n" \
    "ds_read_b32 v63, v31 offset:28\n" \
    "s_waitcnt lgkmcnt(0)\n" \
    "v_readfirstlane_b32 s70, v63\n" \
    "s_cmp_ge_u32 s70, 4\n" \
    "s_cbranch_scc1 213f\n" \
    "s_add_u32 s71, s71, 1\n" \
    "s_cmp_le_u32 s71, 4194304\n" \
    "s_cbranch_scc1 212b\n" \
    "213:\n" \
    "ds_read_b64_tr_b16 v[88:89], v24 offset:16384\n" \
    "ds_read_b64_tr_b16 v[90:91], v24 offset:16640\n" \
    "ds_read_b128 v[92:95], v16 offset:16384\n" \
    "ds_read_b64_tr_b16 v[96:97], v24 offset:18432\n" \
    "ds_read_b64_tr_b16 v[98:99], v24 offset:18688\n" \
    "ds_read_b128 v[100:103], v17 offset:16384\n" \
    "ds_read_b64_tr_b16 v[104:105], v24 offset:20480\n" \
    "ds_read_b64_tr_b16 v[106:107], v24 offset:20736\n" \
    "ds_read_b128 v[108:111], v18 offset:16384\n" \
    "ds_read_b64_tr_b16 v[112:113], v24 offset:22528\n" \
    "ds_read_b64_tr_b16 v[114:115], v24 offset:22784\n" \
    "ds_read_b128 v[116:119], v19 offset:16384\n" \
    "s_waitcnt lgkmcnt(9)\n" \
    "v_mfma_f32_32x32x16_bf16 v[64:79], v[88:91], v[92:95], v[64:79]\n" \
    "ds_read_b64_tr_b16 v[80:81], v24 offset:24576\n" \
    "ds_read_b64_tr_b16 v[82:83], v24 offset:24832\n" \
    "ds_read_b128 v[84:87], v20 offset:16384\n" \
    "s_waitcnt lgkmcnt(9)\n" \
    "v_mfma_f32_32x32x16_bf16 v[64:79], v[96:99], v[100:103], v[64:79]\n" \
    "ds_read_b64_tr_b16 v[88:89], v24 offset:26624\n" \
    "ds_read_b64_tr_b16 v[90:91], v24 offset:26880\n" \
    "ds_read_b128 v[92:95], v21 offset:16384\n" \
    "s_waitcnt lgkmcnt(9)\n" \
    "v_mfma_f32_32x32x16_bf16 v[64:79], v[104:107], v[108:111], v[64:79]\n" \
    "ds_read_b64_tr_b16 v[96:97], v24 offset:28672\n" \
    "ds_read_b64_tr_b16 v[98:99], v24 offset:28928\n" \
    "ds_read_b128 v[100:103], v22 offset:16384\n" \
    "s_waitcnt lgkmcnt(9)\n" \
    "v_mfma_f32_32x32x16_bf16 v[64:79], v[112:115], v[116:119], v[64:79]\n" \
    "ds_read_b64_tr_b16 v[104:105], v24 offset:30720\n" \
    "ds_read_b64_tr_b16 v[106:107], v24 offset:30976\n" \
    "ds_read_b128 v[108:111], v23 offset:16384\n" \
    "s_waitcnt lgkmcnt(9)\n" \
    "v_mfma_f32_32x32x16_bf16 v[64:79], v[80:83], v[84:87], v[64:79]\n" \
    "ds_read_b64_tr_b16 v[112:113], v24 offset:49152\n" \
    "ds_read_b64_tr_b16 v[114:115], v24 offset:49408\n" \
    "ds_read_b128 v[116:119], v16 offset:24576\n" \
    "s_waitcnt lgkmcnt(9)\n" \
    "v_mfma_f32_32x32x16_bf16 v[64:79], v[88:91], v[92:95], v[64:79]\n" \
    "ds_read_b64_tr_b16 v[80:81], v24 offset:51200\n" \
    "ds_read_b64_tr_b16 v[82:83], v24 offset:51456\n" \
    "ds_read_b128 v[84:87], v17 offset:24576\n" \
    "s_waitcnt lgkmcnt(9)\n" \
    "v_mfma_f32_32x32x16_bf16 v[64:79], v[96:99], v[100:103], v[64:79]\n" \
    "ds_read_b64_tr_b16 v[88:89], v24 offset:53248\n" \
    "ds_read_b64_tr_b16 v[90:91], v24 offset:53504\n" \
    "ds_read_b128 v[92:95], v18 offset:24576\n" \
    "s_waitcnt lgkmcnt(9)\n" \
    "v_mfma_f32_32x32x16_bf16 v[64:79], v[104:107], v[108:111], v[64:79]\n" \
    "ds_read_b64_tr_b16 v[96:97], v24 offset:55296\n" \
    "ds_read_b64_tr_b16 v[98:99], v24 offset:55552\n" \
    "ds_read_b128 v[100:103], v19 offset:24576\n" \
    "s_waitcnt lgkmcnt(9)\n" \
    "v_mfma_f32_32x32x16_bf16 v[64:79], v[112:115], v[116:119], v[64:79]\n" \
    "ds_read_b64_tr_b16 v[104:105], v24 offset:57344\n" \
    "ds_read_b64_tr_b16 v[106:107], v24 offset:57600\n" \
    "ds_read_b128 v[108:111], v20 offset:24576\n" \
    "s_waitcnt lgkmcnt(9)\n" \
    "v_mfma_f32_32x32x16_bf16 v[64:79], v[80:83], v[84:87], v[64:79]\n" \
    "ds_read_b64_tr_b16 v[112:113], v24 offset:59392\n" \
    "ds_read_b64_tr_b16 v[114:115], v24 offset:59648\n" \
    "ds_read_b128 v[116:119], v21 offset:24576\n" \
    "s_waitcnt lgkmcnt(9)\n" \
    "v_mfma_f32_32x32x16_bf16 v[64:79], v[88:91], v[92:95], v[64:79]\n" \
    "ds_read_b64_tr_b16 v[80:81], v24 offset:61440\n" \
    "ds_read_b64_tr_b16 v[82:83], v24 offset:61696\n" \
    "ds_read_b128 v[84:87], v22 offset:24576\n" \
    "s_waitcnt lgkmcnt(9)\n" \
    "v_mfma_f32_32x32x16_bf16 v[64:79], v[96:99], v[100:103], v[64:79]\n" \
    "ds_read_b64_tr_b16 v[88:89], v24 offset:63488\n" \
    "ds_read_b64_tr_b16 v[90:91], v24 offset:63744\n" \
    "ds_read_b128 v[92:95], v23 offset:24576\n" \
    "s_waitcnt lgkmcnt(9)\n" \
    "v_mfma_f32_32x32x16_bf16 v[64:79], v[104:107], v[108:111], v[64:79]\n" \
    "s_waitcnt lgkmcnt(6)\n" \
    "v_mfma_f32_32x32x16_bf16 v[64:79], v[112:115], v[116:119], v[64:79]\n" \
    "s_waitcnt lgkmcnt(3)\n" \
    "v_mfma_f32_32x32x16_bf16 v[64:79], v[80:83], v[84:87], v[64:79]\n" \
    "s_waitcnt lgkmcnt(0)\n" \
    "v_mfma_f32_32x32x16_bf16 v[64:79], v[88:91], v[92:95], v[64:79]\n" \
    "v_mov_b64 v[188:189], 0\n" \
    "v_mov_b64 v[190:191], 0\n" \
    "v_mov_b64 v[232:233], -1\n" \
    "v_mov_b64 v[234:235], -1\n" \
    "s_nop 7\n" \
    "v_cvt_pk_bf16_f32 v80, v64, v65\n" \
    "v_cvt_pk_bf16_f32 v81, v66, v67\n" \
    "v_cvt_pk_bf16_f32 v82, v68, v69\n" \
    "v_cvt_pk_bf16_f32 v83, v70, v71\n" \
    "ds_write_b128 v46, v[80:83]\n" \
    "v_cvt_pk_bf16_f32 v84, v72, v73\n" \
    "v_cvt_pk_bf16_f32 v85, v74, v75\n" \
    "v_cvt_pk_bf16_f32 v86, v76, v77\n" \
    "v_cvt_pk_bf16_f32 v87, v78, v79\n" \
    "ds_write_b128 v47, v[84:87]\n" \
    "v_mfma_f32_32x32x16_bf16 v[112:127], v[188:191], v[188:191], 0\n" \
    "v_mfma_f32_32x32x16_bf16 v[128:143], v[188:191], v[188:191], 0\n" \
    "v_mfma_f32_32x32x16_bf16 v[144:159], v[188:191], v[188:191], 0\n" \
    "300:\n"

#define PRIO_MODE 1
#define POISON_AT 0
#define CODE_PF 1
#define F1 1
#define F2 1
#define TSMODE 0
#define NINI 10
#define GATH_AT 1
#define PRIO_START() do { if (w >= 4) __builtin_amdgcn_s_setprio(1); } while (0)
#define PRIO_END() __builtin_amdgcn_s_setprio(0)
__global__ void __launch_bounds__(512, 2)
fused_kernel(const u16* x_, const u16* wq_, const u16* wk_, const u16* wv_, const u16* wo_, const u16* wr_, const u16* wg_,
             const u16* wu_, const u16* wd_, const u16* gw_, u16* out_, char* ws_)
{
    __shared__ __attribute__((aligned(1024))) char smem[L_END];
    LDSP char* lds = (LDSP char*)smem;
    const u32 lb = (u32)(size_t)lds;
    const int tid = threadIdx.x;
    const int b = blockIdx.x;
    u32x16 ka0; u32x8 ka1;
    int w;
    u32x16 EX, PA; u32x4 KV4, WOA;
    u32 skv, sg, swo, skv3;
    f32x16 SX0, SX1, SY0, SY1, OX, OY;
    u32x4 K0, K1, V00, V01, V10, V11, PZV;
#define MT_OUT
#define MT_CLOB
#define MSTAMP(k)
#define V8(a) "v" #a "0", "v" #a "1", "v" #a "2", "v" #a "3", "v" #a "4", "v" #a "5", "v" #a "6", "v" #a "7", "v" #a "8", "v" #a "9"
#define S8(a) "s" #a "0", "s" #a "1", "s" #a "2", "s" #a "3", "s" #a "4", "s" #a "5", "s" #a "6", "s" #a "7", "s" #a "8", "s" #a "9"
#define DBG_CLOB
    {
        const void* kp = (const void*)__builtin_amdgcn_kernarg_segment_ptr();
        asm volatile(FRONT
            : "=&{s[4:19]}"(ka0), "=&{s[20:27]}"(ka1), [w] "=&s"(w),
              "=&{v[32:47]}"(EX), "=&{v[192:195]}"(KV4), "=&{v[236:239]}"(WOA), "=&{v[240:255]}"(PA),
              [skv] "=&s"(skv), [sg] "=&s"(sg), [swo] "=&s"(swo), [skv3] "=&s"(skv3),
              "=&{v[112:127]}"(SY1), "=&{v[128:143]}"(OX), "=&{v[144:159]}"(OY), "=&{v[188:191]}"(V11), "=&{v[232:235]}"(PZV) MT_OUT
            : [kp] "s"(kp), [b] "s"(b), [tid] "v"(tid)
            : "memory", "scc", "vcc", "s62", "s63", "s64", "s65", "s66", "s67", "s68", "s69", S8(7),
              "v14", "v15", "v16", "v17", "v18", "v19", V8(2), "v30", "v31", "v48", "v49", V8(5), V8(6), V8(7), V8(8), V8(9), V8(10), "v110", "v111",
              V8(16), V8(17), "v180", "v181", "v182", "v183", "v184", "v185", "v186", "v187",
              "v196", "v197", "v198", "v199", V8(20), V8(21), V8(22), "v230", "v231" MT_CLOB DBG_CLOB);
    }
#define KP(v, i) ((u64)v[2 * (i)] | ((u64)v[2 * (i) + 1] << 32))
    const u16* x  = (const u16*)KP(ka0, 0);
    const u16* wo = (const u16*)KP(ka0, 4);
    const u16* wd = (const u16*)KP(ka1, 0);
    u16* out = (u16*)KP(ka1, 2); char* ws = (char*)KP(ka1, 3);
    asm volatile("" : "+s"(x), "+s"(wo), "+s"(wd), "+s"(out), "+s"(ws));
    const int lane = tid & 63;
    const int xcd = b & 7, jj = b >> 3;
    const int tb = xcd * 4 + (jj >> 3);
    const int l15 = lane & 15;
    const bool ffn = jj < 28;
    const int fj = xcd * 28 + min(jj, 27);
    u32 epoch;
    u32 cta = lb + L_CT;
    const int tq = w & 3, kh = w >> 2;
    BAR();
    STAMP(10); STAMPC(19);
    MSTAMP(2);

    u32x4 qx, qy;
    u32 kq;
    {
        asm volatile(
            "ds_read_b128 v[168:171], v193\n"
            "ds_read_b128 v[160:163], v47\n"
            "ds_read_b128 v[164:167], v46\n"
            "ds_read_b128 v[172:175], v193 offset:1024\n"
            "ds_read_b128 v[176:179], v43\n"
            "ds_read_b128 v[180:183], v43 offset:512\n"
            "ds_read_b32 %[kq], %[cta] offset:48\n"
            "s_waitcnt lgkmcnt(5)\n"
            "v_mfma_f32_32x32x16_bf16 v[64:79], v[168:171], v[160:163], 0\n"
            "s_waitcnt lgkmcnt(4)\n"
            "v_mfma_f32_32x32x16_bf16 v[96:111], v[168:171], v[164:167], 0\n"
            "s_waitcnt lgkmcnt(0)\n"
            "s_nop 7\n s_nop 3\n"
            : "={v[64:79]}"(SX0), "={v[80:95]}"(SX1), "={v[96:111]}"(SY0),
              "={v[168:171]}"(K0), "={v[172:175]}"(K1), "={v[176:179]}"(V00), "={v[180:183]}"(V01),
              "={v[184:187]}"(V10), "=&{v[160:163]}"(qx), "=&{v[164:167]}"(qy), [kq] "=&v"(kq),
              "+{v[112:127]}"(SY1), "+{v[128:143]}"(OX), "+{v[144:159]}"(OY), "+{v[188:191]}"(V11)
            : "{v[192:195]}"(KV4), "{v[32:47]}"(EX), [cta] "v"(cta)
            : "memory");
    }
    epoch = (u32)__builtin_amdgcn_readfirstlane((int)kq);
#define ATT_OPS \
            : "+{v[64:79]}"(SX0), "+{v[80:95]}"(SX1), "+{v[96:111]}"(SY0), "+{v[112:127]}"(SY1), \
              "+{v[128:143]}"(OX), "+{v[144:159]}"(OY), \
              "+{v[168:171]}"(K0), "+{v[172:175]}"(K1), "+{v[176:179]}"(V00), "+{v[180:183]}"(V01), \
              "+{v[184:187]}"(V10), "+{v[188:191]}"(V11), "+{v[192:195]}"(KV4), [cnt] "=&s"(cnt_)
#define ATT_INS : "{v[160:163]}"(qx), "{v[164:167]}"(qy)
    u32 cnt_;
    const char* kvb = ws + WS_K;
    PRIO_START();
    asm volatile(ATT_A ATT_OPS, "+{v[32:47]}"(EX), "+{v[236:239]}"(WOA), "+{v[240:255]}"(PA), "+{v[232:235]}"(PZV),
                 [skv] "+s"(skv), [sg] "+s"(sg)
                 ATT_INS, [kvb] "s"(kvb), [wsb] "s"(ws) : "memory", "scc");
    LSTAMP(4);
    BAR();
    LSTAMP(8);
    if (tid == 448) { u32 fo = WS_F2 + b * 4; GST32C(ws, fo, epoch); }
    if (b == 0 && tid == 256) { u32 z = 0; GST32C(ws, z, epoch); }
    PRIO_START();
    u32x16 EXD;
    u32 keys;
    asm volatile(ATT_B ATT_OPS, "+{v[32:47]}"(EX), "=&{v[204:219]}"(EXD), "+{v[236:239]}"(WOA), "+{v[240:255]}"(PA), [key] "=&s"(keys)
                 ATT_INS, [kvb] "s"(kvb), [skv3] "s"(skv3), [xb] "s"(x), [wdb] "s"(wd), [cta] "v"(cta)
                 : "memory", "scc", "v220", "v221", "v222", "v223", "v224", "v225", "v226", "v227", "v232", "v233", "v234", "v235");
    LSTAMP(9);
    BAR();
    LSTAMP(11);
    if (ffn && w < 4) {
        float sum = LDF(L_FP + l15 * 4);
#pragma unroll
        for (int i = 1; i < 8; ++i) sum += LDF(L_FP + i * 64 + l15 * 4);
        sum = bfr(sum);
        float up = __shfl(sum, (lane & 7) + 8);
        float gt = __shfl(sum, lane & 7);
        float hv = bfr(bfr(gt * sigm(gt)) * up);
        u32 hb = __float_as_uint(hv);
        float y0 = 0.f, y1 = 0.f;
#pragma unroll
        for (int i = 0; i < 8; ++i) {
            const float hs = __uint_as_float((u32)__builtin_amdgcn_readlane(hb, i));
            y0 += hs * lo16(EXD[8 + i]);
            y1 += hs * hi16(EXD[8 + i]);
        }
        u32x2 gr = {__float_as_uint(y0), __float_as_uint(y1)};
        gr[0] = gr[0] == POISON ? 0x7fc00000u : gr[0];
        gr[1] = gr[1] == POISON ? 0x7fc00000u : gr[1];
        u32 yo = WS_Y + fj * 2048 + (128 * w + 2 * lane) * 4;
        GST64C(ws, yo, gr);
    }
    LSTAMP(22);
    PRIO_START();
    {
        u32 fl, ft;
        asm("s_and_b32 %[t], %[key], 0xffff\n\ts_sub_u32 %[t], 0xffff, %[t]\n\ts_bfe_u32 %[t], %[t], 0x70004\n\ts_cmp_eq_u32 %[t], %[tq]\n\ts_cselect_b32 %[fl], 2, 0\n\ts_xor_b32 %[t], %[t], %[tq]\n\t"
            "s_cmp_lt_u32 %[t], 4\n\ts_cselect_b32 %[t], 1, 0\n\ts_or_b32 %[fl], %[fl], %[t]\n\ts_or_b32 %[fl], %[fl], %[kh]"
            : [fl] "=&s"(fl), [t] "=&s"(ft) : [key] "s"(keys), [tq] "s"(tb * 4 + tq), [kh] "s"(kh * 4) : "scc");
        const u32 cwo = lb + C_WO + kh * 4;
#define BE_CLOB "memory", "scc", "vcc", "s93", "s94", "s95", "s80", "s81", "s82", "s83", "s84", "s85", "s86", "s87", "s88", "s89", "s90", "s91", "s92", \
            "v0","v1","v2","v3","v4","v5","v6","v7","v8","v9","v10","v11","v12","v13","v14","v15","v16","v17","v18","v19","v20","v21","v22","v23","v24","v25","v26","v27","v28","v29","v30","v31", \
            "v32","v33","v34","v35","v36","v37","v38","v39","v40","v41","v42","v43","v44","v45","v46","v47","v48","v49","v50","v51","v52","v53","v54","v55","v56","v57","v58","v59","v60","v61","v62","v63", \
            "v196","v197","v198","v199","v200","v201","v202","v203","v204","v205","v206","v207","v208","v209","v210","v211","v212","v213","v214","v215","v216","v217","v218","v219", \
            "v220","v221","v222","v223","v224","v225","v226","v227","v228","v229","v230","v231","v232","v233","v234","v235"
#define BE_OUTS "+{v[64:79]}"(SX0), "+{v[80:95]}"(SX1), "+{v[96:111]}"(SY0), "+{v[112:127]}"(SY1), "+{v[128:143]}"(OX), "+{v[144:159]}"(OY), \
              "+{v[168:171]}"(K0), "+{v[172:175]}"(K1), "+{v[176:179]}"(V00), "+{v[180:183]}"(V01), "+{v[184:187]}"(V10), "+{v[188:191]}"(V11), \
              "+{v[192:195]}"(KV4), "+{v[236:239]}"(WOA), "+{v[240:255]}"(PA), [swo] "+s"(swo)
#define MT_IN
#define BE_INS ATT_INS MT_IN, [wob] "s"(wo), [wsb] "s"(ws), [xb] "s"(x), [ob] "s"(out), [ep] "s"(epoch), [fl] "s"(fl), [key] "s"(keys), [cwo] "s"(cwo)
        asm volatile(ATT_C : BE_OUTS BE_INS : BE_CLOB);
    }
}

extern "C" void kernel_launch(void* const* d_in, const int* in_sizes, int n_in, void* d_out, int out_size,
                              void* d_ws, size_t ws_size, hipStream_t stream) {
    (void)in_sizes; (void)n_in; (void)out_size; (void)ws_size;
    fused_kernel<<<256, 512, 0, stream>>>((const u16*)d_in[0], (const u16*)d_in[1], (const u16*)d_in[2], (const u16*)d_in[3],
                                          (const u16*)d_in[4], (const u16*)d_in[6], (const u16*)d_in[7], (const u16*)d_in[8],
                                          (const u16*)d_in[9], (const u16*)d_in[10], (u16*)d_out, (char*)d_ws);
}
```
